```python
import torch

def _softmax(x, axis=-1):
    return torch.softmax(x, dim=axis)
def _gelu(x):
    return torch.nn.functional.gelu(x, approximate="tanh")
def _silu(x):
    return torch.nn.functional.silu(x)
def _sigmoid(x):
    return torch.sigmoid(x)
def _layernorm(x, axis=-1, eps=1e-5):
    m = x.mean(dim=axis, keepdim=True)
    v = x.var(dim=axis, keepdim=True, unbiased=False)
    return (x - m) / torch.sqrt(v + eps)
def _scatter_add(tgt, idx, src, axis=0):
    return tgt.clone().index_add_(axis, idx, src)
def _segment_sum(src, seg, n):
    out = torch.zeros((n,) + src.shape[1:], dtype=src.dtype)
    return out.index_add_(0, seg, src)
def _pad(x, pw):
    flat = tuple(v for pair in reversed(pw) for v in pair)
    return torch.nn.functional.pad(x, flat)

kernel_name = 'sequential_preLN__spectral_fnet__swiglu_ffn__prompt'

def setup_inputs():

    _w = (lambda a: (lambda t: t.to(torch.bfloat16) if t.is_floating_point() else t)(torch.as_tensor(a)))
    def _A_setup():
        B, S, D = 1, 2048, 1024
        ns = torch.arange(S)[:, None] * torch.arange(S)[None, :]
        nd = torch.arange(D)[:, None] * torch.arange(D)[None, :]
        Ws_re = torch.cos(-2.0 * torch.pi * ns / S).to(float)
        Ws_im = torch.sin(-2.0 * torch.pi * ns / S).to(float)
        Wd_re = torch.cos(-2.0 * torch.pi * nd / D).to(float)
        Wd_im = torch.sin(-2.0 * torch.pi * nd / D).to(float)
        return {
            "x": _w((lambda *s: torch.randn(*s, dtype=torch.bfloat16) * 0.05)(B, S, D)),
            "dft_seq_re": _w(Ws_re),
            "dft_seq_im": _w(Ws_im),
            "dft_hid_re": _w(Wd_re),
            "dft_hid_im": _w(Wd_im),
        }
    _A_in = _A_setup()

    def _B_setup():
        B, S, D, F = 1, 2048, 1024, 3584
        return {
            "x": _w((lambda *s: torch.randn(*s, dtype=torch.bfloat16) * 0.05)(B, S, D)),
            "w_gate": _w((lambda *s: torch.randn(*s, dtype=torch.bfloat16) * 0.2)(D, F)),
            "w_up": _w((lambda *s: torch.randn(*s, dtype=torch.bfloat16) * 0.2)(D, F)),
            "w_down": _w((lambda *s: torch.randn(*s, dtype=torch.bfloat16) * 0.2)(F, D)),
        }
    _B_in = _B_setup()

    return {
        "A_x": _A_in["x"],
        "A_dft_seq_re": _A_in["dft_seq_re"],
        "A_dft_seq_im": _A_in["dft_seq_im"],
        "A_dft_hid_re": _A_in["dft_hid_re"],
        "A_dft_hid_im": _A_in["dft_hid_im"],
        "B_x": _B_in["x"],
        "B_w_gate": _B_in["w_gate"],
        "B_w_up": _B_in["w_up"],
        "B_w_down": _B_in["w_down"],
    }

def reference(A_x, A_dft_seq_re, A_dft_seq_im, A_dft_hid_re, A_dft_hid_im, B_x, B_w_gate, B_w_up, B_w_down):
    def _A_ref(x, dft_seq_re, dft_seq_im, dft_hid_re, dft_hid_im):
        a = torch.einsum("bsd,ed->bse", x, dft_hid_re)
        b = torch.einsum("bsd,ed->bse", x, dft_hid_im)
        y = torch.einsum("ts,bse->bte", dft_seq_re, a) - torch.einsum("ts,bse->bte", dft_seq_im, b)
        return y

    def _B_ref(x, w_gate, w_up, w_down):
        g = _silu(x @ w_gate)
        u = x @ w_up
        return (g * u) @ w_down

    x = A_x
    h = x + _A_ref(_layernorm(x, axis=-1), A_dft_seq_re, A_dft_seq_im, A_dft_hid_re, A_dft_hid_im)
    h2 = h + _B_ref(_layernorm(h, axis=-1), B_w_gate, B_w_up, B_w_down)
    return h2
```

```cpp
#include <hip/hip_runtime.h>
#include <hip/hip_bf16.h>
#include <type_traits>

#define LAS __attribute__((address_space(3)))
typedef unsigned short u16;
typedef __bf16 bf16x8 __attribute__((ext_vector_type(8)));
typedef __bf16 bf16x4 __attribute__((ext_vector_type(4)));
typedef __bf16 bf16x2 __attribute__((ext_vector_type(2)));
typedef short s16x4 __attribute__((ext_vector_type(4)));
typedef float f32x4 __attribute__((ext_vector_type(4)));
typedef float f32x2 __attribute__((ext_vector_type(2)));
typedef unsigned u32x4 __attribute__((ext_vector_type(4)));
typedef unsigned u32x2 __attribute__((ext_vector_type(2)));

constexpr int SEQ = 2048, DM = 1024, FF = 3584;
constexpr float LN_EPS = 1e-5f;

struct Params {
    const u16* x; const u16* cre; const u16* cim; const u16* wre; const u16* wim;
    const u16* wg; const u16* wu; const u16* wd;
    u16* out; u16* abT; u16* h; u16* act; float* stats;
};

template <int N> __device__ __forceinline__ void wait_vm() { asm volatile("s_waitcnt vmcnt(%0)" ::"n"(N) : "memory"); }
__device__ __forceinline__ void wait_lgkm0() { __builtin_amdgcn_s_waitcnt(0xC07F); asm volatile("s_waitcnt lgkmcnt(0)" ::: "memory"); }
__device__ __forceinline__ void sched0() { __builtin_amdgcn_sched_barrier(0); }
__device__ __forceinline__ void bar() { __builtin_amdgcn_s_barrier(); asm volatile("" ::: "memory"); sched0(); }
__device__ __forceinline__ unsigned pack_bf16(float lo, float hi) { bf16x2 c; c[0] = (__bf16)lo; c[1] = (__bf16)hi; return __builtin_bit_cast(unsigned, c); }
__device__ __forceinline__ float bf_lo(unsigned v) { return __uint_as_float(v << 16); }
__device__ __forceinline__ float bf_hi(unsigned v) { return __uint_as_float(v & 0xffff0000u); }
__device__ __forceinline__ void swap16(float& a, float& b) { auto r = __builtin_amdgcn_permlane16_swap(__float_as_uint(a), __float_as_uint(b), false, false); a = __uint_as_float(r[0]); b = __uint_as_float(r[1]); }
__device__ __forceinline__ void swap32(float& a, float& b) { auto r = __builtin_amdgcn_permlane32_swap(__float_as_uint(a), __float_as_uint(b), false, false); a = __uint_as_float(r[0]); b = __uint_as_float(r[1]); }
__device__ __forceinline__ void swap16u(unsigned& a, unsigned& b) { auto r = __builtin_amdgcn_permlane16_swap(a, b, false, false); a = r[0]; b = r[1]; }
__device__ __forceinline__ void swap32u(unsigned& a, unsigned& b) { auto r = __builtin_amdgcn_permlane32_swap(a, b, false, false); a = r[0]; b = r[1]; }
__device__ __forceinline__ float qsum(float v) { v += __shfl_xor(v, 16); v += __shfl_xor(v, 32); return v; }
__device__ __forceinline__ float dot_ones(bf16x8 a, float c) {
    const u32x4 w = __builtin_bit_cast(u32x4, a); const bf16x2 one = __builtin_bit_cast(bf16x2, 0x3f803f80u);
#pragma unroll
    for (int i = 0; i < 4; ++i) { const unsigned wi = w[i]; c = __builtin_amdgcn_fdot2_f32_bf16(__builtin_bit_cast(bf16x2, wi), one, c, false); }
    return c;
}
__device__ __forceinline__ float dot_self(bf16x8 a, float c) {
    const u32x4 w = __builtin_bit_cast(u32x4, a);
#pragma unroll
    for (int i = 0; i < 4; ++i) { const unsigned wi = w[i]; const bf16x2 b = __builtin_bit_cast(bf16x2, wi); c = __builtin_amdgcn_fdot2_f32_bf16(b, b, c, false); }
    return c;
}

struct Frag { u32x2 lo, hi; __device__ __forceinline__ bf16x8 v() const { return __builtin_bit_cast(bf16x8, __builtin_shufflevector(lo, hi, 0, 1, 2, 3)); } };
template <int OFF> __device__ __forceinline__ u32x2 tr_read(unsigned addr) { u32x2 r; asm volatile("ds_read_b64_tr_b16 %0, %1 offset:%2" : "=v"(r) : "v"(addr), "n"(OFF) : "memory"); return r; }

template <int ID> struct Cfg;
template <> struct Cfg<1> { static constexpr int BM = 128, BN = 128, WM = 2, WN = 2, WK = 2, NS = 4, NK = 16, LDA = DM, LDB = DM, XM = 4, XN = 8, ADV = 4, XOFF = 2; static constexpr bool ATR = false; };
template <> struct Cfg<2> { static constexpr int BM = 128, BN = 64, WM = 2, WN = 2, WK = 2, NS = 5, NK = 64, LDA = 2 * SEQ, LDB = SEQ, XM = 4, XN = 8, ADV = 4, XOFF = 8; static constexpr bool ATR = false; };
template <> struct Cfg<3> { static constexpr int BM = 224, BN = 256, WM = 2, WN = 4, WK = 1, NS = 2, NK = 16, LDA = FF, LDB = DM, XM = 8, XN = 4, ADV = 4, XOFF = 2; static constexpr bool ATR = true; };
template <> struct Cfg<4> { static constexpr int BM = 128, BN = 64, WM = 2, WN = 2, WK = 2, NS = 5, NK = 56, LDA = DM, LDB = FF, XM = 4, XN = 8, ADV = 4, XOFF = 7; static constexpr bool ATR = true; };

template <int ID>
__global__ __launch_bounds__(512, 2) void gemm_k(const Params p) {
#if defined(__HIP_DEVICE_COMPILE__)
    using C = Cfg<ID>;
    constexpr int BM = C::BM, BN = C::BN, WM = C::WM, WN = C::WN, WK = C::WK, NS = C::NS, NK = C::NK;
    constexpr int FA = BM / WM / 16, FB = BN / WN / 16;
    constexpr int A_BYTES = (ID == 3) ? 32768 : BM * 128, B_BYTES = BN * 128, STAGE = A_BYTES + B_BYTES;
    constexpr int PA = A_BYTES / 1024 / 8, PB = B_BYTES / 1024 / 8, PW = PA + PB;
    constexpr int PER = C::XM < C::XN ? C::XM : C::XN;
    constexpr int LDS_BYTES = NS * STAGE + (ID == 3 ? 8192 : 0);
    static_assert(WM * WN * WK == 8 && PA * 8192 == A_BYTES && PB * 8192 == B_BYTES && NK % PER == 0 && NK % 2 == 0, "");
    __shared__ __attribute__((aligned(16))) unsigned char lds_raw[LDS_BYTES];
    LAS unsigned char* lds = (LAS unsigned char*)lds_raw;

    const int tid = threadIdx.x, lane = tid & 63, wid = __builtin_amdgcn_readfirstlane(tid >> 6);
    const int wk = wid % WK, wm = (wid / WK) % WM, wn = wid / (WK * WM);
    const int l15 = lane & 15, q = lane >> 4;
    const int xcd = blockIdx.x & 7, idx = blockIdx.x >> 3, im = idx % C::XM, in = idx / C::XM;
    constexpr int TM = (ID == 1 ? SEQ : ID == 3 ? 2 * FF : DM) / BM, GBM = TM / C::XM;
    const int tm = (xcd % GBM) * C::XM + im, tn = (xcd / GBM) * C::XN + in;
    const int phi = (im + in) % PER, koff = (xcd * C::XOFF) % NK;

    const u16* aptr; const u16* bptr; const u16* bptr2 = nullptr; unsigned abytes, bbytes; int brow0 = tn * BN;
    if constexpr (ID == 1) { aptr = p.x; abytes = SEQ * DM * 2; bptr = tn < 8 ? p.wre : p.wim; bbytes = DM * DM * 2; brow0 = (tn & 7) * BN; }
    if constexpr (ID == 2) { aptr = p.abT; abytes = DM * 2 * SEQ * 2; bptr = p.cre; bptr2 = p.cim; bbytes = SEQ * SEQ * 2; }
    if constexpr (ID == 3) { aptr = wid < 4 ? p.wg : p.wu; abytes = DM * FF * 2; bptr = p.h; bbytes = SEQ * DM * 2; }
    if constexpr (ID == 4) { aptr = p.wd; abytes = FF * DM * 2; bptr = p.act; bbytes = SEQ * FF * 2; }
    unsigned voffA[PA], voffB;
    if constexpr (!C::ATR) {
#pragma unroll
        for (int i = 0; i < PA; ++i) voffA[i] = (unsigned)((tm * BM + (wid * PA + i) * 8 + (lane >> 3)) * C::LDA * 2 + (((lane & 7) ^ (lane >> 3)) << 4));
    } else if constexpr (ID == 3) {
#pragma unroll
        for (int i = 0; i < PA; ++i) { const int ii = (wid & 3) * PA + i, j = l15 ^ (q | (((ii >> 1) & 1) << 2) | ((wid >> 2) << 3));
            voffA[i] = j < 14 ? (unsigned)((ii * 4 + q) * C::LDA * 2 + tm * 224 + j * 16) : 0x7ffffff0u; }
    } else {
#pragma unroll
        for (int i = 0; i < PA; ++i) { const int ii = wid * PA + i, j = l15 ^ ((q | (((ii >> 1) & 1) << 2)) << 1);
            voffA[i] = (unsigned)((ii * 4 + q) * C::LDA * 2 + tm * BM * 2 + j * 16); }
    }
    voffB = (unsigned)((brow0 + wid * PB * 8 + (lane >> 3)) * C::LDB * 2 + (((lane & 7) ^ (lane >> 3)) << 4));

    auto stage = [&](int t, int slot) {
        const bool live = t < NK;
        int k = t + koff + ((t % PER) == phi ? C::ADV : 0); k = k >= NK ? k - NK : k; k = k >= NK ? k - NK : k;
        const auto ra = __builtin_amdgcn_make_buffer_rsrc((void*)aptr, 0, live ? abytes : 0u, 0x00020000);
        const u16* bp = bptr; int kb = k;
        if constexpr (ID == 2) { bp = k < NK / 2 ? bptr : bptr2; kb = k & (NK / 2 - 1); }
        const auto rb = __builtin_amdgcn_make_buffer_rsrc((void*)bp, 0, live ? bbytes : 0u, 0x00020000);
        const int sa = C::ATR ? k * 64 * C::LDA * 2 : k * 128;
        LAS unsigned char* base = lds + slot * STAGE;
#pragma unroll
        for (int i = 0; i < PA; ++i) __builtin_amdgcn_raw_ptr_buffer_load_lds(ra, (LAS void*)(base + (wid * PA + i) * 1024), 16, voffA[i], sa, 0, 0);
#pragma unroll
        for (int i = 0; i < PB; ++i) __builtin_amdgcn_raw_ptr_buffer_load_lds(rb, (LAS void*)(base + A_BYTES + (wid * PB + i) * 1024), 16, voffB, kb * 128 + i * 8 * C::LDB * 2, 0, 0);
    };

    const int rotA = ID == 1 ? 2 * wn : 0, rotB = ID == 1 ? 2 * wm : 0;
    unsigned adA[FA], adB[FB];
    const unsigned kc_sw = (unsigned)(l15 * 128 + (((q + (WK == 2 ? wk * 4 : 0)) ^ (lane & 7)) << 4));
#pragma unroll
    for (int m = 0; m < FA; ++m) {
        if constexpr (!C::ATR) adA[m] = kc_sw + (unsigned)((wm * FA + ((m + rotA) % FA)) * 2048);
        else { const int q4 = l15 >> 2, pp = lane & 3, kr = 8 * q + q4 + (WK == 2 ? wk * 32 : 0);
            if constexpr (ID == 3) adA[m] = (unsigned)((pp >> 1) * 16384 + kr * 256 + (((wm * FA + m) ^ (q4 | ((q & 1) << 2) | ((pp >> 1) << 3))) << 4) + 8 * (pp & 1));
            else adA[m] = (unsigned)(kr * 256 + (((wm * FA + m) ^ (q4 | ((q & 1) << 2))) << 5) + 8 * pp); }
    }
#pragma unroll
    for (int n = 0; n < FB; ++n) adB[n] = (unsigned)A_BYTES + kc_sw + (unsigned)((wn * FB + ((n + rotB) % FB)) * 2048);

    const unsigned lds0 = (unsigned)(size_t)lds;
    auto ldfrag = [&](Frag (&ra)[FA], bf16x8 (&rb)[FB], int slot, auto kkc) {
        constexpr int kk = decltype(kkc)::value;
        const unsigned so = (unsigned)(slot * STAGE);
#pragma unroll
        for (int n = 0; n < FB; ++n) rb[n] = *(const LAS bf16x8*)(lds + so + (kk ? adB[n] ^ 64u : adB[n]));
#pragma unroll
        for (int m = 0; m < FA; ++m) {
            if constexpr (!C::ATR) { const u32x4 w = *(const LAS u32x4*)(lds + so + (kk ? adA[m] ^ 64u : adA[m])); ra[m].lo = (u32x2){w[0], w[1]}; ra[m].hi = (u32x2){w[2], w[3]}; }
            else { const unsigned ad = lds0 + so + adA[m]; ra[m].lo = tr_read<kk * 8192>(ad); ra[m].hi = tr_read<kk * 8192 + 1024>(ad); }
        }
    };

    f32x4 acc[FA][FB];
#pragma unroll
    for (int m = 0; m < FA; ++m)
#pragma unroll
        for (int n = 0; n < FB; ++n) acc[m][n] = (f32x4){0.f, 0.f, 0.f, 0.f};
    float st[ID == 3 ? FA : 6];
#pragma unroll
    for (int i = 0; i < (ID == 3 ? FA : 6); ++i) st[i] = 0.f;

    auto mma = [&](const Frag (&ra)[FA], const bf16x8 (&rb)[FB], int t) {
#pragma unroll
        for (int m = 0; m < FA; ++m)
#pragma unroll
            for (int n = 0; n < FB; ++n) acc[m][n] = __builtin_amdgcn_mfma_f32_16x16x32_bf16(ra[m].v(), rb[n], acc[m][n], 0, 0, 0);
        if constexpr (ID == 1) { st[0] = dot_ones(ra[0].v(), st[0]); st[1] = dot_ones(ra[1].v(), st[1]); st[2] = dot_self(ra[0].v(), st[2]); st[3] = dot_self(ra[1].v(), st[3]);
                                 st[4] = dot_ones(rb[0], st[4]); st[5] = dot_ones(rb[1], st[5]); }
        if constexpr (ID == 3) { if ((t & 3) == wn) {
#pragma unroll
            for (int m = 0; m < FA; ++m) st[m] = dot_ones(ra[m].v(), st[m]); } }
    };

    Frag a0[FA], a1[FA]; bf16x8 b0[FB], b1[FB];
    constexpr std::integral_constant<int, 0> K0{}; constexpr std::integral_constant<int, 1> K1{};
#pragma unroll
    for (int s = 0; s < NS; ++s) stage(s, s);
    wait_vm<(NS - 1) * PW>(); bar();
    ldfrag(a0, b0, 0, K0);
    wait_lgkm0(); sched0();
    int slot = 0;
    if constexpr (WK == 2) {
        for (int t = 0; t < NK; t += 2) {
            int nx = slot + 1 == NS ? 0 : slot + 1;
            wait_vm<(NS - 2) * PW>(); bar();
            stage(t + NS, slot); ldfrag(a1, b1, nx, K0); mma(a0, b0, t);
            sched0(); wait_lgkm0(); sched0();
            slot = nx; nx = slot + 1 == NS ? 0 : slot + 1;
            wait_vm<(NS - 2) * PW>(); bar();
            stage(t + 1 + NS, slot); ldfrag(a0, b0, nx, K0); mma(a1, b1, t + 1);
            sched0(); wait_lgkm0(); sched0();
            slot = nx;
        }
    } else {
        for (int t = 0; t < NK; ++t) {
            const int nx = slot + 1 == NS ? 0 : slot + 1;
            ldfrag(a1, b1, slot, K1); mma(a0, b0, t);
            sched0(); wait_lgkm0(); sched0();
            wait_vm<(NS - 2) * PW>(); bar();
            stage(t + NS, slot); ldfrag(a0, b0, nx, K0); mma(a1, b1, t);
            sched0(); wait_lgkm0(); sched0();
            slot = nx;
        }
    }
    wait_lgkm0(); wait_vm<0>(); bar();

    if constexpr (WK == 2) {
        constexpr int HB = FB / 2;
        LAS f32x4* xch = (LAS f32x4*)lds;
        f32x4 fin[FA][HB];
#pragma unroll
        for (int m = 0; m < FA; ++m)
#pragma unroll
            for (int n = 0; n < HB; ++n) { xch[(wid * FA * HB + m * HB + n) * 64 + lane] = wk ? acc[m][n] : acc[m][n + HB]; fin[m][n] = wk ? acc[m][n + HB] : acc[m][n]; }
        LAS float* tab = (LAS float*)(lds + 8 * FA * HB * 1024);
        if constexpr (ID == 1) {
#pragma unroll
            for (int i = 0; i < 6; ++i) st[i] = qsum(st[i]);
            if (q == 0) {
#pragma unroll
                for (int i = 0; i < 2; ++i) { const int ra = wm * 64 + (rotA + i) * 16 + l15, rb = wn * 64 + (rotB + i) * 16 + l15;
                    tab[wk * 384 + ra] = st[i]; tab[wk * 384 + 128 + ra] = st[2 + i]; tab[wk * 384 + 256 + rb] = st[4 + i]; }
            }
        }
        __syncthreads();
#pragma unroll
        for (int m = 0; m < FA; ++m)
#pragma unroll
            for (int n = 0; n < HB; ++n) fin[m][n] += xch[((wid ^ 1) * FA * HB + m * HB + n) * 64 + lane];
        const int eo = (q & 1) * 16 + (q >> 1) * 8;
        if constexpr (ID == 1) {
            const float sgn = tn < 8 ? 1.f : -1.f;
#pragma unroll
            for (int m = 0; m < FA; ++m) {
                const int r0 = wm * 64 + ((m + rotA) & 3) * 16 + 4 * q;
                const f32x4 s1 = *(const LAS f32x4*)(tab + r0) + *(const LAS f32x4*)(tab + 384 + r0), s2 = *(const LAS f32x4*)(tab + 128 + r0) + *(const LAS f32x4*)(tab + 384 + 128 + r0);
                f32x4 mean = s1 * (1.f / DM), rs;
#pragma unroll
                for (int j = 0; j < 4; ++j) rs[j] = sgn * __builtin_amdgcn_rsqf(fmaxf(s2[j] * (1.f / DM) - mean[j] * mean[j], 0.f) + LN_EPS);
#pragma unroll
                for (int n = 0; n < HB; ++n) { const int rb = wn * 64 + ((n + wk * HB + rotB) & 3) * 16 + l15; const float ws = tab[256 + rb] + tab[384 + 256 + rb];
                    fin[m][n] = (fin[m][n] - mean * ws) * rs; }
            }
#pragma unroll
            for (int n = 0; n < HB; ++n) { const int e = (tn & 7) * BN + wn * 64 + ((n + wk * HB + rotB) & 3) * 16 + l15;
#pragma unroll
                for (int mp = 0; mp < FA / 2; ++mp) {
                    unsigned x0 = pack_bf16(fin[2 * mp][n][0], fin[2 * mp][n][1]), x1 = pack_bf16(fin[2 * mp][n][2], fin[2 * mp][n][3]);
                    unsigned y0 = pack_bf16(fin[2 * mp + 1][n][0], fin[2 * mp + 1][n][1]), y1 = pack_bf16(fin[2 * mp + 1][n][2], fin[2 * mp + 1][n][3]);
                    swap16u(x0, y0); swap16u(x1, y1);
                    const int s = tm * BM + wm * 64 + ((2 * mp + rotA) & 3) * 16 + eo;
                    *(u32x4*)(p.abT + (size_t)e * (2 * SEQ) + (tn >> 3) * SEQ + s) = (u32x4){x0, x1, y0, y1};
                } }
        } else {
            const u16* res = ID == 2 ? p.x : p.h; u16* dst = ID == 2 ? p.h : p.out;
#pragma unroll
            for (int n = 0; n < HB; ++n) { const int t = tn * BN + wn * (FB * 16) + (n + wk * HB) * 16 + l15;
                float v[FA / 2][8];
#pragma unroll
                for (int mp = 0; mp < FA / 2; ++mp) {
                    const size_t off = (size_t)t * DM + tm * BM + wm * 64 + mp * 32 + eo;
                    const u32x4 r = *(const u32x4*)(res + off);
                    f32x4 lo = fin[2 * mp][n], hi = fin[2 * mp + 1][n];
#pragma unroll
                    for (int j = 0; j < 4; ++j) { float a = lo[j], b = hi[j]; swap16(a, b); lo[j] = a; hi[j] = b; }
                    u32x4 o; o[0] = pack_bf16(lo[0] + bf_lo(r[0]), lo[1] + bf_hi(r[0])); o[1] = pack_bf16(lo[2] + bf_lo(r[1]), lo[3] + bf_hi(r[1]));
                    o[2] = pack_bf16(hi[0] + bf_lo(r[2]), hi[1] + bf_hi(r[2])); o[3] = pack_bf16(hi[2] + bf_lo(r[3]), hi[3] + bf_hi(r[3]));
                    *(u32x4*)(dst + off) = o;
#pragma unroll
                    for (int j = 0; j < 4; ++j) { v[mp][2 * j] = bf_lo(o[j]); v[mp][2 * j + 1] = bf_hi(o[j]); }
                }
                if constexpr (ID == 2) {
                    float s = 0.f;
#pragma unroll
                    for (int mp = 0; mp < FA / 2; ++mp)
#pragma unroll
                        for (int j = 0; j < 8; ++j) s += v[mp][j];
                    const float mw = qsum(s) * (1.f / 64.f); float m2 = 0.f;
#pragma unroll
                    for (int mp = 0; mp < FA / 2; ++mp)
#pragma unroll
                        for (int j = 0; j < 8; ++j) { const float d = v[mp][j] - mw; m2 += d * d; }
                    m2 = qsum(m2);
                    if (q == 0) *(f32x2*)(p.stats + ((size_t)t * 16 + tm * 2 + wm) * 2) = (f32x2){mw, m2};
                }
            }
        }
    } else {
        LAS float* cs = (LAS float*)lds;
        LAS f32x2* rowst = (LAS f32x2*)(lds + 4096);
#pragma unroll
        for (int m = 0; m < FA; ++m) { const float s = qsum(st[m]); if (q == 0) cs[wn * 224 + (wm * FA + m) * 16 + l15] = s; }
        if (tid < 256) {
            const f32x4* sp = (const f32x4*)(p.stats + (size_t)(tn * BN + tid) * 32); f32x4 w[8]; float ms = 0.f, m2 = 0.f;
#pragma unroll
            for (int i = 0; i < 8; ++i) { w[i] = sp[i]; ms += w[i][0] + w[i][2]; m2 += w[i][1] + w[i][3]; }
            const float mean = ms * (1.f / 16.f); float dd = 0.f;
#pragma unroll
            for (int i = 0; i < 8; ++i) { const float d0 = w[i][0] - mean, d1 = w[i][2] - mean; dd += d0 * d0 + d1 * d1; }
            rowst[tid] = (f32x2){mean, __builtin_amdgcn_rsqf((m2 + 64.f * dd) * (1.f / DM) + LN_EPS)};
        }
        __syncthreads();
        f32x4 csum[FA];
#pragma unroll
        for (int m = 0; m < FA; ++m) { const LAS float* c0 = cs + (wm * FA + m) * 16 + 4 * q;
            csum[m] = (*(const LAS f32x4*)c0 + *(const LAS f32x4*)(c0 + 224)) + (*(const LAS f32x4*)(c0 + 448) + *(const LAS f32x4*)(c0 + 672)); }
#pragma unroll
        for (int n = 0; n < FB; ++n) {
            const int tl = wn * 64 + n * 16 + l15; const f32x2 rs = rowst[tl];
            unsigned o[8];
#pragma unroll
            for (int m = 0; m < FA; ++m) {
                f32x4 v = (acc[m][n] - rs[0] * csum[m]) * rs[1];
                float g0 = v[0], g1 = v[1], u0 = v[2], u1 = v[3];
                swap32(g0, u0); swap32(g1, u1);
                const float s0 = g0 * __builtin_amdgcn_rcpf(1.f + __builtin_amdgcn_exp2f(-1.44269504f * g0)), s1 = g1 * __builtin_amdgcn_rcpf(1.f + __builtin_amdgcn_exp2f(-1.44269504f * g1));
                o[m] = pack_bf16(s0 * u0, s1 * u1);
            }
            o[7] = 0u;
            u16* dst = p.act + (size_t)(tn * BN + tl) * FF + tm * 112 + wm * 56;
#pragma unroll
            for (int g = 0; g < 2; ++g) {
                unsigned r0 = o[4 * g], r1 = o[4 * g + 1], r2 = o[4 * g + 2], r3 = o[4 * g + 3];
                swap16u(r0, r1); swap16u(r2, r3); swap32u(r0, r2); swap32u(r1, r3);
                if (g == 0 || q < 3) *(u32x4*)(dst + (4 * g + q) * 8) = (u32x4){r0, r2, r1, r3};
            }
        }
    }
#endif
}

extern "C" void kernel_launch(void* const* d_in, const int* in_sizes, int n_in, void* d_out, int out_size, void* d_ws, size_t ws_size, hipStream_t stream) {
    Params p;
    p.x = (const u16*)d_in[0]; p.cre = (const u16*)d_in[1]; p.cim = (const u16*)d_in[2]; p.wre = (const u16*)d_in[3]; p.wim = (const u16*)d_in[4];
    p.wg = (const u16*)d_in[6]; p.wu = (const u16*)d_in[7]; p.wd = (const u16*)d_in[8];
    p.out = (u16*)d_out;
    char* ws = (char*)d_ws;
    p.abT = (u16*)ws; p.h = (u16*)(ws + (8u << 20)); p.act = (u16*)(ws + (12u << 20)); p.stats = (float*)(ws + (28u << 20));
    gemm_k<1><<<256, 512, 0, stream>>>(p);
    gemm_k<2><<<256, 512, 0, stream>>>(p);
    gemm_k<3><<<256, 512, 0, stream>>>(p);
    gemm_k<4><<<256, 512, 0, stream>>>(p);
}
```

```cpp
#include <hip/hip_runtime.h>
#include <hip/hip_bf16.h>
#include <type_traits>

#define LAS __attribute__((address_space(3)))
typedef unsigned short u16;
typedef __bf16 bf16x8 __attribute__((ext_vector_type(8)));
typedef __bf16 bf16x2 __attribute__((ext_vector_type(2)));
typedef float f32x4 __attribute__((ext_vector_type(4)));
typedef float f32x2 __attribute__((ext_vector_type(2)));
typedef unsigned u32x4 __attribute__((ext_vector_type(4)));
typedef unsigned u32x2 __attribute__((ext_vector_type(2)));

constexpr int SEQ = 2048, DM = 1024, FF = 3584;
constexpr float LN_EPS = 1e-5f;
constexpr int SC1 = 16;

struct Params {
    const u16* x; const u16* cre; const u16* cim; const u16* wre; const u16* wim;
    const u16* wg; const u16* wu; const u16* wd;
    u16* out; u16* abT; u16* h; u16* act; u16* xch;
};

template <int N> __device__ __forceinline__ void wait_vm() { asm volatile("s_waitcnt vmcnt(%0)" ::"n"(N) : "memory"); }
__device__ __forceinline__ void wait_lgkm0() { __builtin_amdgcn_s_waitcnt(0xC07F); asm volatile("s_waitcnt lgkmcnt(0)" ::: "memory"); }
__device__ __forceinline__ void sched0() { __builtin_amdgcn_sched_barrier(0); }
__device__ __forceinline__ void bar() { sched0(); asm volatile("" ::: "memory"); __builtin_amdgcn_s_barrier(); asm volatile("" ::: "memory"); sched0(); }
__device__ __forceinline__ unsigned pack_bf16(float lo, float hi) { bf16x2 c; c[0] = (__bf16)lo; c[1] = (__bf16)hi; return __builtin_bit_cast(unsigned, c); }
__device__ __forceinline__ float bf_lo(unsigned v) { return __uint_as_float(v << 16); }
__device__ __forceinline__ float bf_hi(unsigned v) { return __uint_as_float(v & 0xffff0000u); }
__device__ __forceinline__ void swap16(float& a, float& b) { auto r = __builtin_amdgcn_permlane16_swap(__float_as_uint(a), __float_as_uint(b), false, false); a = __uint_as_float(r[0]); b = __uint_as_float(r[1]); }
__device__ __forceinline__ void swap32(float& a, float& b) { auto r = __builtin_amdgcn_permlane32_swap(__float_as_uint(a), __float_as_uint(b), false, false); a = __uint_as_float(r[0]); b = __uint_as_float(r[1]); }
__device__ __forceinline__ void swap16u(unsigned& a, unsigned& b) { auto r = __builtin_amdgcn_permlane16_swap(a, b, false, false); a = r[0]; b = r[1]; }
__device__ __forceinline__ void swap32u(unsigned& a, unsigned& b) { auto r = __builtin_amdgcn_permlane32_swap(a, b, false, false); a = r[0]; b = r[1]; }
__device__ __forceinline__ float qsum(float v) { float a = v, b = v; swap16(a, b); a += b; b = a; swap32(a, b); return a + b; }

struct Frag { u32x2 lo, hi; __device__ __forceinline__ bf16x8 v() const { return __builtin_bit_cast(bf16x8, __builtin_shufflevector(lo, hi, 0, 1, 2, 3)); } };
template <int OFF> __device__ __forceinline__ u32x2 tr_read(unsigned addr) { u32x2 r; asm volatile("ds_read_b64_tr_b16 %0, %1 offset:%2" : "=v"(r) : "v"(addr), "n"(OFF) : "memory"); return r; }
template <int OFF> __device__ __forceinline__ bf16x8 lds_read128(unsigned addr) { bf16x8 r; asm volatile("ds_read_b128 %0, %1 offset:%2" : "=v"(r) : "v"(addr), "n"(OFF) : "memory"); return r; }
template <int N> __device__ __forceinline__ void wait_lgkm() { asm volatile("s_waitcnt lgkmcnt(%0)" ::"n"(N) : "memory"); }
template <int I, int N, class F> __device__ __forceinline__ void static_for(F&& f) { if constexpr (I < N) { f(std::integral_constant<int, I>{}); static_for<I + 1, N>(f); } }
template <int NA> __device__ __forceinline__ void keep(const Frag (&a)[NA], const bf16x8 (&b)[4]) {
#pragma unroll
    for (int i = 0; i < NA; ++i) asm volatile("" ::"v"(a[i].lo), "v"(a[i].hi));
#pragma unroll
    for (int i = 0; i < 4; ++i) asm volatile("" ::"v"(b[i]));
}
__device__ __forceinline__ void vadd(float& a, float b) { asm volatile("v_add_f32 %0, %0, %1" : "+v"(a) : "v"(b)); }
__device__ __forceinline__ void bump(unsigned& a, int d) { asm volatile("v_add_u32 %0, %1, %0" : "+v"(a) : "s"(d)); }
constexpr unsigned SENTINEL = 0xffffffffu;
#define MFMA(a, b, c) __builtin_amdgcn_mfma_f32_16x16x32_bf16(a, b, c, 0, 0, 0)
#define RSRC(ptr, bytes) __builtin_amdgcn_make_buffer_rsrc((void*)(ptr), 0, (bytes), 0x00020000)
#define DMA16(rs, ldsp, voff, soff) __builtin_amdgcn_raw_ptr_buffer_load_lds(rs, (LAS void*)(ldsp), 16, voff, soff, 0, 0)

#define STAMP(i)
#define STAMP_BEGIN
#define STAMP_END(ID)

template <int ID> struct Cfg;
template <> struct Cfg<1> { static constexpr int NK = 16, LDA = DM, LDB = DM, XOFF = 2; static constexpr bool ATR = false; };
template <> struct Cfg<2> { static constexpr int NK = 32, LDA = 2 * SEQ, LDB = SEQ, XOFF = 8; static constexpr bool ATR = false; };
template <> struct Cfg<4> { static constexpr int NK = 28, LDA = DM, LDB = FF, XOFF = 8; static constexpr bool ATR = true; };

template <int ID>
__global__ __launch_bounds__(512, 2) void gemm128_k(const Params p) {
#if defined(__HIP_DEVICE_COMPILE__)
    using C = Cfg<ID>;
    constexpr int NS = 4, NK = C::NK, STAGE = 32768, PW = 8, PER = 4, ADV = 4;
    __shared__ __attribute__((aligned(16))) unsigned char lds_raw[NS * STAGE + (ID == 1 ? 1536 : 0)];
    LAS unsigned char* lds = (LAS unsigned char*)lds_raw;
    STAMP_BEGIN;
    const int tid = threadIdx.x, lane = tid & 63, wid = __builtin_amdgcn_readfirstlane(tid >> 6);
    const int l15 = lane & 15, q = lane >> 4;
    const int xcd = blockIdx.x & 7, idx = blockIdx.x >> 3, im = idx & 3, in = idx >> 2;
    int tm, tn, ks = 0;
    if constexpr (ID == 1) { tm = (xcd & 3) * 4 + im; tn = (xcd >> 2) * 8 + in; }
    else { ks = xcd & 1; tm = ((xcd >> 1) & 1) * 4 + im; tn = (xcd >> 2) * 8 + in; }

    if (wid >= 4) {
        const int lw = wid - 4, phi = (im + in) & 3, koff = ((xcd >> (ID == 1 ? 0 : 1)) * C::XOFF) % NK;
        const u16* aptr; const u16* bptr; unsigned abytes, bbytes; int brow0 = tn * 128, ka0 = 0, kb0 = 0;
        if constexpr (ID == 1) { aptr = p.x; abytes = SEQ * DM * 2; bptr = tn < 8 ? p.wre : p.wim; bbytes = DM * DM * 2; brow0 = (tn & 7) * 128; }
        if constexpr (ID == 2) { aptr = p.abT; abytes = DM * 2 * SEQ * 2; bptr = ks ? p.cim : p.cre; bbytes = SEQ * SEQ * 2; ka0 = ks * NK; }
        if constexpr (ID == 4) { aptr = p.wd; abytes = FF * DM * 2; bptr = p.act; bbytes = SEQ * FF * 2; ka0 = kb0 = ks * NK; }
        const unsigned ksw = (unsigned)(((lane & 7) ^ (lane >> 3)) << 4);
        unsigned voffA[2];
        if constexpr (!C::ATR) voffA[0] = voffA[1] = (unsigned)((tm * 128 + lw * 32 + (lane >> 3)) * C::LDA * 2) + ksw;
        else {
#pragma unroll
            for (int i = 0; i < 2; ++i) voffA[i] = (unsigned)((lw * 16 + q) * C::LDA * 2 + tm * 256 + ((l15 ^ ((q | (i << 2)) << 1)) << 4));
        }
        const unsigned voffB = (unsigned)((brow0 + lw * 32 + (lane >> 3)) * C::LDB * 2) + ksw;
        auto stage = [&](int t, int slot) {
            const bool live = t < NK;
            int k = t + koff + ((t & (PER - 1)) == phi ? ADV : 0); k = k >= NK ? k - NK : k; k = k >= NK ? k - NK : k;
            const auto ra = RSRC(aptr, live ? abytes : 0u); const auto rb = RSRC(bptr, live ? bbytes : 0u);
            LAS unsigned char* base = lds + slot * STAGE + lw * 4096;
#pragma unroll
            for (int i = 0; i < 4; ++i) {
                if constexpr (!C::ATR) DMA16(ra, base + i * 1024, voffA[0], (ka0 + k) * 128 + i * 8 * C::LDA * 2);
                else DMA16(ra, base + i * 1024, voffA[i >> 1], ((ka0 + k) * 64 + i * 4) * C::LDA * 2);
                DMA16(rb, base + 16384 + i * 1024, voffB, (kb0 + k) * 128 + i * 8 * C::LDB * 2);
            }
        };
        stage(0, 0); stage(1, 1);
        wait_vm<PW>(); bar();
        stage(2, 2);
        int slot = 3;
#pragma nounroll
        for (int t = 0; t < NK; ++t) {
            wait_vm<PW>(); bar();
            stage(t + 3, slot);
            slot = (slot + 1) & 3;
            if constexpr (ID != 4) { if (t == NK - 2) {
                const auto rx = RSRC(p.xch + (ID == 1 ? 0 : 256 * 8192), 256 * 16384);
#pragma unroll
                for (int m = 0; m < 4; ++m) __builtin_amdgcn_raw_buffer_store_b128((u32x4){SENTINEL, SENTINEL, SENTINEL, SENTINEL}, rx, (unsigned)(blockIdx.x * 16384 + (lw * 4 + m) * 1024 + lane * 16), 0, SC1);
            } }
        }
        if constexpr (ID == 1) bar();
        return;
    }

    const int wm = wid & 1, wn = wid >> 1;
    const int rotA = ID == 1 ? 2 * wn : 0, rotB = ID == 1 ? 2 * wm : 2 * ks;
    unsigned ad[8];
    const unsigned lds0 = (unsigned)(size_t)lds, kc_sw = lds0 + (unsigned)(l15 * 128 + ((q ^ (lane & 7)) << 4));
#pragma unroll
    for (int i = 0; i < 4; ++i) {
        if constexpr (!C::ATR) ad[i] = (kc_sw ^ (unsigned)((i >> 1) * 64)) + (unsigned)((wm * 4 + ((2 * (i & 1) + rotA) & 3)) * 2048);
        else { const int q4 = l15 >> 2; ad[i] = lds0 + (unsigned)((8 * q + q4) * 256 + (((wm * 4 + i) ^ (q4 | ((q & 1) << 2))) << 5) + 8 * (lane & 3)); }
        ad[4 + i] = 16384u + (kc_sw ^ (unsigned)((i >> 1) * 64)) + (unsigned)((wn * 4 + ((2 * (i & 1) + rotB) & 3)) * 2048);
    }
    constexpr int NR = C::ATR ? 12 : 8;
    auto rd = [&](auto ic, Frag (&ra)[4], bf16x8 (&rb)[4], auto kkc) {
        constexpr int kk = decltype(kkc)::value, i = decltype(ic)::value;
        constexpr int W = C::ATR ? 2 : 1;
        constexpr bool isB = i == 0 || (i > W && i <= W + 3);
        if constexpr (isB) { constexpr int n = i == 0 ? 0 : i - W; rb[n] = lds_read128<(n & 1) * 2048>(ad[4 + kk * 2 + (n >> 1)]); }
        else { constexpr int j = i <= W ? i - 1 : i - 4, m = j / W;
            if constexpr (!C::ATR) { const u32x4 w = __builtin_bit_cast(u32x4, lds_read128<(m & 1) * 2048>(ad[kk * 2 + (m >> 1)])); ra[m].lo = (u32x2){w[0], w[1]}; ra[m].hi = (u32x2){w[2], w[3]}; }
            else if constexpr (j % 2 == 0) ra[m].lo = tr_read<kk * 8192>(ad[m]);
            else ra[m].hi = tr_read<kk * 8192 + 1024>(ad[m]); }
    };
    f32x4 acc[4][4], st[6];
#pragma unroll
    for (int m = 0; m < 4; ++m)
#pragma unroll
        for (int n = 0; n < 4; ++n) acc[m][n] = (f32x4){0.f, 0.f, 0.f, 0.f};
#pragma unroll
    for (int i = 0; i < 6; ++i) st[i] = (f32x4){0.f, 0.f, 0.f, 0.f};
    const bf16x8 ones = __builtin_bit_cast(bf16x8, (u32x4){0x3f803f80u, 0x3f803f80u, 0x3f803f80u, 0x3f803f80u});
    auto phase = [&](const Frag (&ra)[4], const bf16x8 (&rb)[4], Frag (&na)[4], bf16x8 (&nb)[4], int dstep, auto kkc) {
        static_for<0, 16>([&](auto sc) {
            constexpr int s = decltype(sc)::value, m = s >> 2, n = s & 3, W = C::ATR ? 2 : 1;
            if constexpr (n == 0) { wait_lgkm<(3 - m) * W + (s < NR ? s : NR)>(); sched0(); }
            acc[m][n] = MFMA(ra[m].v(), rb[n], acc[m][n]);
            if constexpr (s < NR) rd(sc, na, nb, kkc);
            if constexpr (s >= 12) {
                constexpr int kk = decltype(kkc)::value, j = s - 12;
                if constexpr (C::ATR) { if constexpr (kk == 1) { bump(ad[j], dstep); bump(ad[4 + j], dstep); } }
                else if constexpr (kk == 1) { bump(ad[j], dstep); bump(ad[4 + j], dstep); }
            }
            if constexpr (ID == 1 && s >= 10) {
                constexpr int i = s & 1;
                if constexpr (s < 12) st[i] = MFMA(ones, ra[i].v(), st[i]); else if constexpr (s < 14) st[2 + i] = MFMA(ra[i].v(), ra[i].v(), st[2 + i]); else st[4 + i] = MFMA(ones, rb[i], st[4 + i]); }
            sched0();
        });
    };
    Frag a0[4], a1[4]; bf16x8 b0[4], b1[4];
    constexpr std::integral_constant<int, 0> K0{}; constexpr std::integral_constant<int, 1> K1{};
    bar();
    STAMP(1);
    static_for<0, NR>([&](auto ic) { rd(ic, a0, b0, K0); });
    sched0();
#pragma nounroll
    for (int t = 0; t < NK; ++t) {
        phase(a0, b0, a1, b1, (t & 3) == 3 ? -3 * STAGE : STAGE, K1);
        bar();
        phase(a1, b1, a0, b0, 0, K0);
    }
    wait_lgkm0(); keep(a0, b0); sched0();
    STAMP(2);
    const int eo = (q & 1) * 16 + (q >> 1) * 8;
    if constexpr (ID == 1) {
        LAS float* tab = (LAS float*)(lds + NS * STAGE);
        const float sgn = tn < 8 ? 1.f : -1.f;
        if (q == (l15 >> 2)) {
#pragma unroll
            for (int i = 0; i < 2; ++i) {
                const int j = l15 & 3; const float sq = j == 0 ? st[2 + i][0] : j == 1 ? st[2 + i][1] : j == 2 ? st[2 + i][2] : st[2 + i][3];
                const float mean = st[i][0] * (1.f / DM);
                const int ra = wm * 64 + (rotA + i) * 16 + l15;
                tab[ra] = mean; tab[128 + ra] = sgn * __builtin_amdgcn_rsqf(fmaxf(sq * (1.f / DM) - mean * mean, 0.f) + LN_EPS);
                tab[256 + wn * 64 + (rotB + i) * 16 + l15] = st[4 + i][0];
            }
        }
        wait_lgkm0(); bar();
        const auto ro = RSRC(p.abT, DM * 2 * SEQ * 2);
        float ws[4];
#pragma unroll
        for (int n = 0; n < 4; ++n) ws[n] = tab[256 + wn * 64 + ((n + rotB) & 3) * 16 + l15];
#pragma unroll
        for (int m = 0; m < 4; ++m) {
            const int r0 = wm * 64 + ((m + rotA) & 3) * 16 + 4 * q;
            const f32x4 mean = *(const LAS f32x4*)(tab + r0), rs = *(const LAS f32x4*)(tab + 128 + r0);
#pragma unroll
            for (int n = 0; n < 4; ++n) acc[m][n] = (acc[m][n] - mean * ws[n]) * rs;
        }
#pragma unroll
        for (int n = 0; n < 4; ++n) { const int e = (tn & 7) * 128 + wn * 64 + ((n + rotB) & 3) * 16 + l15;
#pragma unroll
            for (int mp = 0; mp < 2; ++mp) {
                unsigned x0 = pack_bf16(acc[2 * mp][n][0], acc[2 * mp][n][1]), x1 = pack_bf16(acc[2 * mp][n][2], acc[2 * mp][n][3]);
                unsigned y0 = pack_bf16(acc[2 * mp + 1][n][0], acc[2 * mp + 1][n][1]), y1 = pack_bf16(acc[2 * mp + 1][n][2], acc[2 * mp + 1][n][3]);
                swap16u(x0, y0); swap16u(x1, y1);
                const int s = tm * 128 + wm * 64 + ((2 * mp + rotA) & 3) * 16 + eo;
                __builtin_amdgcn_raw_buffer_store_b128((u32x4){x0, x1, y0, y1}, ro, (unsigned)((e * (2 * SEQ) + (tn >> 3) * SEQ + s) * 2), 0, SC1);
            } }
    } else {
        const auto rr = RSRC(ID == 2 ? p.x : p.h, SEQ * DM * 2); const auto ro = RSRC(ID == 2 ? p.h : p.out, SEQ * DM * 2);
        const auto rx = RSRC(p.xch + (ID == 2 ? 0 : 256 * 8192), 256 * 16384);
        unsigned off[2]; u32x4 res[2][2];
#pragma unroll
        for (int m = 0; m < 4; ++m) {
            const u32x4 w = {pack_bf16(acc[m][2][0], acc[m][2][1]), pack_bf16(acc[m][2][2], acc[m][2][3]), pack_bf16(acc[m][3][0], acc[m][3][1]), pack_bf16(acc[m][3][2], acc[m][3][3])};
            __builtin_amdgcn_raw_buffer_store_b128(w, rx, (unsigned)(blockIdx.x * 16384 + (wid * 4 + m) * 1024 + lane * 16), 0, SC1);
        }
#pragma unroll
        for (int n = 0; n < 2; ++n) { off[n] = (unsigned)(((tn * 128 + wn * 64 + (n + rotB) * 16 + l15) * DM + tm * 128 + wm * 64 + eo) * 2);
#pragma unroll
            for (int mp = 0; mp < 2; ++mp) res[n][mp] = __builtin_amdgcn_raw_buffer_load_b128(rr, off[n] + mp * 64, 0, 0); }
        STAMP(4);
        u32x4 px[4];
#pragma nounroll
        for (int spin = 0; spin < (1 << 16); ++spin) {
            asm volatile("" ::: "memory");
#pragma unroll
            for (int m = 0; m < 4; ++m) px[m] = __builtin_amdgcn_raw_buffer_load_b128(rx, (unsigned)((blockIdx.x ^ 1) * 16384 + (wid * 4 + m) * 1024 + lane * 16), 0, SC1);
            bool miss = false;
#pragma unroll
            for (int m = 0; m < 4; ++m)
#pragma unroll
                for (int j = 0; j < 4; ++j) miss |= px[m][j] == SENTINEL;
            if (!__any(miss)) break;
        }
        STAMP(5);
#pragma unroll
        for (int m = 0; m < 4; ++m) {
            acc[m][0] += (f32x4){bf_lo(px[m][0]), bf_hi(px[m][0]), bf_lo(px[m][1]), bf_hi(px[m][1])};
            acc[m][1] += (f32x4){bf_lo(px[m][2]), bf_hi(px[m][2]), bf_lo(px[m][3]), bf_hi(px[m][3])};
        }
#pragma unroll
        for (int n = 0; n < 2; ++n) {
            float v[2][8];
#pragma unroll
            for (int mp = 0; mp < 2; ++mp) {
                const u32x4 r = res[n][mp];
                f32x4 lo = acc[2 * mp][n], hi = acc[2 * mp + 1][n];
#pragma unroll
                for (int j = 0; j < 4; ++j) { float a = lo[j], b = hi[j]; swap16(a, b); lo[j] = a; hi[j] = b; }
                u32x4 o; o[0] = pack_bf16(lo[0] + bf_lo(r[0]), lo[1] + bf_hi(r[0])); o[1] = pack_bf16(lo[2] + bf_lo(r[1]), lo[3] + bf_hi(r[1]));
                o[2] = pack_bf16(hi[0] + bf_lo(r[2]), hi[1] + bf_hi(r[2])); o[3] = pack_bf16(hi[2] + bf_lo(r[3]), hi[3] + bf_hi(r[3]));
                __builtin_amdgcn_raw_buffer_store_b128(o, ro, off[n] + mp * 64, 0, SC1);
#pragma unroll
                for (int j = 0; j < 4; ++j) { v[mp][2 * j] = bf_lo(o[j]); v[mp][2 * j + 1] = bf_hi(o[j]); }
            }
            if constexpr (ID == 2) {
                float s = 0.f;
#pragma unroll
                for (int j = 0; j < 8; ++j) s += v[0][j] + v[1][j];
                const float mw = qsum(s) * (1.f / 64.f); float m2 = 0.f;
#pragma unroll
                for (int j = 0; j < 8; ++j) { const float d0 = v[0][j] - mw, d1 = v[1][j] - mw; m2 += d0 * d0 + d1 * d1; }
                m2 = qsum(m2);
                if (q == 0) *(f32x2*)(p.h + (SEQ + tn * 128 + wn * 64 + (n + rotB) * 16 + l15) * DM + (tm * 2 + wm) * 4) = (f32x2){mw, m2};
            }
        }
    }
    STAMP_END(ID);
#endif
}

#if defined(__HIP_DEVICE_COMPILE__)
template <int GRP>
__device__ __forceinline__ void gemm3_body(const Params& p, LAS unsigned char* lds, const int wid) {
    constexpr int NK = 16, FA = 7, HB0 = 3 * 32768;
    STAMP_BEGIN;
    const int tid = threadIdx.x, lane = tid & 63;
    const int wm = wid & 1, wn = wid >> 1, l15 = lane & 15, q = lane >> 4;
    const int xcd = blockIdx.x & 7, idx = blockIdx.x >> 3, im = idx & 7, in = idx >> 3;
    const int tm = (xcd & 3) * 8 + im, tn = (xcd >> 2) * 4 + in;
    const int phi = (im + in) & 3, koff = (xcd * 2) & (NK - 1);

    const u16* aptr = GRP ? p.wu : p.wg;
    unsigned voffA[2];
#pragma unroll
    for (int i = 0; i < 2; ++i) { const int j = l15 ^ (q | (i << 2) | (GRP << 3));
        voffA[i] = j < 14 ? (unsigned)(((wid & 3) * 16 + q) * FF * 2 + tm * 224 + j * 16) : 0x7ffffff0u; }
    const unsigned voffB = (unsigned)((tn * 256 + wid * 32 + (lane >> 3)) * DM * 2 + (((lane & 7) ^ (lane >> 3)) << 4));
    auto kstep = [&](int t) { return (t + koff + ((t & 3) == phi ? 4 : 0)) & (NK - 1); };
    struct Src { int so; unsigned nrec; };
    auto pin = [](Src x) { asm volatile("" : "+s"(x.so), "+s"(x.nrec)); return x; };
    auto srcW = [&](int t) { return pin(Src{kstep(t) * 64 * FF * 2, t < NK ? (unsigned)(DM * FF * 2) : 0u}); };
    auto srcH = [&](int t) { return pin(Src{t < NK ? kstep(t) * 128 : SEQ * DM * 2, t <= NK ? (unsigned)(2 * SEQ * DM * 2) : 0u}); };
    auto stageW = [&](Src x, int slot, int i) { DMA16(RSRC(aptr, x.nrec), lds + slot * 32768 + wid * 4096 + i * 1024, voffA[i >> 1], x.so + i * 4 * FF * 2); };
    auto stageH = [&](Src x, int slot, int i) { DMA16(RSRC(p.h, x.nrec), lds + HB0 + slot * 32768 + wid * 4096 + i * 1024, voffB, x.so + i * 8 * DM * 2); };
#pragma unroll
    for (int t = 0; t < 2; ++t) {
        const Src xw = srcW(t), xh = srcH(t);
#pragma unroll
        for (int i = 0; i < 4; ++i) stageW(xw, t, i);
#pragma unroll
        for (int i = 0; i < 4; ++i) stageH(xh, t, i);
    }

    unsigned adA[FA];
    const unsigned lds0 = (unsigned)(size_t)lds;
#pragma unroll
    for (int m = 0; m < FA; ++m) { const int q4 = l15 >> 2, pp = lane & 3; int mf = m + 2 * wn; mf = mf >= FA ? mf - FA : mf;
        adA[m] = lds0 + (unsigned)((pp >> 1) * 16384 + (8 * q + q4) * 256 + (((wm * FA + mf) ^ (q4 | ((q & 1) << 2) | ((pp >> 1) << 3))) << 4) + 8 * (pp & 1)); }
    unsigned adB[2]; adB[0] = lds0 + (unsigned)(HB0 + (wn * 64 + l15) * 128 + ((q ^ (lane & 7)) << 4)); adB[1] = adB[0] ^ 64u;
    auto rd = [&](auto ic, Frag (&ra)[FA], bf16x8 (&rb)[4], auto kkc) {
        constexpr int kk = decltype(kkc)::value, i = decltype(ic)::value;
        if constexpr (i < 4) rb[i] = lds_read128<i * 2048>(adB[kk]);
        else if constexpr (i % 2 == 0) ra[(i - 4) / 2].lo = tr_read<kk * 8192>(adA[(i - 4) / 2]);
        else ra[(i - 4) / 2].hi = tr_read<kk * 8192 + 1024>(adA[(i - 4) / 2]);
    };
    f32x4 acc[FA][4];
#pragma unroll
    for (int m = 0; m < FA; ++m)
#pragma unroll
        for (int n = 0; n < 4; ++n) acc[m][n] = (f32x4){0.f, 0.f, 0.f, 0.f};
    float cs0 = 0.f, cs1 = 0.f;
#define DSTR 2
#define PR1 16
#define PR2 4
#define PR3 20

    auto phase = [&](const Frag (&ra)[FA], const bf16x8 (&rb)[4], Frag (&na)[FA], bf16x8 (&nb)[4], int dw, int dh, auto kkc, auto grpc, auto&& dma) {
        static_for<0, 28>([&](auto sc) {
            constexpr int s = decltype(sc)::value, m = s >> 2, n = s & 3;
            if constexpr (decltype(kkc)::value == 0) { if constexpr (s == 0) __builtin_amdgcn_s_setprio(3); if constexpr (s == PR1) __builtin_amdgcn_s_setprio(2); }
            else { if constexpr (s == PR2) __builtin_amdgcn_s_setprio(1); if constexpr (s == PR3) __builtin_amdgcn_s_setprio(0); }
            acc[m][n] = MFMA(ra[m].v(), rb[n], acc[m][n]);
            if constexpr (s < 18) rd(sc, na, nb, kkc);
            if constexpr (s % DSTR == 0 && s < 8 * DSTR && decltype(grpc)::value == ((s / DSTR) & 1)) dma(s / (2 * DSTR));
            if constexpr (decltype(kkc)::value == 1 && s >= 18 && s < 27) { if constexpr (s < 25) bump(adA[s - 18], dw); else bump(adB[s - 25], dh); }
            if constexpr (s >= 1 && s < 17) { constexpr int j = s - 1, f = j >> 3, d = (j >> 1) & 3; const unsigned w = d < 2 ? ra[f].lo[d & 1] : ra[f].hi[d & 1];
                vadd(f ? cs1 : cs0, (j & 1) ? bf_hi(w) : bf_lo(w)); }
            sched0();
        });
        wait_lgkm0(); sched0();
    };
    Frag a0[FA], a1[FA]; bf16x8 b0[4], b1[4];
    constexpr std::integral_constant<int, 0> K0{}; constexpr std::integral_constant<int, 1> K1{};
    wait_vm<8>(); bar();
    STAMP(1);
    static_for<0, 18>([&](auto ic) { rd(ic, a0, b0, K0); });
    wait_lgkm0(); sched0();
    auto kloop = [&](auto grpc) {
        int sw2 = 2;
#pragma nounroll
        for (int t = 0; t < NK; ++t) {
            const int sh = t & 1;
            const Src xw = srcW(t + 2), xh = srcH(t + 2);
            phase(a0, b0, a1, b1, sw2 == 1 ? -65536 : 32768, sh ? -32768 : 32768, K1, grpc, [&](int i) { stageW(xw, sw2, i); });
            wait_vm<4>(); bar();
            phase(a1, b1, a0, b0, 0, 0, K0, grpc, [&](int i) { stageH(xh, sh, i); });
            sw2 = sw2 == 2 ? 0 : sw2 + 1;
        }
    };
    kloop(std::integral_constant<int, GRP>{});
    keep(a0, b0);
    wait_vm<0>(); bar();
    STAMP(2);
    LAS float* cs = (LAS float*)lds;
    cs0 = qsum(cs0); cs1 = qsum(cs1);
    if (q == 0) { cs[(wm * FA + 2 * wn) * 16 + l15] = cs0; if (wn < 3) cs[(wm * FA + 2 * wn + 1) * 16 + l15] = cs1; }
    f32x2 rs[4];
#pragma unroll
    for (int n = 0; n < 4; ++n) {
        const f32x4 s0 = *(const LAS f32x4*)(size_t)(adB[0] + n * 2048), s1 = *(const LAS f32x4*)(size_t)(adB[1] + n * 2048);
        const float mean = qsum((s0[0] + s0[2]) + (s1[0] + s1[2])) * (1.f / 16.f);
        const float d0 = s0[0] - mean, d1 = s0[2] - mean, d2 = s1[0] - mean, d3 = s1[2] - mean;
        const float m2 = qsum((s0[1] + s0[3]) + (s1[1] + s1[3]) + 64.f * ((d0 * d0 + d1 * d1) + (d2 * d2 + d3 * d3)));
        rs[n] = (f32x2){mean, __builtin_amdgcn_rsqf(m2 * (1.f / DM) + LN_EPS)};
    }
    wait_lgkm0(); bar();
    f32x4 csum[FA];
#pragma unroll
    for (int m = 0; m < FA; ++m) { int mf = m + 2 * wn; mf = mf >= FA ? mf - FA : mf; csum[m] = *(const LAS f32x4*)(cs + (wm * FA + mf) * 16 + 4 * q); }
    const auto ro = RSRC(p.act, SEQ * FF * 2);
    unsigned fo[2];
#pragma unroll
    for (int g = 0; g < 2; ++g) { int mf = 4 * g + q + 2 * wn; mf = mf >= FA ? mf - FA : mf; fo[g] = (g == 1 && q == 3) ? 0x7ffffff0u : (unsigned)(tm * 224 + wm * 112 + mf * 16); }
#pragma unroll
    for (int n = 0; n < 4; ++n) {
        unsigned o[8];
#pragma unroll
        for (int m = 0; m < FA; ++m) {
            const f32x4 v = (acc[m][n] - rs[n][0] * csum[m]) * rs[n][1];
            float g0 = v[0], g1 = v[1], u0 = v[2], u1 = v[3];
            swap32(g0, u0); swap32(g1, u1);
            const float s0 = g0 * __builtin_amdgcn_rcpf(1.f + __builtin_amdgcn_exp2f(-1.44269504f * g0)), s1 = g1 * __builtin_amdgcn_rcpf(1.f + __builtin_amdgcn_exp2f(-1.44269504f * g1));
            o[m] = pack_bf16(s0 * u0, s1 * u1);
        }
        o[7] = 0u;
        const unsigned rowoff = (unsigned)((tn * 256 + wn * 64 + n * 16 + l15) * FF * 2);
#pragma unroll
        for (int g = 0; g < 2; ++g) {
            unsigned r0 = o[4 * g], r1 = o[4 * g + 1], r2 = o[4 * g + 2], r3 = o[4 * g + 3];
            swap16u(r0, r1); swap16u(r2, r3); swap32u(r0, r2); swap32u(r1, r3);
            __builtin_amdgcn_raw_buffer_store_b128((u32x4){r0, r2, r1, r3}, ro, rowoff + fo[g], 0, SC1);
        }
    }
    STAMP_END(3);
}
#endif
__global__ __launch_bounds__(512, 2) void gemm3_k(const Params p) {
#if defined(__HIP_DEVICE_COMPILE__)
    __shared__ __attribute__((aligned(16))) unsigned char lds_raw[5 * 32768];
    const int wid = __builtin_amdgcn_readfirstlane(threadIdx.x >> 6);
    if (wid < 4) gemm3_body<0>(p, (LAS unsigned char*)lds_raw, wid); else gemm3_body<1>(p, (LAS unsigned char*)lds_raw, wid);
#endif
}

extern "C" void kernel_launch(void* const* d_in, const int* in_sizes, int n_in, void* d_out, int out_size, void* d_ws, size_t ws_size, hipStream_t stream) {
    Params p;
    p.x = (const u16*)d_in[0]; p.cre = (const u16*)d_in[1]; p.cim = (const u16*)d_in[2]; p.wre = (const u16*)d_in[3]; p.wim = (const u16*)d_in[4];
    p.wg = (const u16*)d_in[6]; p.wu = (const u16*)d_in[7]; p.wd = (const u16*)d_in[8];
    p.out = (u16*)d_out;
    char* ws = (char*)d_ws;
    p.abT = (u16*)ws; p.h = (u16*)(ws + (8u << 20)); p.act = (u16*)(ws + (16u << 20));
    p.xch = (u16*)(ws + (32u << 20));
    gemm128_k<1><<<256, 512, 0, stream>>>(p);
    gemm128_k<2><<<256, 512, 0, stream>>>(p);
    gemm3_k<<<256, 512, 0, stream>>>(p);
    gemm128_k<4><<<256, 512, 0, stream>>>(p);
}
```

```cpp
#include <hip/hip_runtime.h>
#include <hip/hip_bf16.h>
#include <type_traits>

#define LAS __attribute__((address_space(3)))
typedef unsigned short u16;
typedef __bf16 bf16x8 __attribute__((ext_vector_type(8)));
typedef __bf16 bf16x2 __attribute__((ext_vector_type(2)));
typedef float f32x4 __attribute__((ext_vector_type(4)));
typedef float f32x2 __attribute__((ext_vector_type(2)));
typedef unsigned u32x4 __attribute__((ext_vector_type(4)));
typedef unsigned u32x2 __attribute__((ext_vector_type(2)));

constexpr int SEQ = 2048, DM = 1024, FF = 3584;
constexpr float LN_EPS = 1e-5f;
constexpr int SC1 = 16;

struct Params {
    const u16* x; const u16* cre; const u16* cim; const u16* wre; const u16* wim;
    const u16* wg; const u16* wu; const u16* wd;
    u16* out; u16* abT; u16* h; u16* act; u16* xch;
};

template <int N> __device__ __forceinline__ void wait_vm() { asm volatile("s_waitcnt vmcnt(%0)" ::"n"(N) : "memory"); }
__device__ __forceinline__ void wait_lgkm0() { __builtin_amdgcn_s_waitcnt(0xC07F); asm volatile("s_waitcnt lgkmcnt(0)" ::: "memory"); }
__device__ __forceinline__ void sched0() { __builtin_amdgcn_sched_barrier(0); }
__device__ __forceinline__ void bar() { sched0(); asm volatile("" ::: "memory"); __builtin_amdgcn_s_barrier(); asm volatile("" ::: "memory"); sched0(); }
__device__ __forceinline__ unsigned pack_bf16(float lo, float hi) { bf16x2 c; c[0] = (__bf16)lo; c[1] = (__bf16)hi; return __builtin_bit_cast(unsigned, c); }
__device__ __forceinline__ float bf_lo(unsigned v) { return __uint_as_float(v << 16); }
__device__ __forceinline__ float bf_hi(unsigned v) { return __uint_as_float(v & 0xffff0000u); }
__device__ __forceinline__ void swap16(float& a, float& b) { auto r = __builtin_amdgcn_permlane16_swap(__float_as_uint(a), __float_as_uint(b), false, false); a = __uint_as_float(r[0]); b = __uint_as_float(r[1]); }
__device__ __forceinline__ void swap32(float& a, float& b) { auto r = __builtin_amdgcn_permlane32_swap(__float_as_uint(a), __float_as_uint(b), false, false); a = __uint_as_float(r[0]); b = __uint_as_float(r[1]); }
__device__ __forceinline__ void swap16u(unsigned& a, unsigned& b) { auto r = __builtin_amdgcn_permlane16_swap(a, b, false, false); a = r[0]; b = r[1]; }
__device__ __forceinline__ void swap32u(unsigned& a, unsigned& b) { auto r = __builtin_amdgcn_permlane32_swap(a, b, false, false); a = r[0]; b = r[1]; }
__device__ __forceinline__ float qsum(float v) { float a = v, b = v; swap16(a, b); a += b; b = a; swap32(a, b); return a + b; }

struct Frag { u32x2 lo, hi; __device__ __forceinline__ bf16x8 v() const { return __builtin_bit_cast(bf16x8, __builtin_shufflevector(lo, hi, 0, 1, 2, 3)); } };
template <int OFF> __device__ __forceinline__ u32x2 tr_read(unsigned addr) { u32x2 r; asm volatile("ds_read_b64_tr_b16 %0, %1 offset:%2" : "=v"(r) : "v"(addr), "n"(OFF) : "memory"); return r; }
template <int OFF> __device__ __forceinline__ bf16x8 lds_read128(unsigned addr) { bf16x8 r; asm volatile("ds_read_b128 %0, %1 offset:%2" : "=v"(r) : "v"(addr), "n"(OFF) : "memory"); return r; }
template <int N> __device__ __forceinline__ void wait_lgkm() { asm volatile("s_waitcnt lgkmcnt(%0)" ::"n"(N) : "memory"); }
template <int I, int N, class F> __device__ __forceinline__ void static_for(F&& f) { if constexpr (I < N) { f(std::integral_constant<int, I>{}); static_for<I + 1, N>(f); } }
template <int NA> __device__ __forceinline__ void keep(const Frag (&a)[NA], const bf16x8 (&b)[4]) {
#pragma unroll
    for (int i = 0; i < NA; ++i) asm volatile("" ::"v"(a[i].lo), "v"(a[i].hi));
#pragma unroll
    for (int i = 0; i < 4; ++i) asm volatile("" ::"v"(b[i]));
}
__device__ __forceinline__ void vadd(float& a, float b) { asm volatile("v_add_f32 %0, %0, %1" : "+v"(a) : "v"(b)); }
__device__ __forceinline__ void bump(unsigned& a, int d) { asm volatile("v_add_u32 %0, %1, %0" : "+v"(a) : "s"(d)); }
constexpr unsigned SENTINEL = 0xffffffffu;
#define MFMA(a, b, c) __builtin_amdgcn_mfma_f32_16x16x32_bf16(a, b, c, 0, 0, 0)
#define RSRC(ptr, bytes) __builtin_amdgcn_make_buffer_rsrc((void*)(ptr), 0, (bytes), 0x00020000)
#define DMA16(rs, ldsp, voff, soff) __builtin_amdgcn_raw_ptr_buffer_load_lds(rs, (LAS void*)(ldsp), 16, voff, soff, 0, 0)

#define STAMP(i)
#define STAMP_BEGIN
#define STAMP_END(ID)

template <int ID> struct Cfg;
template <> struct Cfg<1> { static constexpr int NK = 16, LDA = DM, LDB = DM, XOFF = 2; static constexpr bool ATR = false; };
template <> struct Cfg<2> { static constexpr int NK = 32, LDA = 2 * SEQ, LDB = SEQ, XOFF = 8; static constexpr bool ATR = false; };
template <> struct Cfg<4> { static constexpr int NK = 28, LDA = DM, LDB = FF, XOFF = 8; static constexpr bool ATR = true; };

template <int ID>
__global__ __launch_bounds__(512, 2) void gemm128_k(const Params p) {
#if defined(__HIP_DEVICE_COMPILE__)
    using C = Cfg<ID>;
    constexpr int NS = 4, NK = C::NK, STAGE = 32768, PW = 8, PER = 4, ADV = 4;
    __shared__ __attribute__((aligned(16))) unsigned char lds_raw[NS * STAGE + (ID == 1 ? 1536 : 0)];
    LAS unsigned char* lds = (LAS unsigned char*)lds_raw;
    STAMP_BEGIN;
    const int tid = threadIdx.x, lane = tid & 63, wid = __builtin_amdgcn_readfirstlane(tid >> 6);
    const int l15 = lane & 15, q = lane >> 4;
    const int xcd = blockIdx.x & 7, idx = blockIdx.x >> 3, im = idx & 3, in = idx >> 2;
    int tm, tn, ks = 0;
    if constexpr (ID == 1) { tm = (xcd & 3) * 4 + im; tn = (xcd >> 2) * 8 + in; }
    else { ks = xcd & 1; tm = ((xcd >> 1) & 1) * 4 + im; tn = (xcd >> 2) * 8 + in; }

    if (wid >= 4) {
        const int lw = wid - 4, phi = (im + in) & 3, koff = ((xcd >> (ID == 1 ? 0 : 1)) * C::XOFF) % NK;
        const u16* aptr; const u16* bptr; unsigned abytes, bbytes; int brow0 = tn * 128, ka0 = 0, kb0 = 0;
        if constexpr (ID == 1) { aptr = p.x; abytes = SEQ * DM * 2; bptr = tn < 8 ? p.wre : p.wim; bbytes = DM * DM * 2; brow0 = (tn & 7) * 128; }
        if constexpr (ID == 2) { aptr = p.abT; abytes = DM * 2 * SEQ * 2; bptr = ks ? p.cim : p.cre; bbytes = SEQ * SEQ * 2; ka0 = ks * NK; }
        if constexpr (ID == 4) { aptr = p.wd; abytes = FF * DM * 2; bptr = p.act; bbytes = SEQ * FF * 2; ka0 = kb0 = ks * NK; }
        const unsigned ksw = (unsigned)(((lane & 7) ^ (lane >> 3)) << 4);
        unsigned voffA[2];
        if constexpr (!C::ATR) voffA[0] = voffA[1] = (unsigned)((tm * 128 + lw * 32 + (lane >> 3)) * C::LDA * 2) + ksw;
        else {
#pragma unroll
            for (int i = 0; i < 2; ++i) voffA[i] = (unsigned)((lw * 16 + q) * C::LDA * 2 + tm * 256 + ((l15 ^ ((q | (i << 2)) << 1)) << 4));
        }
        const unsigned voffB = (unsigned)((brow0 + lw * 32 + (lane >> 3)) * C::LDB * 2) + ksw;
        auto stage = [&](int t, int slot) {
            const bool live = t < NK;
            int k = t + koff + ((t & (PER - 1)) == phi ? ADV : 0); k = k >= NK ? k - NK : k; k = k >= NK ? k - NK : k;
            const auto ra = RSRC(aptr, live ? abytes : 0u); const auto rb = RSRC(bptr, live ? bbytes : 0u);
            LAS unsigned char* base = lds + slot * STAGE + lw * 4096;
#pragma unroll
            for (int i = 0; i < 4; ++i) {
                if constexpr (!C::ATR) DMA16(ra, base + i * 1024, voffA[0], (ka0 + k) * 128 + i * 8 * C::LDA * 2);
                else DMA16(ra, base + i * 1024, voffA[i >> 1], ((ka0 + k) * 64 + i * 4) * C::LDA * 2);
                DMA16(rb, base + 16384 + i * 1024, voffB, (kb0 + k) * 128 + i * 8 * C::LDB * 2);
            }
        };
        stage(0, 0); stage(1, 1);
        wait_vm<PW>(); bar();
        stage(2, 2);
        int slot = 3;
#pragma nounroll
        for (int t = 0; t < NK; ++t) {
            wait_vm<PW>(); bar();
            stage(t + 3, slot);
            slot = (slot + 1) & 3;
            if constexpr (ID != 4) { if (t == NK - 2) {
                const auto rx = RSRC(p.xch + (ID == 1 ? 0 : 256 * 8192), 256 * 16384);
#pragma unroll
                for (int m = 0; m < 4; ++m) __builtin_amdgcn_raw_buffer_store_b128((u32x4){SENTINEL, SENTINEL, SENTINEL, SENTINEL}, rx, (unsigned)(blockIdx.x * 16384 + (lw * 4 + m) * 1024 + lane * 16), 0, SC1);
            } }
        }
        if constexpr (ID == 1) bar();
        return;
    }

    const int wm = wid & 1, wn = wid >> 1;
    const int rotA = ID == 1 ? 2 * wn : 0, rotB = ID == 1 ? 2 * wm : 2 * ks;
    unsigned ad[8];
    const unsigned lds0 = (unsigned)(size_t)lds, kc_sw = lds0 + (unsigned)(l15 * 128 + ((q ^ (lane & 7)) << 4));
#pragma unroll
    for (int i = 0; i < 4; ++i) {
        if constexpr (!C::ATR) ad[i] = (kc_sw ^ (unsigned)((i >> 1) * 64)) + (unsigned)((wm * 4 + ((2 * (i & 1) + rotA) & 3)) * 2048);
        else { const int q4 = l15 >> 2; ad[i] = lds0 + (unsigned)((8 * q + q4) * 256 + (((wm * 4 + i) ^ (q4 | ((q & 1) << 2))) << 5) + 8 * (lane & 3)); }
        ad[4 + i] = 16384u + (kc_sw ^ (unsigned)((i >> 1) * 64)) + (unsigned)((wn * 4 + ((2 * (i & 1) + rotB) & 3)) * 2048);
    }
    constexpr int NR = C::ATR ? 12 : 8;
    auto rd = [&](auto ic, Frag (&ra)[4], bf16x8 (&rb)[4], auto kkc) {
        constexpr int kk = decltype(kkc)::value, i = decltype(ic)::value;
        constexpr int W = C::ATR ? 2 : 1;
        constexpr bool isB = i == 0 || (i > W && i <= W + 3);
        if constexpr (isB) { constexpr int n = i == 0 ? 0 : i - W; rb[n] = lds_read128<(n & 1) * 2048>(ad[4 + kk * 2 + (n >> 1)]); }
        else { constexpr int j = i <= W ? i - 1 : i - 4, m = j / W;
            if constexpr (!C::ATR) { const u32x4 w = __builtin_bit_cast(u32x4, lds_read128<(m & 1) * 2048>(ad[kk * 2 + (m >> 1)])); ra[m].lo = (u32x2){w[0], w[1]}; ra[m].hi = (u32x2){w[2], w[3]}; }
            else if constexpr (j % 2 == 0) ra[m].lo = tr_read<kk * 8192>(ad[m]);
            else ra[m].hi = tr_read<kk * 8192 + 1024>(ad[m]); }
    };
    f32x4 acc[4][4], st[6];
#pragma unroll
    for (int m = 0; m < 4; ++m)
#pragma unroll
        for (int n = 0; n < 4; ++n) acc[m][n] = (f32x4){0.f, 0.f, 0.f, 0.f};
#pragma unroll
    for (int i = 0; i < 6; ++i) st[i] = (f32x4){0.f, 0.f, 0.f, 0.f};
    const bf16x8 ones = __builtin_bit_cast(bf16x8, (u32x4){0x3f803f80u, 0x3f803f80u, 0x3f803f80u, 0x3f803f80u});
    auto phase = [&](const Frag (&ra)[4], const bf16x8 (&rb)[4], Frag (&na)[4], bf16x8 (&nb)[4], int dstep, auto kkc) {
        static_for<0, 16>([&](auto sc) {
            constexpr int s = decltype(sc)::value, m = s >> 2, n = s & 3, W = C::ATR ? 2 : 1;
            if constexpr (n == 0) { wait_lgkm<(3 - m) * W + (s < NR ? s : NR)>(); sched0(); }
            acc[m][n] = MFMA(ra[m].v(), rb[n], acc[m][n]);
            if constexpr (s < NR) rd(sc, na, nb, kkc);
            if constexpr (s >= 12) {
                constexpr int kk = decltype(kkc)::value, j = s - 12;
                if constexpr (C::ATR) { if constexpr (kk == 1) { bump(ad[j], dstep); bump(ad[4 + j], dstep); } }
                else if constexpr (kk == 1) { bump(ad[j], dstep); bump(ad[4 + j], dstep); }
            }
            if constexpr (ID == 1 && s >= 10) {
                constexpr int i = s & 1;
                if constexpr (s < 12) st[i] = MFMA(ones, ra[i].v(), st[i]); else if constexpr (s < 14) st[2 + i] = MFMA(ra[i].v(), ra[i].v(), st[2 + i]); else st[4 + i] = MFMA(ones, rb[i], st[4 + i]); }
            sched0();
        });
    };
    Frag a0[4], a1[4]; bf16x8 b0[4], b1[4];
    constexpr std::integral_constant<int, 0> K0{}; constexpr std::integral_constant<int, 1> K1{};
    bar();
    STAMP(1);
    static_for<0, NR>([&](auto ic) { rd(ic, a0, b0, K0); });
    sched0();
#pragma nounroll
    for (int t = 0; t < NK; ++t) {
        phase(a0, b0, a1, b1, (t & 3) == 3 ? -3 * STAGE : STAGE, K1);
        bar();
        phase(a1, b1, a0, b0, 0, K0);
    }
    wait_lgkm0(); keep(a0, b0); sched0();
    STAMP(2);
    const int eo = (q & 1) * 16 + (q >> 1) * 8;
    if constexpr (ID == 1) {
        LAS float* tab = (LAS float*)(lds + NS * STAGE);
        const float sgn = tn < 8 ? 1.f : -1.f;
        if (q == (l15 >> 2)) {
#pragma unroll
            for (int i = 0; i < 2; ++i) {
                const int j = l15 & 3; const float sq = j == 0 ? st[2 + i][0] : j == 1 ? st[2 + i][1] : j == 2 ? st[2 + i][2] : st[2 + i][3];
                const float mean = st[i][0] * (1.f / DM);
                const int ra = wm * 64 + (rotA + i) * 16 + l15;
                tab[ra] = mean; tab[128 + ra] = sgn * __builtin_amdgcn_rsqf(fmaxf(sq * (1.f / DM) - mean * mean, 0.f) + LN_EPS);
                tab[256 + wn * 64 + (rotB + i) * 16 + l15] = st[4 + i][0];
            }
        }
        wait_lgkm0(); bar();
        const auto ro = RSRC(p.abT, DM * 2 * SEQ * 2);
        float ws[4];
#pragma unroll
        for (int n = 0; n < 4; ++n) ws[n] = tab[256 + wn * 64 + ((n + rotB) & 3) * 16 + l15];
#pragma unroll
        for (int m = 0; m < 4; ++m) {
            const int r0 = wm * 64 + ((m + rotA) & 3) * 16 + 4 * q;
            const f32x4 mean = *(const LAS f32x4*)(tab + r0), rs = *(const LAS f32x4*)(tab + 128 + r0);
#pragma unroll
            for (int n = 0; n < 4; ++n) acc[m][n] = (acc[m][n] - mean * ws[n]) * rs;
        }
#pragma unroll
        for (int n = 0; n < 4; ++n) { const int e = (tn & 7) * 128 + wn * 64 + ((n + rotB) & 3) * 16 + l15;
#pragma unroll
            for (int mp = 0; mp < 2; ++mp) {
                unsigned x0 = pack_bf16(acc[2 * mp][n][0], acc[2 * mp][n][1]), x1 = pack_bf16(acc[2 * mp][n][2], acc[2 * mp][n][3]);
                unsigned y0 = pack_bf16(acc[2 * mp + 1][n][0], acc[2 * mp + 1][n][1]), y1 = pack_bf16(acc[2 * mp + 1][n][2], acc[2 * mp + 1][n][3]);
                swap16u(x0, y0); swap16u(x1, y1);
                const int s = tm * 128 + wm * 64 + ((2 * mp + rotA) & 3) * 16 + eo;
                __builtin_amdgcn_raw_buffer_store_b128((u32x4){x0, x1, y0, y1}, ro, (unsigned)((e * (2 * SEQ) + (tn >> 3) * SEQ + s) * 2), 0, SC1);
            } }
    } else {
        const auto rr = RSRC(ID == 2 ? p.x : p.h, SEQ * DM * 2); const auto ro = RSRC(ID == 2 ? p.h : p.out, SEQ * DM * 2);
        const auto rx = RSRC(p.xch + (ID == 2 ? 0 : 256 * 8192), 256 * 16384);
        unsigned off[2]; u32x4 res[2][2];
#pragma unroll
        for (int m = 0; m < 4; ++m) {
            const u32x4 w = {pack_bf16(acc[m][2][0], acc[m][2][1]), pack_bf16(acc[m][2][2], acc[m][2][3]), pack_bf16(acc[m][3][0], acc[m][3][1]), pack_bf16(acc[m][3][2], acc[m][3][3])};
            __builtin_amdgcn_raw_buffer_store_b128(w, rx, (unsigned)(blockIdx.x * 16384 + (wid * 4 + m) * 1024 + lane * 16), 0, SC1);
        }
#pragma unroll
        for (int n = 0; n < 2; ++n) { off[n] = (unsigned)(((tn * 128 + wn * 64 + (n + rotB) * 16 + l15) * DM + tm * 128 + wm * 64 + eo) * 2);
#pragma unroll
            for (int mp = 0; mp < 2; ++mp) res[n][mp] = __builtin_amdgcn_raw_buffer_load_b128(rr, off[n] + mp * 64, 0, 0); }
        STAMP(4);
        u32x4 px[4];
#pragma nounroll
        for (int spin = 0; spin < (1 << 16); ++spin) {
            asm volatile("" ::: "memory");
#pragma unroll
            for (int m = 0; m < 4; ++m) px[m] = __builtin_amdgcn_raw_buffer_load_b128(rx, (unsigned)((blockIdx.x ^ 1) * 16384 + (wid * 4 + m) * 1024 + lane * 16), 0, SC1);
            bool miss = false;
#pragma unroll
            for (int m = 0; m < 4; ++m)
#pragma unroll
                for (int j = 0; j < 4; ++j) miss |= px[m][j] == SENTINEL;
            if (!__any(miss)) break;
        }
        STAMP(5);
#pragma unroll
        for (int m = 0; m < 4; ++m) {
            acc[m][0] += (f32x4){bf_lo(px[m][0]), bf_hi(px[m][0]), bf_lo(px[m][1]), bf_hi(px[m][1])};
            acc[m][1] += (f32x4){bf_lo(px[m][2]), bf_hi(px[m][2]), bf_lo(px[m][3]), bf_hi(px[m][3])};
        }
#pragma unroll
        for (int n = 0; n < 2; ++n) {
            float v[2][8];
#pragma unroll
            for (int mp = 0; mp < 2; ++mp) {
                const u32x4 r = res[n][mp];
                f32x4 lo = acc[2 * mp][n], hi = acc[2 * mp + 1][n];
#pragma unroll
                for (int j = 0; j < 4; ++j) { float a = lo[j], b = hi[j]; swap16(a, b); lo[j] = a; hi[j] = b; }
                u32x4 o; o[0] = pack_bf16(lo[0] + bf_lo(r[0]), lo[1] + bf_hi(r[0])); o[1] = pack_bf16(lo[2] + bf_lo(r[1]), lo[3] + bf_hi(r[1]));
                o[2] = pack_bf16(hi[0] + bf_lo(r[2]), hi[1] + bf_hi(r[2])); o[3] = pack_bf16(hi[2] + bf_lo(r[3]), hi[3] + bf_hi(r[3]));
                __builtin_amdgcn_raw_buffer_store_b128(o, ro, off[n] + mp * 64, 0, SC1);
#pragma unroll
                for (int j = 0; j < 4; ++j) { v[mp][2 * j] = bf_lo(o[j]); v[mp][2 * j + 1] = bf_hi(o[j]); }
            }
            if constexpr (ID == 2) {
                float s = 0.f;
#pragma unroll
                for (int j = 0; j < 8; ++j) s += v[0][j] + v[1][j];
                const float mw = qsum(s) * (1.f / 64.f); float m2 = 0.f;
#pragma unroll
                for (int j = 0; j < 8; ++j) { const float d0 = v[0][j] - mw, d1 = v[1][j] - mw; m2 += d0 * d0 + d1 * d1; }
                m2 = qsum(m2);
                if (q == 0) *(f32x2*)(p.h + (SEQ + tn * 128 + wn * 64 + (n + rotB) * 16 + l15) * DM + (tm * 2 + wm) * 4) = (f32x2){mw, m2};
            }
        }
    }
    STAMP_END(ID);
#endif
}

#if defined(__HIP_DEVICE_COMPILE__)
template <int GRP>
__device__ __forceinline__ void gemm3_body(const Params& p, LAS unsigned char* lds, const int wid) {
    constexpr int NK = 16, FA = 7, HB0 = 3 * 32768;
    STAMP_BEGIN;
    const int tid = threadIdx.x, lane = tid & 63;
    const int wm = wid & 1, wn = wid >> 1, l15 = lane & 15, q = lane >> 4;
    const int xcd = blockIdx.x & 7, idx = blockIdx.x >> 3, im = idx & 7, in = idx >> 3;
    const int tm = (xcd & 3) * 8 + im, tn = (xcd >> 2) * 4 + in;
    const int phi = (im + in) & 3, koff = (xcd * 2) & (NK - 1);

    const u16* aptr = GRP ? p.wu : p.wg;
    unsigned voffA[2];
#pragma unroll
    for (int i = 0; i < 2; ++i) { const int j = l15 ^ (q | (i << 2) | (GRP << 3));
        voffA[i] = j < 14 ? (unsigned)(((wid & 3) * 16 + q) * FF * 2 + tm * 224 + j * 16) : 0x7ffffff0u; }
    const unsigned voffB = (unsigned)((tn * 256 + wid * 32 + (lane >> 3)) * DM * 2 + (((lane & 7) ^ (lane >> 3)) << 4));
    auto kstep = [&](int t) { return (t + koff + ((t & 3) == phi ? 4 : 0)) & (NK - 1); };
    struct Src { int so; unsigned nrec; };
    auto pin = [](Src x) { asm volatile("" : "+s"(x.so), "+s"(x.nrec)); return x; };
    auto srcW = [&](int t) { return pin(Src{kstep(t) * 64 * FF * 2, t < NK ? (unsigned)(DM * FF * 2) : 0u}); };
    auto srcH = [&](int t) { return pin(Src{t < NK ? kstep(t) * 128 : SEQ * DM * 2, t <= NK ? (unsigned)(2 * SEQ * DM * 2) : 0u}); };
    auto stageW = [&](Src x, int slot, int i) { DMA16(RSRC(aptr, x.nrec), lds + slot * 32768 + wid * 4096 + i * 1024, voffA[i >> 1], x.so + i * 4 * FF * 2); };
    auto stageH = [&](Src x, int slot, int i) { DMA16(RSRC(p.h, x.nrec), lds + HB0 + slot * 32768 + wid * 4096 + i * 1024, voffB, x.so + i * 8 * DM * 2); };
#pragma unroll
    for (int t = 0; t < 2; ++t) {
        const Src xw = srcW(t), xh = srcH(t);
#pragma unroll
        for (int i = 0; i < 4; ++i) stageW(xw, t, i);
#pragma unroll
        for (int i = 0; i < 4; ++i) stageH(xh, t, i);
    }

    unsigned adA[FA];
    const unsigned lds0 = (unsigned)(size_t)lds;
#pragma unroll
    for (int m = 0; m < FA; ++m) { const int q4 = l15 >> 2, pp = lane & 3; int mf = m + 2 * wn; mf = mf >= FA ? mf - FA : mf;
        adA[m] = lds0 + (unsigned)((pp >> 1) * 16384 + (8 * q + q4) * 256 + (((wm * FA + mf) ^ (q4 | ((q & 1) << 2) | ((pp >> 1) << 3))) << 4) + 8 * (pp & 1)); }
    unsigned adB[2]; adB[0] = lds0 + (unsigned)(HB0 + (wn * 64 + l15) * 128 + ((q ^ (lane & 7)) << 4)); adB[1] = adB[0] ^ 64u;
    auto rd = [&](auto ic, Frag (&ra)[FA], bf16x8 (&rb)[4], auto kkc) {
        constexpr int kk = decltype(kkc)::value, i = decltype(ic)::value;
        if constexpr (i < 4) rb[i] = lds_read128<i * 2048>(adB[kk]);
        else if constexpr (i % 2 == 0) ra[(i - 4) / 2].lo = tr_read<kk * 8192>(adA[(i - 4) / 2]);
        else ra[(i - 4) / 2].hi = tr_read<kk * 8192 + 1024>(adA[(i - 4) / 2]);
    };
    f32x4 acc[FA][4];
#pragma unroll
    for (int m = 0; m < FA; ++m)
#pragma unroll
        for (int n = 0; n < 4; ++n) acc[m][n] = (f32x4){0.f, 0.f, 0.f, 0.f};
    float cs0 = 0.f, cs1 = 0.f;
#define DSTR 2
#define PR1 16
#define PR2 4
#define PR3 20

    auto phase = [&](const Frag (&ra)[FA], const bf16x8 (&rb)[4], Frag (&na)[FA], bf16x8 (&nb)[4], int dw, int dh, auto kkc, auto grpc, auto&& dma) {
        static_for<0, 28>([&](auto sc) {
            constexpr int s = decltype(sc)::value, m = s >> 2, n = s & 3;
            if constexpr (decltype(kkc)::value == 0) { if constexpr (s == 0) __builtin_amdgcn_s_setprio(3); if constexpr (s == PR1) __builtin_amdgcn_s_setprio(2); }
            else { if constexpr (s == PR2) __builtin_amdgcn_s_setprio(1); if constexpr (s == PR3) __builtin_amdgcn_s_setprio(0); }
            acc[m][n] = MFMA(ra[m].v(), rb[n], acc[m][n]);
            if constexpr (s < 18) rd(sc, na, nb, kkc);
            if constexpr (s % DSTR == 0 && s < 8 * DSTR && decltype(grpc)::value == ((s / DSTR) & 1)) dma(s / (2 * DSTR));
            if constexpr (decltype(kkc)::value == 1 && s >= 18 && s < 27) { if constexpr (s < 25) bump(adA[s - 18], dw); else bump(adB[s - 25], dh); }
            if constexpr (s >= 1 && s < 17) { constexpr int j = s - 1, f = j >> 3, d = (j >> 1) & 3; const unsigned w = d < 2 ? ra[f].lo[d & 1] : ra[f].hi[d & 1];
                vadd(f ? cs1 : cs0, (j & 1) ? bf_hi(w) : bf_lo(w)); }
            sched0();
        });
        wait_lgkm0(); sched0();
    };
    Frag a0[FA], a1[FA]; bf16x8 b0[4], b1[4];
    constexpr std::integral_constant<int, 0> K0{}; constexpr std::integral_constant<int, 1> K1{};
    wait_vm<8>(); bar();
    STAMP(1);
    static_for<0, 18>([&](auto ic) { rd(ic, a0, b0, K0); });
    wait_lgkm0(); sched0();
    auto kloop = [&](auto grpc) {
        int sw2 = 2;
#pragma nounroll
        for (int t = 0; t < NK; ++t) {
            const int sh = t & 1;
            const Src xw = srcW(t + 2), xh = srcH(t + 2);
            phase(a0, b0, a1, b1, sw2 == 1 ? -65536 : 32768, sh ? -32768 : 32768, K1, grpc, [&](int i) { stageW(xw, sw2, i); });
            wait_vm<4>(); bar();
            phase(a1, b1, a0, b0, 0, 0, K0, grpc, [&](int i) { stageH(xh, sh, i); });
            sw2 = sw2 == 2 ? 0 : sw2 + 1;
        }
    };
    kloop(std::integral_constant<int, GRP>{});
    keep(a0, b0);
    wait_vm<0>(); bar();
    STAMP(2);
    LAS float* cs = (LAS float*)lds;
    cs0 = qsum(cs0); cs1 = qsum(cs1);
    if (q == 0) { cs[(wm * FA + 2 * wn) * 16 + l15] = cs0; if (wn < 3) cs[(wm * FA + 2 * wn + 1) * 16 + l15] = cs1; }
    f32x2 rs[4];
#pragma unroll
    for (int n = 0; n < 4; ++n) {
        const f32x4 s0 = *(const LAS f32x4*)(size_t)(adB[0] + n * 2048), s1 = *(const LAS f32x4*)(size_t)(adB[1] + n * 2048);
        const float mean = qsum((s0[0] + s0[2]) + (s1[0] + s1[2])) * (1.f / 16.f);
        const float d0 = s0[0] - mean, d1 = s0[2] - mean, d2 = s1[0] - mean, d3 = s1[2] - mean;
        const float m2 = qsum((s0[1] + s0[3]) + (s1[1] + s1[3]) + 64.f * ((d0 * d0 + d1 * d1) + (d2 * d2 + d3 * d3)));
        rs[n] = (f32x2){mean, __builtin_amdgcn_rsqf(m2 * (1.f / DM) + LN_EPS)};
    }
    wait_lgkm0(); bar();
    f32x4 csum[FA];
#pragma unroll
    for (int m = 0; m < FA; ++m) { int mf = m + 2 * wn; mf = mf >= FA ? mf - FA : mf; csum[m] = *(const LAS f32x4*)(cs + (wm * FA + mf) * 16 + 4 * q); }
    const auto ro = RSRC(p.act, SEQ * FF * 2);
    unsigned fo[2];
#pragma unroll
    for (int g = 0; g < 2; ++g) { int mf = 4 * g + q + 2 * wn; mf = mf >= FA ? mf - FA : mf; fo[g] = (g == 1 && q == 3) ? 0x7ffffff0u : (unsigned)(tm * 224 + wm * 112 + mf * 16); }
#pragma unroll
    for (int n = 0; n < 4; ++n) {
        unsigned o[8];
#pragma unroll
        for (int m = 0; m < FA; ++m) {
            const f32x4 v = (acc[m][n] - rs[n][0] * csum[m]) * rs[n][1];
            float g0 = v[0], g1 = v[1], u0 = v[2], u1 = v[3];
            swap32(g0, u0); swap32(g1, u1);
            const float s0 = g0 * __builtin_amdgcn_rcpf(1.f + __builtin_amdgcn_exp2f(-1.44269504f * g0)), s1 = g1 * __builtin_amdgcn_rcpf(1.f + __builtin_amdgcn_exp2f(-1.44269504f * g1));
            o[m] = pack_bf16(s0 * u0, s1 * u1);
        }
        o[7] = 0u;
        const unsigned rowoff = (unsigned)((tn * 256 + wn * 64 + n * 16 + l15) * FF * 2);
#pragma unroll
        for (int g = 0; g < 2; ++g) {
            unsigned r0 = o[4 * g], r1 = o[4 * g + 1], r2 = o[4 * g + 2], r3 = o[4 * g + 3];
            swap16u(r0, r1); swap16u(r2, r3); swap32u(r0, r2); swap32u(r1, r3);
            __builtin_amdgcn_raw_buffer_store_b128((u32x4){r0, r2, r1, r3}, ro, rowoff + fo[g], 0, SC1);
        }
    }
    STAMP_END(3);
}
#endif
__global__ __launch_bounds__(512, 2) void gemm3_k(const Params p) {
#if defined(__HIP_DEVICE_COMPILE__)
    __shared__ __attribute__((aligned(16))) unsigned char lds_raw[5 * 32768];
    const int wid = __builtin_amdgcn_readfirstlane(threadIdx.x >> 6);
    if (wid < 4) gemm3_body<0>(p, (LAS unsigned char*)lds_raw, wid); else gemm3_body<1>(p, (LAS unsigned char*)lds_raw, wid);
#endif
}

template __global__ void gemm128_k<1>(const Params); template __global__ void gemm128_k<2>(const Params); template __global__ void gemm128_k<4>(const Params);
template <int MODE> __global__ __launch_bounds__(512) void pre_k(const Params p) {
#if defined(__HIP_DEVICE_COMPILE__)
    const unsigned long long t0 = __builtin_amdgcn_s_memrealtime();
    const int idx = blockIdx.x >> 3, g = idx * 512 + threadIdx.x;
    unsigned a = 0;
    if (MODE & 1) {
        const u16* ptr[13] = {p.x, p.cre, p.cim, p.wre, p.wim, p.wg, p.wu, p.wd, p.out, p.abT, p.h, p.act, p.xch};
        const int pages[13] = {1024, 2048, 2048, 512, 512, 1792, 1792, 1792, 1024, 2048, 2048, 3584, 2048};
        const int gg = (threadIdx.x >> 2) * 32 + idx;
        unsigned v[13];
#pragma unroll
        for (int i = 0; i < 13; ++i) v[i] = *(const unsigned*)(ptr[i] + (size_t)(gg < pages[i] ? gg : 0) * 2048);
#pragma unroll
        for (int i = 0; i < 13; ++i) a += v[i];
    }
    if (MODE & 2) {
        unsigned long long lo, hi; int rel;
        asm volatile("s_getpc_b64 %0\n s_mov_b32 %1, _Z7gemm3_k6Params@rel32@lo+4" : "=s"(hi), "=s"(rel));
        lo = hi + (long long)rel;
        const unsigned long long ad = lo + (unsigned long long)g * 16;
        if (ad + 16 <= hi) { const u32x4 v = *(const u32x4*)ad; a += v[0] + v[3]; }
    }
    if (MODE & 4) {
        const u16* ptr[8] = {p.x, p.wre, p.wim, p.cre, p.cim, p.wg, p.wu, p.wd};
        const int n16[8] = {262144, 131072, 131072, 524288, 524288, 458752, 458752, 458752};
        const int gg = blockIdx.x * 512 + threadIdx.x;
#pragma unroll
        for (int i = 0; i < 8; ++i)
#pragma unroll
            for (int j = 0; j < 4; ++j) { const int e = gg + j * 131072; const u32x4 v = *(const u32x4*)(ptr[i] + (size_t)(e < n16[i] ? e : 0) * 8); a += v[0] + v[3]; }
    }
    if (MODE & 8) {
        const int gg = blockIdx.x * 512 + threadIdx.x;
#pragma unroll
        for (int j = 0; j < 20; ++j) *(u32x4*)(p.abT + (size_t)(gg + j * 131072) * 8) = (u32x4){1u, 2u, 3u, 4u};
#pragma unroll
        for (int j = 0; j < 2; ++j) *(u32x4*)(p.out + (size_t)(gg + j * 131072) * 8) = (u32x4){1u, 2u, 3u, 4u};
        __builtin_amdgcn_fence(__ATOMIC_RELEASE, "agent");
    }
    asm volatile("s_waitcnt vmcnt(0)" : "+v"(a) :: "memory");
    while (__builtin_amdgcn_s_memrealtime() - t0 < 2500) __builtin_amdgcn_s_sleep(2);
    if (a == 0x12345678u && p.x == nullptr) p.out[0] = 0;
#endif
}
extern "C" void kernel_launch(void* const* d_in, const int* in_sizes, int n_in, void* d_out, int out_size, void* d_ws, size_t ws_size, hipStream_t stream) {
    Params p;
    p.x = (const u16*)d_in[0]; p.cre = (const u16*)d_in[1]; p.cim = (const u16*)d_in[2]; p.wre = (const u16*)d_in[3]; p.wim = (const u16*)d_in[4];
    p.wg = (const u16*)d_in[6]; p.wu = (const u16*)d_in[7]; p.wd = (const u16*)d_in[8];
    p.out = (u16*)d_out;
    char* ws = (char*)d_ws;
    p.abT = (u16*)ws; p.h = (u16*)(ws + (8u << 20)); p.act = (u16*)(ws + (16u << 20));
    p.xch = (u16*)(ws + (32u << 20));
    pre_k<4><<<256, 512, 0, stream>>>(p);
    gemm128_k<1><<<256, 512, 0, stream>>>(p);
    gemm128_k<2><<<256, 512, 0, stream>>>(p);
    gemm3_k<<<256, 512, 0, stream>>>(p);
    gemm128_k<4><<<256, 512, 0, stream>>>(p);
}
```

```cpp
#include <hip/hip_runtime.h>
#include <hip/hip_bf16.h>
#include <type_traits>

#define LAS __attribute__((address_space(3)))
typedef unsigned short u16;
typedef __bf16 bf16x8 __attribute__((ext_vector_type(8)));
typedef __bf16 bf16x2 __attribute__((ext_vector_type(2)));
typedef float f32x4 __attribute__((ext_vector_type(4)));
typedef float f32x2 __attribute__((ext_vector_type(2)));
typedef unsigned u32x4 __attribute__((ext_vector_type(4)));
typedef unsigned u32x2 __attribute__((ext_vector_type(2)));

constexpr int SEQ = 2048, DM = 1024, FF = 3584;
constexpr float LN_EPS = 1e-5f;
constexpr int SC1 = 16;

struct Params {
    const u16* x; const u16* cre; const u16* cim; const u16* wre; const u16* wim;
    const u16* wg; const u16* wu; const u16* wd;
    u16* out; u16* abT; u16* h; u16* act; u16* xch; unsigned* flags;
};

template <int N> __device__ __forceinline__ void wait_vm() { asm volatile("s_waitcnt vmcnt(%0)" ::"n"(N) : "memory"); }
__device__ __forceinline__ void wait_lgkm0() { __builtin_amdgcn_s_waitcnt(0xC07F); asm volatile("s_waitcnt lgkmcnt(0)" ::: "memory"); }
__device__ __forceinline__ void sched0() { __builtin_amdgcn_sched_barrier(0); }
__device__ __forceinline__ void bar() { sched0(); asm volatile("" ::: "memory"); __builtin_amdgcn_s_barrier(); asm volatile("" ::: "memory"); sched0(); }
__device__ __forceinline__ unsigned pack_bf16(float lo, float hi) { bf16x2 c; c[0] = (__bf16)lo; c[1] = (__bf16)hi; return __builtin_bit_cast(unsigned, c); }
__device__ __forceinline__ float bf_lo(unsigned v) { return __uint_as_float(v << 16); }
__device__ __forceinline__ float bf_hi(unsigned v) { return __uint_as_float(v & 0xffff0000u); }
__device__ __forceinline__ void swap16(float& a, float& b) { auto r = __builtin_amdgcn_permlane16_swap(__float_as_uint(a), __float_as_uint(b), false, false); a = __uint_as_float(r[0]); b = __uint_as_float(r[1]); }
__device__ __forceinline__ void swap32(float& a, float& b) { auto r = __builtin_amdgcn_permlane32_swap(__float_as_uint(a), __float_as_uint(b), false, false); a = __uint_as_float(r[0]); b = __uint_as_float(r[1]); }
__device__ __forceinline__ void swap16u(unsigned& a, unsigned& b) { auto r = __builtin_amdgcn_permlane16_swap(a, b, false, false); a = r[0]; b = r[1]; }
__device__ __forceinline__ void swap32u(unsigned& a, unsigned& b) { auto r = __builtin_amdgcn_permlane32_swap(a, b, false, false); a = r[0]; b = r[1]; }
__device__ __forceinline__ float qsum(float v) { float a = v, b = v; swap16(a, b); a += b; b = a; swap32(a, b); return a + b; }

struct Frag { u32x2 lo, hi; __device__ __forceinline__ bf16x8 v() const { return __builtin_bit_cast(bf16x8, __builtin_shufflevector(lo, hi, 0, 1, 2, 3)); } };
template <int OFF> __device__ __forceinline__ u32x2 tr_read(unsigned addr) { u32x2 r; asm volatile("ds_read_b64_tr_b16 %0, %1 offset:%2" : "=v"(r) : "v"(addr), "n"(OFF) : "memory"); return r; }
template <int OFF> __device__ __forceinline__ bf16x8 lds_read128(unsigned addr) { bf16x8 r; asm volatile("ds_read_b128 %0, %1 offset:%2" : "=v"(r) : "v"(addr), "n"(OFF) : "memory"); return r; }
template <int N> __device__ __forceinline__ void wait_lgkm() { asm volatile("s_waitcnt lgkmcnt(%0)" ::"n"(N) : "memory"); }
template <int I, int N, class F> __device__ __forceinline__ void static_for(F&& f) { if constexpr (I < N) { f(std::integral_constant<int, I>{}); static_for<I + 1, N>(f); } }
template <int NA> __device__ __forceinline__ void keep(const Frag (&a)[NA], const bf16x8 (&b)[4]) {
#pragma unroll
    for (int i = 0; i < NA; ++i) asm volatile("" ::"v"(a[i].lo), "v"(a[i].hi));
#pragma unroll
    for (int i = 0; i < 4; ++i) asm volatile("" ::"v"(b[i]));
}
__device__ __forceinline__ void vadd(float& a, float b) { asm volatile("v_add_f32 %0, %0, %1" : "+v"(a) : "v"(b)); }
__device__ __forceinline__ void bump(unsigned& a, int d) { asm volatile("v_add_u32 %0, %1, %0" : "+v"(a) : "s"(d)); }
#define MFMA(a, b, c) __builtin_amdgcn_mfma_f32_16x16x32_bf16(a, b, c, 0, 0, 0)
#define RSRC(ptr, bytes) __builtin_amdgcn_make_buffer_rsrc((void*)(ptr), 0, (bytes), 0x00020000)
#define DMA16(rs, ldsp, voff, soff) __builtin_amdgcn_raw_ptr_buffer_load_lds(rs, (LAS void*)(ldsp), 16, voff, soff, 0, 0)
#define DMA16_DEV(rs, ldsp, voff, soff) __builtin_amdgcn_raw_ptr_buffer_load_lds(rs, (LAS void*)(ldsp), 16, voff, soff, 0, SC1)

#define STAMP(i)
#define STAMP_BEGIN
#define STAMP_END(ID)

enum { ROW, CHK, TR };
#define NKS2 28
#define NKS4 24
template <int ID> struct Cfg;
template <> struct Cfg<1> { static constexpr int NKT = 16, NKS = 16, FA = ROW, FB = ROW, LDB = DM; };
template <> struct Cfg<2> { static constexpr int NKT = 64, NKS = NKS2, FA = CHK, FB = ROW, LDB = SEQ; };
template <> struct Cfg<4> { static constexpr int NKT = 56, NKS = NKS4, FA = TR, FB = CHK, LDB = 0; };

template <int ID>
__global__ __launch_bounds__(512, 2) void gemm128_k(const Params p) {
#if defined(__HIP_DEVICE_COMPILE__)
    using C = Cfg<ID>;
    constexpr int STAGE = 32768;
    __shared__ __attribute__((aligned(16))) unsigned char lds_raw[4 * STAGE + (ID == 1 ? 1536 : 0)];
    LAS unsigned char* lds = (LAS unsigned char*)lds_raw;
    STAMP_BEGIN;
    const int tid = threadIdx.x, lane = tid & 63, wid = __builtin_amdgcn_readfirstlane(tid >> 6);
    const int l15 = lane & 15, q = lane >> 4;
    const int xcd = blockIdx.x & 7, idx = blockIdx.x >> 3, im = idx & 3, in = idx >> 2;
    const bool recv = ID != 1 && (xcd & 1);
    const int tm = (ID == 1 ? (xcd & 3) : ((xcd >> 1) & 1)) * 4 + im, tn = (xcd >> 2) * 8 + in;
    const int k0 = recv ? C::NKS : 0, nk = recv ? C::NKT - C::NKS : C::NKS;
    unsigned* const flag = p.flags + ((ID == 4 ? 512 : 0) + (blockIdx.x >> 1) * 4 + (wid & 3)) * 16;
    const unsigned xoff = (unsigned)((blockIdx.x >> 1) * 32768 + (wid & 3) * 8192);

    if (wid >= 4) {
        const int lw = wid - 4, phi = (im + in) & 3;
        const int koff = ID == 1 ? xcd * 2 : ((xcd >> (ID == 2 ? 1 : 2)) & 1) * (nk >> 1);
        if constexpr (ID == 1) { if (lane == 0) p.flags[(blockIdx.x * 4 + lw) * 16] = 0u; }
        const unsigned ksw = (unsigned)(((lane & 7) ^ (lane >> 3)) << 4);
        unsigned voffA[2], voffB;
        if constexpr (C::FA == ROW) voffA[0] = (unsigned)((tm * 128 + lw * 32 + (lane >> 3)) * DM * 2) + ksw;
        if constexpr (C::FA == CHK) voffA[0] = (unsigned)((lw * 2 * DM + tm * 128 + lane) * 16);
        if constexpr (C::FA == TR) {
#pragma unroll
            for (int i = 0; i < 2; ++i) voffA[i] = (unsigned)((lw * 16 + q) * DM * 2 + tm * 256 + ((l15 ^ ((q | (i << 2)) << 1)) << 4));
        }
        if constexpr (C::FB == ROW) voffB = (unsigned)((((ID == 1 ? tn & 7 : tn)) * 128 + lw * 32 + (lane >> 3)) * C::LDB * 2) + ksw;
        else voffB = (unsigned)((lw * 2 * SEQ + tn * 128 + lane) * 16);
        const auto ra = ID == 1 ? RSRC(p.x, SEQ * DM * 2) : ID == 2 ? RSRC(p.abT, 2 * SEQ * DM * 2) : RSRC(p.wd, FF * DM * 2);
        auto stage = [&](int t) {
            int k = (t & 3) != phi ? t : t + 4 < nk ? t + 4 : phi;
            k += koff; k = k0 + (k >= nk ? k - nk : k);
            const auto rb = ID == 1 ? RSRC(tn < 8 ? p.wre : p.wim, DM * DM * 2) : ID == 2 ? RSRC(k < 32 ? p.cre : p.cim, SEQ * SEQ * 2) : RSRC(p.act, SEQ * FF * 2);
            LAS unsigned char* base = lds + (t & 3) * STAGE + lw * 4096;
#pragma unroll
            for (int i = 0; i < 4; ++i) {
                if constexpr (C::FA == ROW) DMA16(ra, base + i * 1024, voffA[0], k * 128 + i * 8 * DM * 2);
                if constexpr (C::FA == CHK) DMA16(ra, base + i * 1024, voffA[0], (k * 8 + (i >> 1)) * DM * 16 + (i & 1) * 1024);
                if constexpr (C::FA == TR) DMA16(ra, base + i * 1024, voffA[i >> 1], (k * 64 + i * 4) * DM * 2);
                if constexpr (C::FB == ROW) DMA16(rb, base + 16384 + i * 1024, voffB, (k & 31) * 128 + i * 8 * C::LDB * 2);
                else DMA16(rb, base + 16384 + i * 1024, voffB, (k * 8 + (i >> 1)) * SEQ * 16 + (i & 1) * 1024);
            }
        };
        stage(0); stage(1);
        wait_vm<8>(); bar();
        stage(2);
#pragma nounroll
        for (int t = 0; t < nk - 3; ++t) {
            wait_vm<8>(); bar();
            stage(t + 3);
        }
        bool got = !recv;
        auto fetch = [&]() {
            if (__builtin_amdgcn_readfirstlane(__hip_atomic_load(flag, __ATOMIC_RELAXED, __HIP_MEMORY_SCOPE_AGENT)) == 0u) return false;
            asm volatile("" ::: "memory");
#pragma unroll
            for (int i = 0; i < 8; ++i) DMA16_DEV(RSRC(p.xch, 128 * 32768), lds + (nk & 3) * STAGE + lw * 8192 + i * 1024, (unsigned)(lane * 16), xoff + i * 1024);
            return true;
        };
        wait_vm<8>(); bar();
        wait_vm<0>();
        unsigned pf[3];
        if constexpr (ID == 1) {
            const int nk2 = (xcd & 1) ? 64 - NKS2 : NKS2, kb = ((xcd & 1) ? NKS2 : 0) + ((xcd >> 1) & 1) * (nk2 >> 1);
            const int row = (xcd >> 2) * 1024 + idx * 32 + lw * 8 + (lane >> 3);
#pragma unroll
            for (int i = 0; i < 3; ++i) { const int j = i * 8 + (lane & 7), k = kb + j;
                const u16* src = (k < 32 ? p.cre : p.cim) + row * SEQ + (j < (nk2 + 1) / 2 && k < 64 ? (k & 31) : (kb & 31)) * 64;
                asm volatile("global_load_dword %0, %1, off" : "=v"(pf[i]) : "v"(src) : "memory"); }
        }
        if (!got) got = fetch();
        bar();
        if (!got) got = fetch();
        bar();
        if (recv) {
#pragma nounroll
            for (int spin = 0; !got && spin < (1 << 20); ++spin) { got = fetch(); __builtin_amdgcn_s_sleep(1); }
            wait_vm<0>(); bar();
        }
        if constexpr (ID == 1) {
            bar();
            wait_vm<0>(); asm volatile("" ::"v"(pf[0]), "v"(pf[1]), "v"(pf[2]));
        }
        return;
    }

    const int wm = wid & 1, wn = wid >> 1;
    const int eo = (q & 1) * 16 + (q >> 1) * 8;
    f32x4 acc[4][4], st[6];
#pragma unroll
    for (int m = 0; m < 4; ++m)
#pragma unroll
        for (int n = 0; n < 4; ++n) acc[m][n] = (f32x4){0.f, 0.f, 0.f, 0.f};
#pragma unroll
    for (int i = 0; i < 6; ++i) st[i] = (f32x4){0.f, 0.f, 0.f, 0.f};
    const int row0 = tn * 128 + wn * 64 + l15, col0 = tm * 128 + wm * 64 + eo;
    if (ID != 1 && !recv) {
        const auto rr = RSRC(ID == 2 ? p.x : p.h, SEQ * DM * 2);
#pragma unroll
        for (int n = 0; n < 4; ++n)
#pragma unroll
            for (int mp = 0; mp < 2; ++mp) {
                u32x4 r = __builtin_amdgcn_raw_buffer_load_b128(rr, (unsigned)(ID == 2 ? ((row0 + n * 16) * DM + col0 + mp * 32) * 2 : ((col0 / 8 + mp * 4) * SEQ + row0 + n * 16) * 16), 0, 0);
                unsigned r0 = r[0], r1 = r[1], r2 = r[2], r3 = r[3];
                swap16u(r0, r2); swap16u(r1, r3);
                acc[2 * mp][n] = (f32x4){bf_lo(r0), bf_hi(r0), bf_lo(r1), bf_hi(r1)}; acc[2 * mp + 1][n] = (f32x4){bf_lo(r2), bf_hi(r2), bf_lo(r3), bf_hi(r3)};
            }
    }
    const int rotA = ID == 1 ? 2 * wn : 0, rotB = ID == 1 ? 2 * wm : 0;
    constexpr int NA = C::FA == CHK ? 1 : 4, NB = C::FB == CHK ? 1 : ID == 1 ? 4 : 2;
    unsigned adA[NA], adB[NB];
    const unsigned lds0 = (unsigned)(size_t)lds, row_sw = lds0 + (unsigned)(l15 * 128 + ((q ^ (lane & 7)) << 4)), chk = lds0 + (unsigned)(q * 2048 + l15 * 16);
#pragma unroll
    for (int i = 0; i < NA; ++i) {
        if constexpr (C::FA == ROW) adA[i] = (row_sw ^ (unsigned)((i >> 1) * 64)) + (unsigned)((wm * 4 + ((2 * (i & 1) + rotA) & 3)) * 2048);
        if constexpr (C::FA == CHK) adA[i] = chk + (unsigned)(wm * 1024);
        if constexpr (C::FA == TR) { const int q4 = l15 >> 2; adA[i] = lds0 + (unsigned)((8 * q + q4) * 256 + (((wm * 4 + i) ^ (q4 | ((q & 1) << 2))) << 5) + 8 * (lane & 3)); }
    }
#pragma unroll
    for (int i = 0; i < NB; ++i) {
        if constexpr (C::FB == CHK) adB[i] = 16384u + chk + (unsigned)(wn * 1024);
        else if constexpr (ID == 1) adB[i] = 16384u + (row_sw ^ (unsigned)((i >> 1) * 64)) + (unsigned)((wn * 4 + ((2 * (i & 1) + rotB) & 3)) * 2048);
        else adB[i] = 16384u + (row_sw ^ (unsigned)(i * 64)) + (unsigned)(wn * 8192);
    }
    constexpr int W = C::FA == TR ? 2 : 1, NR = 4 + 4 * W;
    auto rd = [&](auto ic, Frag (&ra)[4], bf16x8 (&rb)[4], auto kkc) {
        constexpr int kk = decltype(kkc)::value, i = decltype(ic)::value;
        if constexpr (i == 0 || (i > W && i <= W + 3)) { constexpr int n = i == 0 ? 0 : i - W;
            if constexpr (C::FB == CHK) rb[n] = lds_read128<kk * 8192 + n * 256>(adB[0]);
            else if constexpr (ID == 1) rb[n] = lds_read128<(n & 1) * 2048>(adB[kk * 2 + (n >> 1)]);
            else rb[n] = lds_read128<n * 2048>(adB[kk]); }
        else { constexpr int j = i <= W ? i - 1 : i - 4, m = j / W;
            if constexpr (C::FA != TR) { bf16x8 v;
                if constexpr (C::FA == CHK) v = lds_read128<kk * 8192 + m * 256>(adA[0]); else v = lds_read128<(m & 1) * 2048>(adA[kk * 2 + (m >> 1)]);
                const u32x4 w = __builtin_bit_cast(u32x4, v); ra[m].lo = (u32x2){w[0], w[1]}; ra[m].hi = (u32x2){w[2], w[3]}; }
            else if constexpr (j % 2 == 0) ra[m].lo = tr_read<kk * 8192>(adA[m]);
            else ra[m].hi = tr_read<kk * 8192 + 1024>(adA[m]); }
    };
    const bf16x8 ones = __builtin_bit_cast(bf16x8, (u32x4){0x3f803f80u, 0x3f803f80u, 0x3f803f80u, 0x3f803f80u});
    auto phase = [&](const Frag (&ra)[4], const bf16x8 (&rb)[4], Frag (&na)[4], bf16x8 (&nb)[4], int dstep, auto kkc) {
        static_for<0, 16>([&](auto sc) {
            constexpr int s = decltype(sc)::value, m = s >> 2, n = s & 3;
            if constexpr (n == 0) { wait_lgkm<(3 - m) * W + (s < NR ? s : NR)>(); sched0(); }
            acc[m][n] = MFMA(ra[m].v(), rb[n], acc[m][n]);
            if constexpr (s < NR) rd(sc, na, nb, kkc);
            if constexpr (s >= 12 && decltype(kkc)::value == 1) {
                if constexpr (s - 12 < NA) bump(adA[s - 12], dstep);
                if constexpr (s - 12 < NB) bump(adB[s - 12], dstep);
            }
            if constexpr (ID == 1 && s >= 10) {
                constexpr int i = s & 1;
                if constexpr (s < 12) st[i] = MFMA(ones, ra[i].v(), st[i]); else if constexpr (s < 14) st[2 + i] = MFMA(ra[i].v(), ra[i].v(), st[2 + i]); else st[4 + i] = MFMA(ones, rb[i], st[4 + i]); }
            sched0();
        });
    };
    Frag a0[4], a1[4]; bf16x8 b0[4], b1[4];
    constexpr std::integral_constant<int, 0> K0{}; constexpr std::integral_constant<int, 1> K1{};
    bar();
    STAMP(1);
    static_for<0, NR>([&](auto ic) { rd(ic, a0, b0, K0); });
    sched0();
#pragma nounroll
    for (int t = 0; t < nk; ++t) {
        phase(a0, b0, a1, b1, (t & 3) == 3 ? -3 * STAGE : STAGE, K1);
        bar();
        phase(a1, b1, a0, b0, 0, K0);
    }
    wait_lgkm0(); keep(a0, b0); sched0();
    STAMP(2);
    if constexpr (ID == 1) {
        LAS float* tab = (LAS float*)(lds + 4 * STAGE);
        const float sgn = tn < 8 ? 1.f : -1.f;
        if (q == (l15 >> 2)) {
#pragma unroll
            for (int i = 0; i < 2; ++i) {
                const int j = l15 & 3; const float sq = j == 0 ? st[2 + i][0] : j == 1 ? st[2 + i][1] : j == 2 ? st[2 + i][2] : st[2 + i][3];
                const float mean = st[i][0] * (1.f / DM);
                const int ra = wm * 64 + (rotA + i) * 16 + l15;
                tab[ra] = mean; tab[128 + ra] = sgn * __builtin_amdgcn_rsqf(fmaxf(sq * (1.f / DM) - mean * mean, 0.f) + LN_EPS);
                tab[256 + wn * 64 + (rotB + i) * 16 + l15] = st[4 + i][0];
            }
        }
        wait_lgkm0(); bar();
        const auto ro = RSRC(p.abT, 2 * SEQ * DM * 2);
        float ws[4];
#pragma unroll
        for (int n = 0; n < 4; ++n) ws[n] = tab[256 + wn * 64 + ((n + rotB) & 3) * 16 + l15];
#pragma unroll
        for (int m = 0; m < 4; ++m) {
            const int r0 = wm * 64 + ((m + rotA) & 3) * 16 + 4 * q;
            const f32x4 mean = *(const LAS f32x4*)(tab + r0), rs = *(const LAS f32x4*)(tab + 128 + r0);
#pragma unroll
            for (int n = 0; n < 4; ++n) acc[m][n] = (acc[m][n] - mean * ws[n]) * rs;
        }
#pragma unroll
        for (int n = 0; n < 4; ++n) { const int e = (tn & 7) * 128 + wn * 64 + ((n + rotB) & 3) * 16 + l15;
#pragma unroll
            for (int mp = 0; mp < 2; ++mp) {
                unsigned x0 = pack_bf16(acc[2 * mp][n][0], acc[2 * mp][n][1]), x1 = pack_bf16(acc[2 * mp][n][2], acc[2 * mp][n][3]);
                unsigned y0 = pack_bf16(acc[2 * mp + 1][n][0], acc[2 * mp + 1][n][1]), y1 = pack_bf16(acc[2 * mp + 1][n][2], acc[2 * mp + 1][n][3]);
                swap16u(x0, y0); swap16u(x1, y1);
                const int s = (tn >> 3) * SEQ + tm * 128 + wm * 64 + ((2 * mp + rotA) & 3) * 16 + eo;
                __builtin_amdgcn_raw_buffer_store_b128((u32x4){x0, x1, y0, y1}, ro, (unsigned)(((s >> 3) * DM + e) * 16), 0, SC1);
            } }
    } else if (!recv) {
        const auto rx = RSRC(p.xch, 128 * 32768);
#pragma unroll
        for (int m = 0; m < 4; ++m)
#pragma unroll
            for (int h = 0; h < 2; ++h) {
                const f32x4 u = acc[m][2 * h], v = acc[m][2 * h + 1];
                __builtin_amdgcn_raw_buffer_store_b128((u32x4){pack_bf16(u[0], u[1]), pack_bf16(u[2], u[3]), pack_bf16(v[0], v[1]), pack_bf16(v[2], v[3])}, rx, xoff + (unsigned)((m * 2 + h) * 1024 + lane * 16), 0, SC1);
            }
        wait_vm<0>();
        if (lane == 0) __hip_atomic_store(flag, 1u, __ATOMIC_RELAXED, __HIP_MEMORY_SCOPE_AGENT);
        if constexpr (ID == 2) {
            const auto rg = RSRC(p.wg, DM * FF * 2); const auto ru = RSRC(p.wu, DM * FF * 2);
            const unsigned o = (unsigned)((((blockIdx.x >> 1) * 4 + wid) * 128 + lane) * 128);
            const unsigned t0 = __builtin_amdgcn_raw_buffer_load_b32(rg, o, 0, 0), t1 = __builtin_amdgcn_raw_buffer_load_b32(ru, o, 0, 0);
            const unsigned t2 = __builtin_amdgcn_raw_buffer_load_b32(rg, o + 8192, 0, 0), t3 = __builtin_amdgcn_raw_buffer_load_b32(ru, o + 8192, 0, 0);
            asm volatile("" ::"v"(t0), "v"(t1), "v"(t2), "v"(t3));
        }
    } else {
        bar();
        STAMP(4);
        const LAS u32x4* px = (const LAS u32x4*)(lds + (nk & 3) * STAGE + wid * 8192 + lane * 16);
#pragma unroll
        for (int m = 0; m < 4; ++m)
#pragma unroll
            for (int h = 0; h < 2; ++h) { const u32x4 w = px[(m * 2 + h) * 64];
                acc[m][2 * h] += (f32x4){bf_lo(w[0]), bf_hi(w[0]), bf_lo(w[1]), bf_hi(w[1])}; acc[m][2 * h + 1] += (f32x4){bf_lo(w[2]), bf_hi(w[2]), bf_lo(w[3]), bf_hi(w[3])}; }
        const auto ro = RSRC(ID == 2 ? p.h : p.out, SEQ * DM * 2);
#pragma unroll
        for (int n = 0; n < 4; ++n) {
            unsigned o[4][2];
#pragma unroll
            for (int m = 0; m < 4; ++m) { o[m][0] = pack_bf16(acc[m][n][0], acc[m][n][1]); o[m][1] = pack_bf16(acc[m][n][2], acc[m][n][3]); }
            if constexpr (ID == 2) {
                float s = 0.f;
#pragma unroll
                for (int m = 0; m < 4; ++m) s += (bf_lo(o[m][0]) + bf_hi(o[m][0])) + (bf_lo(o[m][1]) + bf_hi(o[m][1]));
                const float mw = qsum(s) * (1.f / 64.f); float m2 = 0.f;
#pragma unroll
                for (int m = 0; m < 4; ++m)
#pragma unroll
                    for (int j = 0; j < 2; ++j) { const float d0 = bf_lo(o[m][j]) - mw, d1 = bf_hi(o[m][j]) - mw; m2 += d0 * d0 + d1 * d1; }
                m2 = qsum(m2);
                if (q == 0) *(f32x2*)(p.h + ((128 + tm) * SEQ + row0 + n * 16) * 8 + wm * 4) = (f32x2){mw, m2};
            }
#pragma unroll
            for (int mp = 0; mp < 2; ++mp) {
                swap16u(o[2 * mp][0], o[2 * mp + 1][0]); swap16u(o[2 * mp][1], o[2 * mp + 1][1]);
                __builtin_amdgcn_raw_buffer_store_b128((u32x4){o[2 * mp][0], o[2 * mp][1], o[2 * mp + 1][0], o[2 * mp + 1][1]}, ro,
                    (unsigned)(ID == 4 ? ((row0 + n * 16) * DM + col0 + mp * 32) * 2 : ((col0 / 8 + mp * 4) * SEQ + row0 + n * 16) * 16), 0, SC1);
            }
        }
        STAMP(5);
    }
    STAMP_END(ID);
#endif
}

#if defined(__HIP_DEVICE_COMPILE__)
template <int GRP>
__device__ __forceinline__ void gemm3_body(const Params& p, LAS unsigned char* lds, const int wid) {
    constexpr int NK = 16, FA = 7, HB0 = 3 * 32768;
    STAMP_BEGIN;
    const int tid = threadIdx.x, lane = tid & 63;
    const int wm = wid & 1, wn = wid >> 1, l15 = lane & 15, q = lane >> 4;
    const int xcd = blockIdx.x & 7, idx = blockIdx.x >> 3, im = idx & 7, in = idx >> 3;
    const int tm = (xcd & 3) * 8 + im, tn = (xcd >> 2) * 4 + in;
    const int phi = (im + in) & 3, koff = (xcd * 2) & (NK - 1);

    const u16* aptr = GRP ? p.wu : p.wg;
    unsigned voffA[2];
#pragma unroll
    for (int i = 0; i < 2; ++i) { const int j = l15 ^ (q | (i << 2) | (GRP << 3));
        voffA[i] = j < 14 ? (unsigned)(((wid & 3) * 16 + q) * FF * 2 + tm * 224 + j * 16) : 0x7ffffff0u; }
    const unsigned voffB = (unsigned)((wid * SEQ + tn * 256 + lane) * 16);
    auto kstep = [&](int t) { return (t + koff + ((t & 3) == phi ? 4 : 0)) & (NK - 1); };
    struct Src { int so; unsigned nrec; };
    auto pin = [](Src x) { asm volatile("" : "+s"(x.so), "+s"(x.nrec)); return x; };
    auto srcW = [&](int t) { return pin(Src{kstep(t) * 64 * FF * 2, t < NK ? (unsigned)(DM * FF * 2) : 0u}); };
    auto srcH = [&](int t) { return pin(Src{(t < NK ? kstep(t) : NK) * 8 * SEQ * 16, t <= NK ? (unsigned)(136 * SEQ * 16) : 0u}); };
    auto stageW = [&](Src x, int slot, int i) { DMA16(RSRC(aptr, x.nrec), lds + slot * 32768 + wid * 4096 + i * 1024, voffA[i >> 1], x.so + i * 4 * FF * 2); };
    auto stageH = [&](Src x, int slot, int i) { DMA16(RSRC(p.h, x.nrec), lds + HB0 + slot * 32768 + wid * 4096 + i * 1024, voffB, x.so + i * 1024); };
#pragma unroll
    for (int t = 0; t < 2; ++t) {
        const Src xw = srcW(t), xh = srcH(t);
#pragma unroll
        for (int i = 0; i < 4; ++i) stageW(xw, t, i);
#pragma unroll
        for (int i = 0; i < 4; ++i) stageH(xh, t, i);
    }

    unsigned adA[FA];
    const unsigned lds0 = (unsigned)(size_t)lds;
#pragma unroll
    for (int m = 0; m < FA; ++m) { const int q4 = l15 >> 2, pp = lane & 3; int mf = m + 2 * wn; mf = mf >= FA ? mf - FA : mf;
        adA[m] = lds0 + (unsigned)((pp >> 1) * 16384 + (8 * q + q4) * 256 + (((wm * FA + mf) ^ (q4 | ((q & 1) << 2) | ((pp >> 1) << 3))) << 4) + 8 * (pp & 1)); }
    unsigned adB = lds0 + (unsigned)(HB0 + q * 4096 + (wn * 64 + l15) * 16);
    auto rd = [&](auto ic, Frag (&ra)[FA], bf16x8 (&rb)[4], auto kkc) {
        constexpr int kk = decltype(kkc)::value, i = decltype(ic)::value;
        if constexpr (i < 4) rb[i] = lds_read128<kk * 16384 + i * 256>(adB);
        else if constexpr (i % 2 == 0) ra[(i - 4) / 2].lo = tr_read<kk * 8192>(adA[(i - 4) / 2]);
        else ra[(i - 4) / 2].hi = tr_read<kk * 8192 + 1024>(adA[(i - 4) / 2]);
    };
    f32x4 acc[FA][4];
#pragma unroll
    for (int m = 0; m < FA; ++m)
#pragma unroll
        for (int n = 0; n < 4; ++n) acc[m][n] = (f32x4){0.f, 0.f, 0.f, 0.f};
    float cs0 = 0.f, cs1 = 0.f;
#define DSTR 2
#define PR1 16
#define PR2 4
#define PR3 20

    auto phase = [&](const Frag (&ra)[FA], const bf16x8 (&rb)[4], Frag (&na)[FA], bf16x8 (&nb)[4], int dw, int dh, auto kkc, auto grpc, auto&& dma) {
        static_for<0, 28>([&](auto sc) {
            constexpr int s = decltype(sc)::value, m = s >> 2, n = s & 3;
            if constexpr (decltype(kkc)::value == 0) { if constexpr (s == 0) __builtin_amdgcn_s_setprio(3); if constexpr (s == PR1) __builtin_amdgcn_s_setprio(2); }
            else { if constexpr (s == PR2) __builtin_amdgcn_s_setprio(1); if constexpr (s == PR3) __builtin_amdgcn_s_setprio(0); }
            acc[m][n] = MFMA(ra[m].v(), rb[n], acc[m][n]);
            if constexpr (s < 18) rd(sc, na, nb, kkc);
            if constexpr (s % DSTR == 0 && s < 8 * DSTR && decltype(grpc)::value == ((s / DSTR) & 1)) dma(s / (2 * DSTR));
            if constexpr (decltype(kkc)::value == 1 && s >= 18 && s < 26) { if constexpr (s < 25) bump(adA[s - 18], dw); else bump(adB, dh); }
            if constexpr (s >= 1 && s < 17) { constexpr int j = s - 1, f = j >> 3, d = (j >> 1) & 3; const unsigned w = d < 2 ? ra[f].lo[d & 1] : ra[f].hi[d & 1];
                vadd(f ? cs1 : cs0, (j & 1) ? bf_hi(w) : bf_lo(w)); }
            sched0();
        });
        wait_lgkm0(); sched0();
    };
    Frag a0[FA], a1[FA]; bf16x8 b0[4], b1[4];
    constexpr std::integral_constant<int, 0> K0{}; constexpr std::integral_constant<int, 1> K1{};
    wait_vm<8>(); bar();
    STAMP(1);
    static_for<0, 18>([&](auto ic) { rd(ic, a0, b0, K0); });
    wait_lgkm0(); sched0();
    auto kloop = [&](auto grpc) {
        int sw2 = 2;
#pragma nounroll
        for (int t = 0; t < NK; ++t) {
            const int sh = t & 1;
            const Src xw = srcW(t + 2), xh = srcH(t + 2);
            phase(a0, b0, a1, b1, sw2 == 1 ? -65536 : 32768, sh ? -32768 : 32768, K1, grpc, [&](int i) { stageW(xw, sw2, i); });
            wait_vm<4>(); bar();
            phase(a1, b1, a0, b0, 0, 0, K0, grpc, [&](int i) { stageH(xh, sh, i); });
            sw2 = sw2 == 2 ? 0 : sw2 + 1;
        }
    };
    kloop(std::integral_constant<int, GRP>{});
    keep(a0, b0); sched0();
    STAMP(2);
    unsigned pf;
    { const u16* src = p.wd + (size_t)min((int)(blockIdx.x * 8 + wid) * 28 + (lane < 28 ? lane : 0), 57343) * 64;
      asm volatile("global_load_dword %0, %1, off" : "=v"(pf) : "v"(src) : "memory"); }
    LAS float* cs = (LAS float*)lds;
    cs0 = qsum(cs0); cs1 = qsum(cs1);
    if (q == 0) { cs[(wm * FA + 2 * wn) * 16 + l15] = cs0; if (wn < 3) cs[(wm * FA + 2 * wn + 1) * 16 + l15] = cs1; }
    float mean[4], c1[4], ve[4];
#pragma unroll
    for (int n = 0; n < 4; ++n) {
        const f32x4 s0 = *(const LAS f32x4*)(size_t)(adB + n * 256), s1 = *(const LAS f32x4*)(size_t)(adB + 16384 + n * 256);
        mean[n] = qsum((s0[0] + s0[2]) + (s1[0] + s1[2])) * (1.f / 16.f);
        const float d0 = s0[0] - mean[n], d1 = s0[2] - mean[n], d2 = s1[0] - mean[n], d3 = s1[2] - mean[n];
        const float m2 = qsum((s0[1] + s0[3]) + (s1[1] + s1[3]) + 64.f * ((d0 * d0 + d1 * d1) + (d2 * d2 + d3 * d3)));
        ve[n] = m2 * (1.f / DM) + LN_EPS; c1[n] = -1.44269504f * __builtin_amdgcn_rsqf(ve[n]);
    }
    wait_lgkm0(); bar();
    STAMP(4);
    const auto ro = RSRC(p.act, SEQ * FF * 2);
#pragma unroll
    for (int m = 0; m < FA; ++m) {
        int mf = m + 2 * wn; mf = mf >= FA ? mf - FA : mf;
        const f32x4 csum = *(const LAS f32x4*)(cs + (wm * FA + mf) * 16 + 4 * q);
        unsigned o[4];
#pragma unroll
        for (int n = 0; n < 4; ++n) {
            const f32x4 v = acc[m][n] - mean[n] * csum;
            float g0 = v[0], g1 = v[1], u0 = v[2], u1 = v[3];
            swap32(g0, u0); swap32(g1, u1);
            const f32x2 g = {g0, g1}, u = {u0, u1}, x = g * c1[n];
            const f32x2 d = (f32x2){__builtin_amdgcn_exp2f(x[0]), __builtin_amdgcn_exp2f(x[1])} * ve[n] + ve[n];
            const f32x2 r = g * u * (f32x2){__builtin_amdgcn_rcpf(d[0]), __builtin_amdgcn_rcpf(d[1])};
            o[n] = pack_bf16(r[0], r[1]);
        }
        swap16u(o[0], o[1]); swap16u(o[2], o[3]); swap32u(o[0], o[2]); swap32u(o[1], o[3]);
        __builtin_amdgcn_raw_buffer_store_b128((u32x4){o[0], o[2], o[1], o[3]}, ro, (unsigned)(((tm * 14 + wm * FA + mf) * SEQ + tn * 256 + wn * 64 + lane) * 16), 0, SC1);
    }
    wait_vm<0>(); asm volatile("" ::"v"(pf));
    STAMP(5);
    STAMP_END(3);
}
#endif
__global__ __launch_bounds__(512, 2) void gemm3_k(const Params p) {
#if defined(__HIP_DEVICE_COMPILE__)
    __shared__ __attribute__((aligned(16))) unsigned char lds_raw[5 * 32768];
    const int wid = __builtin_amdgcn_readfirstlane(threadIdx.x >> 6);
    if (wid < 4) gemm3_body<0>(p, (LAS unsigned char*)lds_raw, wid); else gemm3_body<1>(p, (LAS unsigned char*)lds_raw, wid);
#endif
}

extern "C" void kernel_launch(void* const* d_in, const int* in_sizes, int n_in, void* d_out, int out_size, void* d_ws, size_t ws_size, hipStream_t stream) {
    Params p;
    p.x = (const u16*)d_in[0]; p.cre = (const u16*)d_in[1]; p.cim = (const u16*)d_in[2]; p.wre = (const u16*)d_in[3]; p.wim = (const u16*)d_in[4];
    p.wg = (const u16*)d_in[6]; p.wu = (const u16*)d_in[7]; p.wd = (const u16*)d_in[8];
    p.out = (u16*)d_out;
    char* ws = (char*)d_ws;
    p.abT = (u16*)ws; p.h = (u16*)(ws + (8u << 20)); p.act = (u16*)(ws + (16u << 20));
    p.xch = (u16*)(ws + (32u << 20)); p.flags = (unsigned*)(ws + (40u << 20));
    gemm128_k<1><<<256, 512, 0, stream>>>(p);
    gemm128_k<2><<<256, 512, 0, stream>>>(p);
    gemm3_k<<<256, 512, 0, stream>>>(p);
    gemm128_k<4><<<256, 512, 0, stream>>>(p);
}
```

```cpp
#include <hip/hip_runtime.h>
#include <hip/hip_bf16.h>
#include <type_traits>
#define NO_PF2

#define LAS __attribute__((address_space(3)))
typedef unsigned short u16;
typedef __bf16 bf16x8 __attribute__((ext_vector_type(8)));
typedef __bf16 bf16x2 __attribute__((ext_vector_type(2)));
typedef float f32x4 __attribute__((ext_vector_type(4)));
typedef float f32x2 __attribute__((ext_vector_type(2)));
typedef unsigned u32x4 __attribute__((ext_vector_type(4)));
typedef unsigned u32x2 __attribute__((ext_vector_type(2)));

constexpr int SEQ = 2048, DM = 1024, FF = 3584;
constexpr float LN_EPS = 1e-5f;
constexpr int SC1 = 16;

struct Params {
    const u16* x; const u16* cre; const u16* cim; const u16* wre; const u16* wim;
    const u16* wg; const u16* wu; const u16* wd;
    u16* out; u16* abT; u16* h; u16* act; u16* xch; unsigned* flags;
};

template <int N> __device__ __forceinline__ void wait_vm() { asm volatile("s_waitcnt vmcnt(%0)" ::"n"(N) : "memory"); }
__device__ __forceinline__ void wait_lgkm0() { __builtin_amdgcn_s_waitcnt(0xC07F); asm volatile("s_waitcnt lgkmcnt(0)" ::: "memory"); }
__device__ __forceinline__ void sched0() { __builtin_amdgcn_sched_barrier(0); }
__device__ __forceinline__ void bar() { sched0(); asm volatile("" ::: "memory"); __builtin_amdgcn_s_barrier(); asm volatile("" ::: "memory"); sched0(); }
__device__ __forceinline__ unsigned pack_bf16(float lo, float hi) { bf16x2 c; c[0] = (__bf16)lo; c[1] = (__bf16)hi; return __builtin_bit_cast(unsigned, c); }
__device__ __forceinline__ float bf_lo(unsigned v) { return __uint_as_float(v << 16); }
__device__ __forceinline__ float bf_hi(unsigned v) { return __uint_as_float(v & 0xffff0000u); }
__device__ __forceinline__ void swap16(float& a, float& b) { auto r = __builtin_amdgcn_permlane16_swap(__float_as_uint(a), __float_as_uint(b), false, false); a = __uint_as_float(r[0]); b = __uint_as_float(r[1]); }
__device__ __forceinline__ void swap32(float& a, float& b) { auto r = __builtin_amdgcn_permlane32_swap(__float_as_uint(a), __float_as_uint(b), false, false); a = __uint_as_float(r[0]); b = __uint_as_float(r[1]); }
__device__ __forceinline__ void swap16u(unsigned& a, unsigned& b) { auto r = __builtin_amdgcn_permlane16_swap(a, b, false, false); a = r[0]; b = r[1]; }
__device__ __forceinline__ void swap32u(unsigned& a, unsigned& b) { auto r = __builtin_amdgcn_permlane32_swap(a, b, false, false); a = r[0]; b = r[1]; }
__device__ __forceinline__ float qsum(float v) { float a = v, b = v; swap16(a, b); a += b; b = a; swap32(a, b); return a + b; }

struct Frag { u32x2 lo, hi; __device__ __forceinline__ bf16x8 v() const { return __builtin_bit_cast(bf16x8, __builtin_shufflevector(lo, hi, 0, 1, 2, 3)); } };
template <int OFF> __device__ __forceinline__ u32x2 tr_read(unsigned addr) { u32x2 r; asm volatile("ds_read_b64_tr_b16 %0, %1 offset:%2" : "=v"(r) : "v"(addr), "n"(OFF) : "memory"); return r; }
template <int OFF> __device__ __forceinline__ bf16x8 lds_read128(unsigned addr) { bf16x8 r; asm volatile("ds_read_b128 %0, %1 offset:%2" : "=v"(r) : "v"(addr), "n"(OFF) : "memory"); return r; }
template <int N> __device__ __forceinline__ void wait_lgkm() { asm volatile("s_waitcnt lgkmcnt(%0)" ::"n"(N) : "memory"); }
template <int I, int N, class F> __device__ __forceinline__ void static_for(F&& f) { if constexpr (I < N) { f(std::integral_constant<int, I>{}); static_for<I + 1, N>(f); } }
template <int NA> __device__ __forceinline__ void keep(const Frag (&a)[NA], const bf16x8 (&b)[4]) {
#pragma unroll
    for (int i = 0; i < NA; ++i) asm volatile("" ::"v"(a[i].lo), "v"(a[i].hi));
#pragma unroll
    for (int i = 0; i < 4; ++i) asm volatile("" ::"v"(b[i]));
}
__device__ __forceinline__ void vadd(float& a, float b) { asm volatile("v_add_f32 %0, %0, %1" : "+v"(a) : "v"(b)); }
__device__ __forceinline__ void bump(unsigned& a, int d) { asm volatile("v_add_u32 %0, %1, %0" : "+v"(a) : "s"(d)); }
#define MFMA(a, b, c) __builtin_amdgcn_mfma_f32_16x16x32_bf16(a, b, c, 0, 0, 0)
#define RSRC(ptr, bytes) __builtin_amdgcn_make_buffer_rsrc((void*)(ptr), 0, (bytes), 0x00020000)
#define DMA16(rs, ldsp, voff, soff) __builtin_amdgcn_raw_ptr_buffer_load_lds(rs, (LAS void*)(ldsp), 16, voff, soff, 0, 0)
#define DMA16_DEV(rs, ldsp, voff, soff) __builtin_amdgcn_raw_ptr_buffer_load_lds(rs, (LAS void*)(ldsp), 16, voff, soff, 0, SC1)

#define STAMP(i)
#define STAMP_BEGIN
#define STAMP_END(ID)

enum { ROW, CHK, TR };
#define NKS2 28
#define NKS4 24
template <int ID> struct Cfg;
template <> struct Cfg<1> { static constexpr int NKT = 16, NKS = 16, FA = ROW, FB = ROW, LDB = DM; };
template <> struct Cfg<2> { static constexpr int NKT = 64, NKS = NKS2, FA = CHK, FB = ROW, LDB = SEQ; };
template <> struct Cfg<4> { static constexpr int NKT = 56, NKS = NKS4, FA = TR, FB = CHK, LDB = 0; };

template <int ID>
__global__ __launch_bounds__(512, 2) void gemm128_k(const Params p) {
#if defined(__HIP_DEVICE_COMPILE__)
    using C = Cfg<ID>;
    constexpr int STAGE = 32768;
    __shared__ __attribute__((aligned(16))) unsigned char lds_raw[4 * STAGE + (ID == 1 ? 1536 : 0)];
    LAS unsigned char* lds = (LAS unsigned char*)lds_raw;
    STAMP_BEGIN;
    const int tid = threadIdx.x, lane = tid & 63, wid = __builtin_amdgcn_readfirstlane(tid >> 6);
    const int l15 = lane & 15, q = lane >> 4;
    const int xcd = blockIdx.x & 7, idx = blockIdx.x >> 3, im = idx & 3, in = idx >> 2;
    const bool recv = ID != 1 && (xcd & 1);
    const int tm = (ID == 1 ? (xcd & 3) : ((xcd >> 1) & 1)) * 4 + im, tn = (xcd >> 2) * 8 + in;
    const int k0 = recv ? C::NKS : 0, nk = recv ? C::NKT - C::NKS : C::NKS;
    unsigned* const flag = p.flags + ((ID == 4 ? 512 : 0) + (blockIdx.x >> 1) * 4 + (wid & 3)) * 16;
    const unsigned xoff = (unsigned)((blockIdx.x >> 1) * 32768 + (wid & 3) * 8192);

    if (wid >= 4) {
        const int lw = wid - 4, phi = (im + in) & 3;
        const int koff = ID == 1 ? xcd * 2 : ((xcd >> (ID == 2 ? 1 : 2)) & 1) * (nk >> 1);
        if constexpr (ID == 1) { if (lane == 0) p.flags[(blockIdx.x * 4 + lw) * 16] = 0u; }
        const unsigned ksw = (unsigned)(((lane & 7) ^ (lane >> 3)) << 4);
        unsigned voffA[2], voffB;
        if constexpr (C::FA == ROW) voffA[0] = (unsigned)((tm * 128 + lw * 32 + (lane >> 3)) * DM * 2) + ksw;
        if constexpr (C::FA == CHK) voffA[0] = (unsigned)((lw * 2 * DM + tm * 128 + lane) * 16);
        if constexpr (C::FA == TR) {
#pragma unroll
            for (int i = 0; i < 2; ++i) voffA[i] = (unsigned)((lw * 16 + q) * DM * 2 + tm * 256 + ((l15 ^ ((q | (i << 2)) << 1)) << 4));
        }
        if constexpr (C::FB == ROW) voffB = (unsigned)((((ID == 1 ? tn & 7 : tn)) * 128 + lw * 32 + (lane >> 3)) * C::LDB * 2) + ksw;
        else voffB = (unsigned)((lw * 2 * SEQ + tn * 128 + lane) * 16);
        const auto ra = ID == 1 ? RSRC(p.x, SEQ * DM * 2) : ID == 2 ? RSRC(p.abT, 2 * SEQ * DM * 2) : RSRC(p.wd, FF * DM * 2);
        auto stage = [&](int t) {
            int k = (t & 3) != phi ? t : t + 4 < nk ? t + 4 : phi;
            k += koff; k = k0 + (k >= nk ? k - nk : k);
            const auto rb = ID == 1 ? RSRC(tn < 8 ? p.wre : p.wim, DM * DM * 2) : ID == 2 ? RSRC(k < 32 ? p.cre : p.cim, SEQ * SEQ * 2) : RSRC(p.act, SEQ * FF * 2);
            LAS unsigned char* base = lds + (t & 3) * STAGE + lw * 4096;
#pragma unroll
            for (int i = 0; i < 4; ++i) {
                if constexpr (C::FA == ROW) DMA16(ra, base + i * 1024, voffA[0], k * 128 + i * 8 * DM * 2);
                if constexpr (C::FA == CHK) DMA16(ra, base + i * 1024, voffA[0], (k * 8 + (i >> 1)) * DM * 16 + (i & 1) * 1024);
                if constexpr (C::FA == TR) DMA16(ra, base + i * 1024, voffA[i >> 1], (k * 64 + i * 4) * DM * 2);
                if constexpr (C::FB == ROW) DMA16(rb, base + 16384 + i * 1024, voffB, (k & 31) * 128 + i * 8 * C::LDB * 2);
                else DMA16(rb, base + 16384 + i * 1024, voffB, (k * 8 + (i >> 1)) * SEQ * 16 + (i & 1) * 1024);
            }
        };
        stage(0); stage(1);
        wait_vm<8>(); bar();
        stage(2);
#pragma nounroll
        for (int t = 0; t < nk - 3; ++t) {
            wait_vm<8>(); bar();
            stage(t + 3);
        }
        bool got = !recv;
        auto fetch = [&]() {
            if (__builtin_amdgcn_readfirstlane(__hip_atomic_load(flag, __ATOMIC_RELAXED, __HIP_MEMORY_SCOPE_AGENT)) == 0u) return false;
            asm volatile("" ::: "memory");
#pragma unroll
            for (int i = 0; i < 8; ++i) DMA16_DEV(RSRC(p.xch, 128 * 32768), lds + (nk & 3) * STAGE + lw * 8192 + i * 1024, (unsigned)(lane * 16), xoff + i * 1024);
            return true;
        };
        wait_vm<8>(); bar();
        wait_vm<0>();
        unsigned pf[3];
        if constexpr (ID == 1) {
            const int nk2 = (xcd & 1) ? 64 - NKS2 : NKS2, kb = ((xcd & 1) ? NKS2 : 0) + ((xcd >> 1) & 1) * (nk2 >> 1);
            const int row = (xcd >> 2) * 1024 + idx * 32 + lw * 8 + (lane >> 3);
#pragma unroll
            for (int i = 0; i < 3; ++i) { const int j = i * 8 + (lane & 7), k = kb + j;
                const u16* src = (k < 32 ? p.cre : p.cim) + row * SEQ + (j < (nk2 + 1) / 2 && k < 64 ? (k & 31) : (kb & 31)) * 64;
                asm volatile("global_load_dword %0, %1, off" : "=v"(pf[i]) : "v"(src) : "memory"); }
        }
        if (!got) got = fetch();
        bar();
        if (!got) got = fetch();
        bar();
        if (recv) {
#pragma nounroll
            for (int spin = 0; !got && spin < (1 << 20); ++spin) { got = fetch(); __builtin_amdgcn_s_sleep(1); }
            wait_vm<0>(); bar();
        }
        if constexpr (ID == 1) {
            bar();
            wait_vm<0>(); asm volatile("" ::"v"(pf[0]), "v"(pf[1]), "v"(pf[2]));
        }
        return;
    }

    const int wm = wid & 1, wn = wid >> 1;
    const int eo = (q & 1) * 16 + (q >> 1) * 8;
    f32x4 acc[4][4], st[6];
#pragma unroll
    for (int m = 0; m < 4; ++m)
#pragma unroll
        for (int n = 0; n < 4; ++n) acc[m][n] = (f32x4){0.f, 0.f, 0.f, 0.f};
#pragma unroll
    for (int i = 0; i < 6; ++i) st[i] = (f32x4){0.f, 0.f, 0.f, 0.f};
    const int row0 = tn * 128 + wn * 64 + l15, col0 = tm * 128 + wm * 64 + eo;
    if (ID != 1 && !recv) {
        const auto rr = RSRC(ID == 2 ? p.x : p.h, SEQ * DM * 2);
#pragma unroll
        for (int n = 0; n < 4; ++n)
#pragma unroll
            for (int mp = 0; mp < 2; ++mp) {
                u32x4 r = __builtin_amdgcn_raw_buffer_load_b128(rr, (unsigned)(ID == 2 ? ((row0 + n * 16) * DM + col0 + mp * 32) * 2 : ((col0 / 8 + mp * 4) * SEQ + row0 + n * 16) * 16), 0, 0);
                unsigned r0 = r[0], r1 = r[1], r2 = r[2], r3 = r[3];
                swap16u(r0, r2); swap16u(r1, r3);
                acc[2 * mp][n] = (f32x4){bf_lo(r0), bf_hi(r0), bf_lo(r1), bf_hi(r1)}; acc[2 * mp + 1][n] = (f32x4){bf_lo(r2), bf_hi(r2), bf_lo(r3), bf_hi(r3)};
            }
    }
    const int rotA = ID == 1 ? 2 * wn : 0, rotB = ID == 1 ? 2 * wm : 0;
    constexpr int NA = C::FA == CHK ? 1 : 4, NB = C::FB == CHK ? 1 : ID == 1 ? 4 : 2;
    unsigned adA[NA], adB[NB];
    const unsigned lds0 = (unsigned)(size_t)lds, row_sw = lds0 + (unsigned)(l15 * 128 + ((q ^ (lane & 7)) << 4)), chk = lds0 + (unsigned)(q * 2048 + l15 * 16);
#pragma unroll
    for (int i = 0; i < NA; ++i) {
        if constexpr (C::FA == ROW) adA[i] = (row_sw ^ (unsigned)((i >> 1) * 64)) + (unsigned)((wm * 4 + ((2 * (i & 1) + rotA) & 3)) * 2048);
        if constexpr (C::FA == CHK) adA[i] = chk + (unsigned)(wm * 1024);
        if constexpr (C::FA == TR) { const int q4 = l15 >> 2; adA[i] = lds0 + (unsigned)((8 * q + q4) * 256 + (((wm * 4 + i) ^ (q4 | ((q & 1) << 2))) << 5) + 8 * (lane & 3)); }
    }
#pragma unroll
    for (int i = 0; i < NB; ++i) {
        if constexpr (C::FB == CHK) adB[i] = 16384u + chk + (unsigned)(wn * 1024);
        else if constexpr (ID == 1) adB[i] = 16384u + (row_sw ^ (unsigned)((i >> 1) * 64)) + (unsigned)((wn * 4 + ((2 * (i & 1) + rotB) & 3)) * 2048);
        else adB[i] = 16384u + (row_sw ^ (unsigned)(i * 64)) + (unsigned)(wn * 8192);
    }
    constexpr int W = C::FA == TR ? 2 : 1, NR = 4 + 4 * W;
    auto rd = [&](auto ic, Frag (&ra)[4], bf16x8 (&rb)[4], auto kkc) {
        constexpr int kk = decltype(kkc)::value, i = decltype(ic)::value;
        if constexpr (i == 0 || (i > W && i <= W + 3)) { constexpr int n = i == 0 ? 0 : i - W;
            if constexpr (C::FB == CHK) rb[n] = lds_read128<kk * 8192 + n * 256>(adB[0]);
            else if constexpr (ID == 1) rb[n] = lds_read128<(n & 1) * 2048>(adB[kk * 2 + (n >> 1)]);
            else rb[n] = lds_read128<n * 2048>(adB[kk]); }
        else { constexpr int j = i <= W ? i - 1 : i - 4, m = j / W;
            if constexpr (C::FA != TR) { bf16x8 v;
                if constexpr (C::FA == CHK) v = lds_read128<kk * 8192 + m * 256>(adA[0]); else v = lds_read128<(m & 1) * 2048>(adA[kk * 2 + (m >> 1)]);
                const u32x4 w = __builtin_bit_cast(u32x4, v); ra[m].lo = (u32x2){w[0], w[1]}; ra[m].hi = (u32x2){w[2], w[3]}; }
            else if constexpr (j % 2 == 0) ra[m].lo = tr_read<kk * 8192>(adA[m]);
            else ra[m].hi = tr_read<kk * 8192 + 1024>(adA[m]); }
    };
    const bf16x8 ones = __builtin_bit_cast(bf16x8, (u32x4){0x3f803f80u, 0x3f803f80u, 0x3f803f80u, 0x3f803f80u});
    auto phase = [&](const Frag (&ra)[4], const bf16x8 (&rb)[4], Frag (&na)[4], bf16x8 (&nb)[4], int dstep, auto kkc) {
        static_for<0, 16>([&](auto sc) {
            constexpr int s = decltype(sc)::value, m = s >> 2, n = s & 3;
            if constexpr (n == 0) { wait_lgkm<(3 - m) * W + (s < NR ? s : NR)>(); sched0(); }
            acc[m][n] = MFMA(ra[m].v(), rb[n], acc[m][n]);
            if constexpr (s < NR) rd(sc, na, nb, kkc);
            if constexpr (s >= 12 && decltype(kkc)::value == 1) {
                if constexpr (s - 12 < NA) bump(adA[s - 12], dstep);
                if constexpr (s - 12 < NB) bump(adB[s - 12], dstep);
            }
            if constexpr (ID == 1 && s >= 10) {
                constexpr int i = s & 1;
                if constexpr (s < 12) st[i] = MFMA(ones, ra[i].v(), st[i]); else if constexpr (s < 14) st[2 + i] = MFMA(ra[i].v(), ra[i].v(), st[2 + i]); else st[4 + i] = MFMA(ones, rb[i], st[4 + i]); }
            sched0();
        });
    };
    Frag a0[4], a1[4]; bf16x8 b0[4], b1[4];
    constexpr std::integral_constant<int, 0> K0{}; constexpr std::integral_constant<int, 1> K1{};
    bar();
    STAMP(1);
    static_for<0, NR>([&](auto ic) { rd(ic, a0, b0, K0); });
    sched0();
#pragma nounroll
    for (int t = 0; t < nk; ++t) {
        phase(a0, b0, a1, b1, (t & 3) == 3 ? -3 * STAGE : STAGE, K1);
        bar();
        phase(a1, b1, a0, b0, 0, K0);
    }
    wait_lgkm0(); keep(a0, b0); sched0();
    STAMP(2);
    if constexpr (ID == 1) {
        LAS float* tab = (LAS float*)(lds + 4 * STAGE);
        const float sgn = tn < 8 ? 1.f : -1.f;
        if (q == (l15 >> 2)) {
#pragma unroll
            for (int i = 0; i < 2; ++i) {
                const int j = l15 & 3; const float sq = j == 0 ? st[2 + i][0] : j == 1 ? st[2 + i][1] : j == 2 ? st[2 + i][2] : st[2 + i][3];
                const float mean = st[i][0] * (1.f / DM);
                const int ra = wm * 64 + (rotA + i) * 16 + l15;
                tab[ra] = mean; tab[128 + ra] = sgn * __builtin_amdgcn_rsqf(fmaxf(sq * (1.f / DM) - mean * mean, 0.f) + LN_EPS);
                tab[256 + wn * 64 + (rotB + i) * 16 + l15] = st[4 + i][0];
            }
        }
        wait_lgkm0(); bar();
        const auto ro = RSRC(p.abT, 2 * SEQ * DM * 2);
        float ws[4];
#pragma unroll
        for (int n = 0; n < 4; ++n) ws[n] = tab[256 + wn * 64 + ((n + rotB) & 3) * 16 + l15];
#pragma unroll
        for (int m = 0; m < 4; ++m) {
            const int r0 = wm * 64 + ((m + rotA) & 3) * 16 + 4 * q;
            const f32x4 mean = *(const LAS f32x4*)(tab + r0), rs = *(const LAS f32x4*)(tab + 128 + r0);
#pragma unroll
            for (int n = 0; n < 4; ++n) acc[m][n] = (acc[m][n] - mean * ws[n]) * rs;
        }
#pragma unroll
        for (int n = 0; n < 4; ++n) { const int e = (tn & 7) * 128 + wn * 64 + ((n + rotB) & 3) * 16 + l15;
#pragma unroll
            for (int mp = 0; mp < 2; ++mp) {
                unsigned x0 = pack_bf16(acc[2 * mp][n][0], acc[2 * mp][n][1]), x1 = pack_bf16(acc[2 * mp][n][2], acc[2 * mp][n][3]);
                unsigned y0 = pack_bf16(acc[2 * mp + 1][n][0], acc[2 * mp + 1][n][1]), y1 = pack_bf16(acc[2 * mp + 1][n][2], acc[2 * mp + 1][n][3]);
                swap16u(x0, y0); swap16u(x1, y1);
                const int s = (tn >> 3) * SEQ + tm * 128 + wm * 64 + ((2 * mp + rotA) & 3) * 16 + eo;
                __builtin_amdgcn_raw_buffer_store_b128((u32x4){x0, x1, y0, y1}, ro, (unsigned)(((s >> 3) * DM + e) * 16), 0, SC1);
            } }
    } else if (!recv) {
        const auto rx = RSRC(p.xch, 128 * 32768);
#pragma unroll
        for (int m = 0; m < 4; ++m)
#pragma unroll
            for (int h = 0; h < 2; ++h) {
                const f32x4 u = acc[m][2 * h], v = acc[m][2 * h + 1];
                __builtin_amdgcn_raw_buffer_store_b128((u32x4){pack_bf16(u[0], u[1]), pack_bf16(u[2], u[3]), pack_bf16(v[0], v[1]), pack_bf16(v[2], v[3])}, rx, xoff + (unsigned)((m * 2 + h) * 1024 + lane * 16), 0, SC1);
            }
        wait_vm<0>();
        if (lane == 0) __hip_atomic_store(flag, 1u, __ATOMIC_RELAXED, __HIP_MEMORY_SCOPE_AGENT);
    } else {
        bar();
        STAMP(4);
        const LAS u32x4* px = (const LAS u32x4*)(lds + (nk & 3) * STAGE + wid * 8192 + lane * 16);
#pragma unroll
        for (int m = 0; m < 4; ++m)
#pragma unroll
            for (int h = 0; h < 2; ++h) { const u32x4 w = px[(m * 2 + h) * 64];
                acc[m][2 * h] += (f32x4){bf_lo(w[0]), bf_hi(w[0]), bf_lo(w[1]), bf_hi(w[1])}; acc[m][2 * h + 1] += (f32x4){bf_lo(w[2]), bf_hi(w[2]), bf_lo(w[3]), bf_hi(w[3])}; }
        const auto ro = RSRC(ID == 2 ? p.h : p.out, SEQ * DM * 2);
#pragma unroll
        for (int n = 0; n < 4; ++n) {
            unsigned o[4][2];
#pragma unroll
            for (int m = 0; m < 4; ++m) { o[m][0] = pack_bf16(acc[m][n][0], acc[m][n][1]); o[m][1] = pack_bf16(acc[m][n][2], acc[m][n][3]); }
            if constexpr (ID == 2) {
                float s = 0.f;
#pragma unroll
                for (int m = 0; m < 4; ++m) s += (bf_lo(o[m][0]) + bf_hi(o[m][0])) + (bf_lo(o[m][1]) + bf_hi(o[m][1]));
                const float mw = qsum(s) * (1.f / 64.f); float m2 = 0.f;
#pragma unroll
                for (int m = 0; m < 4; ++m)
#pragma unroll
                    for (int j = 0; j < 2; ++j) { const float d0 = bf_lo(o[m][j]) - mw, d1 = bf_hi(o[m][j]) - mw; m2 += d0 * d0 + d1 * d1; }
                m2 = qsum(m2);
                if (q == 0) *(f32x2*)(p.h + ((128 + tm) * SEQ + row0 + n * 16) * 8 + wm * 4) = (f32x2){mw, m2};
            }
#pragma unroll
            for (int mp = 0; mp < 2; ++mp) {
                swap16u(o[2 * mp][0], o[2 * mp + 1][0]); swap16u(o[2 * mp][1], o[2 * mp + 1][1]);
                __builtin_amdgcn_raw_buffer_store_b128((u32x4){o[2 * mp][0], o[2 * mp][1], o[2 * mp + 1][0], o[2 * mp + 1][1]}, ro,
                    (unsigned)(ID == 4 ? ((row0 + n * 16) * DM + col0 + mp * 32) * 2 : ((col0 / 8 + mp * 4) * SEQ + row0 + n * 16) * 16), 0, SC1);
            }
        }
        STAMP(5);
    }
    STAMP_END(ID);
#endif
}

#if defined(__HIP_DEVICE_COMPILE__)
template <int GRP>
__device__ __forceinline__ void gemm3_body(const Params& p, LAS unsigned char* lds, const int wid) {
    constexpr int NK = 16, FA = 7, HB0 = 3 * 32768;
    STAMP_BEGIN;
    const int tid = threadIdx.x, lane = tid & 63;
    const int wm = wid & 1, wn = wid >> 1, l15 = lane & 15, q = lane >> 4;
    const int xcd = blockIdx.x & 7, idx = blockIdx.x >> 3, im = idx & 7, in = idx >> 3;
    const int tm = (xcd & 3) * 8 + im, tn = (xcd >> 2) * 4 + in;
    const int phi = (im + in) & 3, koff = (xcd * 2) & (NK - 1);

    const u16* aptr = GRP ? p.wu : p.wg;
    unsigned voffA[2];
#pragma unroll
    for (int i = 0; i < 2; ++i) { const int j = l15 ^ (q | (i << 2) | (GRP << 3));
        voffA[i] = j < 14 ? (unsigned)(((wid & 3) * 16 + q) * FF * 2 + tm * 224 + j * 16) : 0x7ffffff0u; }
    const unsigned voffB = (unsigned)((wid * SEQ + tn * 256 + lane) * 16);
    auto kstep = [&](int t) { return (t + koff + ((t & 3) == phi ? 4 : 0)) & (NK - 1); };
    struct Src { int so; unsigned nrec; };
    auto pin = [](Src x) { asm volatile("" : "+s"(x.so), "+s"(x.nrec)); return x; };
    auto srcW = [&](int t) { return pin(Src{kstep(t) * 64 * FF * 2, t < NK ? (unsigned)(DM * FF * 2) : 0u}); };
    auto srcH = [&](int t) { return pin(Src{(t < NK ? kstep(t) : NK) * 8 * SEQ * 16, t <= NK ? (unsigned)(136 * SEQ * 16) : 0u}); };
    auto stageW = [&](Src x, int slot, int i) { DMA16(RSRC(aptr, x.nrec), lds + slot * 32768 + wid * 4096 + i * 1024, voffA[i >> 1], x.so + i * 4 * FF * 2); };
    auto stageH = [&](Src x, int slot, int i) { DMA16(RSRC(p.h, x.nrec), lds + HB0 + slot * 32768 + wid * 4096 + i * 1024, voffB, x.so + i * 1024); };
#pragma unroll
    for (int t = 0; t < 2; ++t) {
        const Src xw = srcW(t), xh = srcH(t);
#pragma unroll
        for (int i = 0; i < 4; ++i) stageW(xw, t, i);
#pragma unroll
        for (int i = 0; i < 4; ++i) stageH(xh, t, i);
    }

    unsigned adA[FA];
    const unsigned lds0 = (unsigned)(size_t)lds;
#pragma unroll
    for (int m = 0; m < FA; ++m) { const int q4 = l15 >> 2, pp = lane & 3; int mf = m + 2 * wn; mf = mf >= FA ? mf - FA : mf;
        adA[m] = lds0 + (unsigned)((pp >> 1) * 16384 + (8 * q + q4) * 256 + (((wm * FA + mf) ^ (q4 | ((q & 1) << 2) | ((pp >> 1) << 3))) << 4) + 8 * (pp & 1)); }
    unsigned adB = lds0 + (unsigned)(HB0 + q * 4096 + (wn * 64 + l15) * 16);
    auto rd = [&](auto ic, Frag (&ra)[FA], bf16x8 (&rb)[4], auto kkc) {
        constexpr int kk = decltype(kkc)::value, i = decltype(ic)::value;
        if constexpr (i < 4) rb[i] = lds_read128<kk * 16384 + i * 256>(adB);
        else if constexpr (i % 2 == 0) ra[(i - 4) / 2].lo = tr_read<kk * 8192>(adA[(i - 4) / 2]);
        else ra[(i - 4) / 2].hi = tr_read<kk * 8192 + 1024>(adA[(i - 4) / 2]);
    };
    f32x4 acc[FA][4];
#pragma unroll
    for (int m = 0; m < FA; ++m)
#pragma unroll
        for (int n = 0; n < 4; ++n) acc[m][n] = (f32x4){0.f, 0.f, 0.f, 0.f};
    float cs0 = 0.f, cs1 = 0.f;
#define DSTR 2
#define PR1 16
#define PR2 4
#define PR3 20

    auto phase = [&](const Frag (&ra)[FA], const bf16x8 (&rb)[4], Frag (&na)[FA], bf16x8 (&nb)[4], int dw, int dh, auto kkc, auto grpc, auto&& dma) {
        static_for<0, 28>([&](auto sc) {
            constexpr int s = decltype(sc)::value, m = s >> 2, n = s & 3;
            if constexpr (decltype(kkc)::value == 0) { if constexpr (s == 0) __builtin_amdgcn_s_setprio(3); if constexpr (s == PR1) __builtin_amdgcn_s_setprio(2); }
            else { if constexpr (s == PR2) __builtin_amdgcn_s_setprio(1); if constexpr (s == PR3) __builtin_amdgcn_s_setprio(0); }
            acc[m][n] = MFMA(ra[m].v(), rb[n], acc[m][n]);
            if constexpr (s < 18) rd(sc, na, nb, kkc);
            if constexpr (s % DSTR == 0 && s < 8 * DSTR && decltype(grpc)::value == ((s / DSTR) & 1)) dma(s / (2 * DSTR));
            if constexpr (decltype(kkc)::value == 1 && s >= 18 && s < 26) { if constexpr (s < 25) bump(adA[s - 18], dw); else bump(adB, dh); }
            if constexpr (s >= 1 && s < 17) { constexpr int j = s - 1, f = j >> 3, d = (j >> 1) & 3; const unsigned w = d < 2 ? ra[f].lo[d & 1] : ra[f].hi[d & 1];
                vadd(f ? cs1 : cs0, (j & 1) ? bf_hi(w) : bf_lo(w)); }
            sched0();
        });
        wait_lgkm0(); sched0();
    };
    Frag a0[FA], a1[FA]; bf16x8 b0[4], b1[4];
    constexpr std::integral_constant<int, 0> K0{}; constexpr std::integral_constant<int, 1> K1{};
    wait_vm<8>(); bar();
    STAMP(1);
    static_for<0, 18>([&](auto ic) { rd(ic, a0, b0, K0); });
    wait_lgkm0(); sched0();
    auto kloop = [&](auto grpc) {
        int sw2 = 2;
#pragma nounroll
        for (int t = 0; t < NK; ++t) {
            const int sh = t & 1;
            const Src xw = srcW(t + 2), xh = srcH(t + 2);
            phase(a0, b0, a1, b1, sw2 == 1 ? -65536 : 32768, sh ? -32768 : 32768, K1, grpc, [&](int i) { stageW(xw, sw2, i); });
            wait_vm<4>(); bar();
            phase(a1, b1, a0, b0, 0, 0, K0, grpc, [&](int i) { stageH(xh, sh, i); });
            sw2 = sw2 == 2 ? 0 : sw2 + 1;
        }
    };
    kloop(std::integral_constant<int, GRP>{});
    keep(a0, b0); sched0();
    STAMP(2);
    unsigned pf;
    { const u16* src = p.wd + (size_t)min((int)(blockIdx.x * 8 + wid) * 28 + (lane < 28 ? lane : 0), 57343) * 64;
      asm volatile("global_load_dword %0, %1, off" : "=v"(pf) : "v"(src) : "memory"); }
    LAS float* cs = (LAS float*)lds;
    cs0 = qsum(cs0); cs1 = qsum(cs1);
    if (q == 0) { cs[(wm * FA + 2 * wn) * 16 + l15] = cs0; if (wn < 3) cs[(wm * FA + 2 * wn + 1) * 16 + l15] = cs1; }
    float mean[4], c1[4], ve[4];
#pragma unroll
    for (int n = 0; n < 4; ++n) {
        const f32x4 s0 = *(const LAS f32x4*)(size_t)(adB + n * 256), s1 = *(const LAS f32x4*)(size_t)(adB + 16384 + n * 256);
        mean[n] = qsum((s0[0] + s0[2]) + (s1[0] + s1[2])) * (1.f / 16.f);
        const float d0 = s0[0] - mean[n], d1 = s0[2] - mean[n], d2 = s1[0] - mean[n], d3 = s1[2] - mean[n];
        const float m2 = qsum((s0[1] + s0[3]) + (s1[1] + s1[3]) + 64.f * ((d0 * d0 + d1 * d1) + (d2 * d2 + d3 * d3)));
        ve[n] = m2 * (1.f / DM) + LN_EPS; c1[n] = -1.44269504f * __builtin_amdgcn_rsqf(ve[n]);
    }
    wait_lgkm0(); bar();
    STAMP(4);
    const auto ro = RSRC(p.act, SEQ * FF * 2);
#pragma unroll
    for (int m = 0; m < FA; ++m) {
        int mf = m + 2 * wn; mf = mf >= FA ? mf - FA : mf;
        const f32x4 csum = *(const LAS f32x4*)(cs + (wm * FA + mf) * 16 + 4 * q);
        unsigned o[4];
#pragma unroll
        for (int n = 0; n < 4; ++n) {
            const f32x4 v = acc[m][n] - mean[n] * csum;
            float g0 = v[0], g1 = v[1], u0 = v[2], u1 = v[3];
            swap32(g0, u0); swap32(g1, u1);
            const f32x2 g = {g0, g1}, u = {u0, u1}, x = g * c1[n];
            const f32x2 d = (f32x2){__builtin_amdgcn_exp2f(x[0]), __builtin_amdgcn_exp2f(x[1])} * ve[n] + ve[n];
            const f32x2 r = g * u * (f32x2){__builtin_amdgcn_rcpf(d[0]), __builtin_amdgcn_rcpf(d[1])};
            o[n] = pack_bf16(r[0], r[1]);
        }
        swap16u(o[0], o[1]); swap16u(o[2], o[3]); swap32u(o[0], o[2]); swap32u(o[1], o[3]);
        __builtin_amdgcn_raw_buffer_store_b128((u32x4){o[0], o[2], o[1], o[3]}, ro, (unsigned)(((tm * 14 + wm * FA + mf) * SEQ + tn * 256 + wn * 64 + lane) * 16), 0, SC1);
    }
    wait_vm<0>(); asm volatile("" ::"v"(pf));
    STAMP(5);
    STAMP_END(3);
}
#endif
__global__ __launch_bounds__(512, 2) void gemm3_k(const Params p) {
#if defined(__HIP_DEVICE_COMPILE__)
    __shared__ __attribute__((aligned(16))) unsigned char lds_raw[5 * 32768];
    const int wid = __builtin_amdgcn_readfirstlane(threadIdx.x >> 6);
    if (wid < 4) gemm3_body<0>(p, (LAS unsigned char*)lds_raw, wid); else gemm3_body<1>(p, (LAS unsigned char*)lds_raw, wid);
#endif
}

extern "C" void kernel_launch(void* const* d_in, const int* in_sizes, int n_in, void* d_out, int out_size, void* d_ws, size_t ws_size, hipStream_t stream) {
    Params p;
    p.x = (const u16*)d_in[0]; p.cre = (const u16*)d_in[1]; p.cim = (const u16*)d_in[2]; p.wre = (const u16*)d_in[3]; p.wim = (const u16*)d_in[4];
    p.wg = (const u16*)d_in[6]; p.wu = (const u16*)d_in[7]; p.wd = (const u16*)d_in[8];
    p.out = (u16*)d_out;
    char* ws = (char*)d_ws;
    p.abT = (u16*)ws; p.h = (u16*)(ws + (8u << 20)); p.act = (u16*)(ws + (16u << 20));
    p.xch = (u16*)(ws + (32u << 20)); p.flags = (unsigned*)(ws + (40u << 20));
    gemm128_k<1><<<256, 512, 0, stream>>>(p);
    gemm128_k<2><<<256, 512, 0, stream>>>(p);
    gemm3_k<<<256, 512, 0, stream>>>(p);
    gemm128_k<4><<<256, 512, 0, stream>>>(p);
}
```

```cpp
#include <hip/hip_runtime.h>
#include <hip/hip_bf16.h>
#include <type_traits>

#define LAS __attribute__((address_space(3)))
typedef unsigned short u16;
typedef __bf16 bf16x8 __attribute__((ext_vector_type(8)));
typedef __bf16 bf16x2 __attribute__((ext_vector_type(2)));
typedef float f32x4 __attribute__((ext_vector_type(4)));
typedef float f32x2 __attribute__((ext_vector_type(2)));
typedef unsigned u32x4 __attribute__((ext_vector_type(4)));
typedef unsigned u32x2 __attribute__((ext_vector_type(2)));

constexpr int SEQ = 2048, DM = 1024, FF = 3584;
constexpr float LN_EPS = 1e-5f;
constexpr int SC1 = 16;

struct Params {
    const u16* x; const u16* cre; const u16* cim; const u16* wre; const u16* wim;
    const u16* wg; const u16* wu; const u16* wd;
    u16* out; u16* abT; u16* h; u16* act; u16* xch; unsigned long long* flags; float* csum;
};

template <int N> __device__ __forceinline__ void wait_vm() { asm volatile("s_waitcnt vmcnt(%0)" ::"n"(N) : "memory"); }
__device__ __forceinline__ void wait_lgkm0() { __builtin_amdgcn_s_waitcnt(0xC07F); asm volatile("s_waitcnt lgkmcnt(0)" ::: "memory"); }
__device__ __forceinline__ void sched0() { __builtin_amdgcn_sched_barrier(0); }
__device__ __forceinline__ void bar() { sched0(); asm volatile("" ::: "memory"); __builtin_amdgcn_s_barrier(); asm volatile("" ::: "memory"); sched0(); }
__device__ __forceinline__ unsigned pack_bf16(float lo, float hi) { bf16x2 c; c[0] = (__bf16)lo; c[1] = (__bf16)hi; return __builtin_bit_cast(unsigned, c); }
__device__ __forceinline__ float bf_lo(unsigned v) { return __uint_as_float(v << 16); }
__device__ __forceinline__ float bf_hi(unsigned v) { return __uint_as_float(v & 0xffff0000u); }
__device__ __forceinline__ void swap16(float& a, float& b) { auto r = __builtin_amdgcn_permlane16_swap(__float_as_uint(a), __float_as_uint(b), false, false); a = __uint_as_float(r[0]); b = __uint_as_float(r[1]); }
__device__ __forceinline__ void swap32(float& a, float& b) { auto r = __builtin_amdgcn_permlane32_swap(__float_as_uint(a), __float_as_uint(b), false, false); a = __uint_as_float(r[0]); b = __uint_as_float(r[1]); }
__device__ __forceinline__ void swap16u(unsigned& a, unsigned& b) { auto r = __builtin_amdgcn_permlane16_swap(a, b, false, false); a = r[0]; b = r[1]; }
__device__ __forceinline__ void swap32u(unsigned& a, unsigned& b) { auto r = __builtin_amdgcn_permlane32_swap(a, b, false, false); a = r[0]; b = r[1]; }
__device__ __forceinline__ float qsum(float v) { float a = v, b = v; swap16(a, b); a += b; b = a; swap32(a, b); return a + b; }

struct Frag { u32x2 lo, hi; __device__ __forceinline__ bf16x8 v() const { return __builtin_bit_cast(bf16x8, __builtin_shufflevector(lo, hi, 0, 1, 2, 3)); } };
template <int OFF> __device__ __forceinline__ u32x2 tr_read(unsigned addr) { u32x2 r; asm volatile("ds_read_b64_tr_b16 %0, %1 offset:%2" : "=v"(r) : "v"(addr), "n"(OFF) : "memory"); return r; }
template <int OFF> __device__ __forceinline__ bf16x8 lds_read128(unsigned addr) { bf16x8 r; asm volatile("ds_read_b128 %0, %1 offset:%2" : "=v"(r) : "v"(addr), "n"(OFF) : "memory"); return r; }
template <int N> __device__ __forceinline__ void wait_lgkm() { asm volatile("s_waitcnt lgkmcnt(%0)" ::"n"(N) : "memory"); }
template <int I, int N, class F> __device__ __forceinline__ void static_for(F&& f) { if constexpr (I < N) { f(std::integral_constant<int, I>{}); static_for<I + 1, N>(f); } }
template <int NA> __device__ __forceinline__ void keep(const Frag (&a)[NA], const bf16x8 (&b)[4]) {
#pragma unroll
    for (int i = 0; i < NA; ++i) asm volatile("" ::"v"(a[i].lo), "v"(a[i].hi));
#pragma unroll
    for (int i = 0; i < 4; ++i) asm volatile("" ::"v"(b[i]));
}
__device__ __forceinline__ void bump(unsigned& a, int d) { asm volatile("v_add_u32 %0, %1, %0" : "+v"(a) : "s"(d)); }
#define MFMA(a, b, c) __builtin_amdgcn_mfma_f32_16x16x32_bf16(a, b, c, 0, 0, 0)
#define RSRC(ptr, bytes) __builtin_amdgcn_make_buffer_rsrc((void*)(ptr), 0, (bytes), 0x00020000)
#define DMA16(rs, ldsp, voff, soff) __builtin_amdgcn_raw_ptr_buffer_load_lds(rs, (LAS void*)(ldsp), 16, voff, soff, 0, 0)
#define DMA16_DEV(rs, ldsp, voff, soff) __builtin_amdgcn_raw_ptr_buffer_load_lds(rs, (LAS void*)(ldsp), 16, voff, soff, 0, SC1)

#define STAMP(i)
#define STAMP_BEGIN
#define STAMP_END(ID)

typedef unsigned long long u64;
constexpr int FLAG_STRIDE = 8, F_PAIR = 1, F_AB = F_PAIR + 1024, F_H = F_AB + 256, F_ACT = F_H + 256, F_CS = F_ACT + 256;
__device__ __forceinline__ u64 flag_load(u64* w) { return __hip_atomic_load(w, __ATOMIC_RELAXED, __HIP_MEMORY_SCOPE_AGENT); }
__device__ __forceinline__ void flag_store(u64* w, u64 v) { __hip_atomic_store(w, v, __ATOMIC_RELAXED, __HIP_MEMORY_SCOPE_AGENT); }
__device__ __forceinline__ u64 uniform64(u64 v) { return ((u64)(unsigned)__builtin_amdgcn_readfirstlane((unsigned)(v >> 32)) << 32) | (unsigned)__builtin_amdgcn_readfirstlane((unsigned)v); }
__device__ __forceinline__ void wait_group(u64* word, bool need, u64 ep) {
#pragma nounroll
    for (int spin = 0; spin < (1 << 20); ++spin) {
        if (!__any(need && flag_load(word) != ep)) break;
        __builtin_amdgcn_s_sleep(2);
    }
}
#if defined(__HIP_DEVICE_COMPILE__)
__device__ __forceinline__ void touch(unsigned& sink, unsigned voff, const void* base, unsigned bytes) {
    asm volatile("s_nop 4\n\tbuffer_load_dword %0, %1, %2, 0 offen" : "+v"(sink) : "v"(voff), "s"(RSRC(base, bytes)) : "memory");
}
struct G3W {
    const u16* ptr; unsigned voff[2]; int phi, koff;
    __device__ __forceinline__ G3W(const Params& p, int wid, int lane) {
        const int xcd = blockIdx.x & 7, idx = blockIdx.x >> 3, tm = (xcd & 3) * 8 + (idx & 7), q = lane >> 4;
        ptr = wid < 4 ? p.wg : p.wu; phi = ((idx & 7) + (idx >> 3)) & 3; koff = xcd * 2;
#pragma unroll
        for (int i = 0; i < 2; ++i) { const int j = (lane & 15) ^ (q | (i << 2) | ((wid >> 2) << 3));
            voff[i] = j < 14 ? (unsigned)(((wid & 3) * 16 + q) * FF * 2 + tm * 224 + j * 16) : 0x7ffffff0u; }
    }
    __device__ __forceinline__ int kstep(int t) const { return (t + koff + ((t & 3) == phi ? 4 : 0)) & 15; }
    __device__ __forceinline__ void piece(LAS unsigned char* lds, int wid, const void* base, int so, unsigned nrec, int slot, int i) const { DMA16_DEV(RSRC(base, nrec), lds + slot * 32768 + wid * 4096 + i * 1024, voff[i >> 1], so + i * 4 * FF * 2); }
    __device__ __forceinline__ void early(LAS unsigned char* lds, int wid, int t) const {
#pragma unroll
        for (int i = 0; i < 4; ++i) piece(lds, wid, ptr, kstep(t) * 64 * FF * 2, DM * FF * 2, t, i);
    }
};
#define CSW 8
__device__ __forceinline__ void colsum_partial(const Params& p, LAS unsigned char* lds, int lw) {
    const int lane = threadIdx.x & 63, slab = blockIdx.x >> 1, mat = slab >= 56, c0 = (slab - mat * 56) * 64;
    const auto rw = RSRC(mat ? p.wu : p.wg, slab < 112 ? DM * FF * 2 : 0);
    const unsigned vo = (unsigned)(((lw * 256 + (lane >> 3)) * FF + c0 + (lane & 7) * 8) * 2);
    u32x4 v[CSW]; f32x4 s0 = {0.f, 0.f, 0.f, 0.f}, s1 = s0;
#pragma unroll
    for (int i = 0; i < CSW; ++i) v[i] = __builtin_amdgcn_raw_buffer_load_b128(rw, vo, i * 8 * FF * 2, 0);
#pragma nounroll
    for (int j = CSW; j < 32 + CSW; j += CSW)
#pragma unroll
        for (int i = 0; i < CSW; ++i) {
            const u32x4 w = v[i];
            s0 += (f32x4){bf_lo(w[0]), bf_hi(w[0]), bf_lo(w[1]), bf_hi(w[1])}; s1 += (f32x4){bf_lo(w[2]), bf_hi(w[2]), bf_lo(w[3]), bf_hi(w[3])};
            v[i] = __builtin_amdgcn_raw_buffer_load_b128(rw, j < 32 ? vo : 0x7ffffff0u, (j + i) * 8 * FF * 2, 0);
            sched0();
        }
    LAS float* tab = (LAS float*)(lds + 131072) + (lw * 8 + (lane >> 3)) * 64 + (lane & 7) * 8;
    *(LAS f32x4*)tab = s0; *(LAS f32x4*)(tab + 4) = s1;
    wait_lgkm0();
}
__device__ __forceinline__ void colsum_final(const Params& p, LAS unsigned char* lds, u64 ep) {
    const int lane = threadIdx.x & 63, slab = blockIdx.x >> 1, mat = slab >= 56, c0 = (slab - mat * 56) * 64;
    if (slab >= 112) return;
    const LAS float* tab = (const LAS float*)(lds + 131072);
    float a = 0.f, b = 0.f;
#pragma unroll
    for (int i = 0; i < 32; i += 2) { a += tab[i * 64 + lane]; b += tab[(i + 1) * 64 + lane]; }
    const int f = c0 + lane, t = f / 112, r = f - t * 112, h = r >= 56;
    __hip_atomic_store(p.csum + (mat * 2 + h) * 1792 + t * 56 + r - h * 56, a + b, __ATOMIC_RELAXED, __HIP_MEMORY_SCOPE_AGENT);
    wait_vm<0>();
    if (lane == 0) flag_store(p.flags + (F_CS + slab) * FLAG_STRIDE, ep);
}
#endif

enum { ROW, CHK, TR };
#define NKS2 28
#define NKS4 25
template <int ID> struct Cfg;
template <> struct Cfg<1> { static constexpr int NKT = 16, NKS = 16, FA = ROW, FB = ROW, LDB = DM; };
template <> struct Cfg<2> { static constexpr int NKT = 64, NKS = NKS2, FA = CHK, FB = ROW, LDB = SEQ; };
template <> struct Cfg<4> { static constexpr int NKT = 56, NKS = NKS4, FA = TR, FB = CHK, LDB = 0; };

#if defined(__HIP_DEVICE_COMPILE__)
template <int ID, int PRE>
__device__ __forceinline__ void g128_load(const Params& p, LAS unsigned char* lds, const int wid, u64& ep) {
    using C = Cfg<ID>;
    constexpr int STAGE = 32768, XLDS = 4 * STAGE;
    const int lane = threadIdx.x & 63, l15 = lane & 15, q = lane >> 4;
    const int xcd = blockIdx.x & 7, idx = blockIdx.x >> 3, im = idx & 3, in = idx >> 2;
    const bool recv = ID != 1 && (xcd & 1) == (ID == 2);
    const int tm = (ID == 1 ? (xcd & 3) : ((xcd >> 1) & 1)) * 4 + im, tn = (xcd >> 2) * 8 + in;
    const int k0 = recv ? C::NKS : 0, nk = recv ? C::NKT - C::NKS : C::NKS;
    u64* const flag = p.flags + (F_PAIR + (ID == 4 ? 512 : 0) + (blockIdx.x >> 1) * 4 + (wid & 3)) * FLAG_STRIDE;
    const unsigned xoff = (unsigned)(((ID == 4 ? 128 : 0) + (blockIdx.x >> 1)) * 32768 + (wid & 3) * 8192);
    const int lw = wid - 4, phi = (im + in) & 3;
    const int koff = ID == 1 ? xcd * 2 : ((xcd >> (ID == 2 ? 1 : 2)) & 1) * (nk >> 1);
    const unsigned ksw = (unsigned)(((lane & 7) ^ (lane >> 3)) << 4);
    unsigned voffA[2], voffB;
    if constexpr (C::FA == ROW) voffA[0] = (unsigned)((tm * 128 + lw * 32 + (lane >> 3)) * DM * 2) + ksw;
    if constexpr (C::FA == CHK) voffA[0] = (unsigned)((lw * 2 * DM + tm * 128 + lane) * 16);
    if constexpr (C::FA == TR) {
#pragma unroll
        for (int i = 0; i < 2; ++i) voffA[i] = (unsigned)((lw * 16 + q) * DM * 2 + tm * 256 + ((l15 ^ ((q | (i << 2)) << 1)) << 4));
    }
    if constexpr (C::FB == ROW) voffB = (unsigned)((((ID == 1 ? tn & 7 : tn)) * 128 + lw * 32 + (lane >> 3)) * C::LDB * 2) + ksw;
    else voffB = (unsigned)((lw * 2 * SEQ + tn * 128 + lane) * 16);
    const auto ra = ID == 1 ? RSRC(p.x, SEQ * DM * 2) : ID == 2 ? RSRC(p.abT, 2 * SEQ * DM * 2) : RSRC(p.wd, FF * DM * 2);
    auto stage = [&](int t, bool doA, bool doB) {
        int k = (t & 3) != phi ? t : t + 4 < nk ? t + 4 : phi;
        k += koff; k = k0 + (k >= nk ? k - nk : k);
        const auto rb = ID == 1 ? RSRC(tn < 8 ? p.wre : p.wim, DM * DM * 2) : ID == 2 ? RSRC(k < 32 ? p.cre : p.cim, SEQ * SEQ * 2) : RSRC(p.act, SEQ * FF * 2);
        LAS unsigned char* base = lds + (t & 3) * STAGE + lw * 4096;
#pragma unroll
        for (int i = 0; i < 4; ++i) {
            if (doA) {
                if constexpr (C::FA == ROW) DMA16(ra, base + i * 1024, voffA[0], k * 128 + i * 8 * DM * 2);
                if constexpr (C::FA == CHK) DMA16_DEV(ra, base + i * 1024, voffA[0], (k * 8 + (i >> 1)) * DM * 16 + (i & 1) * 1024);
                if constexpr (C::FA == TR) DMA16(ra, base + i * 1024, voffA[i >> 1], (k * 64 + i * 4) * DM * 2);
            }
            if (doB) {
                if constexpr (C::FB == ROW) DMA16(rb, base + 16384 + i * 1024, voffB, (k & 31) * 128 + i * 8 * C::LDB * 2);
                else DMA16_DEV(rb, base + 16384 + i * 1024, voffB, (k * 8 + (i >> 1)) * SEQ * 16 + (i & 1) * 1024);
            }
        }
    };
    if constexpr (PRE >= 0) { stage(PRE, ID == 4, ID == 2); return; }
    if constexpr (ID != 1) {
        u64* words = p.flags + (ID == 2 ? F_AB + tm * 32 : F_ACT + (tn >> 1) * 32) * FLAG_STRIDE;
        if (wid == 4 && lane == 0) flag_store(ID == 2 ? p.flags + (F_AB + (tn & 7) * 32 + (tn >> 3) * 16 + (xcd & 3) * 4 + im) * FLAG_STRIDE : words + ((xcd & 3) * 8 + (idx & 7)) * FLAG_STRIDE, ep);
        if (wid == 4) { const int j = lane & 31; wait_group(words + j * FLAG_STRIDE, ID == 2 ? 2 * j + 2 > k0 && 2 * j < k0 + nk : j * 14 + 14 > k0 * 8 && j * 14 < (k0 + nk) * 8, ep); }
        bar();
        stage(0, ID == 2, ID == 4); stage(1, ID == 2, ID == 4); stage(2, ID == 2, ID == 4);
        wait_vm<8>(); bar();
        wait_vm<4>();
    } else {
        stage(0, true, true); stage(1, true, true);
        wait_vm<8>(); bar();
        stage(2, true, true);
    }
#pragma nounroll
    for (int t = 0; t < nk - 3; ++t) {
        wait_vm<8>(); bar();
        stage(t + 3, true, true);
    }
    bool got = !recv;
    auto fetch = [&]() {
        if (uniform64(flag_load(flag)) != ep) return false;
        asm volatile("" ::: "memory");
#pragma unroll
        for (int i = 0; i < 8; ++i) DMA16_DEV(RSRC(p.xch, 256 * 32768), lds + XLDS + lw * 8192 + i * 1024, (unsigned)(lane * 16), xoff + i * 1024);
        return true;
    };
    wait_vm<8>(); bar();
    wait_vm<0>();
    if (!got) got = fetch();
    if constexpr (ID == 1) g128_load<2, 0>(p, lds, wid, ep);
    bar();
    if (!got) got = fetch();
    if constexpr (ID == 1) g128_load<2, 1>(p, lds, wid, ep);
    bar();
    if constexpr (ID == 1) g128_load<2, 2>(p, lds, wid, ep);
    if (recv) {
#pragma nounroll
        for (int spin = 0; !got && spin < (1 << 20); ++spin) { got = fetch(); __builtin_amdgcn_s_sleep(1); }
        wait_vm<0>(); bar();
        if constexpr (ID == 2) {
#pragma unroll
            for (int t = 0; t < 2; ++t) { G3W(p, lw, lane).early(lds, lw, t); G3W(p, wid, lane).early(lds, wid, t); }
        }
    }
    if constexpr (ID == 2) { if (!recv) colsum_partial(p, lds, lw); }
    if constexpr (ID == 1) { bar(); ep = uniform64(*(const LAS u64*)(lds + XLDS + 1536)); }
    if constexpr (ID != 4) bar();
}

template <int ID>
__device__ __forceinline__ void g128_compute(const Params& p, LAS unsigned char* lds, const int wid, u64& ep) {
    using C = Cfg<ID>;
    constexpr int STAGE = 32768, XLDS = 4 * STAGE;
    const int lane = threadIdx.x & 63, l15 = lane & 15, q = lane >> 4;
    const int xcd = blockIdx.x & 7, idx = blockIdx.x >> 3, im = idx & 3, in = idx >> 2;
    const bool recv = ID != 1 && (xcd & 1) == (ID == 2);
    const int tm = (ID == 1 ? (xcd & 3) : ((xcd >> 1) & 1)) * 4 + im, tn = (xcd >> 2) * 8 + in;
    const int k0 = recv ? C::NKS : 0, nk = recv ? C::NKT - C::NKS : C::NKS;
    u64* const flag = p.flags + (F_PAIR + (ID == 4 ? 512 : 0) + (blockIdx.x >> 1) * 4 + (wid & 3)) * FLAG_STRIDE;
    const unsigned xoff = (unsigned)(((ID == 4 ? 128 : 0) + (blockIdx.x >> 1)) * 32768 + (wid & 3) * 8192);
    STAMP_BEGIN;
    const int wm = wid & 1, wn = wid >> 1;
    const int eo = (q & 1) * 16 + (q >> 1) * 8;
    f32x4 acc[4][4], st[6];
#pragma unroll
    for (int m = 0; m < 4; ++m)
#pragma unroll
        for (int n = 0; n < 4; ++n) acc[m][n] = (f32x4){0.f, 0.f, 0.f, 0.f};
#pragma unroll
    for (int i = 0; i < 6; ++i) st[i] = (f32x4){0.f, 0.f, 0.f, 0.f};
    const int row0 = tn * 128 + wn * 64 + l15, col0 = tm * 128 + wm * 64 + eo;
    if (ID != 1 && !recv) {
        const auto rr = RSRC(ID == 2 ? p.x : p.h, SEQ * DM * 2);
#pragma unroll
        for (int n = 0; n < 4; ++n)
#pragma unroll
            for (int mp = 0; mp < 2; ++mp) {
                u32x4 r = __builtin_amdgcn_raw_buffer_load_b128(rr, (unsigned)(ID == 2 ? ((row0 + n * 16) * DM + col0 + mp * 32) * 2 : ((col0 / 8 + mp * 4) * SEQ + row0 + n * 16) * 16), 0, ID == 4 ? SC1 : 0);
                unsigned r0 = r[0], r1 = r[1], r2 = r[2], r3 = r[3];
                swap16u(r0, r2); swap16u(r1, r3);
                acc[2 * mp][n] = (f32x4){bf_lo(r0), bf_hi(r0), bf_lo(r1), bf_hi(r1)}; acc[2 * mp + 1][n] = (f32x4){bf_lo(r2), bf_hi(r2), bf_lo(r3), bf_hi(r3)};
            }
    }
    if constexpr (ID == 1) {
        ep = uniform64(flag_load(p.flags)) + 1;
        if (threadIdx.x == 0) *(LAS u64*)(lds + XLDS + 1536) = ep;
        if (idx == 0) {
            unsigned long long pc; int lo, hi;
            asm volatile("s_getpc_b64 %0\n s_mov_b32 %1, _Z7fused_k6Params@rel32@lo+4\n s_mov_b32 %2, _Z6tail_kv@rel32@lo+12" : "=s"(pc), "=s"(lo), "=s"(hi));
            const unsigned t0 = *(const unsigned*)(pc + (long long)min(lo + (int)threadIdx.x * 128, hi)); asm volatile("" ::"v"(t0));
        }
    }
    const int rotA = ID == 1 ? 2 * wn : 0, rotB = ID == 1 ? 2 * wm : 0;
    constexpr int NA = C::FA == CHK ? 1 : 4, NB = C::FB == CHK ? 1 : ID == 1 ? 4 : 2;
    unsigned adA[NA], adB[NB];
    const unsigned lds0 = (unsigned)(size_t)lds, row_sw = lds0 + (unsigned)(l15 * 128 + ((q ^ (lane & 7)) << 4)), chk = lds0 + (unsigned)(q * 2048 + l15 * 16);
#pragma unroll
    for (int i = 0; i < NA; ++i) {
        if constexpr (C::FA == ROW) adA[i] = (row_sw ^ (unsigned)((i >> 1) * 64)) + (unsigned)((wm * 4 + ((2 * (i & 1) + rotA) & 3)) * 2048);
        if constexpr (C::FA == CHK) adA[i] = chk + (unsigned)(wm * 1024);
        if constexpr (C::FA == TR) { const int q4 = l15 >> 2; adA[i] = lds0 + (unsigned)((8 * q + q4) * 256 + (((wm * 4 + i) ^ (q4 | ((q & 1) << 2))) << 5) + 8 * (lane & 3)); }
    }
#pragma unroll
    for (int i = 0; i < NB; ++i) {
        if constexpr (C::FB == CHK) adB[i] = 16384u + chk + (unsigned)(wn * 1024);
        else if constexpr (ID == 1) adB[i] = 16384u + (row_sw ^ (unsigned)((i >> 1) * 64)) + (unsigned)((wn * 4 + ((2 * (i & 1) + rotB) & 3)) * 2048);
        else adB[i] = 16384u + (row_sw ^ (unsigned)(i * 64)) + (unsigned)(wn * 8192);
    }
    constexpr int W = C::FA == TR ? 2 : 1, NR = 4 + 4 * W;
    auto rd = [&](auto ic, Frag (&ra)[4], bf16x8 (&rb)[4], auto kkc) {
        constexpr int kk = decltype(kkc)::value, i = decltype(ic)::value;
        if constexpr (i == 0 || (i > W && i <= W + 3)) { constexpr int n = i == 0 ? 0 : i - W;
            if constexpr (C::FB == CHK) rb[n] = lds_read128<kk * 8192 + n * 256>(adB[0]);
            else if constexpr (ID == 1) rb[n] = lds_read128<(n & 1) * 2048>(adB[kk * 2 + (n >> 1)]);
            else rb[n] = lds_read128<n * 2048>(adB[kk]); }
        else { constexpr int j = i <= W ? i - 1 : i - 4, m = j / W;
            if constexpr (C::FA != TR) { bf16x8 v;
                if constexpr (C::FA == CHK) v = lds_read128<kk * 8192 + m * 256>(adA[0]); else v = lds_read128<(m & 1) * 2048>(adA[kk * 2 + (m >> 1)]);
                const u32x4 w = __builtin_bit_cast(u32x4, v); ra[m].lo = (u32x2){w[0], w[1]}; ra[m].hi = (u32x2){w[2], w[3]}; }
            else if constexpr (j % 2 == 0) ra[m].lo = tr_read<kk * 8192>(adA[m]);
            else ra[m].hi = tr_read<kk * 8192 + 1024>(adA[m]); }
    };
    const bf16x8 ones = __builtin_bit_cast(bf16x8, (u32x4){0x3f803f80u, 0x3f803f80u, 0x3f803f80u, 0x3f803f80u});
    auto phase = [&](const Frag (&ra)[4], const bf16x8 (&rb)[4], Frag (&na)[4], bf16x8 (&nb)[4], int dstep, auto kkc) {
        static_for<0, 16>([&](auto sc) {
            constexpr int s = decltype(sc)::value, m = s >> 2, n = s & 3;
            if constexpr (n == 0) { wait_lgkm<(3 - m) * W + (s < NR ? s : NR)>(); sched0(); }
            acc[m][n] = MFMA(ra[m].v(), rb[n], acc[m][n]);
            if constexpr (s < NR) rd(sc, na, nb, kkc);
            if constexpr (s >= 12 && decltype(kkc)::value == 1) {
                if constexpr (s - 12 < NA) bump(adA[s - 12], dstep);
                if constexpr (s - 12 < NB) bump(adB[s - 12], dstep);
            }
            if constexpr (ID == 1 && s >= 10) {
                constexpr int i = s & 1;
                if constexpr (s < 12) st[i] = MFMA(ones, ra[i].v(), st[i]); else if constexpr (s < 14) st[2 + i] = MFMA(ra[i].v(), ra[i].v(), st[2 + i]); else st[4 + i] = MFMA(ones, rb[i], st[4 + i]); }
            sched0();
        });
    };
    Frag a0[4], a1[4]; bf16x8 b0[4], b1[4];
    constexpr std::integral_constant<int, 0> K0{}; constexpr std::integral_constant<int, 1> K1{};
    if constexpr (ID != 1) bar();
    bar();
    STAMP(1);
    static_for<0, NR>([&](auto ic) { rd(ic, a0, b0, K0); });
    sched0();
#pragma nounroll
    for (int t = 0; t < nk; ++t) {
        phase(a0, b0, a1, b1, (t & 3) == 3 ? -3 * STAGE : STAGE, K1);
        bar();
        phase(a1, b1, a0, b0, 0, K0);
    }
    wait_lgkm0(); keep(a0, b0); sched0();
    STAMP(2);
    if constexpr (ID == 1) {
        LAS float* tab = (LAS float*)(lds + XLDS);
        const float sgn = tn < 8 ? 1.f : -1.f;
        if (q == (l15 >> 2)) {
#pragma unroll
            for (int i = 0; i < 2; ++i) {
                const int j = l15 & 3; const float sq = j == 0 ? st[2 + i][0] : j == 1 ? st[2 + i][1] : j == 2 ? st[2 + i][2] : st[2 + i][3];
                const float mean = st[i][0] * (1.f / DM);
                const int ra = wm * 64 + (rotA + i) * 16 + l15;
                tab[ra] = mean; tab[128 + ra] = sgn * __builtin_amdgcn_rsqf(fmaxf(sq * (1.f / DM) - mean * mean, 0.f) + LN_EPS);
                tab[256 + wn * 64 + (rotB + i) * 16 + l15] = st[4 + i][0];
            }
        }
        wait_lgkm0(); bar();
        const auto ro = RSRC(p.abT, 2 * SEQ * DM * 2);
        float ws[4];
#pragma unroll
        for (int n = 0; n < 4; ++n) ws[n] = tab[256 + wn * 64 + ((n + rotB) & 3) * 16 + l15];
#pragma unroll
        for (int m = 0; m < 4; ++m) {
            const int r0 = wm * 64 + ((m + rotA) & 3) * 16 + 4 * q;
            const f32x4 mean = *(const LAS f32x4*)(tab + r0), rs = *(const LAS f32x4*)(tab + 128 + r0);
#pragma unroll
            for (int n = 0; n < 4; ++n) acc[m][n] = (acc[m][n] - mean * ws[n]) * rs;
        }
#pragma unroll
        for (int n = 0; n < 4; ++n) { const int e = (tn & 7) * 128 + wn * 64 + ((n + rotB) & 3) * 16 + l15;
#pragma unroll
            for (int mp = 0; mp < 2; ++mp) {
                unsigned x0 = pack_bf16(acc[2 * mp][n][0], acc[2 * mp][n][1]), x1 = pack_bf16(acc[2 * mp][n][2], acc[2 * mp][n][3]);
                unsigned y0 = pack_bf16(acc[2 * mp + 1][n][0], acc[2 * mp + 1][n][1]), y1 = pack_bf16(acc[2 * mp + 1][n][2], acc[2 * mp + 1][n][3]);
                swap16u(x0, y0); swap16u(x1, y1);
                const int s = (tn >> 3) * SEQ + tm * 128 + wm * 64 + ((2 * mp + rotA) & 3) * 16 + eo;
                __builtin_amdgcn_raw_buffer_store_b128((u32x4){x0, x1, y0, y1}, ro, (unsigned)(((s >> 3) * DM + e) * 16), 0, SC1);
            } }
        wait_vm<0>(); bar();
    } else if (!recv) {
        const auto rx = RSRC(p.xch, 256 * 32768);
#pragma unroll
        for (int m = 0; m < 4; ++m)
#pragma unroll
            for (int h = 0; h < 2; ++h) {
                const f32x4 u = acc[m][2 * h], v = acc[m][2 * h + 1];
                __builtin_amdgcn_raw_buffer_store_b128((u32x4){pack_bf16(u[0], u[1]), pack_bf16(u[2], u[3]), pack_bf16(v[0], v[1]), pack_bf16(v[2], v[3])}, rx, xoff + (unsigned)((m * 2 + h) * 1024 + lane * 16), 0, SC1);
            }
        wait_vm<0>();
        if (lane == 0) flag_store(flag, ep);
        if constexpr (ID == 2) bar();
    } else {
        bar();
        STAMP(4);
        const unsigned px = (unsigned)(size_t)lds + (unsigned)(XLDS + wid * 8192 + lane * 16);
        u32x4 pw[8];
        static_for<0, 8>([&](auto ic) { pw[decltype(ic)::value] = __builtin_bit_cast(u32x4, lds_read128<decltype(ic)::value * 1024>(px)); });
        wait_lgkm0(); sched0();
#pragma unroll
        for (int m = 0; m < 4; ++m)
#pragma unroll
            for (int h = 0; h < 2; ++h) { const u32x4 w = pw[m * 2 + h];
                acc[m][2 * h] += (f32x4){bf_lo(w[0]), bf_hi(w[0]), bf_lo(w[1]), bf_hi(w[1])}; acc[m][2 * h + 1] += (f32x4){bf_lo(w[2]), bf_hi(w[2]), bf_lo(w[3]), bf_hi(w[3])}; }
        const auto ro = RSRC(ID == 2 ? p.h : p.out, ID == 2 ? 136 * SEQ * 16 : SEQ * DM * 2);
#pragma unroll
        for (int n = 0; n < 4; ++n) {
            unsigned o[4][2];
#pragma unroll
            for (int m = 0; m < 4; ++m) { o[m][0] = pack_bf16(acc[m][n][0], acc[m][n][1]); o[m][1] = pack_bf16(acc[m][n][2], acc[m][n][3]); }
            if constexpr (ID == 2) {
                const f32x4 s4 = (acc[0][n] + acc[1][n]) + (acc[2][n] + acc[3][n]);
                const float mw = qsum((s4[0] + s4[1]) + (s4[2] + s4[3])) * (1.f / 64.f);
                const f32x4 d0 = acc[0][n] - mw, d1 = acc[1][n] - mw, d2 = acc[2][n] - mw, d3 = acc[3][n] - mw, q4 = (d0 * d0 + d1 * d1) + (d2 * d2 + d3 * d3);
                float m2 = (q4[0] + q4[1]) + (q4[2] + q4[3]);
                m2 = qsum(m2);
                if (q == 0) __builtin_amdgcn_raw_buffer_store_b64((u32x2){__float_as_uint(mw), __float_as_uint(m2)}, ro, (unsigned)(((128 + tm) * SEQ + row0 + n * 16) * 16 + wm * 8), 0, SC1);
            }
#pragma unroll
            for (int mp = 0; mp < 2; ++mp) {
                swap16u(o[2 * mp][0], o[2 * mp + 1][0]); swap16u(o[2 * mp][1], o[2 * mp + 1][1]);
                __builtin_amdgcn_raw_buffer_store_b128((u32x4){o[2 * mp][0], o[2 * mp][1], o[2 * mp + 1][0], o[2 * mp + 1][1]}, ro,
                    (unsigned)(ID == 4 ? ((row0 + n * 16) * DM + col0 + mp * 32) * 2 : ((col0 / 8 + mp * 4) * SEQ + row0 + n * 16) * 16), 0, SC1);
            }
        }
        STAMP(5);
        if constexpr (ID == 2) { wait_vm<0>(); bar(); }
    }
    STAMP_END(ID);
}
#endif

#if defined(__HIP_DEVICE_COMPILE__)
template <int GRP>
__device__ __forceinline__ void gemm3_body(const Params& p, LAS unsigned char* lds, const int wid, u64& ep) {
    constexpr int NK = 16, FA = 7, HB0 = 3 * 32768;
    STAMP_BEGIN;
    const int tid = threadIdx.x, lane = tid & 63;
    const int wm = wid & 1, wn = wid >> 1, l15 = lane & 15, q = lane >> 4;
    const int xcd = blockIdx.x & 7, idx = blockIdx.x >> 3, im = idx & 7, in = idx >> 3;
    const int tm = (xcd & 3) * 8 + im, tn = (xcd >> 2) * 4 + in;
    const G3W gw(p, wid, lane);
    unsigned pf = 0;
    if (!(xcd & 1)) {
        if (wid == 1) colsum_final(p, lds, ep);
        gw.early(lds, wid, 0); gw.early(lds, wid, 1);
    }
    if (wid == 0) {
        u64* words = p.flags + (F_H + tn * 32) * FLAG_STRIDE;
        if (lane == 0 && (xcd & 1)) flag_store(words + tm * FLAG_STRIDE, ep);
        const int sl = (tm * 112 >> 6) + (lane & 3);
        wait_group(lane < 32 ? words + lane * FLAG_STRIDE : p.flags + (F_CS + ((lane >> 2) & 1) * 56 + sl) * FLAG_STRIDE, lane < 32 ? (lane & 8) != 0 : lane < 40 && sl * 64 < tm * 112 + 112, ep);
        if (blockIdx.x == 0 && lane == 0) flag_store(p.flags, ep);
    }
    bar();
    const unsigned voffB = (unsigned)((wid * SEQ + tn * 256 + lane) * 16);
    struct Src { const void* base; int so; unsigned nrec; };
    auto pin = [](Src x) { asm volatile("" : "+s"(x.base), "+s"(x.so), "+s"(x.nrec)); return x; };
    auto srcW = [&](int t) { return pin(Src{t < NK ? (const void*)gw.ptr : (const void*)(p.csum + GRP * 3584), t < NK ? gw.kstep(t) * 64 * FF * 2 : 0, t < NK ? (unsigned)(DM * FF * 2) : t == NK ? (unsigned)(2 * FF * 2) : 0u}); };
    auto srcH = [&](int t) { Src x{nullptr, (t < NK ? gw.kstep(t) : NK) * 8 * SEQ * 16, t <= NK ? (unsigned)(136 * SEQ * 16) : 0u}; asm volatile("" : "+s"(x.so), "+s"(x.nrec)); return x; };
    auto stageW = [&](Src x, int slot, int i) { gw.piece(lds, wid, x.base, x.so, x.nrec, slot, i); };
    auto stageH = [&](Src x, int slot, int i) { DMA16_DEV(RSRC(p.h, x.nrec), lds + HB0 + slot * 32768 + wid * 4096 + i * 1024, voffB, x.so + i * 1024); };
#pragma unroll
    for (int t = 0; t < 2; ++t) {
        const Src xh = srcH(t);
#pragma unroll
        for (int i = 0; i < 4; ++i) stageH(xh, t, i);
    }

    unsigned adA[FA];
    const unsigned lds0 = (unsigned)(size_t)lds;
#pragma unroll
    for (int m = 0; m < FA; ++m) { const int q4 = l15 >> 2, pp = lane & 3, mf = m;
        adA[m] = lds0 + (unsigned)((pp >> 1) * 16384 + (8 * q + q4) * 256 + (((wm * FA + mf) ^ (q4 | ((q & 1) << 2) | ((pp >> 1) << 3))) << 4) + 8 * (pp & 1)); }
    unsigned adB = lds0 + (unsigned)(HB0 + q * 4096 + (wn * 64 + l15) * 16);
    auto rd = [&](auto ic, Frag (&ra)[FA], bf16x8 (&rb)[4], auto kkc) {
        constexpr int kk = decltype(kkc)::value, i = decltype(ic)::value;
        if constexpr (i < 4) rb[i] = lds_read128<kk * 16384 + i * 256>(adB);
        else if constexpr (i % 2 == 0) ra[(i - 4) / 2].lo = tr_read<kk * 8192>(adA[(i - 4) / 2]);
        else ra[(i - 4) / 2].hi = tr_read<kk * 8192 + 1024>(adA[(i - 4) / 2]);
    };
    f32x4 acc[FA][4];
#pragma unroll
    for (int m = 0; m < FA; ++m)
#pragma unroll
        for (int n = 0; n < 4; ++n) acc[m][n] = (f32x4){0.f, 0.f, 0.f, 0.f};
#define DSTR 2
#define PR1 16
#define PR2 4
#define PR3 20

    auto phase = [&](const Frag (&ra)[FA], const bf16x8 (&rb)[4], Frag (&na)[FA], bf16x8 (&nb)[4], int dw, int dh, auto kkc, auto grpc, auto&& dma) {
        static_for<0, 28>([&](auto sc) {
            constexpr int s = decltype(sc)::value, m = s >> 2, n = s & 3;
            if constexpr (decltype(kkc)::value == 0) { if constexpr (s == 0) __builtin_amdgcn_s_setprio(3); if constexpr (s == PR1) __builtin_amdgcn_s_setprio(2); }
            else { if constexpr (s == PR2) __builtin_amdgcn_s_setprio(1); if constexpr (s == PR3) __builtin_amdgcn_s_setprio(0); }
            acc[m][n] = MFMA(ra[m].v(), rb[n], acc[m][n]);
            if constexpr (s < 18) rd(sc, na, nb, kkc);
            if constexpr (s % DSTR == 0 && s < 8 * DSTR && decltype(grpc)::value == ((s / DSTR) & 1)) dma(s / (2 * DSTR));
            if constexpr (decltype(kkc)::value == 1 && s >= 18 && s < 26) { if constexpr (s < 25) bump(adA[s - 18], dw); else bump(adB, dh); }
            sched0();
        });
        wait_lgkm0(); sched0();
    };
    Frag a0[FA], a1[FA]; bf16x8 b0[4], b1[4];
    constexpr std::integral_constant<int, 0> K0{}; constexpr std::integral_constant<int, 1> K1{};
    wait_vm<4>(); bar();
    STAMP(1);
    static_for<0, 18>([&](auto ic) { rd(ic, a0, b0, K0); });
    wait_lgkm0(); sched0();
    const unsigned pfo = (unsigned)(((blockIdx.x * 8 + wid) * 28 + (lane < 28 ? lane : 0)) * 128); const int pft = 1 + idx % 12;
    auto kloop = [&](auto grpc) {
        int sw2 = 2;
#pragma nounroll
        for (int t = 0; t < NK; ++t) {
            const int sh = t & 1;
            const Src xw = srcW(t + 2), xh = srcH(t + 2);
            phase(a0, b0, a1, b1, sw2 == 1 ? -65536 : 32768, sh ? -32768 : 32768, K1, grpc, [&](int i) { stageW(xw, sw2, i);
                if (i == 3) touch(pf, pfo, p.wd, t == pft ? FF * DM * 2 : 0);
            });
            wait_vm<5>(); bar();
            phase(a1, b1, a0, b0, 0, 0, K0, grpc, [&](int i) { stageH(xh, sh, i); });
            sw2 = sw2 == 2 ? 0 : sw2 + 1;
        }
    };
    kloop(std::integral_constant<int, GRP>{});
    keep(a0, b0); sched0();
    STAMP(2);
    __builtin_amdgcn_s_setprio(0);
    f32x4 csv[FA];
    static_for<0, FA>([&](auto mc) { csv[decltype(mc)::value] = __builtin_bit_cast(f32x4, lds_read128<32768>(lds0 + (unsigned)((q >> 1) * 16384 + wm * 256 + (((decltype(mc)::value * 2 + (q & 1)) ^ (wm | ((q >> 1) << 3))) << 4)))); });
    float mean[4], c1[4], ve[4];
#pragma unroll
    for (int n = 0; n < 4; ++n) {
        const f32x4 s0 = *(const LAS f32x4*)(size_t)(adB + n * 256), s1 = *(const LAS f32x4*)(size_t)(adB + 16384 + n * 256);
        mean[n] = qsum((s0[0] + s0[2]) + (s1[0] + s1[2])) * (1.f / 16.f);
        const float d0 = s0[0] - mean[n], d1 = s0[2] - mean[n], d2 = s1[0] - mean[n], d3 = s1[2] - mean[n];
        const float m2 = qsum((s0[1] + s0[3]) + (s1[1] + s1[3]) + 64.f * ((d0 * d0 + d1 * d1) + (d2 * d2 + d3 * d3)));
        ve[n] = m2 * (1.f / DM) + LN_EPS; c1[n] = -1.44269504f * __builtin_amdgcn_rsqf(ve[n]);
    }
    wait_lgkm0(); wait_vm<0>(); asm volatile("; pf %0" ::"v"(pf)); bar();
    STAMP(4);
    if constexpr (GRP == 1) { g128_load<4, 0>(p, lds, wid, ep); g128_load<4, 1>(p, lds, wid, ep); g128_load<4, 2>(p, lds, wid, ep); }
    const auto ro = RSRC(p.act, SEQ * FF * 2);
#pragma unroll
    for (int m = 0; m < FA; ++m) {
        const f32x4 csum = csv[m];
        unsigned o[4];
#pragma unroll
        for (int n = 0; n < 4; ++n) {
            const f32x4 v = acc[m][n] - mean[n] * csum;
            float g0 = v[0], g1 = v[1], u0 = v[2], u1 = v[3];
            swap32(g0, u0); swap32(g1, u1);
            const f32x2 g = {g0, g1}, u = {u0, u1}, x = g * c1[n];
            const f32x2 d = (f32x2){__builtin_amdgcn_exp2f(x[0]), __builtin_amdgcn_exp2f(x[1])} * ve[n] + ve[n];
            const f32x2 r = g * u * (f32x2){__builtin_amdgcn_rcpf(d[0]), __builtin_amdgcn_rcpf(d[1])};
            o[n] = pack_bf16(r[0], r[1]);
        }
        swap16u(o[0], o[1]); swap16u(o[2], o[3]); swap32u(o[0], o[2]); swap32u(o[1], o[3]);
        __builtin_amdgcn_raw_buffer_store_b128((u32x4){o[0], o[2], o[1], o[3]}, ro, (unsigned)(((tm * 14 + wm * FA + m) * SEQ + tn * 256 + wn * 64 + lane) * 16), 0, SC1);
    }
    STAMP(5);
    wait_vm<0>(); bar();
    STAMP_END(3);
}
#endif

__global__ __launch_bounds__(512, 2) void fused_k(const Params p) {
#if defined(__HIP_DEVICE_COMPILE__)
    __shared__ __attribute__((aligned(16))) unsigned char lds_raw[5 * 32768];
    LAS unsigned char* lds = (LAS unsigned char*)lds_raw;
    const int wid = __builtin_amdgcn_readfirstlane(threadIdx.x >> 6);
    u64 ep = 0;
    if (wid < 4) { g128_compute<1>(p, lds, wid, ep); g128_compute<2>(p, lds, wid, ep); gemm3_body<0>(p, lds, wid, ep); g128_compute<4>(p, lds, wid, ep); }
    else { g128_load<1, -1>(p, lds, wid, ep); g128_load<2, -1>(p, lds, wid, ep); gemm3_body<1>(p, lds, wid, ep); g128_load<4, -1>(p, lds, wid, ep); }
#endif
}
__global__ void tail_k() {}

extern "C" void kernel_launch(void* const* d_in, const int* in_sizes, int n_in, void* d_out, int out_size, void* d_ws, size_t ws_size, hipStream_t stream) {
    Params p;
    p.x = (const u16*)d_in[0]; p.cre = (const u16*)d_in[1]; p.cim = (const u16*)d_in[2]; p.wre = (const u16*)d_in[3]; p.wim = (const u16*)d_in[4];
    p.wg = (const u16*)d_in[6]; p.wu = (const u16*)d_in[7]; p.wd = (const u16*)d_in[8];
    p.out = (u16*)d_out;
    char* ws = (char*)d_ws;
    p.abT = (u16*)ws; p.h = (u16*)(ws + (8u << 20)); p.act = (u16*)(ws + (16u << 20));
    p.xch = (u16*)(ws + (32u << 20)); p.flags = (unsigned long long*)(ws + (40u << 20)); p.csum = (float*)(ws + (40u << 20) + (512u << 10));
    fused_k<<<256, 512, 0, stream>>>(p);
}
```

```cpp
#include <hip/hip_runtime.h>
#include <hip/hip_bf16.h>
#include <type_traits>

#define LAS __attribute__((address_space(3)))
typedef unsigned short u16;
typedef __bf16 bf16x8 __attribute__((ext_vector_type(8)));
typedef __bf16 bf16x2 __attribute__((ext_vector_type(2)));
typedef float f32x4 __attribute__((ext_vector_type(4)));
typedef float f32x2 __attribute__((ext_vector_type(2)));
typedef unsigned u32x4 __attribute__((ext_vector_type(4)));
typedef unsigned u32x2 __attribute__((ext_vector_type(2)));

constexpr int SEQ = 2048, DM = 1024, FF = 3584;
constexpr float LN_EPS = 1e-5f;
constexpr int SC1 = 16;

struct Params {
    const u16* x; const u16* cre; const u16* cim; const u16* wre; const u16* wim;
    const u16* wg; const u16* wu; const u16* wd;
    u16* out; u16* abT; u16* h; u16* act; u16* xch; unsigned long long* flags; float* csum;
};

template <int N> __device__ __forceinline__ void wait_vm() { asm volatile("s_waitcnt vmcnt(%0)" ::"n"(N) : "memory"); }
__device__ __forceinline__ void wait_lgkm0() { __builtin_amdgcn_s_waitcnt(0xC07F); asm volatile("s_waitcnt lgkmcnt(0)" ::: "memory"); }
__device__ __forceinline__ void sched0() { __builtin_amdgcn_sched_barrier(0); }
__device__ __forceinline__ void bar() { sched0(); asm volatile("" ::: "memory"); __builtin_amdgcn_s_barrier(); asm volatile("" ::: "memory"); sched0(); }
__device__ __forceinline__ unsigned pack_bf16(float lo, float hi) { bf16x2 c; c[0] = (__bf16)lo; c[1] = (__bf16)hi; return __builtin_bit_cast(unsigned, c); }
__device__ __forceinline__ float bf_lo(unsigned v) { return __uint_as_float(v << 16); }
__device__ __forceinline__ float bf_hi(unsigned v) { return __uint_as_float(v & 0xffff0000u); }
__device__ __forceinline__ void swap16(float& a, float& b) { auto r = __builtin_amdgcn_permlane16_swap(__float_as_uint(a), __float_as_uint(b), false, false); a = __uint_as_float(r[0]); b = __uint_as_float(r[1]); }
__device__ __forceinline__ void swap32(float& a, float& b) { auto r = __builtin_amdgcn_permlane32_swap(__float_as_uint(a), __float_as_uint(b), false, false); a = __uint_as_float(r[0]); b = __uint_as_float(r[1]); }
__device__ __forceinline__ void swap16u(unsigned& a, unsigned& b) { auto r = __builtin_amdgcn_permlane16_swap(a, b, false, false); a = r[0]; b = r[1]; }
__device__ __forceinline__ void swap32u(unsigned& a, unsigned& b) { auto r = __builtin_amdgcn_permlane32_swap(a, b, false, false); a = r[0]; b = r[1]; }
__device__ __forceinline__ float qsum(float v) { float a = v, b = v; swap16(a, b); a += b; b = a; swap32(a, b); return a + b; }

struct Frag { u32x2 lo, hi; __device__ __forceinline__ bf16x8 v() const { return __builtin_bit_cast(bf16x8, __builtin_shufflevector(lo, hi, 0, 1, 2, 3)); } };
template <int OFF> __device__ __forceinline__ u32x2 tr_read(unsigned addr) { u32x2 r; asm volatile("ds_read_b64_tr_b16 %0, %1 offset:%2" : "=v"(r) : "v"(addr), "n"(OFF) : "memory"); return r; }
template <int OFF> __device__ __forceinline__ bf16x8 lds_read128(unsigned addr) { bf16x8 r; asm volatile("ds_read_b128 %0, %1 offset:%2" : "=v"(r) : "v"(addr), "n"(OFF) : "memory"); return r; }
template <int N> __device__ __forceinline__ void wait_lgkm() { asm volatile("s_waitcnt lgkmcnt(%0)" ::"n"(N) : "memory"); }
template <int I, int N, class F> __device__ __forceinline__ void static_for(F&& f) { if constexpr (I < N) { f(std::integral_constant<int, I>{}); static_for<I + 1, N>(f); } }
template <int NA> __device__ __forceinline__ void keep(const Frag (&a)[NA], const bf16x8 (&b)[4]) {
#pragma unroll
    for (int i = 0; i < NA; ++i) asm volatile("" ::"v"(a[i].lo), "v"(a[i].hi));
#pragma unroll
    for (int i = 0; i < 4; ++i) asm volatile("" ::"v"(b[i]));
}
__device__ __forceinline__ void bump(unsigned& a, int d) { asm volatile("v_add_u32 %0, %1, %0" : "+v"(a) : "s"(d)); }
#define MFMA(a, b, c) __builtin_amdgcn_mfma_f32_16x16x32_bf16(a, b, c, 0, 0, 0)
#define RSRC(ptr, bytes) __builtin_amdgcn_make_buffer_rsrc((void*)(ptr), 0, (bytes), 0x00020000)
#define DMA16(rs, ldsp, voff, soff) __builtin_amdgcn_raw_ptr_buffer_load_lds(rs, (LAS void*)(ldsp), 16, voff, soff, 0, 0)
#define DMA16_DEV(rs, ldsp, voff, soff) __builtin_amdgcn_raw_ptr_buffer_load_lds(rs, (LAS void*)(ldsp), 16, voff, soff, 0, SC1)

#define STAMP(i)
#define STAMP_BEGIN
#define STAMP_END(ID)

typedef unsigned long long u64;
constexpr int FLAG_STRIDE = 8, F_PAIR = 1, F_AB = F_PAIR + 1024, F_H = F_AB + 256, F_ACT = F_H + 256, F_CS = F_ACT + 256;
__device__ __forceinline__ u64 flag_load(u64* w) { return __hip_atomic_load(w, __ATOMIC_RELAXED, __HIP_MEMORY_SCOPE_AGENT); }
__device__ __forceinline__ void flag_store(u64* w, u64 v) { __hip_atomic_store(w, v, __ATOMIC_RELAXED, __HIP_MEMORY_SCOPE_AGENT); }
__device__ __forceinline__ u64 uniform64(u64 v) { return ((u64)(unsigned)__builtin_amdgcn_readfirstlane((unsigned)(v >> 32)) << 32) | (unsigned)__builtin_amdgcn_readfirstlane((unsigned)v); }
__device__ __forceinline__ void wait_group(u64* word, bool need, u64 ep) {
#pragma nounroll
    for (int spin = 0; spin < (1 << 20); ++spin) {
        if (!__any(need && flag_load(word) != ep)) break;
        __builtin_amdgcn_s_sleep(2);
    }
}
#if defined(__HIP_DEVICE_COMPILE__)
__device__ __forceinline__ void touch(unsigned& sink, unsigned voff, const void* base, unsigned bytes) {
    asm volatile("s_nop 4\n\tbuffer_load_dword %0, %1, %2, 0 offen" : "+v"(sink) : "v"(voff), "s"(RSRC(base, bytes)) : "memory");
}
struct G3W {
    const u16* ptr; unsigned voff[2]; int phi, koff;
    __device__ __forceinline__ G3W(const Params& p, int wid, int lane) {
        const int xcd = blockIdx.x & 7, idx = blockIdx.x >> 3, tm = (xcd & 3) * 8 + (idx & 7), q = lane >> 4;
        ptr = wid < 4 ? p.wg : p.wu; phi = ((idx & 7) + (idx >> 3)) & 3; koff = xcd * 2;
#pragma unroll
        for (int i = 0; i < 2; ++i) { const int j = (lane & 15) ^ (q | (i << 2) | ((wid >> 2) << 3));
            voff[i] = j < 14 ? (unsigned)(((wid & 3) * 16 + q) * FF * 2 + tm * 224 + j * 16) : 0x7ffffff0u; }
    }
    __device__ __forceinline__ int kstep(int t) const { return (t + koff + ((t & 3) == phi ? 4 : 0)) & 15; }
    __device__ __forceinline__ void piece(LAS unsigned char* lds, int wid, const void* base, int so, unsigned nrec, int slot, int i) const { DMA16_DEV(RSRC(base, nrec), lds + slot * 32768 + wid * 4096 + i * 1024, voff[i >> 1], so + i * 4 * FF * 2); }
    __device__ __forceinline__ void early(LAS unsigned char* lds, int wid, int t) const {
#pragma unroll
        for (int i = 0; i < 4; ++i) piece(lds, wid, ptr, kstep(t) * 64 * FF * 2, DM * FF * 2, t, i);
    }
};
#define CSW 8
__device__ __forceinline__ void colsum_partial(const Params& p, LAS unsigned char* lds, int lw) {
    const int lane = threadIdx.x & 63, slab = blockIdx.x >> 1, mat = slab >= 56, c0 = (slab - mat * 56) * 64;
    const auto rw = RSRC(mat ? p.wu : p.wg, slab < 112 ? DM * FF * 2 : 0);
    const unsigned vo = (unsigned)(((lw * 256 + (lane >> 3)) * FF + c0 + (lane & 7) * 8) * 2);
    u32x4 v[CSW]; f32x4 s0 = {0.f, 0.f, 0.f, 0.f}, s1 = s0;
#pragma unroll
    for (int i = 0; i < CSW; ++i) v[i] = __builtin_amdgcn_raw_buffer_load_b128(rw, vo, i * 8 * FF * 2, 0);
#pragma nounroll
    for (int j = CSW; j < 32 + CSW; j += CSW)
#pragma unroll
        for (int i = 0; i < CSW; ++i) {
            const u32x4 w = v[i];
            s0 += (f32x4){bf_lo(w[0]), bf_hi(w[0]), bf_lo(w[1]), bf_hi(w[1])}; s1 += (f32x4){bf_lo(w[2]), bf_hi(w[2]), bf_lo(w[3]), bf_hi(w[3])};
            v[i] = __builtin_amdgcn_raw_buffer_load_b128(rw, j < 32 ? vo : 0x7ffffff0u, (j + i) * 8 * FF * 2, 0);
            sched0();
        }
    LAS float* tab = (LAS float*)(lds + 131072) + (lw * 8 + (lane >> 3)) * 64 + (lane & 7) * 8;
    *(LAS f32x4*)tab = s0; *(LAS f32x4*)(tab + 4) = s1;
    wait_lgkm0();
}
__device__ __forceinline__ void colsum_final(const Params& p, LAS unsigned char* lds, u64 ep) {
    const int lane = threadIdx.x & 63, slab = blockIdx.x >> 1, mat = slab >= 56, c0 = (slab - mat * 56) * 64;
    if (slab >= 112) return;
    const LAS float* tab = (const LAS float*)(lds + 131072);
    float a = 0.f, b = 0.f;
#pragma unroll
    for (int i = 0; i < 32; i += 2) { a += tab[i * 64 + lane]; b += tab[(i + 1) * 64 + lane]; }
    const int f = c0 + lane, t = f / 112, r = f - t * 112, h = r >= 56;
    __hip_atomic_store(p.csum + (mat * 2 + h) * 1792 + t * 56 + r - h * 56, a + b, __ATOMIC_RELAXED, __HIP_MEMORY_SCOPE_AGENT);
    wait_vm<0>();
    if (lane == 0) flag_store(p.flags + (F_CS + slab) * FLAG_STRIDE, ep);
}
#endif

enum { ROW, CHK, TR };
#define NKS2 28
#define OUT_AUX 16
#define CAUX 0
#define NKS4 25
template <int ID> struct Cfg;
template <> struct Cfg<1> { static constexpr int NKT = 16, NKS = 16, FA = ROW, FB = ROW, LDB = DM; };
template <> struct Cfg<2> { static constexpr int NKT = 64, NKS = NKS2, FA = CHK, FB = ROW, LDB = SEQ; };
template <> struct Cfg<4> { static constexpr int NKT = 56, NKS = NKS4, FA = TR, FB = CHK, LDB = 0; };

#if defined(__HIP_DEVICE_COMPILE__)
template <int ID, int PRE>
__device__ __forceinline__ void g128_load(const Params& p, LAS unsigned char* lds, const int wid, u64& ep) {
    using C = Cfg<ID>;
    constexpr int STAGE = 32768, XLDS = 4 * STAGE;
    const int lane = threadIdx.x & 63, l15 = lane & 15, q = lane >> 4;
    const int xcd = blockIdx.x & 7, idx = blockIdx.x >> 3, im = idx & 3, in = idx >> 2;
    const bool recv = ID != 1 && (xcd & 1) == (ID == 2);
    const int tm = (ID == 1 ? (xcd & 3) : ((xcd >> 1) & 1)) * 4 + im, tn = (xcd >> 2) * 8 + in;
    const int k0 = recv ? C::NKS : 0, nk = recv ? C::NKT - C::NKS : C::NKS;
    u64* const flag = p.flags + (F_PAIR + (ID == 4 ? 512 : 0) + (blockIdx.x >> 1) * 4 + (wid & 3)) * FLAG_STRIDE;
    const unsigned xoff = (unsigned)(((ID == 4 ? 128 : 0) + (blockIdx.x >> 1)) * 32768 + (wid & 3) * 8192);
    const int lw = wid - 4, phi = (im + in) & 3;
    const int koff = ID == 1 ? xcd * 2 : ((xcd >> (ID == 2 ? 1 : 2)) & 1) * (nk >> 1);
    const unsigned ksw = (unsigned)(((lane & 7) ^ (lane >> 3)) << 4);
    unsigned voffA[2], voffB;
    if constexpr (C::FA == ROW) voffA[0] = (unsigned)((tm * 128 + lw * 32 + (lane >> 3)) * DM * 2) + ksw;
    if constexpr (C::FA == CHK) voffA[0] = (unsigned)((lw * 2 * DM + tm * 128 + lane) * 16);
    if constexpr (C::FA == TR) {
#pragma unroll
        for (int i = 0; i < 2; ++i) voffA[i] = (unsigned)((lw * 16 + q) * DM * 2 + tm * 256 + ((l15 ^ ((q | (i << 2)) << 1)) << 4));
    }
    if constexpr (C::FB == ROW) voffB = (unsigned)((((ID == 1 ? tn & 7 : tn)) * 128 + lw * 32 + (lane >> 3)) * C::LDB * 2) + ksw;
    else voffB = (unsigned)((lw * 2 * SEQ + tn * 128 + lane) * 16);
    const auto ra = ID == 1 ? RSRC(p.x, SEQ * DM * 2) : ID == 2 ? RSRC(p.abT, 2 * SEQ * DM * 2) : RSRC(p.wd, FF * DM * 2);
    auto stage = [&](int t, bool doA, bool doB) {
        int k = (t & 3) != phi ? t : t + 4 < nk ? t + 4 : phi;
        k += koff; k = k0 + (k >= nk ? k - nk : k);
        const auto rb = ID == 1 ? RSRC(tn < 8 ? p.wre : p.wim, DM * DM * 2) : ID == 2 ? RSRC(k < 32 ? p.cre : p.cim, SEQ * SEQ * 2) : RSRC(p.act, SEQ * FF * 2);
        LAS unsigned char* base = lds + (t & 3) * STAGE + lw * 4096;
#pragma unroll
        for (int i = 0; i < 4; ++i) {
            if (doA) {
                if constexpr (C::FA == ROW) DMA16(ra, base + i * 1024, voffA[0], k * 128 + i * 8 * DM * 2);
                if constexpr (C::FA == CHK) DMA16_DEV(ra, base + i * 1024, voffA[0], (k * 8 + (i >> 1)) * DM * 16 + (i & 1) * 1024);
                if constexpr (C::FA == TR) DMA16(ra, base + i * 1024, voffA[i >> 1], (k * 64 + i * 4) * DM * 2);
            }
            if (doB) {
                if constexpr (C::FB == ROW) __builtin_amdgcn_raw_ptr_buffer_load_lds(rb, (LAS void*)(base + 16384 + i * 1024), 16, voffB, (k & 31) * 128 + i * 8 * C::LDB * 2, 0, ID == 2 ? CAUX : 0);
                else DMA16_DEV(rb, base + 16384 + i * 1024, voffB, (k * 8 + (i >> 1)) * SEQ * 16 + (i & 1) * 1024);
            }
        }
    };
    if constexpr (PRE >= 0) { stage(PRE, ID == 4, ID == 2); return; }
    if constexpr (ID != 1) {
        u64* words = p.flags + (ID == 2 ? F_AB + tm * 32 : F_ACT + (tn >> 1) * 32) * FLAG_STRIDE;
        if (wid == 4 && lane == 0) flag_store(ID == 2 ? p.flags + (F_AB + (tn & 7) * 32 + (tn >> 3) * 16 + (xcd & 3) * 4 + im) * FLAG_STRIDE : words + ((xcd & 3) * 8 + (idx & 7)) * FLAG_STRIDE, ep);
        if (wid == 4) { const int j = lane & 31; wait_group(words + j * FLAG_STRIDE, ID == 2 ? 2 * j + 2 > k0 && 2 * j < k0 + nk : j * 14 + 14 > k0 * 8 && j * 14 < (k0 + nk) * 8, ep); }
        bar();
        stage(0, ID == 2, ID == 4); stage(1, ID == 2, ID == 4); stage(2, ID == 2, ID == 4);
        wait_vm<8>(); bar();
        wait_vm<4>();
    } else {
        stage(0, true, true); stage(1, true, true);
        wait_vm<8>(); bar();
        stage(2, true, true);
    }
#pragma nounroll
    for (int t = 0; t < nk - 3; ++t) {
        wait_vm<8>(); bar();
        stage(t + 3, true, true);
    }
    bool got = !recv;
    auto fetch = [&]() {
        if (uniform64(flag_load(flag)) != ep) return false;
        asm volatile("" ::: "memory");
#pragma unroll
        for (int i = 0; i < 8; ++i) DMA16_DEV(RSRC(p.xch, 256 * 32768), lds + XLDS + lw * 8192 + i * 1024, (unsigned)(lane * 16), xoff + i * 1024);
        return true;
    };
    wait_vm<8>(); bar();
    wait_vm<0>();
    if (!got) got = fetch();
    if constexpr (ID == 1) g128_load<2, 0>(p, lds, wid, ep);
    bar();
    if (!got) got = fetch();
    if constexpr (ID == 1) g128_load<2, 1>(p, lds, wid, ep);
    bar();
    if constexpr (ID == 1) g128_load<2, 2>(p, lds, wid, ep);
    if (recv) {
#pragma nounroll
        for (int spin = 0; !got && spin < (1 << 20); ++spin) { got = fetch(); __builtin_amdgcn_s_sleep(1); }
        wait_vm<0>(); bar();
        if constexpr (ID == 2) {
#pragma unroll
            for (int t = 0; t < 2; ++t) { G3W(p, lw, lane).early(lds, lw, t); G3W(p, wid, lane).early(lds, wid, t); }
        }
    }
    if constexpr (ID == 2) { if (!recv) colsum_partial(p, lds, lw); }
    if constexpr (ID == 1) { bar(); ep = uniform64(*(const LAS u64*)(lds + XLDS + 1536)); }
    if constexpr (ID != 4) bar();
}

template <int ID>
__device__ __forceinline__ void g128_compute(const Params& p, LAS unsigned char* lds, const int wid, u64& ep) {
    using C = Cfg<ID>;
    constexpr int STAGE = 32768, XLDS = 4 * STAGE;
    const int lane = threadIdx.x & 63, l15 = lane & 15, q = lane >> 4;
    const int xcd = blockIdx.x & 7, idx = blockIdx.x >> 3, im = idx & 3, in = idx >> 2;
    const bool recv = ID != 1 && (xcd & 1) == (ID == 2);
    const int tm = (ID == 1 ? (xcd & 3) : ((xcd >> 1) & 1)) * 4 + im, tn = (xcd >> 2) * 8 + in;
    const int k0 = recv ? C::NKS : 0, nk = recv ? C::NKT - C::NKS : C::NKS;
    u64* const flag = p.flags + (F_PAIR + (ID == 4 ? 512 : 0) + (blockIdx.x >> 1) * 4 + (wid & 3)) * FLAG_STRIDE;
    const unsigned xoff = (unsigned)(((ID == 4 ? 128 : 0) + (blockIdx.x >> 1)) * 32768 + (wid & 3) * 8192);
    STAMP_BEGIN;
    const int wm = wid & 1, wn = wid >> 1;
    const int eo = (q & 1) * 16 + (q >> 1) * 8;
    f32x4 acc[4][4], st[6];
#pragma unroll
    for (int m = 0; m < 4; ++m)
#pragma unroll
        for (int n = 0; n < 4; ++n) acc[m][n] = (f32x4){0.f, 0.f, 0.f, 0.f};
#pragma unroll
    for (int i = 0; i < 6; ++i) st[i] = (f32x4){0.f, 0.f, 0.f, 0.f};
    const int row0 = tn * 128 + wn * 64 + l15, col0 = tm * 128 + wm * 64 + eo;
    if (ID != 1 && !recv) {
        const auto rr = RSRC(ID == 2 ? p.x : p.h, SEQ * DM * 2);
#pragma unroll
        for (int n = 0; n < 4; ++n)
#pragma unroll
            for (int mp = 0; mp < 2; ++mp) {
                u32x4 r = __builtin_amdgcn_raw_buffer_load_b128(rr, (unsigned)(ID == 2 ? ((row0 + n * 16) * DM + col0 + mp * 32) * 2 : ((col0 / 8 + mp * 4) * SEQ + row0 + n * 16) * 16), 0, ID == 4 ? SC1 : 0);
                unsigned r0 = r[0], r1 = r[1], r2 = r[2], r3 = r[3];
                swap16u(r0, r2); swap16u(r1, r3);
                acc[2 * mp][n] = (f32x4){bf_lo(r0), bf_hi(r0), bf_lo(r1), bf_hi(r1)}; acc[2 * mp + 1][n] = (f32x4){bf_lo(r2), bf_hi(r2), bf_lo(r3), bf_hi(r3)};
            }
    }
    if constexpr (ID == 1) {
        ep = uniform64(flag_load(p.flags)) + 1;
        if (threadIdx.x == 0) *(LAS u64*)(lds + XLDS + 1536) = ep;
        if (idx == 0) {
            unsigned long long pc; int lo, hi;
            asm volatile("s_getpc_b64 %0\n s_mov_b32 %1, _Z7fused_k6Params@rel32@lo+4\n s_mov_b32 %2, _Z6tail_kv@rel32@lo+12" : "=s"(pc), "=s"(lo), "=s"(hi));
            const unsigned t0 = *(const unsigned*)(pc + (long long)min(lo + (int)threadIdx.x * 128, hi)); asm volatile("" ::"v"(t0));
        }
    }
    const int rotA = ID == 1 ? 2 * wn : 0, rotB = ID == 1 ? 2 * wm : 0;
    constexpr int NA = C::FA == CHK ? 1 : 4, NB = C::FB == CHK ? 1 : ID == 1 ? 4 : 2;
    unsigned adA[NA], adB[NB];
    const unsigned lds0 = (unsigned)(size_t)lds, row_sw = lds0 + (unsigned)(l15 * 128 + ((q ^ (lane & 7)) << 4)), chk = lds0 + (unsigned)(q * 2048 + l15 * 16);
#pragma unroll
    for (int i = 0; i < NA; ++i) {
        if constexpr (C::FA == ROW) adA[i] = (row_sw ^ (unsigned)((i >> 1) * 64)) + (unsigned)((wm * 4 + ((2 * (i & 1) + rotA) & 3)) * 2048);
        if constexpr (C::FA == CHK) adA[i] = chk + (unsigned)(wm * 1024);
        if constexpr (C::FA == TR) { const int q4 = l15 >> 2; adA[i] = lds0 + (unsigned)((8 * q + q4) * 256 + (((wm * 4 + i) ^ (q4 | ((q & 1) << 2))) << 5) + 8 * (lane & 3)); }
    }
#pragma unroll
    for (int i = 0; i < NB; ++i) {
        if constexpr (C::FB == CHK) adB[i] = 16384u + chk + (unsigned)(wn * 1024);
        else if constexpr (ID == 1) adB[i] = 16384u + (row_sw ^ (unsigned)((i >> 1) * 64)) + (unsigned)((wn * 4 + ((2 * (i & 1) + rotB) & 3)) * 2048);
        else adB[i] = 16384u + (row_sw ^ (unsigned)(i * 64)) + (unsigned)(wn * 8192);
    }
    constexpr int W = C::FA == TR ? 2 : 1, NR = 4 + 4 * W;
    auto rd = [&](auto ic, Frag (&ra)[4], bf16x8 (&rb)[4], auto kkc) {
        constexpr int kk = decltype(kkc)::value, i = decltype(ic)::value;
        if constexpr (i == 0 || (i > W && i <= W + 3)) { constexpr int n = i == 0 ? 0 : i - W;
            if constexpr (C::FB == CHK) rb[n] = lds_read128<kk * 8192 + n * 256>(adB[0]);
            else if constexpr (ID == 1) rb[n] = lds_read128<(n & 1) * 2048>(adB[kk * 2 + (n >> 1)]);
            else rb[n] = lds_read128<n * 2048>(adB[kk]); }
        else { constexpr int j = i <= W ? i - 1 : i - 4, m = j / W;
            if constexpr (C::FA != TR) { bf16x8 v;
                if constexpr (C::FA == CHK) v = lds_read128<kk * 8192 + m * 256>(adA[0]); else v = lds_read128<(m & 1) * 2048>(adA[kk * 2 + (m >> 1)]);
                const u32x4 w = __builtin_bit_cast(u32x4, v); ra[m].lo = (u32x2){w[0], w[1]}; ra[m].hi = (u32x2){w[2], w[3]}; }
            else if constexpr (j % 2 == 0) ra[m].lo = tr_read<kk * 8192>(adA[m]);
            else ra[m].hi = tr_read<kk * 8192 + 1024>(adA[m]); }
    };
    const bf16x8 ones = __builtin_bit_cast(bf16x8, (u32x4){0x3f803f80u, 0x3f803f80u, 0x3f803f80u, 0x3f803f80u});
    auto phase = [&](const Frag (&ra)[4], const bf16x8 (&rb)[4], Frag (&na)[4], bf16x8 (&nb)[4], int dstep, auto kkc) {
        static_for<0, 16>([&](auto sc) {
            constexpr int s = decltype(sc)::value, m = s >> 2, n = s & 3;
            if constexpr (n == 0) { wait_lgkm<(3 - m) * W + (s < NR ? s : NR)>(); sched0(); }
            acc[m][n] = MFMA(ra[m].v(), rb[n], acc[m][n]);
            if constexpr (s < NR) rd(sc, na, nb, kkc);
            if constexpr (s >= 12 && decltype(kkc)::value == 1) {
                if constexpr (s - 12 < NA) bump(adA[s - 12], dstep);
                if constexpr (s - 12 < NB) bump(adB[s - 12], dstep);
            }
            if constexpr (ID == 1 && s >= 10) {
                constexpr int i = s & 1;
                if constexpr (s < 12) st[i] = MFMA(ones, ra[i].v(), st[i]); else if constexpr (s < 14) st[2 + i] = MFMA(ra[i].v(), ra[i].v(), st[2 + i]); else st[4 + i] = MFMA(ones, rb[i], st[4 + i]); }
            sched0();
        });
    };
    Frag a0[4], a1[4]; bf16x8 b0[4], b1[4];
    constexpr std::integral_constant<int, 0> K0{}; constexpr std::integral_constant<int, 1> K1{};
    if constexpr (ID != 1) bar();
    bar();
    STAMP(1);
    static_for<0, NR>([&](auto ic) { rd(ic, a0, b0, K0); });
    sched0();
#pragma nounroll
    for (int t = 0; t < nk; ++t) {
        phase(a0, b0, a1, b1, (t & 3) == 3 ? -3 * STAGE : STAGE, K1);
        bar();
        phase(a1, b1, a0, b0, 0, K0);
    }
    wait_lgkm0(); keep(a0, b0); sched0();
    STAMP(2);
    if constexpr (ID == 1) {
        LAS float* tab = (LAS float*)(lds + XLDS);
        const float sgn = tn < 8 ? 1.f : -1.f;
        if (q == (l15 >> 2)) {
#pragma unroll
            for (int i = 0; i < 2; ++i) {
                const int j = l15 & 3; const float sq = j == 0 ? st[2 + i][0] : j == 1 ? st[2 + i][1] : j == 2 ? st[2 + i][2] : st[2 + i][3];
                const float mean = st[i][0] * (1.f / DM);
                const int ra = wm * 64 + (rotA + i) * 16 + l15;
                tab[ra] = mean; tab[128 + ra] = sgn * __builtin_amdgcn_rsqf(fmaxf(sq * (1.f / DM) - mean * mean, 0.f) + LN_EPS);
                tab[256 + wn * 64 + (rotB + i) * 16 + l15] = st[4 + i][0];
            }
        }
        wait_lgkm0(); bar();
        const auto ro = RSRC(p.abT, 2 * SEQ * DM * 2);
        float ws[4];
#pragma unroll
        for (int n = 0; n < 4; ++n) ws[n] = tab[256 + wn * 64 + ((n + rotB) & 3) * 16 + l15];
#pragma unroll
        for (int m = 0; m < 4; ++m) {
            const int r0 = wm * 64 + ((m + rotA) & 3) * 16 + 4 * q;
            const f32x4 mean = *(const LAS f32x4*)(tab + r0), rs = *(const LAS f32x4*)(tab + 128 + r0);
#pragma unroll
            for (int n = 0; n < 4; ++n) acc[m][n] = (acc[m][n] - mean * ws[n]) * rs;
        }
#pragma unroll
        for (int n = 0; n < 4; ++n) { const int e = (tn & 7) * 128 + wn * 64 + ((n + rotB) & 3) * 16 + l15;
#pragma unroll
            for (int mp = 0; mp < 2; ++mp) {
                unsigned x0 = pack_bf16(acc[2 * mp][n][0], acc[2 * mp][n][1]), x1 = pack_bf16(acc[2 * mp][n][2], acc[2 * mp][n][3]);
                unsigned y0 = pack_bf16(acc[2 * mp + 1][n][0], acc[2 * mp + 1][n][1]), y1 = pack_bf16(acc[2 * mp + 1][n][2], acc[2 * mp + 1][n][3]);
                swap16u(x0, y0); swap16u(x1, y1);
                const int s = (tn >> 3) * SEQ + tm * 128 + wm * 64 + ((2 * mp + rotA) & 3) * 16 + eo;
                __builtin_amdgcn_raw_buffer_store_b128((u32x4){x0, x1, y0, y1}, ro, (unsigned)(((s >> 3) * DM + e) * 16), 0, SC1);
            } }
        wait_vm<0>(); bar();
    } else if (!recv) {
        const auto rx = RSRC(p.xch, 256 * 32768);
#pragma unroll
        for (int m = 0; m < 4; ++m)
#pragma unroll
            for (int h = 0; h < 2; ++h) {
                const f32x4 u = acc[m][2 * h], v = acc[m][2 * h + 1];
                __builtin_amdgcn_raw_buffer_store_b128((u32x4){pack_bf16(u[0], u[1]), pack_bf16(u[2], u[3]), pack_bf16(v[0], v[1]), pack_bf16(v[2], v[3])}, rx, xoff + (unsigned)((m * 2 + h) * 1024 + lane * 16), 0, SC1);
            }
        wait_vm<0>();
        if (lane == 0) flag_store(flag, ep);
        if constexpr (ID == 2) bar();
    } else {
        bar();
        STAMP(4);
        const unsigned px = (unsigned)(size_t)lds + (unsigned)(XLDS + wid * 8192 + lane * 16);
        u32x4 pw[8];
        static_for<0, 8>([&](auto ic) { pw[decltype(ic)::value] = __builtin_bit_cast(u32x4, lds_read128<decltype(ic)::value * 1024>(px)); });
        wait_lgkm0(); sched0();
#pragma unroll
        for (int m = 0; m < 4; ++m)
#pragma unroll
            for (int h = 0; h < 2; ++h) { const u32x4 w = pw[m * 2 + h];
                acc[m][2 * h] += (f32x4){bf_lo(w[0]), bf_hi(w[0]), bf_lo(w[1]), bf_hi(w[1])}; acc[m][2 * h + 1] += (f32x4){bf_lo(w[2]), bf_hi(w[2]), bf_lo(w[3]), bf_hi(w[3])}; }
        const auto ro = RSRC(ID == 2 ? p.h : p.out, ID == 2 ? 136 * SEQ * 16 : SEQ * DM * 2);
#pragma unroll
        for (int n = 0; n < 4; ++n) {
            unsigned o[4][2];
#pragma unroll
            for (int m = 0; m < 4; ++m) { o[m][0] = pack_bf16(acc[m][n][0], acc[m][n][1]); o[m][1] = pack_bf16(acc[m][n][2], acc[m][n][3]); }
            if constexpr (ID == 2) {
                const f32x4 s4 = (acc[0][n] + acc[1][n]) + (acc[2][n] + acc[3][n]);
                const float mw = qsum((s4[0] + s4[1]) + (s4[2] + s4[3])) * (1.f / 64.f);
                const f32x4 d0 = acc[0][n] - mw, d1 = acc[1][n] - mw, d2 = acc[2][n] - mw, d3 = acc[3][n] - mw, q4 = (d0 * d0 + d1 * d1) + (d2 * d2 + d3 * d3);
                float m2 = (q4[0] + q4[1]) + (q4[2] + q4[3]);
                m2 = qsum(m2);
                if (q == 0) __builtin_amdgcn_raw_buffer_store_b64((u32x2){__float_as_uint(mw), __float_as_uint(m2)}, ro, (unsigned)(((128 + tm) * SEQ + row0 + n * 16) * 16 + wm * 8), 0, SC1);
            }
#pragma unroll
            for (int mp = 0; mp < 2; ++mp) {
                swap16u(o[2 * mp][0], o[2 * mp + 1][0]); swap16u(o[2 * mp][1], o[2 * mp + 1][1]);
                __builtin_amdgcn_raw_buffer_store_b128((u32x4){o[2 * mp][0], o[2 * mp][1], o[2 * mp + 1][0], o[2 * mp + 1][1]}, ro,
                    (unsigned)(ID == 4 ? ((row0 + n * 16) * DM + col0 + mp * 32) * 2 : ((col0 / 8 + mp * 4) * SEQ + row0 + n * 16) * 16), 0, ID == 4 ? OUT_AUX : SC1);
            }
        }
        STAMP(5);
        if constexpr (ID == 2) { wait_vm<0>(); bar(); }
    }
    STAMP_END(ID);
}
#endif

#if defined(__HIP_DEVICE_COMPILE__)
template <int GRP>
__device__ __forceinline__ void gemm3_body(const Params& p, LAS unsigned char* lds, const int wid, u64& ep) {
    constexpr int NK = 16, FA = 7, HB0 = 3 * 32768;
    STAMP_BEGIN;
    const int tid = threadIdx.x, lane = tid & 63;
    const int wm = wid & 1, wn = wid >> 1, l15 = lane & 15, q = lane >> 4;
    const int xcd = blockIdx.x & 7, idx = blockIdx.x >> 3, im = idx & 7, in = idx >> 3;
    const int tm = (xcd & 3) * 8 + im, tn = (xcd >> 2) * 4 + in;
    const G3W gw(p, wid, lane);
    unsigned pf = 0;
    if (!(xcd & 1)) {
        if (wid == 1) colsum_final(p, lds, ep);
        gw.early(lds, wid, 0); gw.early(lds, wid, 1);
    }
    if (wid == 0) {
        u64* words = p.flags + (F_H + tn * 32) * FLAG_STRIDE;
        if (lane == 0 && (xcd & 1)) flag_store(words + tm * FLAG_STRIDE, ep);
        const int sl = (tm * 112 >> 6) + (lane & 3);
        wait_group(lane < 32 ? words + lane * FLAG_STRIDE : p.flags + (F_CS + ((lane >> 2) & 1) * 56 + sl) * FLAG_STRIDE, lane < 32 ? (lane & 8) != 0 : lane < 40 && sl * 64 < tm * 112 + 112, ep);
        if (blockIdx.x == 0 && lane == 0) flag_store(p.flags, ep);
    }
    bar();
    const unsigned voffB = (unsigned)((wid * SEQ + tn * 256 + lane) * 16);
    struct Src { const void* base; int so; unsigned nrec; };
    auto pin = [](Src x) { asm volatile("" : "+s"(x.base), "+s"(x.so), "+s"(x.nrec)); return x; };
    auto srcW = [&](int t) { return pin(Src{t < NK ? (const void*)gw.ptr : (const void*)(p.csum + GRP * 3584), t < NK ? gw.kstep(t) * 64 * FF * 2 : 0, t < NK ? (unsigned)(DM * FF * 2) : t == NK ? (unsigned)(2 * FF * 2) : 0u}); };
    auto srcH = [&](int t) { Src x{nullptr, (t < NK ? gw.kstep(t) : NK) * 8 * SEQ * 16, t <= NK ? (unsigned)(136 * SEQ * 16) : 0u}; asm volatile("" : "+s"(x.so), "+s"(x.nrec)); return x; };
    auto stageW = [&](Src x, int slot, int i) { gw.piece(lds, wid, x.base, x.so, x.nrec, slot, i); };
    auto stageH = [&](Src x, int slot, int i) { DMA16_DEV(RSRC(p.h, x.nrec), lds + HB0 + slot * 32768 + wid * 4096 + i * 1024, voffB, x.so + i * 1024); };
#pragma unroll
    for (int t = 0; t < 2; ++t) {
        const Src xh = srcH(t);
#pragma unroll
        for (int i = 0; i < 4; ++i) stageH(xh, t, i);
    }

    unsigned adA[FA];
    const unsigned lds0 = (unsigned)(size_t)lds;
#pragma unroll
    for (int m = 0; m < FA; ++m) { const int q4 = l15 >> 2, pp = lane & 3, mf = m;
        adA[m] = lds0 + (unsigned)((pp >> 1) * 16384 + (8 * q + q4) * 256 + (((wm * FA + mf) ^ (q4 | ((q & 1) << 2) | ((pp >> 1) << 3))) << 4) + 8 * (pp & 1)); }
    unsigned adB = lds0 + (unsigned)(HB0 + q * 4096 + (wn * 64 + l15) * 16);
    auto rd = [&](auto ic, Frag (&ra)[FA], bf16x8 (&rb)[4], auto kkc) {
        constexpr int kk = decltype(kkc)::value, i = decltype(ic)::value;
        if constexpr (i < 4) rb[i] = lds_read128<kk * 16384 + i * 256>(adB);
        else if constexpr (i % 2 == 0) ra[(i - 4) / 2].lo = tr_read<kk * 8192>(adA[(i - 4) / 2]);
        else ra[(i - 4) / 2].hi = tr_read<kk * 8192 + 1024>(adA[(i - 4) / 2]);
    };
    f32x4 acc[FA][4];
#pragma unroll
    for (int m = 0; m < FA; ++m)
#pragma unroll
        for (int n = 0; n < 4; ++n) acc[m][n] = (f32x4){0.f, 0.f, 0.f, 0.f};
#define DSTR 2
#define PR1 16
#define PR2 4
#define PR3 20

    auto phase = [&](const Frag (&ra)[FA], const bf16x8 (&rb)[4], Frag (&na)[FA], bf16x8 (&nb)[4], int dw, int dh, auto kkc, auto grpc, auto&& dma) {
        static_for<0, 28>([&](auto sc) {
            constexpr int s = decltype(sc)::value, m = s >> 2, n = s & 3;
            if constexpr (decltype(kkc)::value == 0) { if constexpr (s == 0) __builtin_amdgcn_s_setprio(3); if constexpr (s == PR1) __builtin_amdgcn_s_setprio(2); }
            else { if constexpr (s == PR2) __builtin_amdgcn_s_setprio(1); if constexpr (s == PR3) __builtin_amdgcn_s_setprio(0); }
            acc[m][n] = MFMA(ra[m].v(), rb[n], acc[m][n]);
            if constexpr (s < 18) rd(sc, na, nb, kkc);
            if constexpr (s % DSTR == 0 && s < 8 * DSTR && decltype(grpc)::value == ((s / DSTR) & 1)) dma(s / (2 * DSTR));
            if constexpr (decltype(kkc)::value == 1 && s >= 18 && s < 26) { if constexpr (s < 25) bump(adA[s - 18], dw); else bump(adB, dh); }
            sched0();
        });
        wait_lgkm0(); sched0();
    };
    Frag a0[FA], a1[FA]; bf16x8 b0[4], b1[4];
    constexpr std::integral_constant<int, 0> K0{}; constexpr std::integral_constant<int, 1> K1{};
    wait_vm<4>(); bar();
    STAMP(1);
    static_for<0, 18>([&](auto ic) { rd(ic, a0, b0, K0); });
    wait_lgkm0(); sched0();
    const unsigned pfo = (unsigned)(((blockIdx.x * 8 + wid) * 28 + (lane < 28 ? lane : 0)) * 128); const int pft = 1 + idx % 12;
    auto kloop = [&](auto grpc) {
        int sw2 = 2;
#pragma nounroll
        for (int t = 0; t < NK; ++t) {
            const int sh = t & 1;
            const Src xw = srcW(t + 2), xh = srcH(t + 2);
            phase(a0, b0, a1, b1, sw2 == 1 ? -65536 : 32768, sh ? -32768 : 32768, K1, grpc, [&](int i) { stageW(xw, sw2, i);
                if (i == 3) touch(pf, pfo, p.wd, t == pft ? FF * DM * 2 : 0);
            });
            wait_vm<5>(); bar();
            phase(a1, b1, a0, b0, 0, 0, K0, grpc, [&](int i) { stageH(xh, sh, i); });
            sw2 = sw2 == 2 ? 0 : sw2 + 1;
        }
    };
    kloop(std::integral_constant<int, GRP>{});
    keep(a0, b0); sched0();
    STAMP(2);
    __builtin_amdgcn_s_setprio(0);
    f32x4 csv[FA];
    static_for<0, FA>([&](auto mc) { csv[decltype(mc)::value] = __builtin_bit_cast(f32x4, lds_read128<32768>(lds0 + (unsigned)((q >> 1) * 16384 + wm * 256 + (((decltype(mc)::value * 2 + (q & 1)) ^ (wm | ((q >> 1) << 3))) << 4)))); });
    float mean[4], c1[4], ve[4];
#pragma unroll
    for (int n = 0; n < 4; ++n) {
        const f32x4 s0 = *(const LAS f32x4*)(size_t)(adB + n * 256), s1 = *(const LAS f32x4*)(size_t)(adB + 16384 + n * 256);
        mean[n] = qsum((s0[0] + s0[2]) + (s1[0] + s1[2])) * (1.f / 16.f);
        const float d0 = s0[0] - mean[n], d1 = s0[2] - mean[n], d2 = s1[0] - mean[n], d3 = s1[2] - mean[n];
        const float m2 = qsum((s0[1] + s0[3]) + (s1[1] + s1[3]) + 64.f * ((d0 * d0 + d1 * d1) + (d2 * d2 + d3 * d3)));
        ve[n] = m2 * (1.f / DM) + LN_EPS; c1[n] = -1.44269504f * __builtin_amdgcn_rsqf(ve[n]);
    }
    wait_lgkm0(); wait_vm<0>(); asm volatile("; pf %0" ::"v"(pf)); bar();
    STAMP(4);
    if constexpr (GRP == 1) { g128_load<4, 0>(p, lds, wid, ep); g128_load<4, 1>(p, lds, wid, ep); g128_load<4, 2>(p, lds, wid, ep); }
    const auto ro = RSRC(p.act, SEQ * FF * 2);
#pragma unroll
    for (int m = 0; m < FA; ++m) {
        const f32x4 csum = csv[m];
        unsigned o[4];
#pragma unroll
        for (int n = 0; n < 4; ++n) {
            const f32x4 v = acc[m][n] - mean[n] * csum;
            float g0 = v[0], g1 = v[1], u0 = v[2], u1 = v[3];
            swap32(g0, u0); swap32(g1, u1);
            const f32x2 g = {g0, g1}, u = {u0, u1}, x = g * c1[n];
            const f32x2 d = (f32x2){__builtin_amdgcn_exp2f(x[0]), __builtin_amdgcn_exp2f(x[1])} * ve[n] + ve[n];
            const f32x2 r = g * u * (f32x2){__builtin_amdgcn_rcpf(d[0]), __builtin_amdgcn_rcpf(d[1])};
            o[n] = pack_bf16(r[0], r[1]);
        }
        swap16u(o[0], o[1]); swap16u(o[2], o[3]); swap32u(o[0], o[2]); swap32u(o[1], o[3]);
        __builtin_amdgcn_raw_buffer_store_b128((u32x4){o[0], o[2], o[1], o[3]}, ro, (unsigned)(((tm * 14 + wm * FA + m) * SEQ + tn * 256 + wn * 64 + lane) * 16), 0, SC1);
    }
    STAMP(5);
    wait_vm<0>(); bar();
    STAMP_END(3);
}
#endif

__global__ __launch_bounds__(512, 2) void fused_k(const Params p) {
#if defined(__HIP_DEVICE_COMPILE__)
    __shared__ __attribute__((aligned(16))) unsigned char lds_raw[5 * 32768];
    LAS unsigned char* lds = (LAS unsigned char*)lds_raw;
    const int wid = __builtin_amdgcn_readfirstlane(threadIdx.x >> 6);
    u64 ep = 0;
    if (wid < 4) { g128_compute<1>(p, lds, wid, ep); g128_compute<2>(p, lds, wid, ep); gemm3_body<0>(p, lds, wid, ep); g128_compute<4>(p, lds, wid, ep); }
    else { g128_load<1, -1>(p, lds, wid, ep); g128_load<2, -1>(p, lds, wid, ep); gemm3_body<1>(p, lds, wid, ep); g128_load<4, -1>(p, lds, wid, ep); }
#endif
}
__global__ void tail_k() {}

extern "C" void kernel_launch(void* const* d_in, const int* in_sizes, int n_in, void* d_out, int out_size, void* d_ws, size_t ws_size, hipStream_t stream) {
    Params p;
    p.x = (const u16*)d_in[0]; p.cre = (const u16*)d_in[1]; p.cim = (const u16*)d_in[2]; p.wre = (const u16*)d_in[3]; p.wim = (const u16*)d_in[4];
    p.wg = (const u16*)d_in[6]; p.wu = (const u16*)d_in[7]; p.wd = (const u16*)d_in[8];
    p.out = (u16*)d_out;
    char* ws = (char*)d_ws;
    p.abT = (u16*)ws; p.h = (u16*)(ws + (8u << 20)); p.act = (u16*)(ws + (16u << 20));
    p.xch = (u16*)(ws + (32u << 20)); p.flags = (unsigned long long*)(ws + (40u << 20)); p.csum = (float*)(ws + (40u << 20) + (512u << 10));
    fused_k<<<256, 512, 0, stream>>>(p);
}
```

```cpp
#include <hip/hip_runtime.h>
#include <hip/hip_bf16.h>
#include <type_traits>

#define LAS __attribute__((address_space(3)))
typedef unsigned short u16;
typedef __bf16 bf16x8 __attribute__((ext_vector_type(8)));
typedef __bf16 bf16x2 __attribute__((ext_vector_type(2)));
typedef float f32x4 __attribute__((ext_vector_type(4)));
typedef float f32x2 __attribute__((ext_vector_type(2)));
typedef unsigned u32x4 __attribute__((ext_vector_type(4)));
typedef unsigned u32x2 __attribute__((ext_vector_type(2)));

constexpr int SEQ = 2048, DM = 1024, FF = 3584;
#define ABP 1024
constexpr int ABT_BYTES = 512 * ABP * 16;
constexpr float LN_EPS = 1e-5f;
constexpr int SC1 = 16;

struct Params {
    const u16* x; const u16* cre; const u16* cim; const u16* wre; const u16* wim;
    const u16* wg; const u16* wu; const u16* wd;
    u16* out; u16* abT; u16* h; u16* act; u16* xch; unsigned long long* flags; float* csum;
};

template <int N> __device__ __forceinline__ void wait_vm() { asm volatile("s_waitcnt vmcnt(%0)" ::"n"(N) : "memory"); }
__device__ __forceinline__ void wait_lgkm0() { __builtin_amdgcn_s_waitcnt(0xC07F); asm volatile("s_waitcnt lgkmcnt(0)" ::: "memory"); }
__device__ __forceinline__ void sched0() { __builtin_amdgcn_sched_barrier(0); }
__device__ __forceinline__ void bar() { sched0(); asm volatile("" ::: "memory"); __builtin_amdgcn_s_barrier(); asm volatile("" ::: "memory"); sched0(); }
__device__ __forceinline__ unsigned pack_bf16(float lo, float hi) { bf16x2 c; c[0] = (__bf16)lo; c[1] = (__bf16)hi; return __builtin_bit_cast(unsigned, c); }
__device__ __forceinline__ float bf_lo(unsigned v) { return __uint_as_float(v << 16); }
__device__ __forceinline__ float bf_hi(unsigned v) { return __uint_as_float(v & 0xffff0000u); }
__device__ __forceinline__ void swap16(float& a, float& b) { auto r = __builtin_amdgcn_permlane16_swap(__float_as_uint(a), __float_as_uint(b), false, false); a = __uint_as_float(r[0]); b = __uint_as_float(r[1]); }
__device__ __forceinline__ void swap32(float& a, float& b) { auto r = __builtin_amdgcn_permlane32_swap(__float_as_uint(a), __float_as_uint(b), false, false); a = __uint_as_float(r[0]); b = __uint_as_float(r[1]); }
__device__ __forceinline__ void swap16u(unsigned& a, unsigned& b) { auto r = __builtin_amdgcn_permlane16_swap(a, b, false, false); a = r[0]; b = r[1]; }
__device__ __forceinline__ void swap32u(unsigned& a, unsigned& b) { auto r = __builtin_amdgcn_permlane32_swap(a, b, false, false); a = r[0]; b = r[1]; }
__device__ __forceinline__ float qsum(float v) { float a = v, b = v; swap16(a, b); a += b; b = a; swap32(a, b); return a + b; }

struct Frag { u32x2 lo, hi; __device__ __forceinline__ bf16x8 v() const { return __builtin_bit_cast(bf16x8, __builtin_shufflevector(lo, hi, 0, 1, 2, 3)); } };
template <int OFF> __device__ __forceinline__ u32x2 tr_read(unsigned addr) { u32x2 r; asm volatile("ds_read_b64_tr_b16 %0, %1 offset:%2" : "=v"(r) : "v"(addr), "n"(OFF) : "memory"); return r; }
template <int OFF> __device__ __forceinline__ bf16x8 lds_read128(unsigned addr) { bf16x8 r; asm volatile("ds_read_b128 %0, %1 offset:%2" : "=v"(r) : "v"(addr), "n"(OFF) : "memory"); return r; }
template <int N> __device__ __forceinline__ void wait_lgkm() { asm volatile("s_waitcnt lgkmcnt(%0)" ::"n"(N) : "memory"); }
template <int I, int N, class F> __device__ __forceinline__ void static_for(F&& f) { if constexpr (I < N) { f(std::integral_constant<int, I>{}); static_for<I + 1, N>(f); } }
template <int NA> __device__ __forceinline__ void keep(const Frag (&a)[NA], const bf16x8 (&b)[4]) {
#pragma unroll
    for (int i = 0; i < NA; ++i) asm volatile("" ::"v"(a[i].lo), "v"(a[i].hi));
#pragma unroll
    for (int i = 0; i < 4; ++i) asm volatile("" ::"v"(b[i]));
}
__device__ __forceinline__ void bump(unsigned& a, int d) { asm volatile("v_add_u32 %0, %1, %0" : "+v"(a) : "s"(d)); }
#define MFMA(a, b, c) __builtin_amdgcn_mfma_f32_16x16x32_bf16(a, b, c, 0, 0, 0)
#define RSRC(ptr, bytes) __builtin_amdgcn_make_buffer_rsrc((void*)(ptr), 0, (bytes), 0x00020000)
#define DMA16(rs, ldsp, voff, soff) __builtin_amdgcn_raw_ptr_buffer_load_lds(rs, (LAS void*)(ldsp), 16, voff, soff, 0, 0)
#define DMA16_DEV(rs, ldsp, voff, soff) __builtin_amdgcn_raw_ptr_buffer_load_lds(rs, (LAS void*)(ldsp), 16, voff, soff, 0, SC1)

#define STAMP(i)
#define STAMP_BEGIN
#define STAMP_END(ID)

typedef unsigned long long u64;
#define FSTR 8
constexpr int FLAG_STRIDE = FSTR, F_PAIR = 1, F_AB = F_PAIR + 1536, F_H = F_AB + 256, F_ACT = F_H + 256, F_CS = F_ACT + 256;
#define PSCOPE __HIP_MEMORY_SCOPE_AGENT
#define PSLEEP 2
__device__ __forceinline__ u64 flag_load(u64* w) { return __hip_atomic_load(w, __ATOMIC_RELAXED, PSCOPE); }
__device__ __forceinline__ void flag_store(u64* w, u64 v) { __hip_atomic_store(w, v, __ATOMIC_RELAXED, PSCOPE); }
__device__ __forceinline__ u64 uniform64(u64 v) { return ((u64)(unsigned)__builtin_amdgcn_readfirstlane((unsigned)(v >> 32)) << 32) | (unsigned)__builtin_amdgcn_readfirstlane((unsigned)v); }
__device__ __forceinline__ void wait_group(u64* word, bool need, u64 ep) {
#pragma nounroll
    for (int spin = 0; spin < (1 << 20); ++spin) {
        if (!__any(need && flag_load(word) != ep)) break;
        __builtin_amdgcn_s_sleep(PSLEEP);
    }
}
#if defined(__HIP_DEVICE_COMPILE__)
__device__ __forceinline__ void touch(unsigned& sink, unsigned voff, const void* base, unsigned bytes) {
    asm volatile("s_nop 4\n\tbuffer_load_dword %0, %1, %2, 0 offen" : "+v"(sink) : "v"(voff), "s"(RSRC(base, bytes)) : "memory");
}
struct G3W {
    const u16* ptr; unsigned voff[2]; int phi, koff;
    __device__ __forceinline__ G3W(const Params& p, int wid, int lane) {
        const int xcd = blockIdx.x & 7, idx = blockIdx.x >> 3, tm = (xcd & 3) * 8 + (idx & 7), q = lane >> 4;
        ptr = wid < 4 ? p.wg : p.wu; phi = ((idx & 7) + (idx >> 3)) & 3; koff = xcd * 2;
#pragma unroll
        for (int i = 0; i < 2; ++i) { const int j = (lane & 15) ^ (((q | (i << 2)) << 1) | (wid >> 2));
            voff[i] = j < 14 ? (unsigned)(((wid & 3) * 16 + q) * FF * 2 + tm * 224 + j * 16) : 0x7ffffff0u; }
    }
    __device__ __forceinline__ int kstep(int t) const { return (t + koff + ((t & 3) == phi ? 4 : 0)) & 15; }
    __device__ __forceinline__ void piece(LAS unsigned char* lds, int wid, const void* base, int so, unsigned nrec, int slot, int i) const { DMA16_DEV(RSRC(base, nrec), lds + slot * 32768 + wid * 4096 + i * 1024, voff[i >> 1], so + i * 4 * FF * 2); }
    __device__ __forceinline__ void early(LAS unsigned char* lds, int wid, int t) const {
#pragma unroll
        for (int i = 0; i < 4; ++i) piece(lds, wid, ptr, kstep(t) * 64 * FF * 2, DM * FF * 2, t, i);
    }
};
#define CSW 8
#define CSD 24
__device__ __forceinline__ void colsum_partial(const Params& p, LAS unsigned char* lds, int lw) {
    const int lane = threadIdx.x & 63, slab = blockIdx.x >> 1, mat = slab >= 56, c0 = (slab - mat * 56) * 64;
    const auto rw = RSRC(mat ? p.wu : p.wg, slab < 112 ? DM * FF * 2 : 0);
    const unsigned vo = (unsigned)(((lw * 256 + (lane >> 3)) * FF + c0 + (lane & 7) * 8) * 2);
    u32x4 v[CSW]; f32x4 s0 = {0.f, 0.f, 0.f, 0.f}, s1 = s0;
    if (CSD) __builtin_amdgcn_s_sleep(CSD);
#pragma unroll
    for (int i = 0; i < CSW; ++i) v[i] = __builtin_amdgcn_raw_buffer_load_b128(rw, vo, i * 8 * FF * 2, 0);
#pragma nounroll
    for (int j = CSW; j < 32 + CSW; j += CSW)
#pragma unroll
        for (int i = 0; i < CSW; ++i) {
            const u32x4 w = v[i];
            s0 += (f32x4){bf_lo(w[0]), bf_hi(w[0]), bf_lo(w[1]), bf_hi(w[1])}; s1 += (f32x4){bf_lo(w[2]), bf_hi(w[2]), bf_lo(w[3]), bf_hi(w[3])};
            v[i] = __builtin_amdgcn_raw_buffer_load_b128(rw, j < 32 ? vo : 0x7ffffff0u, (j + i) * 8 * FF * 2, 0);
            sched0();
        }
    LAS float* tab = (LAS float*)(lds + 131072) + (lw * 8 + (lane >> 3)) * 64 + (lane & 7) * 8;
    *(LAS f32x4*)tab = s0; *(LAS f32x4*)(tab + 4) = s1;
    wait_lgkm0();
}
__device__ __forceinline__ void colsum_final(const Params& p, LAS unsigned char* lds, u64 ep) {
    const int lane = threadIdx.x & 63, slab = blockIdx.x >> 1, mat = slab >= 56, c0 = (slab - mat * 56) * 64;
    if (slab >= 112) return;
    const LAS float* tab = (const LAS float*)(lds + 131072);
    float a = 0.f, b = 0.f;
#pragma unroll
    for (int i = 0; i < 32; i += 2) { a += tab[i * 64 + lane]; b += tab[(i + 1) * 64 + lane]; }
    const int f = c0 + lane, t = f / 112, r = f - t * 112, h = r >= 56;
    __hip_atomic_store(p.csum + (mat * 2 + h) * 1792 + t * 56 + r - h * 56, a + b, __ATOMIC_RELAXED, __HIP_MEMORY_SCOPE_AGENT);
    wait_vm<0>();
    if (lane == 0) flag_store(p.flags + (F_CS + slab) * FLAG_STRIDE, ep);
}
#endif

enum { ROW, CHK, TR };
#define NKS2 28
#define FLAGD 1
#define WLATE 1
#define NKS4 24
template <int ID> struct Cfg;
template <> struct Cfg<1> { static constexpr int NKT = 16, NKS = 16, FA = ROW, FB = ROW, LDB = DM; };
template <> struct Cfg<2> { static constexpr int NKT = 64, NKS = NKS2, FA = CHK, FB = ROW, LDB = SEQ; };
template <> struct Cfg<4> { static constexpr int NKT = 56, NKS = NKS4, FA = TR, FB = CHK, LDB = 0; };

#if defined(__HIP_DEVICE_COMPILE__)
template <int ID, int PRE>
__device__ __forceinline__ void g128_load(const Params& p, LAS unsigned char* lds, const int wid, u64& ep) {
    using C = Cfg<ID>;
    constexpr int STAGE = 32768, XLDS = 4 * STAGE;
    const int lane = threadIdx.x & 63, l15 = lane & 15, q = lane >> 4;
    const int xcd = blockIdx.x & 7, idx = blockIdx.x >> 3, im = idx & 3, in = idx >> 2;
    constexpr int NH = ID == 4 ? C::NKT - 2 * C::NKS : 0;
    const int ks = (xcd & 1) ^ (ID == 2); const bool recv = ID == 4 || (ID == 2 && ks);
    const int tm = (ID == 1 ? (xcd & 3) : ((xcd >> 1) & 1)) * 4 + im, tn = (xcd >> 2) * 8 + in;
    const int k0 = ID != 1 && ks ? C::NKS : 0, nk = ID == 2 && ks ? C::NKT - C::NKS : C::NKS;
    const int xb = ID == 4 ? 128 + (blockIdx.x ^ (wid >> 2)) : blockIdx.x >> 1;
    u64* const flag = p.flags + (F_PAIR + xb * 4 + (wid & 3)) * FLAG_STRIDE;
    const unsigned xoff = (unsigned)(ID == 4 ? (128 + xb) * 16384 + (wid & 3) * 4096 : xb * 32768 + (wid & 3) * 8192);
    const int lw = wid - 4, phi = (im + in) & 3;
    const int koff = ID == 1 ? xcd * 2 : ((xcd >> (ID == 2 ? 1 : 2)) & 1) * (nk >> 1);
    const unsigned ksw = (unsigned)(((lane & 7) ^ (lane >> 3)) << 4);
    unsigned voffA[2], voffB;
    if constexpr (C::FA == ROW) voffA[0] = (unsigned)((tm * 128 + lw * 32 + (lane >> 3)) * DM * 2) + ksw;
    if constexpr (C::FA == CHK) voffA[0] = (unsigned)((lw * 2 * ABP + tm * 128 + lane) * 16);
    if constexpr (C::FA == TR) {
#pragma unroll
        for (int i = 0; i < 2; ++i) voffA[i] = (unsigned)((lw * 16 + q) * DM * 2 + tm * 256 + ((l15 ^ ((q | (i << 2)) << 1)) << 4));
    }
    if constexpr (C::FB == ROW) voffB = (unsigned)((((ID == 1 ? tn & 7 : tn)) * 128 + lw * 32 + (lane >> 3)) * C::LDB * 2) + ksw;
    else voffB = (unsigned)((lw * 2 * SEQ + tn * 128 + lane) * 16);
    const auto ra = ID == 1 ? RSRC(p.x, SEQ * DM * 2) : ID == 2 ? RSRC(p.abT, ABT_BYTES) : RSRC(p.wd, FF * DM * 2);
    auto stage = [&](int t, bool doA, bool doB, bool half = false) {
        int k = (t & 3) != phi ? t : t + 4 < nk ? t + 4 : phi;
        k += koff; k = k0 + (k >= nk ? k - nk : k);
        if (half) { k = t - nk + phi; k = 2 * C::NKS + (k >= NH ? k - NH : k); }
        const auto rb = ID == 1 ? RSRC(tn < 8 ? p.wre : p.wim, DM * DM * 2) : ID == 2 ? RSRC(k < 32 ? p.cre : p.cim, SEQ * SEQ * 2) : RSRC(p.act, SEQ * FF * 2);
        LAS unsigned char* base = lds + (t & 3) * STAGE + lw * 4096;
#pragma unroll
        for (int i = 0; i < 4; ++i) {
            if (doA) {
                if constexpr (C::FA == ROW) DMA16(ra, base + i * 1024, voffA[0], k * 128 + i * 8 * DM * 2);
                if constexpr (C::FA == CHK) DMA16_DEV(ra, base + i * 1024, voffA[0], (k * 8 + (i >> 1)) * ABP * 16 + (i & 1) * 1024);
                if constexpr (C::FA == TR) DMA16(ra, base + i * 1024, voffA[i >> 1], (k * 64 + i * 4) * DM * 2);
            }
            if (doB) {
                if constexpr (C::FB == ROW) DMA16(rb, base + 16384 + i * 1024, voffB, (k & 31) * 128 + i * 8 * C::LDB * 2);
                else if (!(half && (i & 1))) DMA16_DEV(rb, base + 16384 + i * 1024, voffB, (k * 8 + (i >> 1)) * SEQ * 16 + ((i & 1) ^ ks) * 1024);
            }
        }
    };
    if constexpr (PRE >= 0) { stage(PRE, ID == 4, ID == 2); return; }
    if constexpr (ID != 1) {
        u64* words = p.flags + (ID == 2 ? F_AB + tm * 32 : F_ACT + (tn >> 1) * 32) * FLAG_STRIDE;
        if (wid == 4 && lane == 0) flag_store(ID == 2 ? p.flags + (F_AB + (tn & 7) * 32 + (tn >> 3) * 16 + (xcd & 3) * 4 + im) * FLAG_STRIDE : words + ((xcd & 3) * 8 + (idx & 7)) * FLAG_STRIDE, ep);
        if constexpr (ID == 4) { if (wid != 4) __builtin_amdgcn_s_sleep(2); stage(1, true, false); stage(2, true, false); }
        if (wid == 4) { const int j = lane & 31; wait_group(words + j * FLAG_STRIDE, ID == 2 ? 2 * j + 2 > k0 && 2 * j < k0 + nk : j * 14 + 14 > k0 * 8 && (j * 14 < (k0 + nk) * 8 || j * 14 + 14 > 2 * C::NKS * 8), ep); }
        bar();
        stage(0, ID == 2, ID == 4); stage(1, ID == 2, ID == 4); stage(2, ID == 2, ID == 4);
        wait_vm<8>(); bar();
        wait_vm<4>();
    } else {
        stage(0, true, true); stage(1, true, true);
        wait_vm<8>(); bar();
        stage(2, true, true);
    }
#pragma nounroll
    for (int t = 0; t < nk - 3; ++t) {
        wait_vm<8>(); bar();
        stage(t + 3, true, true);
    }
    if constexpr (NH > 0) {
        wait_vm<8>(); bar(); stage(nk, true, true, true);
#pragma nounroll
        for (int t = nk - 2; t < nk + NH - 3; ++t) { wait_vm<6>(); bar(); stage(t + 3, true, true, true); }
    }
    bool got = !recv;
    auto fetch = [&](u64 seen) {
        if (uniform64(seen) != ep) return false;
        asm volatile("" ::: "memory");
#pragma unroll
        for (int i = 0; i < (ID == 4 ? 4 : 8); ++i) DMA16_DEV(RSRC(p.xch, 256 * 32768), lds + XLDS + lw * 8192 + i * 1024, (unsigned)(lane * 16), xoff + i * 1024);
        return true;
    };
    wait_vm<NH ? 6 : 8>(); bar();
    u64 seen0 = 0;
    if constexpr (ID != 1) seen0 = flag_load(flag);
    wait_vm<0>();
    if (!got) got = fetch(seen0);
    if constexpr (ID == 1) g128_load<2, 0>(p, lds, wid, ep);
    bar();
    if constexpr (ID != 1) seen0 = flag_load(flag);
    if constexpr (ID == 1) g128_load<2, 1>(p, lds, wid, ep);
    bar();
    if (!got) got = fetch(seen0);
    if constexpr (ID == 1) g128_load<2, 2>(p, lds, wid, ep);
    if (recv) {
#pragma nounroll
        for (int spin = 0; !got && spin < (1 << 20); ++spin) { got = fetch(flag_load(flag)); __builtin_amdgcn_s_sleep(1); }
        wait_vm<0>(); bar();
        if constexpr (ID == 2 && !WLATE) {
#pragma unroll
            for (int t = 0; t < 2; ++t) { G3W(p, lw, lane).early(lds, lw, t); G3W(p, wid, lane).early(lds, wid, t); }
        }
    }
    if constexpr (ID == 2) { if (!recv) colsum_partial(p, lds, lw); }
    if constexpr (ID == 1) { bar(); ep = uniform64(*(const LAS u64*)(lds + XLDS + 1536)); }
    if constexpr (ID != 4) bar();
}

template <int ID>
__device__ __forceinline__ void g128_compute(const Params& p, LAS unsigned char* lds, const int wid, u64& ep) {
    using C = Cfg<ID>;
    constexpr int STAGE = 32768, XLDS = 4 * STAGE;
    const int lane = threadIdx.x & 63, l15 = lane & 15, q = lane >> 4;
    const int xcd = blockIdx.x & 7, idx = blockIdx.x >> 3, im = idx & 3, in = idx >> 2;
    constexpr int NH = ID == 4 ? C::NKT - 2 * C::NKS : 0;
    const int ks = (xcd & 1) ^ (ID == 2); const bool recv = ID == 4 || (ID == 2 && ks);
    const int tm = (ID == 1 ? (xcd & 3) : ((xcd >> 1) & 1)) * 4 + im, tn = (xcd >> 2) * 8 + in;
    const int k0 = ID != 1 && ks ? C::NKS : 0, nk = ID == 2 && ks ? C::NKT - C::NKS : C::NKS;
    const int xb = ID == 4 ? 128 + (blockIdx.x ^ (wid >> 2)) : blockIdx.x >> 1;
    u64* const flag = p.flags + (F_PAIR + xb * 4 + (wid & 3)) * FLAG_STRIDE;
    const unsigned xoff = (unsigned)(ID == 4 ? (128 + xb) * 16384 + (wid & 3) * 4096 : xb * 32768 + (wid & 3) * 8192);
    STAMP_BEGIN;
    const int wm = wid & 1, wn = wid >> 1;
    const int eo = (q & 1) * 16 + (q >> 1) * 8;
    f32x4 acc[4][4], st[6];
#pragma unroll
    for (int m = 0; m < 4; ++m)
#pragma unroll
        for (int n = 0; n < 4; ++n) acc[m][n] = (f32x4){0.f, 0.f, 0.f, 0.f};
#pragma unroll
    for (int i = 0; i < 6; ++i) st[i] = (f32x4){0.f, 0.f, 0.f, 0.f};
    const int row0 = tn * 128 + (ID == 4 ? ks * 64 + wn * 32 : wn * 64) + l15, col0 = tm * 128 + wm * 64 + eo;
    if constexpr (ID != 1) {
        constexpr int NN = ID == 4 ? 2 : 4;
        if (ID == 4 || !recv) {
            if constexpr (ID == 2) { wait_vm<0>(); bar(); __builtin_amdgcn_s_sleep(4); }
            u32x4 r[NN][2];
#pragma unroll
            for (int n = 0; n < NN; ++n)
#pragma unroll
                for (int mp = 0; mp < 2; ++mp)
                    r[n][mp] = __builtin_amdgcn_raw_buffer_load_b128(RSRC(ID == 2 ? p.x : p.h, SEQ * DM * 2), (unsigned)(ID == 2 ? ((row0 + n * 16) * DM + col0 + mp * 32) * 2 : ((col0 / 8 + mp * 4) * SEQ + row0 + n * 16) * 16), 0, ID == 4 ? SC1 : 0);
            if constexpr (ID == 4) { wait_vm<4>(); bar(); }
#pragma unroll
            for (int n = 0; n < NN; ++n)
#pragma unroll
                for (int mp = 0; mp < 2; ++mp) {
                    unsigned r0 = r[n][mp][0], r1 = r[n][mp][1], r2 = r[n][mp][2], r3 = r[n][mp][3];
                    swap16u(r0, r2); swap16u(r1, r3);
                    acc[2 * mp][n] = (f32x4){bf_lo(r0), bf_hi(r0), bf_lo(r1), bf_hi(r1)}; acc[2 * mp + 1][n] = (f32x4){bf_lo(r2), bf_hi(r2), bf_lo(r3), bf_hi(r3)};
                }
        } else { wait_vm<0>(); bar(); }
    }
    unsigned pfc = 0;
    if constexpr (ID == 1) {
        ep = uniform64(flag_load(p.flags)) + 1;
        if (threadIdx.x == 0) *(LAS u64*)(lds + XLDS + 1536) = ep;
        if (idx == 0) {
            unsigned long long pc; int lo, hi;
            asm volatile("s_getpc_b64 %0\n s_mov_b32 %1, _Z7fused_k6Params@rel32@lo+4\n s_mov_b32 %2, _Z6tail_kv@rel32@lo+12" : "=s"(pc), "=s"(lo), "=s"(hi));
            touch(pfc, threadIdx.x * 128, (const void*)(pc + (long long)lo), (unsigned)max(hi - lo, 0)); touch(pfc, threadIdx.x * 128 + 32768, (const void*)(pc + (long long)lo), (unsigned)max(hi - lo, 0));
        }
    }
    const int rotA = ID == 1 ? 2 * wn : 0, rotB = ID == 1 ? 2 * wm : 0;
    constexpr int NA = C::FA == CHK ? 1 : 4, NB = C::FB == CHK ? 1 : ID == 1 ? 4 : 2;
    unsigned adA[NA], adB[NB];
    const unsigned lds0 = (unsigned)(size_t)lds, row_sw = lds0 + (unsigned)(l15 * 128 + ((q ^ (lane & 7)) << 4)), chk = lds0 + (unsigned)(q * 2048 + l15 * 16);
#pragma unroll
    for (int i = 0; i < NA; ++i) {
        if constexpr (C::FA == ROW) adA[i] = (row_sw ^ (unsigned)((i >> 1) * 64)) + (unsigned)((wm * 4 + ((2 * (i & 1) + rotA) & 3)) * 2048);
        if constexpr (C::FA == CHK) adA[i] = chk + (unsigned)(wm * 1024);
        if constexpr (C::FA == TR) { const int q4 = l15 >> 2; adA[i] = lds0 + (unsigned)((8 * q + q4) * 256 + (((wm * 4 + i) ^ (q4 | ((q & 1) << 2))) << 5) + 8 * (lane & 3)); }
    }
#pragma unroll
    for (int i = 0; i < NB; ++i) {
        if constexpr (C::FB == CHK) adB[i] = 16384u + chk + (unsigned)(wn * 512);
        else if constexpr (ID == 1) adB[i] = 16384u + (row_sw ^ (unsigned)((i >> 1) * 64)) + (unsigned)((wn * 4 + ((2 * (i & 1) + rotB) & 3)) * 2048);
        else adB[i] = 16384u + (row_sw ^ (unsigned)(i * 64)) + (unsigned)(wn * 8192);
    }
    constexpr int W = C::FA == TR ? 2 : 1, NR = 4 + 4 * W;
    auto rd = [&](auto ic, Frag (&ra)[4], bf16x8 (&rb)[4], auto kkc) {
        constexpr int kk = decltype(kkc)::value, i = decltype(ic)::value;
        if constexpr (i == 0 || (i > W && i <= W + 3)) { constexpr int n = i == 0 ? 0 : i - W;
            if constexpr (C::FB == CHK) rb[n] = lds_read128<kk * 8192 + (n >> 1) * 1024 + (n & 1) * 256>(adB[0]);
            else if constexpr (ID == 1) rb[n] = lds_read128<(n & 1) * 2048>(adB[kk * 2 + (n >> 1)]);
            else rb[n] = lds_read128<n * 2048>(adB[kk]); }
        else { constexpr int j = i <= W ? i - 1 : i - 4, m = j / W;
            if constexpr (C::FA != TR) { bf16x8 v;
                if constexpr (C::FA == CHK) v = lds_read128<kk * 8192 + m * 256>(adA[0]); else v = lds_read128<(m & 1) * 2048>(adA[kk * 2 + (m >> 1)]);
                const u32x4 w = __builtin_bit_cast(u32x4, v); ra[m].lo = (u32x2){w[0], w[1]}; ra[m].hi = (u32x2){w[2], w[3]}; }
            else if constexpr (j % 2 == 0) ra[m].lo = tr_read<kk * 8192>(adA[m]);
            else ra[m].hi = tr_read<kk * 8192 + 1024>(adA[m]); }
    };
    const bf16x8 ones = __builtin_bit_cast(bf16x8, (u32x4){0x3f803f80u, 0x3f803f80u, 0x3f803f80u, 0x3f803f80u});
    auto phase = [&](const Frag (&ra)[4], const bf16x8 (&rb)[4], Frag (&na)[4], bf16x8 (&nb)[4], int dstep, auto kkc) {
        static_for<0, 16>([&](auto sc) {
            constexpr int s = decltype(sc)::value, m = s >> 2, n = s & 3;
            if constexpr (n == 0) { wait_lgkm<(3 - m) * W + (s < NR ? s : NR)>(); sched0(); }
            acc[m][n] = MFMA(ra[m].v(), rb[n], acc[m][n]);
            if constexpr (s < NR) rd(sc, na, nb, kkc);
            if constexpr (s >= 12 && decltype(kkc)::value == 1) {
                if constexpr (s - 12 < NA) bump(adA[s - 12], dstep);
                if constexpr (s - 12 < NB) bump(adB[s - 12], dstep);
            }
            if constexpr (ID == 1 && s >= 10) {
                constexpr int i = s & 1;
                if constexpr (s < 12) st[i] = MFMA(ones, ra[i].v(), st[i]); else if constexpr (s < 14) st[2 + i] = MFMA(ra[i].v(), ra[i].v(), st[2 + i]); else st[4 + i] = MFMA(ones, rb[i], st[4 + i]); }
            sched0();
        });
    };
    Frag a0[4], a1[4]; bf16x8 b0[4], b1[4];
    constexpr std::integral_constant<int, 0> K0{}; constexpr std::integral_constant<int, 1> K1{};
    if constexpr (ID != 1) bar();
    bar();
    STAMP(1);
    static_for<0, NR>([&](auto ic) { rd(ic, a0, b0, K0); });
    sched0();
#pragma nounroll
    for (int t = 0; t < nk; ++t) {
        phase(a0, b0, a1, b1, (t & 3) == 3 ? -3 * STAGE : STAGE, K1);
        bar();
        phase(a1, b1, a0, b0, 0, K0);
    }
    wait_lgkm0(); keep(a0, b0); sched0();
    auto send = [&](int h0) {
        const auto rx = RSRC(p.xch, 256 * 32768);
#pragma unroll
        for (int m = 0; m < 4; ++m)
#pragma unroll
            for (int h = h0; h < 2; ++h) {
                const f32x4 u = acc[m][2 * h], v = acc[m][2 * h + 1];
                __builtin_amdgcn_raw_buffer_store_b128((u32x4){pack_bf16(u[0], u[1]), pack_bf16(u[2], u[3]), pack_bf16(v[0], v[1]), pack_bf16(v[2], v[3])}, rx, xoff + (unsigned)((h0 ? m : m * 2 + h) * 1024 + lane * 16), 0, SC1);
            }
    };
    if constexpr (NH > 0) {
        send(1); sched0();
        auto phaseH = [&](const Frag (&ra)[4], const bf16x8 (&rb)[4], Frag (&na)[4], bf16x8 (&nb)[4], int dstep, auto kkc) {
            static_for<0, 8>([&](auto sc) {
                constexpr int s = decltype(sc)::value;
                if constexpr ((s & 1) == 0) { wait_lgkm<6 - s + (s < 5 ? 2 * s : 10)>(); sched0(); }
                acc[s >> 1][s & 1] = MFMA(ra[s >> 1].v(), rb[s & 1], acc[s >> 1][s & 1]);
                if constexpr (s < 5) { rd(std::integral_constant<int, s < 2 ? 2 * s : 2 * s + 2>{}, na, nb, kkc); rd(std::integral_constant<int, s < 2 ? 2 * s + 1 : 2 * s + 3>{}, na, nb, kkc); }
                if constexpr (s >= 5 && decltype(kkc)::value == 1) { if constexpr (s < 7) { bump(adA[2 * (s - 5)], dstep); bump(adA[2 * (s - 5) + 1], dstep); } else bump(adB[0], dstep); }
                sched0();
            });
        };
#pragma nounroll
        for (int t = nk; t < nk + NH; ++t) {
            phaseH(a0, b0, a1, b1, (t & 3) == 3 ? -3 * STAGE : STAGE, K1);
            bar();
            phaseH(a1, b1, a0, b0, 0, K0);
            if (t == nk + FLAGD) { wait_vm<0>(); if (lane == 0) flag_store(flag, ep); }
        }
        wait_lgkm0();
        asm volatile("" ::"v"(b0[0]), "v"(b0[1]));
#pragma unroll
        for (int i = 0; i < 4; ++i) asm volatile("" ::"v"(a0[i].lo), "v"(a0[i].hi));
        sched0();
    }
    STAMP(2);
    if constexpr (ID == 1) {
        wait_vm<0>(); asm volatile("; pfc %0" ::"v"(pfc));
        LAS float* tab = (LAS float*)(lds + XLDS);
        const float sgn = tn < 8 ? 1.f : -1.f;
        if (q == (l15 >> 2)) {
#pragma unroll
            for (int i = 0; i < 2; ++i) {
                const int j = l15 & 3; const float sq = j == 0 ? st[2 + i][0] : j == 1 ? st[2 + i][1] : j == 2 ? st[2 + i][2] : st[2 + i][3];
                const float mean = st[i][0] * (1.f / DM);
                const int ra = wm * 64 + (rotA + i) * 16 + l15;
                tab[ra] = mean; tab[128 + ra] = sgn * __builtin_amdgcn_rsqf(fmaxf(sq * (1.f / DM) - mean * mean, 0.f) + LN_EPS);
                tab[256 + wn * 64 + (rotB + i) * 16 + l15] = st[4 + i][0];
            }
        }
        wait_lgkm0(); bar();
        const auto ro = RSRC(p.abT, ABT_BYTES);
        float ws[4];
#pragma unroll
        for (int n = 0; n < 4; ++n) ws[n] = tab[256 + wn * 64 + ((n + rotB) & 3) * 16 + l15];
#pragma unroll
        for (int m = 0; m < 4; ++m) {
            const int r0 = wm * 64 + ((m + rotA) & 3) * 16 + 4 * q;
            const f32x4 mean = *(const LAS f32x4*)(tab + r0), rs = *(const LAS f32x4*)(tab + 128 + r0);
#pragma unroll
            for (int n = 0; n < 4; ++n) acc[m][n] = (acc[m][n] - mean * ws[n]) * rs;
        }
#pragma unroll
        for (int n = 0; n < 4; ++n) { const int e = (tn & 7) * 128 + wn * 64 + ((n + rotB) & 3) * 16 + l15;
#pragma unroll
            for (int mp = 0; mp < 2; ++mp) {
                unsigned x0 = pack_bf16(acc[2 * mp][n][0], acc[2 * mp][n][1]), x1 = pack_bf16(acc[2 * mp][n][2], acc[2 * mp][n][3]);
                unsigned y0 = pack_bf16(acc[2 * mp + 1][n][0], acc[2 * mp + 1][n][1]), y1 = pack_bf16(acc[2 * mp + 1][n][2], acc[2 * mp + 1][n][3]);
                swap16u(x0, y0); swap16u(x1, y1);
                const int s = (tn >> 3) * SEQ + tm * 128 + wm * 64 + ((2 * mp + rotA) & 3) * 16 + eo;
                __builtin_amdgcn_raw_buffer_store_b128((u32x4){x0, x1, y0, y1}, ro, (unsigned)(((s >> 3) * ABP + e) * 16), 0, SC1);
            } }
    } else if (!recv) {
        send(0);
        wait_vm<0>();
        if (lane == 0) flag_store(flag, ep);
        if constexpr (ID == 2) bar();
    } else {
        bar();
        STAMP(4);
        const unsigned px = (unsigned)(size_t)lds + (unsigned)(XLDS + wid * 8192 + lane * 16);
        constexpr int HH = ID == 4 ? 1 : 2;
        u32x4 pw[4 * HH];
        static_for<0, 4 * HH>([&](auto ic) { pw[decltype(ic)::value] = __builtin_bit_cast(u32x4, lds_read128<decltype(ic)::value * 1024>(px)); });
        wait_lgkm0(); sched0();
#pragma unroll
        for (int m = 0; m < 4; ++m)
#pragma unroll
            for (int h = 0; h < HH; ++h) { const u32x4 w = pw[m * HH + h];
                acc[m][2 * h] += (f32x4){bf_lo(w[0]), bf_hi(w[0]), bf_lo(w[1]), bf_hi(w[1])}; acc[m][2 * h + 1] += (f32x4){bf_lo(w[2]), bf_hi(w[2]), bf_lo(w[3]), bf_hi(w[3])}; }
        const auto ro = RSRC(ID == 2 ? p.h : p.out, ID == 2 ? 136 * SEQ * 16 : SEQ * DM * 2);
#pragma unroll
        for (int n = 0; n < 2 * HH; ++n) {
            unsigned o[4][2];
#pragma unroll
            for (int m = 0; m < 4; ++m) { o[m][0] = pack_bf16(acc[m][n][0], acc[m][n][1]); o[m][1] = pack_bf16(acc[m][n][2], acc[m][n][3]); }
            if constexpr (ID == 2) {
                const f32x4 s4 = (acc[0][n] + acc[1][n]) + (acc[2][n] + acc[3][n]);
                const float mw = qsum((s4[0] + s4[1]) + (s4[2] + s4[3])) * (1.f / 64.f);
                const f32x4 d0 = acc[0][n] - mw, d1 = acc[1][n] - mw, d2 = acc[2][n] - mw, d3 = acc[3][n] - mw, q4 = (d0 * d0 + d1 * d1) + (d2 * d2 + d3 * d3);
                float m2 = (q4[0] + q4[1]) + (q4[2] + q4[3]);
                m2 = qsum(m2);
                if (q == 0) __builtin_amdgcn_raw_buffer_store_b64((u32x2){__float_as_uint(mw), __float_as_uint(m2)}, ro, (unsigned)(((128 + tm) * SEQ + row0 + n * 16) * 16 + wm * 8), 0, SC1);
            }
#pragma unroll
            for (int mp = 0; mp < 2; ++mp) {
                swap16u(o[2 * mp][0], o[2 * mp + 1][0]); swap16u(o[2 * mp][1], o[2 * mp + 1][1]);
                __builtin_amdgcn_raw_buffer_store_b128((u32x4){o[2 * mp][0], o[2 * mp][1], o[2 * mp + 1][0], o[2 * mp + 1][1]}, ro,
                    (unsigned)(ID == 4 ? ((row0 + n * 16) * DM + col0 + mp * 32) * 2 : ((col0 / 8 + mp * 4) * SEQ + row0 + n * 16) * 16), 0, SC1);
            }
        }
        STAMP(5);
        if constexpr (ID == 2) { wait_vm<0>(); bar(); }
    }
    STAMP_END(ID);
}
#endif

#if defined(__HIP_DEVICE_COMPILE__)
template <int GRP>
__device__ __forceinline__ void gemm3_body(const Params& p, LAS unsigned char* lds, const int wid, u64& ep) {
    constexpr int NK = 16, FA = 7, HB0 = 3 * 32768;
    STAMP_BEGIN;
    const int tid = threadIdx.x, lane = tid & 63;
    const int wm = wid & 1, wn = wid >> 1, l15 = lane & 15, q = lane >> 4;
    const int xcd = blockIdx.x & 7, idx = blockIdx.x >> 3, im = idx & 7, in = idx >> 3;
    const int tm = (xcd & 3) * 8 + im, tn = (xcd >> 2) * 4 + in;
    const G3W gw(p, wid, lane);
    unsigned pf = 0;
    if (xcd & 1) {
        if (wid == 1) colsum_final(p, lds, ep);
        gw.early(lds, wid, 0); gw.early(lds, wid, 1);
    }
    u64* words = p.flags + (F_H + tn * 32) * FLAG_STRIDE;
    if (threadIdx.x == 0 && !(xcd & 1)) flag_store(words + tm * FLAG_STRIDE, ep);
    if (WLATE && !(xcd & 1)) { if (wid) __builtin_amdgcn_s_sleep(2); gw.early(lds, wid, 0); gw.early(lds, wid, 1); }
    if (wid == 0) {
        const int sl = (tm * 112 >> 6) + (lane & 3);
        wait_group(lane < 32 ? words + lane * FLAG_STRIDE : p.flags + (F_CS + ((lane >> 2) & 1) * 56 + sl) * FLAG_STRIDE, lane < 32 ? (lane & 8) == 0 : lane < 40 && sl * 64 < tm * 112 + 112, ep);
        if (blockIdx.x == 0 && lane == 0) flag_store(p.flags, ep);
    }
    bar();
    const unsigned voffB = (unsigned)((wid * SEQ + tn * 256 + lane) * 16);
    struct Src { const void* base; int so; unsigned nrec; };
    auto pin = [](Src x) { asm volatile("" : "+s"(x.base), "+s"(x.so), "+s"(x.nrec)); return x; };
    auto srcW = [&](int t) { return pin(Src{t < NK ? (const void*)gw.ptr : (const void*)(p.csum + GRP * 3584), t < NK ? gw.kstep(t) * 64 * FF * 2 : 0, t < NK ? (unsigned)(DM * FF * 2) : t == NK ? (unsigned)(2 * FF * 2) : 0u}); };
    auto srcH = [&](int t) { Src x{nullptr, (t < NK ? gw.kstep(t) : NK) * 8 * SEQ * 16, 136 * SEQ * 16}; asm volatile("" : "+s"(x.so)); return x; };
    auto stageW = [&](Src x, int slot, int i) { gw.piece(lds, wid, x.base, x.so, x.nrec, slot, i); };
    auto stageH = [&](Src x, int slot, int i) { DMA16_DEV(RSRC(p.h, x.nrec), lds + HB0 + slot * 32768 + wid * 4096 + i * 1024, voffB, x.so + i * 1024); };
#pragma unroll
    for (int t = 0; t < 2; ++t) {
        const Src xh = srcH(t);
#pragma unroll
        for (int i = 0; i < 4; ++i) stageH(xh, t, i);
    }

    unsigned adA[FA];
    const unsigned lds0 = (unsigned)(size_t)lds;
#pragma unroll
    for (int m = 0; m < FA; ++m) { const int q4 = l15 >> 2, pp = lane & 3, reg = m < 6 ? m & 1 : pp >> 1, ch = m < 6 ? wm * 8 + (m & 6) + (pp >> 1) : 6 + wm;
        adA[m] = lds0 + (unsigned)(reg * 16384 + (8 * q + q4) * 256 + ((ch ^ (((q4 | ((q & 1) << 2)) << 1) | reg)) << 4) + 8 * (pp & 1)); }
    unsigned adB = lds0 + (unsigned)(HB0 + q * 4096 + (wn * 64 + l15) * 16);
    auto rd = [&](auto ic, Frag (&ra)[FA], bf16x8 (&rb)[4], auto kkc) {
        constexpr int kk = decltype(kkc)::value, i = decltype(ic)::value;
        if constexpr (i < 4) rb[i] = lds_read128<kk * 16384 + i * 256>(adB);
        else if constexpr (i % 2 == 0) ra[(i - 4) / 2].lo = tr_read<kk * 8192>(adA[(i - 4) / 2]);
        else ra[(i - 4) / 2].hi = tr_read<kk * 8192 + 1024>(adA[(i - 4) / 2]);
    };
    f32x4 acc[FA][4];
#pragma unroll
    for (int m = 0; m < FA; ++m)
#pragma unroll
        for (int n = 0; n < 4; ++n) acc[m][n] = (f32x4){0.f, 0.f, 0.f, 0.f};
#define DSTR 2
#define LASTP 3
#define PR1 18
#define PR2 6
#define PR3 22

    auto phase = [&](const Frag (&ra)[FA], const bf16x8 (&rb)[4], Frag (&na)[FA], bf16x8 (&nb)[4], int dw, int dh, auto kkc, auto grpc, auto&& dma, auto&& prep) {
        static_for<0, 28>([&](auto sc) {
            constexpr int s = decltype(sc)::value, m = s >> 2, n = s & 3;
            if constexpr (decltype(kkc)::value == 0) { if constexpr (s == 0) __builtin_amdgcn_s_setprio(3); if constexpr (s == PR1) __builtin_amdgcn_s_setprio(2); }
            else { if constexpr (s == PR2) __builtin_amdgcn_s_setprio(1); if constexpr (s == PR3) __builtin_amdgcn_s_setprio(0); }
            acc[m][n] = MFMA(ra[m].v(), rb[n], acc[m][n]);
            sched0();
            if constexpr (s < 18) rd(sc, na, nb, kkc);
            if constexpr (s == 20) prep();
            if constexpr (s % DSTR == 0 && s < 8 * DSTR && decltype(grpc)::value == ((s / DSTR) & 1)) dma(s / (2 * DSTR));
            if constexpr (decltype(kkc)::value == 1 && s >= 18 && s < 26) { if constexpr (s < 25) bump(adA[s - 18], dw); else bump(adB, dh); }
            sched0();
        });
        wait_lgkm0(); sched0();
    };
    Frag a0[FA], a1[FA]; bf16x8 b0[4], b1[4];
    constexpr std::integral_constant<int, 0> K0{}; constexpr std::integral_constant<int, 1> K1{};
    wait_vm<4>(); bar();
    STAMP(1);
    static_for<0, 18>([&](auto ic) { rd(ic, a0, b0, K0); });
    wait_lgkm0(); sched0();
    const unsigned pfo = (unsigned)(((blockIdx.x * 8 + wid) * 28 + (lane < 28 ? lane : 0)) * 128); const int pft = 1 + idx % 12;
    constexpr std::integral_constant<int, GRP> grpc{};
    Src xw = srcW(2), xh;
    auto phaseA = [&](int t, int sw2) {
        phase(a0, b0, a1, b1, sw2 == 1 ? -65536 : 32768, (t & 1) ? -32768 : 32768, K1, grpc, [&](int i) { stageW(xw, sw2, i);
            if (i == 3) touch(pf, pfo, p.wd, t == pft ? FF * DM * 2 : 0);
        }, [&]() { xh = srcH(t + 2); });
        wait_vm<5>();
    };
    phaseA(0, 2);
    int sw2 = 0;
#pragma nounroll
    for (int t = 0; t < NK - 1; ++t) {
        bar();
        phase(a1, b1, a0, b0, 0, 0, K0, grpc, [&](int i) { stageH(xh, t & 1, i); }, [&]() { xw = srcW(t + 3); });
        phaseA(t + 1, sw2);
        sw2 = sw2 == 2 ? 0 : sw2 + 1;
    }
    bar();
    __builtin_amdgcn_s_setprio(GRP ? 0 : LASTP);
    f32x4 csv[FA], sp[4][2];
    static_for<0, 16>([&](auto sc) {
        constexpr int s = decltype(sc)::value;
        acc[s >> 2][s & 3] = MFMA(a1[s >> 2].v(), b1[s & 3], acc[s >> 2][s & 3]);
        if constexpr (s < 8) sp[s >> 1][s & 1] = __builtin_bit_cast(f32x4, lds_read128<(s & 1) * 16384 + (s >> 1) * 256>(adB));
        else if constexpr (s < 15) { constexpr int m = s - 8; const int reg = m < 6 ? m & 1 : q >> 1, c4 = m < 6 ? wm * 2 + (m & 6) * 2 + q : (wm ? 0 : 12) + (q & 1);
            csv[m] = __builtin_bit_cast(f32x4, lds_read128<32768>(lds0 + (unsigned)(reg * 16384 + wm * 256 + ((c4 ^ ((wm << 1) | reg)) << 4)))); }
        if constexpr (GRP == 1 && s == 2) g128_load<4, 0>(p, lds, wid, ep);
        sched0();
    });
    wait_lgkm0(); wait_vm<GRP ? 4 : 0>(); asm volatile("; pf %0" ::"v"(pf)); sched0();
    float mean[4], c1[4], ve[4];
#pragma unroll
    for (int n = 0; n < 4; ++n) {
        const f32x4 s0 = sp[n][0], s1 = sp[n][1];
        mean[n] = qsum((s0[0] + s0[2]) + (s1[0] + s1[2])) * (1.f / 16.f);
        const float d0 = s0[0] - mean[n], d1 = s0[2] - mean[n], d2 = s1[0] - mean[n], d3 = s1[2] - mean[n];
        const float m2 = qsum((s0[1] + s0[3]) + (s1[1] + s1[3]) + 64.f * ((d0 * d0 + d1 * d1) + (d2 * d2 + d3 * d3)));
        ve[n] = m2 * (1.f / DM) + LN_EPS; c1[n] = -1.44269504f * __builtin_amdgcn_rsqf(ve[n]);
    }
#pragma unroll
    for (int s = 16; s < 28; ++s) acc[s >> 2][s & 3] = MFMA(a1[s >> 2].v(), b1[s & 3], acc[s >> 2][s & 3]);
#pragma unroll
    for (int s = 16; s < 28; ++s) { __builtin_amdgcn_sched_group_barrier(0x008, 1, 0); __builtin_amdgcn_sched_group_barrier(0x002, 10, 0); }
    sched0();
    STAMP(2);
    const auto ro = RSRC(p.act, SEQ * FF * 2);
    auto silu2 = [&](f32x2 g, f32x2 u, int n) {
        const f32x2 x = g * c1[n], d = (f32x2){__builtin_amdgcn_exp2f(x[0]), __builtin_amdgcn_exp2f(x[1])} * ve[n] + ve[n];
        const f32x2 r = g * u * (f32x2){__builtin_amdgcn_rcpf(d[0]), __builtin_amdgcn_rcpf(d[1])};
        return pack_bf16(r[0], r[1]);
    };
#pragma unroll
    for (int j = 0; j < 3; ++j)
#pragma unroll
        for (int np = 0; np < 2; ++np) {
            unsigned o[2][2];
#pragma unroll
            for (int i = 0; i < 2; ++i) { const int n = 2 * np + i; const f32x4 g = acc[2 * j][n] - mean[n] * csv[2 * j], u = acc[2 * j + 1][n] - mean[n] * csv[2 * j + 1];
                o[i][0] = silu2((f32x2){g[0], g[1]}, (f32x2){u[0], u[1]}, n); o[i][1] = silu2((f32x2){g[2], g[3]}, (f32x2){u[2], u[3]}, n); }
            swap16u(o[0][0], o[1][0]); swap16u(o[0][1], o[1][1]);
            __builtin_amdgcn_raw_buffer_store_b128((u32x4){o[0][0], o[0][1], o[1][0], o[1][1]}, ro, (unsigned)(((tm * 14 + wm * 8 + 2 * j + (q >> 1)) * SEQ + tn * 256 + wn * 64 + np * 32 + (lane & 31)) * 16), 0, SC1);
        }
    {
        unsigned o[4];
#pragma unroll
        for (int n = 0; n < 4; ++n) {
            const f32x4 v = acc[6][n] - mean[n] * csv[6];
            float g0 = v[0], g1 = v[1], u0 = v[2], u1 = v[3];
            swap32(g0, u0); swap32(g1, u1);
            o[n] = silu2((f32x2){g0, g1}, (f32x2){u0, u1}, n);
        }
        swap16u(o[0], o[1]); swap16u(o[2], o[3]); swap32u(o[0], o[2]); swap32u(o[1], o[3]);
        __builtin_amdgcn_raw_buffer_store_b128((u32x4){o[0], o[2], o[1], o[3]}, ro, (unsigned)(((tm * 14 + 6 + wm) * SEQ + tn * 256 + wn * 64 + lane) * 16), 0, SC1);
    }
    STAMP(5);
    __builtin_amdgcn_s_setprio(0);
    if constexpr (GRP == 1) { wait_vm<0>(); bar(); }
    STAMP_END(3);
}
#endif

__global__ __launch_bounds__(512, 2) void fused_k(const Params p) {
#if defined(__HIP_DEVICE_COMPILE__)
    __shared__ __attribute__((aligned(16))) unsigned char lds_raw[5 * 32768];
    LAS unsigned char* lds = (LAS unsigned char*)lds_raw;
    const int wid = __builtin_amdgcn_readfirstlane(threadIdx.x >> 6);
    u64 ep = 0;
    if (wid < 4) { g128_compute<1>(p, lds, wid, ep); g128_compute<2>(p, lds, wid, ep); gemm3_body<0>(p, lds, wid, ep); g128_compute<4>(p, lds, wid, ep); }
    else { g128_load<1, -1>(p, lds, wid, ep); g128_load<2, -1>(p, lds, wid, ep); gemm3_body<1>(p, lds, wid, ep); g128_load<4, -1>(p, lds, wid, ep); }
#endif
}
__global__ void tail_k() {}

extern "C" void kernel_launch(void* const* d_in, const int* in_sizes, int n_in, void* d_out, int out_size, void* d_ws, size_t ws_size, hipStream_t stream) {
    Params p;
    p.x = (const u16*)d_in[0]; p.cre = (const u16*)d_in[1]; p.cim = (const u16*)d_in[2]; p.wre = (const u16*)d_in[3]; p.wim = (const u16*)d_in[4];
    p.wg = (const u16*)d_in[6]; p.wu = (const u16*)d_in[7]; p.wd = (const u16*)d_in[8];
    p.out = (u16*)d_out;
    char* ws = (char*)d_ws;
    p.abT = (u16*)(ws + (ABP == 1024 ? 0u : 48u << 20)); p.h = (u16*)(ws + (8u << 20)); p.act = (u16*)(ws + (16u << 20));
    p.xch = (u16*)(ws + (32u << 20)); p.flags = (unsigned long long*)(ws + (40u << 20)); p.csum = (float*)(ws + (40u << 20) + (512u << 10));
    fused_k<<<256, 512, 0, stream>>>(p);
}
```

```cpp
#include <hip/hip_runtime.h>
#include <hip/hip_bf16.h>
#include <type_traits>

#define LAS __attribute__((address_space(3)))
typedef unsigned short u16;
typedef __bf16 bf16x8 __attribute__((ext_vector_type(8)));
typedef __bf16 bf16x2 __attribute__((ext_vector_type(2)));
typedef float f32x4 __attribute__((ext_vector_type(4)));
typedef float f32x2 __attribute__((ext_vector_type(2)));
typedef unsigned u32x4 __attribute__((ext_vector_type(4)));
typedef unsigned u32x2 __attribute__((ext_vector_type(2)));

constexpr int SEQ = 2048, DM = 1024, FF = 3584;
#define ABP 1024
constexpr int ABT_BYTES = 512 * ABP * 16;
#define STA 16
constexpr float LN_EPS = 1e-5f;
constexpr int SC1 = 16;

struct Params {
    const u16* x; const u16* cre; const u16* cim; const u16* wre; const u16* wim;
    const u16* wg; const u16* wu; const u16* wd;
    u16* out; u16* abT; u16* h; u16* act; u16* xch; unsigned long long* flags; float* csum; float* ln1;
};

template <int N> __device__ __forceinline__ void wait_vm() { asm volatile("s_waitcnt vmcnt(%0)" ::"n"(N) : "memory"); }
__device__ __forceinline__ void wait_lgkm0() { __builtin_amdgcn_s_waitcnt(0xC07F); asm volatile("s_waitcnt lgkmcnt(0)" ::: "memory"); }
__device__ __forceinline__ void sched0() { __builtin_amdgcn_sched_barrier(0); }
__device__ __forceinline__ void bar() { sched0(); asm volatile("" ::: "memory"); __builtin_amdgcn_s_barrier(); asm volatile("" ::: "memory"); sched0(); }
__device__ __forceinline__ unsigned pack_bf16(float lo, float hi) { bf16x2 c; c[0] = (__bf16)lo; c[1] = (__bf16)hi; return __builtin_bit_cast(unsigned, c); }
__device__ __forceinline__ float bf_lo(unsigned v) { return __uint_as_float(v << 16); }
__device__ __forceinline__ float bf_hi(unsigned v) { return __uint_as_float(v & 0xffff0000u); }
__device__ __forceinline__ void swap16(float& a, float& b) { auto r = __builtin_amdgcn_permlane16_swap(__float_as_uint(a), __float_as_uint(b), false, false); a = __uint_as_float(r[0]); b = __uint_as_float(r[1]); }
__device__ __forceinline__ void swap32(float& a, float& b) { auto r = __builtin_amdgcn_permlane32_swap(__float_as_uint(a), __float_as_uint(b), false, false); a = __uint_as_float(r[0]); b = __uint_as_float(r[1]); }
__device__ __forceinline__ void swap16u(unsigned& a, unsigned& b) { auto r = __builtin_amdgcn_permlane16_swap(a, b, false, false); a = r[0]; b = r[1]; }
__device__ __forceinline__ void swap32u(unsigned& a, unsigned& b) { auto r = __builtin_amdgcn_permlane32_swap(a, b, false, false); a = r[0]; b = r[1]; }
__device__ __forceinline__ float qsum(float v) { float a = v, b = v; swap16(a, b); a += b; b = a; swap32(a, b); return a + b; }

template <int CTRL> __device__ __forceinline__ float dpp_add(float v) { return v + __int_as_float(__builtin_amdgcn_update_dpp(0, __float_as_int(v), CTRL, 0xf, 0xf, false)); }
__device__ __forceinline__ float hsum(float v) { v = dpp_add<0x128>(v); v = dpp_add<0x124>(v); v = dpp_add<0x122>(v); v = dpp_add<0x121>(v); float b = v; swap16(v, b); return v + b; }

struct Frag { u32x2 lo, hi; __device__ __forceinline__ bf16x8 v() const { return __builtin_bit_cast(bf16x8, __builtin_shufflevector(lo, hi, 0, 1, 2, 3)); } };
template <int OFF> __device__ __forceinline__ u32x2 tr_read(unsigned addr) { u32x2 r; asm volatile("ds_read_b64_tr_b16 %0, %1 offset:%2" : "=v"(r) : "v"(addr), "n"(OFF) : "memory"); return r; }
template <int OFF> __device__ __forceinline__ bf16x8 lds_read128(unsigned addr) { bf16x8 r; asm volatile("ds_read_b128 %0, %1 offset:%2" : "=v"(r) : "v"(addr), "n"(OFF) : "memory"); return r; }
template <int OFF> __device__ __forceinline__ float lds_read32(unsigned addr) { float r; asm volatile("ds_read_b32 %0, %1 offset:%2" : "=v"(r) : "v"(addr), "n"(OFF) : "memory"); return r; }
template <int N> __device__ __forceinline__ void wait_lgkm() { asm volatile("s_waitcnt lgkmcnt(%0)" ::"n"(N) : "memory"); }
template <int I, int N, class F> __device__ __forceinline__ void static_for(F&& f) { if constexpr (I < N) { f(std::integral_constant<int, I>{}); static_for<I + 1, N>(f); } }
template <int NA> __device__ __forceinline__ void keep(const Frag (&a)[NA], const bf16x8 (&b)[4]) {
#pragma unroll
    for (int i = 0; i < NA; ++i) asm volatile("" ::"v"(a[i].lo), "v"(a[i].hi));
#pragma unroll
    for (int i = 0; i < 4; ++i) asm volatile("" ::"v"(b[i]));
}
__device__ __forceinline__ void bump(unsigned& a, int d) { asm volatile("v_add_u32 %0, %1, %0" : "+v"(a) : "s"(d)); }
#define MFMA(a, b, c) __builtin_amdgcn_mfma_f32_16x16x32_bf16(a, b, c, 0, 0, 0)
#define RSRC(ptr, bytes) __builtin_amdgcn_make_buffer_rsrc((void*)(ptr), 0, (bytes), 0x00020000)
#define DMA16(rs, ldsp, voff, soff) __builtin_amdgcn_raw_ptr_buffer_load_lds(rs, (LAS void*)(ldsp), 16, voff, soff, 0, 0)
#define DMA16_DEV(rs, ldsp, voff, soff) __builtin_amdgcn_raw_ptr_buffer_load_lds(rs, (LAS void*)(ldsp), 16, voff, soff, 0, SC1)

#define STAMP(i)
#define STAMP_BEGIN
#define STAMP_END(ID)

typedef unsigned long long u64;
#define FSTR 8
constexpr int FLAG_STRIDE = FSTR, F_PAIR = 1, F_AB = F_PAIR + 1536, F_H = F_AB + 256, F_ACT = F_H + 256, F_CS = F_ACT + 256, F_LN = F_CS + 128;
#define PSCOPE __HIP_MEMORY_SCOPE_AGENT
#define PSLEEP 2
__device__ __forceinline__ u64 flag_load(u64* w) { return __hip_atomic_load(w, __ATOMIC_RELAXED, PSCOPE); }
__device__ __forceinline__ void flag_store(u64* w, u64 v) { __hip_atomic_store(w, v, __ATOMIC_RELAXED, PSCOPE); }
__device__ __forceinline__ u64 uniform64(u64 v) { return ((u64)(unsigned)__builtin_amdgcn_readfirstlane((unsigned)(v >> 32)) << 32) | (unsigned)__builtin_amdgcn_readfirstlane((unsigned)v); }
__device__ __forceinline__ void wait_group(u64* word, bool need, u64 ep) {
#pragma nounroll
    for (int spin = 0; spin < (1 << 20); ++spin) {
        if (!__any(need && flag_load(word) != ep)) break;
        __builtin_amdgcn_s_sleep(PSLEEP);
    }
}
#if defined(__HIP_DEVICE_COMPILE__)
__device__ __forceinline__ void touch(unsigned& sink, unsigned voff, const void* base, unsigned bytes) {
    asm volatile("s_nop 4\n\tbuffer_load_dword %0, %1, %2, 0 offen" : "+v"(sink) : "v"(voff), "s"(RSRC(base, bytes)) : "memory");
}
__device__ __forceinline__ void flag_peek(u64& v, const u64* w) { asm volatile("global_load_dwordx2 %0, %1, off sc1" : "+v"(v) : "v"(w) : "memory"); }
struct G3W {
    const u16* ptr; unsigned voff[2]; int phi, koff;
    __device__ __forceinline__ G3W(const Params& p, int wid, int lane) {
        const int xcd = blockIdx.x & 7, idx = blockIdx.x >> 3, tm = (xcd & 3) * 8 + (idx & 7), q = lane >> 4;
        ptr = wid < 4 ? p.wg : p.wu; phi = ((idx & 7) + (idx >> 3)) & 3; koff = xcd * 2;
#pragma unroll
        for (int i = 0; i < 2; ++i) { const int j = (lane & 15) ^ (((q | (i << 2)) << 1) | (wid >> 2));
            voff[i] = j < 14 ? (unsigned)(((wid & 3) * 16 + q) * FF * 2 + tm * 224 + j * 16) : 0x7ffffff0u; }
    }
    __device__ __forceinline__ int kstep(int t) const { return (t + koff + ((t & 3) == phi ? 4 : 0)) & 15; }
    __device__ __forceinline__ void piece(LAS unsigned char* lds, int wid, const void* base, int so, unsigned nrec, int slot, int i) const { DMA16_DEV(RSRC(base, nrec), lds + slot * 32768 + wid * 4096 + i * 1024, voff[i >> 1], so + i * 4 * FF * 2); }
    __device__ __forceinline__ void early(LAS unsigned char* lds, int wid, int t) const {
#pragma unroll
        for (int i = 0; i < 4; ++i) piece(lds, wid, ptr, kstep(t) * 64 * FF * 2, DM * FF * 2, t, i);
    }
};
#define CSW 4
#define CSD 0
__device__ __forceinline__ void colsum_partial(const Params& p, LAS unsigned char* lds, int lw) {
    const int lane = threadIdx.x & 63, slab = blockIdx.x >> 1, mat = slab >= 56, c0 = (slab - mat * 56) * 64;
    const auto rw = RSRC(mat ? p.wu : p.wg, slab < 112 ? DM * FF * 2 : 0);
    const unsigned vo = (unsigned)(((lw * 256 + (lane >> 3)) * FF + c0 + (lane & 7) * 8) * 2);
    u32x4 v[8]; f32x4 s0 = {0.f, 0.f, 0.f, 0.f}, s1 = s0;
    auto add = [&](const u32x4 w) { s0 += (f32x4){bf_lo(w[0]), bf_hi(w[0]), bf_lo(w[1]), bf_hi(w[1])}; s1 += (f32x4){bf_lo(w[2]), bf_hi(w[2]), bf_lo(w[3]), bf_hi(w[3])}; };
    if (CSD) __builtin_amdgcn_s_sleep(CSD);
#pragma unroll
    for (int i = 0; i < CSW; ++i) v[i] = __builtin_amdgcn_raw_buffer_load_b128(rw, vo, i * 8 * FF * 2, 0);
#pragma unroll
    for (int i = 0; i < 8; ++i) { add(v[i % CSW]); v[i % CSW] = __builtin_amdgcn_raw_buffer_load_b128(rw, vo, (CSW + i) * 8 * FF * 2, 0); sched0(); }
#pragma unroll
    for (int i = CSW; i < 8; ++i) v[i] = __builtin_amdgcn_raw_buffer_load_b128(rw, vo, (8 + i) * 8 * FF * 2, 0);
#pragma nounroll
    for (int j = 16; j < 40; j += 8)
#pragma unroll
        for (int i = 0; i < 8; ++i) { add(v[i]); v[i] = __builtin_amdgcn_raw_buffer_load_b128(rw, j < 32 ? vo : 0x7ffffff0u, (j + i) * 8 * FF * 2, 0); sched0(); }
    LAS float* tab = (LAS float*)(lds + 131072) + (lw * 8 + (lane >> 3)) * 64 + (lane & 7) * 8;
    *(LAS f32x4*)tab = s0; *(LAS f32x4*)(tab + 4) = s1;
    wait_lgkm0();
}
__device__ __forceinline__ void colsum_final(const Params& p, LAS unsigned char* lds, u64 ep) {
    const int lane = threadIdx.x & 63, slab = blockIdx.x >> 1, mat = slab >= 56, c0 = (slab - mat * 56) * 64;
    if (slab >= 112) return;
    const LAS float* tab = (const LAS float*)(lds + 131072);
    float a = 0.f, b = 0.f;
#pragma unroll
    for (int i = 0; i < 32; i += 2) { a += tab[i * 64 + lane]; b += tab[(i + 1) * 64 + lane]; }
    const int f = c0 + lane, t = f / 112, r = f - t * 112, h = r >= 56;
    __hip_atomic_store(p.csum + (mat * 2 + h) * 1792 + t * 56 + r - h * 56, a + b, __ATOMIC_RELAXED, __HIP_MEMORY_SCOPE_AGENT);
    wait_vm<0>();
    if (lane == 0) flag_store(p.flags + (F_CS + slab) * FLAG_STRIDE, ep);
}
#endif

enum { ROW, CHK, TR };
#define NKS2 27
#define FLAGD 1
#define W1E 1
#define LNT0 1
#define LNT1 5
#define NPRE2 1
#define RE4 1
#define RO4 3
template <int ID> struct Cfg;
template <> struct Cfg<1> { static constexpr int NKT = 16, NKS = 16, FA = ROW, FB = ROW, LDB = DM; };
template <> struct Cfg<2> { static constexpr int NKT = 64, NKS = NKS2, FA = CHK, FB = ROW, LDB = SEQ; };
template <> struct Cfg<4> { static constexpr int NKT = 56, NKS = 0, FA = TR, FB = CHK, LDB = 0; };

#if defined(__HIP_DEVICE_COMPILE__)
template <int ID, int PRE>
__device__ __forceinline__ void g128_load(const Params& p, LAS unsigned char* lds, const int wid, u64& ep) {
    using C = Cfg<ID>;
    constexpr int STAGE = 32768, XLDS = 4 * STAGE;
    const int lane = threadIdx.x & 63, l15 = lane & 15, q = lane >> 4;
    const int xcd = blockIdx.x & 7, idx = blockIdx.x >> 3, im = idx & 3, in = idx >> 2;
    constexpr int NH = ID == 4 ? 2 * (RE4 + RO4) : 0;
    const int ks = (xcd & 1) ^ (ID == 2); const bool recv = ID == 4 || (ID == 2 && ks);
    const int tm = (ID == 1 ? (xcd & 3) : ((xcd >> 1) & 1)) * 4 + im, tn = (xcd >> 2) * 8 + in;
    const int k0 = ID != 1 && ks ? C::NKS : 0, nk = ID == 4 ? 28 - 2 * (ks ? RO4 : RE4) : ID == 2 && ks ? C::NKT - C::NKS : C::NKS;
    const int xb = ID == 4 ? 128 + (blockIdx.x ^ (wid >> 2)) : blockIdx.x >> 1;
    u64* const flag = p.flags + (F_PAIR + xb * 4 + (wid & 3)) * FLAG_STRIDE;
    const unsigned xoff = (unsigned)(ID == 4 ? (128 + xb) * 16384 + (wid & 3) * 4096 : xb * 32768 + (wid & 3) * 8192);
    const int lw = wid - 4, phi = (im + in) & 3;
    const int koff = ID == 1 ? xcd * 2 : ((xcd >> (ID == 2 ? 1 : 2)) & 1) * (nk >> 1);
    const unsigned ksw = (unsigned)(((lane & 7) ^ (lane >> 3)) << 4);
    unsigned voffA[2], voffB;
    if constexpr (C::FA == ROW) voffA[0] = (unsigned)((tm * 128 + lw * 32 + (lane >> 3)) * DM * 2) + ksw;
    if constexpr (C::FA == CHK) voffA[0] = (unsigned)((lw * 2 * ABP + tm * 128 + lane) * 16);
    if constexpr (C::FA == TR) {
#pragma unroll
        for (int i = 0; i < 2; ++i) voffA[i] = (unsigned)((lw * 16 + q) * DM * 2 + tm * 256 + ((l15 ^ ((q | (i << 2)) << 1)) << 4));
    }
    if constexpr (C::FB == ROW) voffB = (unsigned)((((ID == 1 ? tn & 7 : tn)) * 128 + lw * 32 + (lane >> 3)) * C::LDB * 2) + ksw;
    else voffB = (unsigned)((lw * 2 * SEQ + tn * 128 + lane) * 16);
    const auto ra = ID == 1 ? RSRC(p.x, SEQ * DM * 2) : ID == 2 ? RSRC(p.abT, ABT_BYTES) : RSRC(p.wd, FF * DM * 2);
    auto stage = [&](int t, bool doA, bool doB, bool half = false) {
        int k = (t & 3) != phi ? t : t + 4 < nk ? t + 4 : phi;
        k += koff; k = k >= nk ? k - nk : k;
        if constexpr (ID == 4) k = ks * 14 + (k < (nk >> 1) ? k : k + 28 - (nk >> 1)); else k += k0;
        if (half) { int u = t - nk + phi; u = u >= NH ? u - NH : u; const int h = u >= RE4 + RO4, v = u - h * (RE4 + RO4); k = h * 28 + (v < RE4 ? 14 - RE4 + v : 28 - RO4 - RE4 + v); }
        const auto rb = ID == 1 ? RSRC(tn < 8 ? p.wre : p.wim, DM * DM * 2) : ID == 2 ? RSRC(k < 32 ? p.cre : p.cim, SEQ * SEQ * 2) : RSRC(p.act, SEQ * FF * 2);
        LAS unsigned char* base = lds + (t & 3) * STAGE + lw * 4096;
#pragma unroll
        for (int i = 0; i < 4; ++i) {
            if (doA) {
                if constexpr (C::FA == ROW) DMA16(ra, base + i * 1024, voffA[0], k * 128 + i * 8 * DM * 2);
                if constexpr (C::FA == CHK) DMA16_DEV(ra, base + i * 1024, voffA[0], (k * 8 + (i >> 1)) * ABP * 16 + (i & 1) * 1024);
                if constexpr (C::FA == TR) DMA16(ra, base + i * 1024, voffA[i >> 1], (k * 64 + i * 4) * DM * 2);
            }
            if (doB) {
                if constexpr (C::FB == ROW) DMA16(rb, base + 16384 + i * 1024, voffB, (k & 31) * 128 + i * 8 * C::LDB * 2);
                else if (!(half && (i & 1))) DMA16_DEV(rb, base + 16384 + i * 1024, voffB, (k * 8 + (i >> 1)) * SEQ * 16 + ((i & 1) ^ ks) * 1024);
            }
        }
    };
    if constexpr (PRE >= 0) { stage(PRE, ID == 4, ID == 2); return; }
    if constexpr (ID != 1) {
        u64* words = p.flags + (ID == 2 ? F_AB + tm * 32 : F_ACT + (tn >> 1) * 32) * FLAG_STRIDE;
        if (wid == 4 && lane == 0) flag_store(ID == 2 ? p.flags + (F_AB + (tn & 7) * 32 + (tn >> 3) * 16 + (xcd & 3) * 4 + im) * FLAG_STRIDE : words + ((xcd & 3) * 8 + (idx & 7)) * FLAG_STRIDE, ep);
        if constexpr (ID == 4) { if (wid != 4) __builtin_amdgcn_s_sleep(2); stage(1, true, false); stage(2, true, false); }
        if constexpr (ID == 2 && NPRE2 < 3) { if (wid != 4) __builtin_amdgcn_s_sleep(2); if (NPRE2 < 2) stage(1, false, true); stage(2, false, true); }
        if (wid == 4) { const int j = lane & 31; wait_group(words + j * FLAG_STRIDE, ID == 2 ? 2 * j + 2 > k0 && 2 * j < k0 + nk : ((j >> 3) & 1) == ks, ep); }
        bar();
        stage(0, ID == 2, ID == 4); stage(1, ID == 2, ID == 4); stage(2, ID == 2, ID == 4);
        wait_vm<8>(); bar();
        wait_vm<4>();
    } else {
        stage(0, true, true); stage(1, true, true);
        wait_vm<8>(); bar();
        stage(2, true, true);
        const unsigned a = (unsigned)(size_t)lds + XLDS;
        const float e0 = lds_read32<3072>(a), e1 = lds_read32<3076>(a);
        wait_lgkm0(); sched0();
        ep = uniform64(((u64)__float_as_uint(e1) << 32) | __float_as_uint(e0));
    }
    u64 pseen = 0; u64* const pwords = p.flags + (F_ACT + (tn >> 1) * 32 + (lane & 31)) * FLAG_STRIDE;
#pragma nounroll
    for (int t = 0; t < nk - 3; ++t) {
        wait_vm<8>();
        if constexpr (ID == 4) { if (t == nk - 4 && wid == 4 && __any(pseen != ep)) wait_group(pwords, true, ep); }
        bar();
        if constexpr (ID == 4) { if (t == nk - 5 && wid == 4) flag_peek(pseen, pwords); }
        stage(t + 3, true, true);
        if constexpr (ID == 1) { if (t == LNT1 && wid == 4 && lane == 0) flag_store(p.flags + (F_LN + tm * 16 + tn) * FLAG_STRIDE, ep); }
    }
    if constexpr (NH > 0) {
        wait_vm<8>(); bar(); stage(nk, true, true, true);
#pragma nounroll
        for (int t = nk - 2; t < nk + NH - 3; ++t) { wait_vm<6>(); bar(); stage(t + 3, true, true, true); }
    }
    bool got = !recv;
    auto fetch = [&](u64 seen) {
        if (uniform64(seen) != ep) return false;
        asm volatile("" ::: "memory");
#pragma unroll
        for (int i = 0; i < (ID == 4 ? 4 : 8); ++i) DMA16_DEV(RSRC(p.xch, 256 * 32768), lds + XLDS + lw * 8192 + i * 1024, (unsigned)(lane * 16), xoff + i * 1024);
        return true;
    };
    wait_vm<NH ? 6 : 8>(); bar();
    u64* const lnw = p.flags + (F_LN + (lane & 16 ? l15 * 16 + tn : tm * 16 + l15)) * FLAG_STRIDE;
    u64 seen0 = flag_load(ID == 1 ? lnw : flag);
    wait_vm<0>();
    if (!got) got = fetch(seen0);
    if constexpr (ID == 1) { if (wid == 4) {
        if (__any(seen0 != ep)) wait_group(lnw, true, ep);
        asm volatile("" ::: "memory");
#pragma unroll
        for (int i = 0; i < 3; ++i) DMA16_DEV(RSRC(p.ln1, 3 * SEQ * 4), lds + XLDS + i * 1024, lane < 32 ? (unsigned)(lane * 16) : 0x7ffffff0u, (i * SEQ + (i < 2 ? tm : tn) * 128) * 4);
    } }
    if constexpr (ID == 1) g128_load<2, 0>(p, lds, wid, ep);
    bar();
    if constexpr (ID != 1) seen0 = flag_load(flag);
    if constexpr (ID == 2) { if (!got) got = fetch(seen0); }
    if constexpr (ID == 1 && NPRE2 > 1) g128_load<2, 1>(p, lds, wid, ep);
    bar();
    if (!got) got = fetch(seen0);
    if constexpr (ID == 1 && NPRE2 > 2) g128_load<2, 2>(p, lds, wid, ep);
    if (recv) {
#pragma nounroll
        for (int spin = 0; !got && spin < (1 << 20); ++spin) { got = fetch(flag_load(flag)); __builtin_amdgcn_s_sleep(1); }
        wait_vm<0>(); bar();
    }
    if constexpr (ID == 2) { if (!recv) colsum_partial(p, lds, lw); }
    if constexpr (ID == 1) { wait_vm<4 * NPRE2>(); bar(); }
    if constexpr (ID != 4) bar();
}

template <int ID>
__device__ __forceinline__ void g128_compute(const Params& p, LAS unsigned char* lds, const int wid, u64& ep) {
    using C = Cfg<ID>;
    constexpr int STAGE = 32768, XLDS = 4 * STAGE;
    const int lane = threadIdx.x & 63, l15 = lane & 15, q = lane >> 4;
    const int xcd = blockIdx.x & 7, idx = blockIdx.x >> 3, im = idx & 3, in = idx >> 2;
    constexpr int NH = ID == 4 ? 2 * (RE4 + RO4) : 0;
    const int ks = (xcd & 1) ^ (ID == 2); const bool recv = ID == 4 || (ID == 2 && ks);
    const int tm = (ID == 1 ? (xcd & 3) : ((xcd >> 1) & 1)) * 4 + im, tn = (xcd >> 2) * 8 + in;
    const int k0 = ID != 1 && ks ? C::NKS : 0, nk = ID == 4 ? 28 - 2 * (ks ? RO4 : RE4) : ID == 2 && ks ? C::NKT - C::NKS : C::NKS;
    const int xb = ID == 4 ? 128 + (blockIdx.x ^ (wid >> 2)) : blockIdx.x >> 1;
    u64* const flag = p.flags + (F_PAIR + xb * 4 + (wid & 3)) * FLAG_STRIDE;
    const unsigned xoff = (unsigned)(ID == 4 ? (128 + xb) * 16384 + (wid & 3) * 4096 : xb * 32768 + (wid & 3) * 8192);
    STAMP_BEGIN;
    const int wm = wid & 1, wn = wid >> 1;
    const int eo = (q & 1) * 16 + (q >> 1) * 8;
    f32x4 acc[4][4];
#pragma unroll
    for (int m = 0; m < 4; ++m)
#pragma unroll
        for (int n = 0; n < 4; ++n) acc[m][n] = (f32x4){0.f, 0.f, 0.f, 0.f};
    const int row0 = tn * 128 + (ID == 4 ? ks * 64 + wn * 32 : wn * 64) + l15, col0 = tm * 128 + wm * 64 + eo;
    if constexpr (ID != 1) {
        constexpr int NN = ID == 4 ? 2 : 4;
        if (ID == 4 || !recv) {
            if constexpr (ID == 2) { wait_vm<0>(); bar(); __builtin_amdgcn_s_sleep(4); }
            u32x4 r[NN][2];
#pragma unroll
            for (int n = 0; n < NN; ++n)
#pragma unroll
                for (int mp = 0; mp < 2; ++mp)
                    r[n][mp] = __builtin_amdgcn_raw_buffer_load_b128(RSRC(ID == 2 ? p.x : p.h, SEQ * DM * 2), (unsigned)(ID == 2 ? ((row0 + n * 16) * DM + col0 + mp * 32) * 2 : ((col0 / 8 + mp * 4) * SEQ + row0 + n * 16) * 16), 0, ID == 4 ? SC1 : 0);
            if constexpr (ID == 4) { wait_vm<4>(); bar(); }
#pragma unroll
            for (int n = 0; n < NN; ++n)
#pragma unroll
                for (int mp = 0; mp < 2; ++mp) {
                    unsigned r0 = r[n][mp][0], r1 = r[n][mp][1], r2 = r[n][mp][2], r3 = r[n][mp][3];
                    swap16u(r0, r2); swap16u(r1, r3);
                    acc[2 * mp][n] = (f32x4){bf_lo(r0), bf_hi(r0), bf_lo(r1), bf_hi(r1)}; acc[2 * mp + 1][n] = (f32x4){bf_lo(r2), bf_hi(r2), bf_lo(r3), bf_hi(r3)};
                }
        } else { wait_vm<0>(); bar(); }
    }
    unsigned pfc = 0;
    if constexpr (ID == 1) {
        ep = uniform64(flag_load(p.flags)) + 1;
        if (threadIdx.x == 0) *(LAS u64*)(lds + XLDS + 3072) = ep;
        wait_lgkm0();
        if (idx == 0) {
            unsigned long long pc; int lo, hi;
            asm volatile("s_getpc_b64 %0\n s_mov_b32 %1, _Z7fused_k6Params@rel32@lo+4\n s_mov_b32 %2, _Z6tail_kv@rel32@lo+12" : "=s"(pc), "=s"(lo), "=s"(hi));
            touch(pfc, threadIdx.x * 128, (const void*)(pc + (long long)lo), (unsigned)max(hi - lo, 0)); touch(pfc, threadIdx.x * 128 + 32768, (const void*)(pc + (long long)lo), (unsigned)max(hi - lo, 0));
        }
    }
    constexpr int NA = C::FA == CHK ? 1 : C::FA == ROW ? 2 : 4, NB = C::FB == CHK ? 1 : 2;
    unsigned adA[NA], adB[NB];
    const unsigned lds0 = (unsigned)(size_t)lds, row_sw = lds0 + (unsigned)(l15 * 128 + ((q ^ (lane & 7)) << 4)), chk = lds0 + (unsigned)(q * 2048 + l15 * 16);
#pragma unroll
    for (int i = 0; i < NA; ++i) {
        if constexpr (C::FA == ROW) adA[i] = (row_sw ^ (unsigned)(i * 64)) + (unsigned)(wm * 8192);
        if constexpr (C::FA == CHK) adA[i] = chk + (unsigned)(wm * 1024);
        if constexpr (C::FA == TR) { const int q4 = l15 >> 2; adA[i] = lds0 + (unsigned)((8 * q + q4) * 256 + (((wm * 4 + i) ^ (q4 | ((q & 1) << 2))) << 5) + 8 * (lane & 3)); }
    }
#pragma unroll
    for (int i = 0; i < NB; ++i) adB[i] = C::FB == CHK ? 16384u + chk + (unsigned)(wn * 512) : 16384u + (row_sw ^ (unsigned)(i * 64)) + (unsigned)(wn * 8192);
    constexpr int W = C::FA == TR ? 2 : 1, NR = 4 + 4 * W;
    auto rd = [&](auto ic, Frag (&ra)[4], bf16x8 (&rb)[4], auto kkc) {
        constexpr int kk = decltype(kkc)::value, i = decltype(ic)::value;
        if constexpr (i == 0 || (i > W && i <= W + 3)) { constexpr int n = i == 0 ? 0 : i - W;
            if constexpr (C::FB == CHK) rb[n] = lds_read128<kk * 8192 + (n >> 1) * 1024 + (n & 1) * 256>(adB[0]);
            else rb[n] = lds_read128<n * 2048>(adB[kk]); }
        else { constexpr int j = i <= W ? i - 1 : i - 4, m = j / W;
            if constexpr (C::FA != TR) { bf16x8 v;
                if constexpr (C::FA == CHK) v = lds_read128<kk * 8192 + m * 256>(adA[0]); else v = lds_read128<m * 2048>(adA[kk]);
                const u32x4 w = __builtin_bit_cast(u32x4, v); ra[m].lo = (u32x2){w[0], w[1]}; ra[m].hi = (u32x2){w[2], w[3]}; }
            else if constexpr (j % 2 == 0) ra[m].lo = tr_read<kk * 8192>(adA[m]);
            else ra[m].hi = tr_read<kk * 8192 + 1024>(adA[m]); }
    };
    auto phase = [&](const Frag (&ra)[4], const bf16x8 (&rb)[4], Frag (&na)[4], bf16x8 (&nb)[4], int dstep, auto kkc) {
        static_for<0, 16>([&](auto sc) {
            constexpr int s = decltype(sc)::value, m = s >> 2, n = s & 3;
            if constexpr (n == 0) { wait_lgkm<(3 - m) * W + (s < NR ? s : NR)>(); sched0(); }
            acc[m][n] = MFMA(ra[m].v(), rb[n], acc[m][n]);
            if constexpr (s < NR) rd(sc, na, nb, kkc);
            if constexpr (s >= 12 && decltype(kkc)::value == 1) {
                if constexpr (s - 12 < NA) bump(adA[s - 12], dstep);
                if constexpr (s - 12 < NB) bump(adB[s - 12], dstep);
            }
            sched0();
        });
    };
    Frag a0[4], a1[4]; bf16x8 b0[4], b1[4];
    constexpr std::integral_constant<int, 0> K0{}; constexpr std::integral_constant<int, 1> K1{};
    if constexpr (ID != 1) bar();
    bar();
    STAMP(1);
    static_for<0, NR>([&](auto ic) { rd(ic, a0, b0, K0); });
    sched0();
    const int rx = tm * 128 + tn * 8 + wid * 2 + (lane >> 5), rw = tn * 128 + tm * 8 + wid * 2 + (lane >> 5);
    u32x4 xv[4], wv[4];
    if constexpr (ID == 1) {
#pragma unroll
        for (int i = 0; i < 4; ++i) { xv[i] = __builtin_amdgcn_raw_buffer_load_b128(RSRC(p.x, SEQ * DM * 2), (unsigned)(rx * 2048 + (lane & 31) * 16 + i * 512), 0, 0);
                                      wv[i] = __builtin_amdgcn_raw_buffer_load_b128(RSRC(tn < 8 ? p.wre : p.wim, DM * DM * 2), (unsigned)((rw & 1023) * 2048 + (lane & 31) * 16 + i * 512), 0, 0); }
        sched0();
    }
    auto steps = [&](int t0, int t1) {
#pragma nounroll
        for (int t = t0; t < t1; ++t) {
            phase(a0, b0, a1, b1, (t & 3) == 3 ? -3 * STAGE : STAGE, K1);
            if constexpr (ID == 1) { if (t == LNT1) wait_vm<0>(); }
            bar();
            phase(a1, b1, a0, b0, 0, K0);
        }
    };
    if constexpr (ID == 1) {
        steps(0, LNT0);
        wait_lgkm0(); sched0();
        f32x2 sx = {0.f, 0.f}, sw = sx, sq = sx;
#pragma unroll
        for (int i = 0; i < 16; ++i) { sx += (f32x2){bf_lo(xv[i >> 2][i & 3]), bf_hi(xv[i >> 2][i & 3])}; sw += (f32x2){bf_lo(wv[i >> 2][i & 3]), bf_hi(wv[i >> 2][i & 3])}; }
        const float mean = hsum(sx[0] + sx[1]) * (1.f / DM);
#pragma unroll
        for (int i = 0; i < 16; ++i) { const f32x2 d = (f32x2){bf_lo(xv[i >> 2][i & 3]), bf_hi(xv[i >> 2][i & 3])} - mean; sq += d * d; }
        const float rstd = __builtin_amdgcn_rsqf(hsum(sq[0] + sq[1]) * (1.f / DM) + LN_EPS), wsum = hsum(sw[0] + sw[1]);
        const int j = lane & 31;
        __builtin_amdgcn_raw_buffer_store_b32(__float_as_uint(j == 0 ? mean : j == 1 ? rstd : wsum), RSRC(p.ln1, 3 * SEQ * 4), j < 3 ? (unsigned)((j * SEQ + (j < 2 ? rx : rw)) * 4) : 0x7ffffff0u, 0, SC1);
        sched0();
        steps(LNT0, nk);
    } else steps(0, nk);
    wait_lgkm0(); keep(a0, b0); sched0();
    auto send = [&](int h0) {
        const auto rx = RSRC(p.xch, 256 * 32768);
#pragma unroll
        for (int m = 0; m < 4; ++m)
#pragma unroll
            for (int h = h0; h < 2; ++h) {
                const f32x4 u = acc[m][2 * h], v = acc[m][2 * h + 1];
                __builtin_amdgcn_raw_buffer_store_b128((u32x4){pack_bf16(u[0], u[1]), pack_bf16(u[2], u[3]), pack_bf16(v[0], v[1]), pack_bf16(v[2], v[3])}, rx, xoff + (unsigned)((h0 ? m : m * 2 + h) * 1024 + lane * 16), 0, SC1);
            }
    };
    if constexpr (NH > 0) {
        send(1); sched0();
        auto phaseH = [&](const Frag (&ra)[4], const bf16x8 (&rb)[4], Frag (&na)[4], bf16x8 (&nb)[4], int dstep, auto kkc) {
            static_for<0, 8>([&](auto sc) {
                constexpr int s = decltype(sc)::value;
                if constexpr ((s & 1) == 0) { wait_lgkm<6 - s + (s < 5 ? 2 * s : 10)>(); sched0(); }
                acc[s >> 1][s & 1] = MFMA(ra[s >> 1].v(), rb[s & 1], acc[s >> 1][s & 1]);
                if constexpr (s < 5) { rd(std::integral_constant<int, s < 2 ? 2 * s : 2 * s + 2>{}, na, nb, kkc); rd(std::integral_constant<int, s < 2 ? 2 * s + 1 : 2 * s + 3>{}, na, nb, kkc); }
                if constexpr (s >= 5 && decltype(kkc)::value == 1) { if constexpr (s < 7) { bump(adA[2 * (s - 5)], dstep); bump(adA[2 * (s - 5) + 1], dstep); } else bump(adB[0], dstep); }
                sched0();
            });
        };
#pragma nounroll
        for (int t = nk; t < nk + NH; ++t) {
            phaseH(a0, b0, a1, b1, (t & 3) == 3 ? -3 * STAGE : STAGE, K1);
            bar();
            phaseH(a1, b1, a0, b0, 0, K0);
            if (t == nk + FLAGD) { wait_vm<0>(); if (lane == 0) flag_store(flag, ep); }
        }
        wait_lgkm0();
        asm volatile("" ::"v"(b0[0]), "v"(b0[1]));
#pragma unroll
        for (int i = 0; i < 4; ++i) asm volatile("" ::"v"(a0[i].lo), "v"(a0[i].hi));
        sched0();
    }
    STAMP(2);
    if constexpr (ID == 1) {
        wait_vm<0>(); asm volatile("; pfc %0" ::"v"(pfc));
        bar();
        const auto ro = RSRC(p.abT, ABT_BYTES);
        const float sgn = tn < 8 ? 1.f : -1.f;
        f32x4 mean[4], rs[4]; float ws[4];
        static_for<0, 4>([&](auto ic) { constexpr int i = decltype(ic)::value; const unsigned a = lds0 + (unsigned)(XLDS + wm * 256 + q * 16);
            mean[i] = __builtin_bit_cast(f32x4, lds_read128<i * 64>(a)); rs[i] = __builtin_bit_cast(f32x4, lds_read128<1024 + i * 64>(a));
            ws[i] = lds_read32<2048 + i * 64>(lds0 + (unsigned)(XLDS + wn * 256 + l15 * 4)); });
        wait_lgkm0(); sched0();
#pragma unroll
        for (int m = 0; m < 4; ++m) {
            const f32x4 r = rs[m] * sgn;
#pragma unroll
            for (int n = 0; n < 4; ++n) acc[m][n] = (acc[m][n] - mean[m] * ws[n]) * r;
        }
#pragma unroll
        for (int n = 0; n < 4; ++n) { const int e = (tn & 7) * 128 + wn * 64 + n * 16 + l15;
#pragma unroll
            for (int mp = 0; mp < 2; ++mp) {
                unsigned x0 = pack_bf16(acc[2 * mp][n][0], acc[2 * mp][n][1]), x1 = pack_bf16(acc[2 * mp][n][2], acc[2 * mp][n][3]);
                unsigned y0 = pack_bf16(acc[2 * mp + 1][n][0], acc[2 * mp + 1][n][1]), y1 = pack_bf16(acc[2 * mp + 1][n][2], acc[2 * mp + 1][n][3]);
                swap16u(x0, y0); swap16u(x1, y1);
                const int s = (tn >> 3) * SEQ + col0 + mp * 32;
                __builtin_amdgcn_raw_buffer_store_b128((u32x4){x0, x1, y0, y1}, ro, (unsigned)(((s >> 3) * ABP + e) * 16), 0, STA);
            } }
    } else if (!recv) {
        send(0);
        wait_vm<0>();
        if (lane == 0) flag_store(flag, ep);
        if constexpr (ID == 2) bar();
    } else {
        bar();
        STAMP(4);
        const unsigned px = (unsigned)(size_t)lds + (unsigned)(XLDS + wid * 8192 + lane * 16);
        constexpr int HH = ID == 4 ? 1 : 2;
        u32x4 pw[4 * HH];
        static_for<0, 4 * HH>([&](auto ic) { pw[decltype(ic)::value] = __builtin_bit_cast(u32x4, lds_read128<decltype(ic)::value * 1024>(px)); });
        wait_lgkm0(); sched0();
#pragma unroll
        for (int m = 0; m < 4; ++m)
#pragma unroll
            for (int h = 0; h < HH; ++h) { const u32x4 w = pw[m * HH + h];
                acc[m][2 * h] += (f32x4){bf_lo(w[0]), bf_hi(w[0]), bf_lo(w[1]), bf_hi(w[1])}; acc[m][2 * h + 1] += (f32x4){bf_lo(w[2]), bf_hi(w[2]), bf_lo(w[3]), bf_hi(w[3])}; }
        const auto ro = RSRC(ID == 2 ? p.h : p.out, ID == 2 ? 136 * SEQ * 16 : SEQ * DM * 2);
#pragma unroll
        for (int n = 0; n < 2 * HH; ++n) {
            unsigned o[4][2];
#pragma unroll
            for (int m = 0; m < 4; ++m) { o[m][0] = pack_bf16(acc[m][n][0], acc[m][n][1]); o[m][1] = pack_bf16(acc[m][n][2], acc[m][n][3]); }
            if constexpr (ID == 2) {
                const f32x4 s4 = (acc[0][n] + acc[1][n]) + (acc[2][n] + acc[3][n]);
                const float mw = qsum((s4[0] + s4[1]) + (s4[2] + s4[3])) * (1.f / 64.f);
                const f32x4 d0 = acc[0][n] - mw, d1 = acc[1][n] - mw, d2 = acc[2][n] - mw, d3 = acc[3][n] - mw, q4 = (d0 * d0 + d1 * d1) + (d2 * d2 + d3 * d3);
                float m2 = (q4[0] + q4[1]) + (q4[2] + q4[3]);
                m2 = qsum(m2);
                if (q == 0) __builtin_amdgcn_raw_buffer_store_b64((u32x2){__float_as_uint(mw), __float_as_uint(m2)}, ro, (unsigned)(((128 + tm) * SEQ + row0 + n * 16) * 16 + wm * 8), 0, SC1);
            }
#pragma unroll
            for (int mp = 0; mp < 2; ++mp) {
                swap16u(o[2 * mp][0], o[2 * mp + 1][0]); swap16u(o[2 * mp][1], o[2 * mp + 1][1]);
                __builtin_amdgcn_raw_buffer_store_b128((u32x4){o[2 * mp][0], o[2 * mp][1], o[2 * mp + 1][0], o[2 * mp + 1][1]}, ro,
                    (unsigned)(ID == 4 ? ((row0 + n * 16) * DM + col0 + mp * 32) * 2 : ((col0 / 8 + mp * 4) * SEQ + row0 + n * 16) * 16), 0, SC1);
            }
        }
        STAMP(5);
        if constexpr (ID == 2) { wait_vm<0>(); bar(); }
    }
    STAMP_END(ID);
}
#endif

#if defined(__HIP_DEVICE_COMPILE__)
template <int GRP>
__device__ __forceinline__ void gemm3_body(const Params& p, LAS unsigned char* lds, const int wid, u64& ep) {
    constexpr int NK = 16, FA = 7, HB0 = 3 * 32768;
    STAMP_BEGIN;
    const int tid = threadIdx.x, lane = tid & 63;
    const int wm = wid & 1, wn = wid >> 1, l15 = lane & 15, q = lane >> 4;
    const int xcd = blockIdx.x & 7, idx = blockIdx.x >> 3, im = idx & 7, in = idx >> 3;
    const int tm = (xcd & 3) * 8 + im, tn = (xcd >> 2) * 4 + in;
    const G3W gw(p, wid, lane);
    unsigned pf = 0;
    u64* words = p.flags + (F_H + tn * 32) * FLAG_STRIDE;
    if (threadIdx.x == 0 && !(xcd & 1)) flag_store(words + tm * FLAG_STRIDE, ep);
    if (wid) __builtin_amdgcn_s_sleep(2);
    gw.early(lds, wid, 0);
    if (W1E) gw.early(lds, wid, 1);
    if ((xcd & 1) && wid == 1) colsum_final(p, lds, ep);
    if (wid == 0) {
        wait_group(words + (lane & 31) * FLAG_STRIDE, (lane & 8) == 0, ep);
        if (blockIdx.x == 0 && lane == 0) flag_store(p.flags, ep);
    }
    bar();
    const unsigned voffB = (unsigned)((wid * SEQ + tn * 256 + lane) * 16);
    struct Src { const void* base; int so; unsigned nrec; };
    auto pin = [](Src x) { asm volatile("" : "+s"(x.base), "+s"(x.so), "+s"(x.nrec)); return x; };
    auto srcW = [&](int t) { return pin(Src{t < NK ? (const void*)gw.ptr : (const void*)(p.csum + GRP * 3584), t < NK ? gw.kstep(t) * 64 * FF * 2 : 0, t < NK ? (unsigned)(DM * FF * 2) : t == NK ? (unsigned)(2 * FF * 2) : 0u}); };
    auto srcH = [&](int t) { Src x{nullptr, (t < NK ? gw.kstep(t) : NK) * 8 * SEQ * 16, 136 * SEQ * 16}; asm volatile("" : "+s"(x.so)); return x; };
    auto stageW = [&](Src x, int slot, int i) { gw.piece(lds, wid, x.base, x.so, x.nrec, slot, i); };
    auto stageH = [&](Src x, int slot, int i) { DMA16_DEV(RSRC(p.h, x.nrec), lds + HB0 + slot * 32768 + wid * 4096 + i * 1024, voffB, x.so + i * 1024); };
#pragma unroll
    for (int t = 0; t < 2; ++t) {
        const Src xh = srcH(t);
#pragma unroll
        for (int i = 0; i < 4; ++i) stageH(xh, t, i);
        if (!W1E && t == 0) gw.early(lds, wid, 1);
    }

    unsigned adA[FA];
    const unsigned lds0 = (unsigned)(size_t)lds;
#pragma unroll
    for (int m = 0; m < FA; ++m) { const int q4 = l15 >> 2, pp = lane & 3, reg = m < 6 ? m & 1 : pp >> 1, ch = m < 6 ? wm * 8 + (m & 6) + (pp >> 1) : 6 + wm;
        adA[m] = lds0 + (unsigned)(reg * 16384 + (8 * q + q4) * 256 + ((ch ^ (((q4 | ((q & 1) << 2)) << 1) | reg)) << 4) + 8 * (pp & 1)); }
    unsigned adB = lds0 + (unsigned)(HB0 + q * 4096 + (wn * 64 + l15) * 16);
    auto rd = [&](auto ic, Frag (&ra)[FA], bf16x8 (&rb)[4], auto kkc) {
        constexpr int kk = decltype(kkc)::value, i = decltype(ic)::value;
        if constexpr (i < 4) rb[i] = lds_read128<kk * 16384 + i * 256>(adB);
        else if constexpr (i % 2 == 0) ra[(i - 4) / 2].lo = tr_read<kk * 8192>(adA[(i - 4) / 2]);
        else ra[(i - 4) / 2].hi = tr_read<kk * 8192 + 1024>(adA[(i - 4) / 2]);
    };
    f32x4 acc[FA][4];
#pragma unroll
    for (int m = 0; m < FA; ++m)
#pragma unroll
        for (int n = 0; n < 4; ++n) acc[m][n] = (f32x4){0.f, 0.f, 0.f, 0.f};
#define DSTR 2
#define LASTP 3
#define PR1 18
#define PR2 6
#define PR3 22

    auto phase = [&](const Frag (&ra)[FA], const bf16x8 (&rb)[4], Frag (&na)[FA], bf16x8 (&nb)[4], int dw, int dh, auto kkc, auto grpc, auto&& dma, auto&& prep) {
        static_for<0, 28>([&](auto sc) {
            constexpr int s = decltype(sc)::value, m = s >> 2, n = s & 3;
            if constexpr (decltype(kkc)::value == 0) { if constexpr (s == 0) __builtin_amdgcn_s_setprio(3); if constexpr (s == PR1) __builtin_amdgcn_s_setprio(2); }
            else { if constexpr (s == PR2) __builtin_amdgcn_s_setprio(1); if constexpr (s == PR3) __builtin_amdgcn_s_setprio(0); }
            acc[m][n] = MFMA(ra[m].v(), rb[n], acc[m][n]);
            sched0();
            if constexpr (s < 18) rd(sc, na, nb, kkc);
            if constexpr (s == 20) prep();
            if constexpr (s % DSTR == 0 && s < 8 * DSTR && decltype(grpc)::value == ((s / DSTR) & 1)) dma(s / (2 * DSTR));
            if constexpr (decltype(kkc)::value == 1 && s >= 18 && s < 26) { if constexpr (s < 25) bump(adA[s - 18], dw); else bump(adB, dh); }
            sched0();
        });
        wait_lgkm0(); sched0();
    };
    Frag a0[FA], a1[FA]; bf16x8 b0[4], b1[4];
    constexpr std::integral_constant<int, 0> K0{}; constexpr std::integral_constant<int, 1> K1{};
    wait_vm<W1E ? 4 : 8>(); bar();
    STAMP(1);
    static_for<0, 18>([&](auto ic) { rd(ic, a0, b0, K0); });
    wait_lgkm0(); sched0();
    const unsigned pfo = (unsigned)(((blockIdx.x * 8 + wid) * 28 + (lane < 28 ? lane : 0)) * 128); const int pft = 1 + idx % 12;
    constexpr std::integral_constant<int, GRP> grpc{};
    Src xw = srcW(2), xh;
    auto phaseA = [&](int t, int sw2) {
        phase(a0, b0, a1, b1, sw2 == 1 ? -65536 : 32768, (t & 1) ? -32768 : 32768, K1, grpc, [&](int i) { stageW(xw, sw2, i);
            if (i == 3) touch(pf, pfo, p.wd, t == pft ? FF * DM * 2 : 0);
        }, [&]() { xh = srcH(t + 2); });
        wait_vm<5>();
    };
    u64 cseen = 0;
    phaseA(0, 2);
    int sw2 = 0;
#pragma nounroll
    for (int t = 0; t < NK - 1; ++t) {
        if (__builtin_expect(t >= 11 && t <= 12 && (wid & 3) == 0, 0)) {
            const int sl = min((tm * 112 >> 6) + (lane & 3), (tm * 112 + 111) >> 6);
            u64* w = p.flags + (F_CS + GRP * 56 + sl) * FLAG_STRIDE;
            if (t == 11) flag_peek(cseen, w); else if (__any(cseen != ep)) wait_group(w, true, ep);
        }
        bar();
        phase(a1, b1, a0, b0, 0, 0, K0, grpc, [&](int i) { stageH(xh, t & 1, i); }, [&]() { xw = srcW(t + 3); });
        phaseA(t + 1, sw2);
        sw2 = sw2 == 2 ? 0 : sw2 + 1;
    }
    bar();
    __builtin_amdgcn_s_setprio(GRP ? 0 : LASTP);
    f32x4 csv[FA], sp[4][2];
    static_for<0, 16>([&](auto sc) {
        constexpr int s = decltype(sc)::value;
        acc[s >> 2][s & 3] = MFMA(a1[s >> 2].v(), b1[s & 3], acc[s >> 2][s & 3]);
        if constexpr (s < 8) sp[s >> 1][s & 1] = __builtin_bit_cast(f32x4, lds_read128<(s & 1) * 16384 + (s >> 1) * 256>(adB));
        else if constexpr (s < 15) { constexpr int m = s - 8; const int reg = m < 6 ? m & 1 : q >> 1, c4 = m < 6 ? wm * 2 + (m & 6) * 2 + q : (wm ? 0 : 12) + (q & 1);
            csv[m] = __builtin_bit_cast(f32x4, lds_read128<32768>(lds0 + (unsigned)(reg * 16384 + wm * 256 + ((c4 ^ ((wm << 1) | reg)) << 4)))); }
        if constexpr (GRP == 1 && s == 2) g128_load<4, 0>(p, lds, wid, ep);
        sched0();
    });
    wait_lgkm0(); wait_vm<GRP ? 4 : 0>(); asm volatile("; pf %0" ::"v"(pf)); sched0();
    float mean[4], c1[4], ve[4];
#pragma unroll
    for (int n = 0; n < 4; ++n) {
        const f32x4 s0 = sp[n][0], s1 = sp[n][1];
        mean[n] = qsum((s0[0] + s0[2]) + (s1[0] + s1[2])) * (1.f / 16.f);
        const float d0 = s0[0] - mean[n], d1 = s0[2] - mean[n], d2 = s1[0] - mean[n], d3 = s1[2] - mean[n];
        const float m2 = qsum((s0[1] + s0[3]) + (s1[1] + s1[3]) + 64.f * ((d0 * d0 + d1 * d1) + (d2 * d2 + d3 * d3)));
        ve[n] = m2 * (1.f / DM) + LN_EPS; c1[n] = -1.44269504f * __builtin_amdgcn_rsqf(ve[n]);
    }
#pragma unroll
    for (int s = 16; s < 28; ++s) acc[s >> 2][s & 3] = MFMA(a1[s >> 2].v(), b1[s & 3], acc[s >> 2][s & 3]);
#pragma unroll
    for (int s = 16; s < 28; ++s) { __builtin_amdgcn_sched_group_barrier(0x008, 1, 0); __builtin_amdgcn_sched_group_barrier(0x002, 10, 0); }
    sched0();
    STAMP(2);
    const auto ro = RSRC(p.act, SEQ * FF * 2);
    auto silu2 = [&](f32x2 g, f32x2 u, int n) {
        const f32x2 x = g * c1[n], d = (f32x2){__builtin_amdgcn_exp2f(x[0]), __builtin_amdgcn_exp2f(x[1])} * ve[n] + ve[n];
        const f32x2 r = g * u * (f32x2){__builtin_amdgcn_rcpf(d[0]), __builtin_amdgcn_rcpf(d[1])};
        return pack_bf16(r[0], r[1]);
    };
#pragma unroll
    for (int j = 0; j < 3; ++j)
#pragma unroll
        for (int np = 0; np < 2; ++np) {
            unsigned o[2][2];
#pragma unroll
            for (int i = 0; i < 2; ++i) { const int n = 2 * np + i; const f32x4 g = acc[2 * j][n] - mean[n] * csv[2 * j], u = acc[2 * j + 1][n] - mean[n] * csv[2 * j + 1];
                o[i][0] = silu2((f32x2){g[0], g[1]}, (f32x2){u[0], u[1]}, n); o[i][1] = silu2((f32x2){g[2], g[3]}, (f32x2){u[2], u[3]}, n); }
            swap16u(o[0][0], o[1][0]); swap16u(o[0][1], o[1][1]);
            __builtin_amdgcn_raw_buffer_store_b128((u32x4){o[0][0], o[0][1], o[1][0], o[1][1]}, ro, (unsigned)(((tm * 14 + wm * 8 + 2 * j + (q >> 1)) * SEQ + tn * 256 + wn * 64 + np * 32 + (lane & 31)) * 16), 0, STA);
        }
    {
        unsigned o[4];
#pragma unroll
        for (int n = 0; n < 4; ++n) {
            const f32x4 v = acc[6][n] - mean[n] * csv[6];
            float g0 = v[0], g1 = v[1], u0 = v[2], u1 = v[3];
            swap32(g0, u0); swap32(g1, u1);
            o[n] = silu2((f32x2){g0, g1}, (f32x2){u0, u1}, n);
        }
        swap16u(o[0], o[1]); swap16u(o[2], o[3]); swap32u(o[0], o[2]); swap32u(o[1], o[3]);
        __builtin_amdgcn_raw_buffer_store_b128((u32x4){o[0], o[2], o[1], o[3]}, ro, (unsigned)(((tm * 14 + 6 + wm) * SEQ + tn * 256 + wn * 64 + lane) * 16), 0, STA);
    }
    STAMP(5);
    __builtin_amdgcn_s_setprio(0);
    if constexpr (GRP == 1) { wait_vm<0>(); bar(); }
    STAMP_END(3);
}
#endif

__global__ __launch_bounds__(512, 2) void fused_k(const Params p) {
#if defined(__HIP_DEVICE_COMPILE__)
    __shared__ __attribute__((aligned(16))) unsigned char lds_raw[5 * 32768];
    LAS unsigned char* lds = (LAS unsigned char*)lds_raw;
    const int wid = __builtin_amdgcn_readfirstlane(threadIdx.x >> 6);
    u64 ep = 0;
    if (wid < 4) { g128_compute<1>(p, lds, wid, ep); g128_compute<2>(p, lds, wid, ep); gemm3_body<0>(p, lds, wid, ep); g128_compute<4>(p, lds, wid, ep); }
    else { g128_load<1, -1>(p, lds, wid, ep); g128_load<2, -1>(p, lds, wid, ep); gemm3_body<1>(p, lds, wid, ep); g128_load<4, -1>(p, lds, wid, ep); }
#endif
}
__global__ void tail_k() {}

extern "C" void kernel_launch(void* const* d_in, const int* in_sizes, int n_in, void* d_out, int out_size, void* d_ws, size_t ws_size, hipStream_t stream) {
    Params p;
    p.x = (const u16*)d_in[0]; p.cre = (const u16*)d_in[1]; p.cim = (const u16*)d_in[2]; p.wre = (const u16*)d_in[3]; p.wim = (const u16*)d_in[4];
    p.wg = (const u16*)d_in[6]; p.wu = (const u16*)d_in[7]; p.wd = (const u16*)d_in[8];
    p.out = (u16*)d_out;
    char* ws = (char*)d_ws;
    p.abT = (u16*)(ws + (ABP == 1024 ? 0u : 48u << 20)); p.h = (u16*)(ws + (8u << 20)); p.act = (u16*)(ws + (16u << 20));
    p.xch = (u16*)(ws + (32u << 20)); p.flags = (unsigned long long*)(ws + (40u << 20)); p.csum = (float*)(ws + (40u << 20) + (512u << 10)); p.ln1 = (float*)(ws + (40u << 20) + (768u << 10));
    fused_k<<<256, 512, 0, stream>>>(p);
}
```

```cpp
#include <hip/hip_runtime.h>
#include <hip/hip_bf16.h>
#include <type_traits>

#define LAS __attribute__((address_space(3)))
typedef unsigned short u16;
typedef __bf16 bf16x8 __attribute__((ext_vector_type(8)));
typedef __bf16 bf16x2 __attribute__((ext_vector_type(2)));
typedef float f32x4 __attribute__((ext_vector_type(4)));
typedef float f32x2 __attribute__((ext_vector_type(2)));
typedef unsigned u32x4 __attribute__((ext_vector_type(4)));
typedef unsigned u32x2 __attribute__((ext_vector_type(2)));

constexpr int SEQ = 2048, DM = 1024, FF = 3584;
#define ABP 1024
constexpr int ABT_BYTES = 512 * ABP * 16;
#define STA 16
constexpr float LN_EPS = 1e-5f;
constexpr int SC1 = 16;

struct Params {
    const u16* x; const u16* cre; const u16* cim; const u16* wre; const u16* wim;
    const u16* wg; const u16* wu; const u16* wd;
    u16* out; u16* abT; u16* h; u16* act; u16* xch; unsigned long long* flags; float* csum; float* ln1;
};

template <int N> __device__ __forceinline__ void wait_vm() { asm volatile("s_waitcnt vmcnt(%0)" ::"n"(N) : "memory"); }
__device__ __forceinline__ void wait_lgkm0() { __builtin_amdgcn_s_waitcnt(0xC07F); asm volatile("s_waitcnt lgkmcnt(0)" ::: "memory"); }
__device__ __forceinline__ void sched0() { __builtin_amdgcn_sched_barrier(0); }
__device__ __forceinline__ void bar() { sched0(); asm volatile("" ::: "memory"); __builtin_amdgcn_s_barrier(); asm volatile("" ::: "memory"); sched0(); }
__device__ __forceinline__ unsigned pack_bf16(float lo, float hi) { bf16x2 c; c[0] = (__bf16)lo; c[1] = (__bf16)hi; return __builtin_bit_cast(unsigned, c); }
__device__ __forceinline__ float bf_lo(unsigned v) { return __uint_as_float(v << 16); }
__device__ __forceinline__ float bf_hi(unsigned v) { return __uint_as_float(v & 0xffff0000u); }
__device__ __forceinline__ void swap16(float& a, float& b) { auto r = __builtin_amdgcn_permlane16_swap(__float_as_uint(a), __float_as_uint(b), false, false); a = __uint_as_float(r[0]); b = __uint_as_float(r[1]); }
__device__ __forceinline__ void swap32(float& a, float& b) { auto r = __builtin_amdgcn_permlane32_swap(__float_as_uint(a), __float_as_uint(b), false, false); a = __uint_as_float(r[0]); b = __uint_as_float(r[1]); }
__device__ __forceinline__ void swap16u(unsigned& a, unsigned& b) { auto r = __builtin_amdgcn_permlane16_swap(a, b, false, false); a = r[0]; b = r[1]; }
__device__ __forceinline__ void swap32u(unsigned& a, unsigned& b) { auto r = __builtin_amdgcn_permlane32_swap(a, b, false, false); a = r[0]; b = r[1]; }
__device__ __forceinline__ float qsum(float v) { float a = v, b = v; swap16(a, b); a += b; b = a; swap32(a, b); return a + b; }

template <int CTRL> __device__ __forceinline__ float dpp_add(float v) { return v + __int_as_float(__builtin_amdgcn_update_dpp(0, __float_as_int(v), CTRL, 0xf, 0xf, false)); }
__device__ __forceinline__ float hsum(float v) { v = dpp_add<0x128>(v); v = dpp_add<0x124>(v); v = dpp_add<0x122>(v); v = dpp_add<0x121>(v); float b = v; swap16(v, b); return v + b; }

struct Frag { u32x2 lo, hi; __device__ __forceinline__ bf16x8 v() const { return __builtin_bit_cast(bf16x8, __builtin_shufflevector(lo, hi, 0, 1, 2, 3)); } };
template <int OFF> __device__ __forceinline__ u32x2 tr_read(unsigned addr) { u32x2 r; asm volatile("ds_read_b64_tr_b16 %0, %1 offset:%2" : "=v"(r) : "v"(addr), "n"(OFF) : "memory"); return r; }
template <int OFF> __device__ __forceinline__ bf16x8 lds_read128(unsigned addr) { bf16x8 r; asm volatile("ds_read_b128 %0, %1 offset:%2" : "=v"(r) : "v"(addr), "n"(OFF) : "memory"); return r; }
template <int OFF> __device__ __forceinline__ float lds_read32(unsigned addr) { float r; asm volatile("ds_read_b32 %0, %1 offset:%2" : "=v"(r) : "v"(addr), "n"(OFF) : "memory"); return r; }
template <int N> __device__ __forceinline__ void wait_lgkm() { asm volatile("s_waitcnt lgkmcnt(%0)" ::"n"(N) : "memory"); }
template <int I, int N, class F> __device__ __forceinline__ void static_for(F&& f) { if constexpr (I < N) { f(std::integral_constant<int, I>{}); static_for<I + 1, N>(f); } }
template <int NA> __device__ __forceinline__ void keep(const Frag (&a)[NA], const bf16x8 (&b)[4]) {
#pragma unroll
    for (int i = 0; i < NA; ++i) asm volatile("" ::"v"(a[i].lo), "v"(a[i].hi));
#pragma unroll
    for (int i = 0; i < 4; ++i) asm volatile("" ::"v"(b[i]));
}
__device__ __forceinline__ void bump(unsigned& a, int d) { asm volatile("v_add_u32 %0, %1, %0" : "+v"(a) : "s"(d)); }
#define MFMA(a, b, c) __builtin_amdgcn_mfma_f32_16x16x32_bf16(a, b, c, 0, 0, 0)
#define RSRC(ptr, bytes) __builtin_amdgcn_make_buffer_rsrc((void*)(ptr), 0, (bytes), 0x00020000)
#define DMA16(rs, ldsp, voff, soff) __builtin_amdgcn_raw_ptr_buffer_load_lds(rs, (LAS void*)(ldsp), 16, voff, soff, 0, 0)
#define DMA16_DEV(rs, ldsp, voff, soff) __builtin_amdgcn_raw_ptr_buffer_load_lds(rs, (LAS void*)(ldsp), 16, voff, soff, 0, SC1)

#define STAMP(i)
#define STAMP_BEGIN
#define STAMP_END(ID)

typedef unsigned long long u64;
#define FSTR 8
constexpr int FLAG_STRIDE = FSTR, F_PAIR = 1, F_AB = F_PAIR + 1536, F_H = F_AB + 256, F_ACT = F_H + 256, F_CS = F_ACT + 256, F_LN = F_CS + 128;
#define PSCOPE __HIP_MEMORY_SCOPE_AGENT
#define PSLEEP 2
__device__ __forceinline__ u64 flag_load(u64* w) { return __hip_atomic_load(w, __ATOMIC_RELAXED, PSCOPE); }
__device__ __forceinline__ void flag_store(u64* w, u64 v) { __hip_atomic_store(w, v, __ATOMIC_RELAXED, PSCOPE); }
__device__ __forceinline__ u64 uniform64(u64 v) { return ((u64)(unsigned)__builtin_amdgcn_readfirstlane((unsigned)(v >> 32)) << 32) | (unsigned)__builtin_amdgcn_readfirstlane((unsigned)v); }
__device__ __forceinline__ void wait_group(u64* word, bool need, u64 ep) {
#pragma nounroll
    for (int spin = 0; spin < (1 << 20); ++spin) {
        if (!__any(need && flag_load(word) != ep)) break;
        __builtin_amdgcn_s_sleep(PSLEEP);
    }
}
#if defined(__HIP_DEVICE_COMPILE__)
__device__ __forceinline__ void touch(unsigned& sink, unsigned voff, const void* base, unsigned bytes) {
    asm volatile("s_nop 4\n\tbuffer_load_dword %0, %1, %2, 0 offen" : "+v"(sink) : "v"(voff), "s"(RSRC(base, bytes)) : "memory");
}
__device__ __forceinline__ void flag_peek(u64& v, const u64* w) { asm volatile("global_load_dwordx2 %0, %1, off sc1" : "+v"(v) : "v"(w) : "memory"); }
struct G3W {
    const u16* ptr; unsigned voff[2]; int phi, koff;
    __device__ __forceinline__ G3W(const Params& p, int wid, int lane) {
        const int xcd = blockIdx.x & 7, idx = blockIdx.x >> 3, tm = (xcd & 3) * 8 + (idx & 7), q = lane >> 4;
        ptr = wid < 4 ? p.wg : p.wu; phi = ((idx & 7) + (idx >> 3)) & 3; koff = xcd * 2;
#pragma unroll
        for (int i = 0; i < 2; ++i) { const int j = (lane & 15) ^ (((q | (i << 2)) << 1) | (wid >> 2));
            voff[i] = j < 14 ? (unsigned)(((wid & 3) * 16 + q) * FF * 2 + tm * 224 + j * 16) : 0x7ffffff0u; }
    }
    __device__ __forceinline__ int kstep(int t) const { return (t + koff + ((t & 3) == phi ? 4 : 0)) & 15; }
    __device__ __forceinline__ void piece(LAS unsigned char* lds, int wid, const void* base, int so, unsigned nrec, int slot, int i) const { DMA16_DEV(RSRC(base, nrec), lds + slot * 32768 + wid * 4096 + i * 1024, voff[i >> 1], so + i * 4 * FF * 2); }
    __device__ __forceinline__ void early(LAS unsigned char* lds, int wid, int t) const {
#pragma unroll
        for (int i = 0; i < 4; ++i) piece(lds, wid, ptr, kstep(t) * 64 * FF * 2, DM * FF * 2, t, i);
    }
};
#define CSW 4
#define CSD 0
__device__ __forceinline__ void colsum_partial(const Params& p, LAS unsigned char* lds, int lw) {
    const int lane = threadIdx.x & 63, slab = blockIdx.x >> 1, mat = slab >= 56, c0 = (slab - mat * 56) * 64;
    const auto rw = RSRC(mat ? p.wu : p.wg, slab < 112 ? DM * FF * 2 : 0);
    const unsigned vo = (unsigned)(((lw * 256 + (lane >> 3)) * FF + c0 + (lane & 7) * 8) * 2);
    u32x4 v[8]; f32x4 s0 = {0.f, 0.f, 0.f, 0.f}, s1 = s0;
    auto add = [&](const u32x4 w) { s0 += (f32x4){bf_lo(w[0]), bf_hi(w[0]), bf_lo(w[1]), bf_hi(w[1])}; s1 += (f32x4){bf_lo(w[2]), bf_hi(w[2]), bf_lo(w[3]), bf_hi(w[3])}; };
    if (CSD) __builtin_amdgcn_s_sleep(CSD);
#pragma unroll
    for (int i = 0; i < CSW; ++i) v[i] = __builtin_amdgcn_raw_buffer_load_b128(rw, vo, i * 8 * FF * 2, 0);
#pragma unroll
    for (int i = 0; i < 8; ++i) { add(v[i % CSW]); v[i % CSW] = __builtin_amdgcn_raw_buffer_load_b128(rw, vo, (CSW + i) * 8 * FF * 2, 0); sched0(); }
#pragma unroll
    for (int i = CSW; i < 8; ++i) v[i] = __builtin_amdgcn_raw_buffer_load_b128(rw, vo, (8 + i) * 8 * FF * 2, 0);
#pragma nounroll
    for (int j = 16; j < 40; j += 8)
#pragma unroll
        for (int i = 0; i < 8; ++i) { add(v[i]); v[i] = __builtin_amdgcn_raw_buffer_load_b128(rw, j < 32 ? vo : 0x7ffffff0u, (j + i) * 8 * FF * 2, 0); sched0(); }
    LAS float* tab = (LAS float*)(lds + 131072) + (lw * 8 + (lane >> 3)) * 64 + (lane & 7) * 8;
    *(LAS f32x4*)tab = s0; *(LAS f32x4*)(tab + 4) = s1;
    wait_lgkm0();
}
__device__ __forceinline__ void colsum_final(const Params& p, LAS unsigned char* lds, u64 ep) {
    const int lane = threadIdx.x & 63, slab = blockIdx.x >> 1, mat = slab >= 56, c0 = (slab - mat * 56) * 64;
    if (slab >= 112) return;
    const LAS float* tab = (const LAS float*)(lds + 131072);
    float a = 0.f, b = 0.f;
#pragma unroll
    for (int i = 0; i < 32; i += 2) { a += tab[i * 64 + lane]; b += tab[(i + 1) * 64 + lane]; }
    const int f = c0 + lane, t = f / 112, r = f - t * 112, h = r >= 56;
    __hip_atomic_store(p.csum + (mat * 2 + h) * 1792 + t * 56 + r - h * 56, a + b, __ATOMIC_RELAXED, __HIP_MEMORY_SCOPE_AGENT);
    wait_vm<0>();
    if (lane == 0) flag_store(p.flags + (F_CS + slab) * FLAG_STRIDE, ep);
}
#endif

enum { ROW, CHK, TR };
#define NKS2 27
#define FLAGD 1
#define W1E 1
#define LNT0 1
#define LNT1 5
#define NPRE2 1
#define NKS4 24
template <int ID> struct Cfg;
template <> struct Cfg<1> { static constexpr int NKT = 16, NKS = 16, FA = ROW, FB = ROW, LDB = DM; };
template <> struct Cfg<2> { static constexpr int NKT = 64, NKS = NKS2, FA = CHK, FB = ROW, LDB = SEQ; };
template <> struct Cfg<4> { static constexpr int NKT = 56, NKS = NKS4, FA = TR, FB = CHK, LDB = 0; };

#if defined(__HIP_DEVICE_COMPILE__)
template <int ID, int PRE>
__device__ __forceinline__ void g128_load(const Params& p, LAS unsigned char* lds, const int wid, u64& ep) {
    using C = Cfg<ID>;
    constexpr int STAGE = 32768, XLDS = 4 * STAGE;
    const int lane = threadIdx.x & 63, l15 = lane & 15, q = lane >> 4;
    const int xcd = blockIdx.x & 7, idx = blockIdx.x >> 3, im = idx & 3, in = idx >> 2;
    constexpr int NH = ID == 4 ? C::NKT - 2 * C::NKS : 0;
    const int ks = (xcd & 1) ^ (ID == 2); const bool recv = ID == 4 || (ID == 2 && ks);
    const int tm = (ID == 1 ? (xcd & 3) : ((xcd >> 1) & 1)) * 4 + im, tn = (xcd >> 2) * 8 + in;
    const int k0 = ID != 1 && ks ? C::NKS : 0, nk = ID == 2 && ks ? C::NKT - C::NKS : C::NKS;
    const int xb = ID == 4 ? 128 + (blockIdx.x ^ (wid >> 2)) : blockIdx.x >> 1;
    u64* const flag = p.flags + (F_PAIR + xb * 4 + (wid & 3)) * FLAG_STRIDE;
    const unsigned xoff = (unsigned)(ID == 4 ? (128 + xb) * 16384 + (wid & 3) * 4096 : xb * 32768 + (wid & 3) * 8192);
    const int lw = wid - 4, phi = (im + in) & 3;
    const int koff = ID == 1 ? xcd * 2 : ((xcd >> (ID == 2 ? 1 : 2)) & 1) * (nk >> 1);
    const unsigned ksw = (unsigned)(((lane & 7) ^ (lane >> 3)) << 4);
    unsigned voffA[2], voffB;
    if constexpr (C::FA == ROW) voffA[0] = (unsigned)((tm * 128 + lw * 32 + (lane >> 3)) * DM * 2) + ksw;
    if constexpr (C::FA == CHK) voffA[0] = (unsigned)((lw * 2 * ABP + tm * 128 + lane) * 16);
    if constexpr (C::FA == TR) {
#pragma unroll
        for (int i = 0; i < 2; ++i) voffA[i] = (unsigned)((lw * 16 + q) * DM * 2 + tm * 256 + ((l15 ^ ((q | (i << 2)) << 1)) << 4));
    }
    if constexpr (C::FB == ROW) voffB = (unsigned)((((ID == 1 ? tn & 7 : tn)) * 128 + lw * 32 + (lane >> 3)) * C::LDB * 2) + ksw;
    else voffB = (unsigned)((lw * 2 * SEQ + tn * 128 + lane) * 16);
    const auto ra = ID == 1 ? RSRC(p.x, SEQ * DM * 2) : ID == 2 ? RSRC(p.abT, ABT_BYTES) : RSRC(p.wd, FF * DM * 2);
    auto stage = [&](int t, bool doA, bool doB, bool half = false) {
        int k = (t & 3) != phi ? t : t + 4 < nk ? t + 4 : phi;
        k += koff; k = k0 + (k >= nk ? k - nk : k);
        if (half) { k = t - nk + phi; k = 2 * C::NKS + (k >= NH ? k - NH : k); }
        const auto rb = ID == 1 ? RSRC(tn < 8 ? p.wre : p.wim, DM * DM * 2) : ID == 2 ? RSRC(k < 32 ? p.cre : p.cim, SEQ * SEQ * 2) : RSRC(p.act, SEQ * FF * 2);
        LAS unsigned char* base = lds + (t & 3) * STAGE + lw * 4096;
#pragma unroll
        for (int i = 0; i < 4; ++i) {
            if (doA) {
                if constexpr (C::FA == ROW) DMA16(ra, base + i * 1024, voffA[0], k * 128 + i * 8 * DM * 2);
                if constexpr (C::FA == CHK) DMA16_DEV(ra, base + i * 1024, voffA[0], (k * 8 + (i >> 1)) * ABP * 16 + (i & 1) * 1024);
                if constexpr (C::FA == TR) DMA16(ra, base + i * 1024, voffA[i >> 1], (k * 64 + i * 4) * DM * 2);
            }
            if (doB) {
                if constexpr (C::FB == ROW) DMA16(rb, base + 16384 + i * 1024, voffB, (k & 31) * 128 + i * 8 * C::LDB * 2);
                else if (!(half && (i & 1))) DMA16_DEV(rb, base + 16384 + i * 1024, voffB, (k * 8 + (i >> 1)) * SEQ * 16 + ((i & 1) ^ ks) * 1024);
            }
        }
    };
    if constexpr (PRE >= 0) { stage(PRE, ID == 4, ID == 2); return; }
    if constexpr (ID != 1) {
        u64* words = p.flags + (ID == 2 ? F_AB + tm * 32 : F_ACT + (tn >> 1) * 32) * FLAG_STRIDE;
        if (wid == 4 && lane == 0) flag_store(ID == 2 ? p.flags + (F_AB + (tn & 7) * 32 + (tn >> 3) * 16 + (xcd & 3) * 4 + im) * FLAG_STRIDE : words + ((xcd & 3) * 8 + (idx & 7)) * FLAG_STRIDE, ep);
        if constexpr (ID == 4) { if (wid != 4) __builtin_amdgcn_s_sleep(2); stage(1, true, false); stage(2, true, false); }
        if constexpr (ID == 2 && NPRE2 < 3) { if (wid != 4) __builtin_amdgcn_s_sleep(2); if (NPRE2 < 2) stage(1, false, true); stage(2, false, true); }
        if (wid == 4) { const int j = lane & 31; wait_group(words + j * FLAG_STRIDE, ID == 2 ? 2 * j + 2 > k0 && 2 * j < k0 + nk : j * 14 + 14 > k0 * 8 && (j * 14 < (k0 + nk) * 8 || j * 14 + 14 > 2 * C::NKS * 8), ep); }
        bar();
        stage(0, ID == 2, ID == 4); stage(1, ID == 2, ID == 4); stage(2, ID == 2, ID == 4);
        wait_vm<8>(); bar();
        wait_vm<4>();
    } else {
        stage(0, true, true); stage(1, true, true);
        wait_vm<8>(); bar();
        stage(2, true, true);
        const unsigned a = (unsigned)(size_t)lds + XLDS;
        const float e0 = lds_read32<3072>(a), e1 = lds_read32<3076>(a);
        wait_lgkm0(); sched0();
        ep = uniform64(((u64)__float_as_uint(e1) << 32) | __float_as_uint(e0));
    }
#pragma nounroll
    for (int t = 0; t < nk - 3; ++t) {
        wait_vm<8>(); bar();
        stage(t + 3, true, true);
        if constexpr (ID == 1) { if (t == LNT1 && wid == 4 && lane == 0) flag_store(p.flags + (F_LN + tm * 16 + tn) * FLAG_STRIDE, ep); }
    }
    if constexpr (NH > 0) {
        wait_vm<8>(); bar(); stage(nk, true, true, true);
#pragma nounroll
        for (int t = nk - 2; t < nk + NH - 3; ++t) { wait_vm<6>(); bar(); stage(t + 3, true, true, true); }
    }
    bool got = !recv;
    auto fetch = [&](u64 seen) {
        if (uniform64(seen) != ep) return false;
        asm volatile("" ::: "memory");
#pragma unroll
        for (int i = 0; i < (ID == 4 ? 4 : 8); ++i) DMA16_DEV(RSRC(p.xch, 256 * 32768), lds + XLDS + lw * 8192 + i * 1024, (unsigned)(lane * 16), xoff + i * 1024);
        return true;
    };
    wait_vm<NH ? 6 : 8>(); bar();
    u64* const lnw = p.flags + (F_LN + (lane & 16 ? l15 * 16 + tn : tm * 16 + l15)) * FLAG_STRIDE;
    u64 seen0 = flag_load(ID == 1 ? lnw : flag);
    wait_vm<0>();
    if (!got) got = fetch(seen0);
    if constexpr (ID == 1) { if (wid == 4) {
        if (__any(seen0 != ep)) wait_group(lnw, true, ep);
        asm volatile("" ::: "memory");
#pragma unroll
        for (int i = 0; i < 3; ++i) DMA16_DEV(RSRC(p.ln1, 3 * SEQ * 4), lds + XLDS + i * 1024, lane < 32 ? (unsigned)(lane * 16) : 0x7ffffff0u, (i * SEQ + (i < 2 ? tm : tn) * 128) * 4);
    } }
    if constexpr (ID == 1) g128_load<2, 0>(p, lds, wid, ep);
    bar();
    if constexpr (ID != 1) seen0 = flag_load(flag);
    if constexpr (ID == 2) { if (!got) got = fetch(seen0); }
    if constexpr (ID == 1 && NPRE2 > 1) g128_load<2, 1>(p, lds, wid, ep);
    bar();
    if (!got) got = fetch(seen0);
    if constexpr (ID == 1 && NPRE2 > 2) g128_load<2, 2>(p, lds, wid, ep);
    if (recv) {
#pragma nounroll
        for (int spin = 0; !got && spin < (1 << 20); ++spin) { got = fetch(flag_load(flag)); __builtin_amdgcn_s_sleep(1); }
        wait_vm<0>(); bar();
    }
    if constexpr (ID == 2) { if (!recv) colsum_partial(p, lds, lw); }
    if constexpr (ID == 1) { wait_vm<4 * NPRE2>(); bar(); }
    if constexpr (ID != 4) bar();
}

template <int ID>
__device__ __forceinline__ void g128_compute(const Params& p, LAS unsigned char* lds, const int wid, u64& ep) {
    using C = Cfg<ID>;
    constexpr int STAGE = 32768, XLDS = 4 * STAGE;
    const int lane = threadIdx.x & 63, l15 = lane & 15, q = lane >> 4;
    const int xcd = blockIdx.x & 7, idx = blockIdx.x >> 3, im = idx & 3, in = idx >> 2;
    constexpr int NH = ID == 4 ? C::NKT - 2 * C::NKS : 0;
    const int ks = (xcd & 1) ^ (ID == 2); const bool recv = ID == 4 || (ID == 2 && ks);
    const int tm = (ID == 1 ? (xcd & 3) : ((xcd >> 1) & 1)) * 4 + im, tn = (xcd >> 2) * 8 + in;
    const int k0 = ID != 1 && ks ? C::NKS : 0, nk = ID == 2 && ks ? C::NKT - C::NKS : C::NKS;
    const int xb = ID == 4 ? 128 + (blockIdx.x ^ (wid >> 2)) : blockIdx.x >> 1;
    u64* const flag = p.flags + (F_PAIR + xb * 4 + (wid & 3)) * FLAG_STRIDE;
    const unsigned xoff = (unsigned)(ID == 4 ? (128 + xb) * 16384 + (wid & 3) * 4096 : xb * 32768 + (wid & 3) * 8192);
    STAMP_BEGIN;
    const int wm = wid & 1, wn = wid >> 1;
    const int eo = (q & 1) * 16 + (q >> 1) * 8;
    f32x4 acc[4][4];
#pragma unroll
    for (int m = 0; m < 4; ++m)
#pragma unroll
        for (int n = 0; n < 4; ++n) acc[m][n] = (f32x4){0.f, 0.f, 0.f, 0.f};
    const int row0 = tn * 128 + (ID == 4 ? ks * 64 + wn * 32 : wn * 64) + l15, col0 = tm * 128 + wm * 64 + eo;
    if constexpr (ID != 1) {
        constexpr int NN = ID == 4 ? 2 : 4;
        if (ID == 4 || !recv) {
            if constexpr (ID == 2) { wait_vm<0>(); bar(); __builtin_amdgcn_s_sleep(4); }
            u32x4 r[NN][2];
#pragma unroll
            for (int n = 0; n < NN; ++n)
#pragma unroll
                for (int mp = 0; mp < 2; ++mp)
                    r[n][mp] = __builtin_amdgcn_raw_buffer_load_b128(RSRC(ID == 2 ? p.x : p.h, SEQ * DM * 2), (unsigned)(ID == 2 ? ((row0 + n * 16) * DM + col0 + mp * 32) * 2 : ((col0 / 8 + mp * 4) * SEQ + row0 + n * 16) * 16), 0, ID == 4 ? SC1 : 0);
            if constexpr (ID == 4) { wait_vm<4>(); bar(); }
#pragma unroll
            for (int n = 0; n < NN; ++n)
#pragma unroll
                for (int mp = 0; mp < 2; ++mp) {
                    unsigned r0 = r[n][mp][0], r1 = r[n][mp][1], r2 = r[n][mp][2], r3 = r[n][mp][3];
                    swap16u(r0, r2); swap16u(r1, r3);
                    acc[2 * mp][n] = (f32x4){bf_lo(r0), bf_hi(r0), bf_lo(r1), bf_hi(r1)}; acc[2 * mp + 1][n] = (f32x4){bf_lo(r2), bf_hi(r2), bf_lo(r3), bf_hi(r3)};
                }
        } else { wait_vm<0>(); bar(); }
    }
    unsigned pfc = 0;
    if constexpr (ID == 1) {
        ep = uniform64(flag_load(p.flags)) + 1;
        if (threadIdx.x == 0) *(LAS u64*)(lds + XLDS + 3072) = ep;
        wait_lgkm0();
        if (idx == 0) {
            unsigned long long pc; int lo, hi;
            asm volatile("s_getpc_b64 %0\n s_mov_b32 %1, _Z7fused_k6Params@rel32@lo+4\n s_mov_b32 %2, _Z6tail_kv@rel32@lo+12" : "=s"(pc), "=s"(lo), "=s"(hi));
            touch(pfc, threadIdx.x * 128, (const void*)(pc + (long long)lo), (unsigned)max(hi - lo, 0)); touch(pfc, threadIdx.x * 128 + 32768, (const void*)(pc + (long long)lo), (unsigned)max(hi - lo, 0));
        }
    }
    constexpr int NA = C::FA == CHK ? 1 : C::FA == ROW ? 2 : 4, NB = C::FB == CHK ? 1 : 2;
    unsigned adA[NA], adB[NB];
    const unsigned lds0 = (unsigned)(size_t)lds, row_sw = lds0 + (unsigned)(l15 * 128 + ((q ^ (lane & 7)) << 4)), chk = lds0 + (unsigned)(q * 2048 + l15 * 16);
#pragma unroll
    for (int i = 0; i < NA; ++i) {
        if constexpr (C::FA == ROW) adA[i] = (row_sw ^ (unsigned)(i * 64)) + (unsigned)(wm * 8192);
        if constexpr (C::FA == CHK) adA[i] = chk + (unsigned)(wm * 1024);
        if constexpr (C::FA == TR) { const int q4 = l15 >> 2; adA[i] = lds0 + (unsigned)((8 * q + q4) * 256 + (((wm * 4 + i) ^ (q4 | ((q & 1) << 2))) << 5) + 8 * (lane & 3)); }
    }
#pragma unroll
    for (int i = 0; i < NB; ++i) adB[i] = C::FB == CHK ? 16384u + chk + (unsigned)(wn * 512) : 16384u + (row_sw ^ (unsigned)(i * 64)) + (unsigned)(wn * 8192);
    constexpr int W = C::FA == TR ? 2 : 1, NR = 4 + 4 * W;
    auto rd = [&](auto ic, Frag (&ra)[4], bf16x8 (&rb)[4], auto kkc) {
        constexpr int kk = decltype(kkc)::value, i = decltype(ic)::value;
        if constexpr (i == 0 || (i > W && i <= W + 3)) { constexpr int n = i == 0 ? 0 : i - W;
            if constexpr (C::FB == CHK) rb[n] = lds_read128<kk * 8192 + (n >> 1) * 1024 + (n & 1) * 256>(adB[0]);
            else rb[n] = lds_read128<n * 2048>(adB[kk]); }
        else { constexpr int j = i <= W ? i - 1 : i - 4, m = j / W;
            if constexpr (C::FA != TR) { bf16x8 v;
                if constexpr (C::FA == CHK) v = lds_read128<kk * 8192 + m * 256>(adA[0]); else v = lds_read128<m * 2048>(adA[kk]);
                const u32x4 w = __builtin_bit_cast(u32x4, v); ra[m].lo = (u32x2){w[0], w[1]}; ra[m].hi = (u32x2){w[2], w[3]}; }
            else if constexpr (j % 2 == 0) ra[m].lo = tr_read<kk * 8192>(adA[m]);
            else ra[m].hi = tr_read<kk * 8192 + 1024>(adA[m]); }
    };
    auto phase = [&](const Frag (&ra)[4], const bf16x8 (&rb)[4], Frag (&na)[4], bf16x8 (&nb)[4], int dstep, auto kkc) {
        static_for<0, 16>([&](auto sc) {
            constexpr int s = decltype(sc)::value, m = s >> 2, n = s & 3;
            if constexpr (n == 0) { wait_lgkm<(3 - m) * W + (s < NR ? s : NR)>(); sched0(); }
            acc[m][n] = MFMA(ra[m].v(), rb[n], acc[m][n]);
            if constexpr (s < NR) rd(sc, na, nb, kkc);
            if constexpr (s >= 12 && decltype(kkc)::value == 1) {
                if constexpr (s - 12 < NA) bump(adA[s - 12], dstep);
                if constexpr (s - 12 < NB) bump(adB[s - 12], dstep);
            }
            sched0();
        });
    };
    Frag a0[4], a1[4]; bf16x8 b0[4], b1[4];
    constexpr std::integral_constant<int, 0> K0{}; constexpr std::integral_constant<int, 1> K1{};
    if constexpr (ID != 1) bar();
    bar();
    STAMP(1);
    static_for<0, NR>([&](auto ic) { rd(ic, a0, b0, K0); });
    sched0();
    const int rx = tm * 128 + tn * 8 + wid * 2 + (lane >> 5), rw = tn * 128 + tm * 8 + wid * 2 + (lane >> 5);
    u32x4 xv[4], wv[4];
    if constexpr (ID == 1) {
#pragma unroll
        for (int i = 0; i < 4; ++i) { xv[i] = __builtin_amdgcn_raw_buffer_load_b128(RSRC(p.x, SEQ * DM * 2), (unsigned)(rx * 2048 + (lane & 31) * 16 + i * 512), 0, 0);
                                      wv[i] = __builtin_amdgcn_raw_buffer_load_b128(RSRC(tn < 8 ? p.wre : p.wim, DM * DM * 2), (unsigned)((rw & 1023) * 2048 + (lane & 31) * 16 + i * 512), 0, 0); }
        sched0();
    }
    auto steps = [&](int t0, int t1) {
#pragma nounroll
        for (int t = t0; t < t1; ++t) {
            phase(a0, b0, a1, b1, (t & 3) == 3 ? -3 * STAGE : STAGE, K1);
            if constexpr (ID == 1) { if (t == LNT1) wait_vm<0>(); }
            bar();
            phase(a1, b1, a0, b0, 0, K0);
        }
    };
    if constexpr (ID == 1) {
        steps(0, LNT0);
        wait_lgkm0(); sched0();
        f32x2 sx = {0.f, 0.f}, sw = sx, sq = sx;
#pragma unroll
        for (int i = 0; i < 16; ++i) { sx += (f32x2){bf_lo(xv[i >> 2][i & 3]), bf_hi(xv[i >> 2][i & 3])}; sw += (f32x2){bf_lo(wv[i >> 2][i & 3]), bf_hi(wv[i >> 2][i & 3])}; }
        const float mean = hsum(sx[0] + sx[1]) * (1.f / DM);
#pragma unroll
        for (int i = 0; i < 16; ++i) { const f32x2 d = (f32x2){bf_lo(xv[i >> 2][i & 3]), bf_hi(xv[i >> 2][i & 3])} - mean; sq += d * d; }
        const float rstd = __builtin_amdgcn_rsqf(hsum(sq[0] + sq[1]) * (1.f / DM) + LN_EPS), wsum = hsum(sw[0] + sw[1]);
        const int j = lane & 31;
        __builtin_amdgcn_raw_buffer_store_b32(__float_as_uint(j == 0 ? mean : j == 1 ? rstd : wsum), RSRC(p.ln1, 3 * SEQ * 4), j < 3 ? (unsigned)((j * SEQ + (j < 2 ? rx : rw)) * 4) : 0x7ffffff0u, 0, SC1);
        sched0();
        steps(LNT0, nk);
    } else steps(0, nk);
    wait_lgkm0(); keep(a0, b0); sched0();
    auto send = [&](int h0) {
        const auto rx = RSRC(p.xch, 256 * 32768);
#pragma unroll
        for (int m = 0; m < 4; ++m)
#pragma unroll
            for (int h = h0; h < 2; ++h) {
                const f32x4 u = acc[m][2 * h], v = acc[m][2 * h + 1];
                __builtin_amdgcn_raw_buffer_store_b128((u32x4){pack_bf16(u[0], u[1]), pack_bf16(u[2], u[3]), pack_bf16(v[0], v[1]), pack_bf16(v[2], v[3])}, rx, xoff + (unsigned)((h0 ? m : m * 2 + h) * 1024 + lane * 16), 0, SC1);
            }
    };
    if constexpr (NH > 0) {
        send(1); sched0();
        auto phaseH = [&](const Frag (&ra)[4], const bf16x8 (&rb)[4], Frag (&na)[4], bf16x8 (&nb)[4], int dstep, auto kkc) {
            static_for<0, 8>([&](auto sc) {
                constexpr int s = decltype(sc)::value;
                if constexpr ((s & 1) == 0) { wait_lgkm<6 - s + (s < 5 ? 2 * s : 10)>(); sched0(); }
                acc[s >> 1][s & 1] = MFMA(ra[s >> 1].v(), rb[s & 1], acc[s >> 1][s & 1]);
                if constexpr (s < 5) { rd(std::integral_constant<int, s < 2 ? 2 * s : 2 * s + 2>{}, na, nb, kkc); rd(std::integral_constant<int, s < 2 ? 2 * s + 1 : 2 * s + 3>{}, na, nb, kkc); }
                if constexpr (s >= 5 && decltype(kkc)::value == 1) { if constexpr (s < 7) { bump(adA[2 * (s - 5)], dstep); bump(adA[2 * (s - 5) + 1], dstep); } else bump(adB[0], dstep); }
                sched0();
            });
        };
#pragma nounroll
        for (int t = nk; t < nk + NH; ++t) {
            phaseH(a0, b0, a1, b1, (t & 3) == 3 ? -3 * STAGE : STAGE, K1);
            bar();
            phaseH(a1, b1, a0, b0, 0, K0);
            if (t == nk + FLAGD) { wait_vm<0>(); if (lane == 0) flag_store(flag, ep); }
        }
        wait_lgkm0();
        asm volatile("" ::"v"(b0[0]), "v"(b0[1]));
#pragma unroll
        for (int i = 0; i < 4; ++i) asm volatile("" ::"v"(a0[i].lo), "v"(a0[i].hi));
        sched0();
    }
    STAMP(2);
    if constexpr (ID == 1) {
        wait_vm<0>(); asm volatile("; pfc %0" ::"v"(pfc));
        bar();
        const auto ro = RSRC(p.abT, ABT_BYTES);
        const float sgn = tn < 8 ? 1.f : -1.f;
        f32x4 mean[4], rs[4]; float ws[4];
        static_for<0, 4>([&](auto ic) { constexpr int i = decltype(ic)::value; const unsigned a = lds0 + (unsigned)(XLDS + wm * 256 + q * 16);
            mean[i] = __builtin_bit_cast(f32x4, lds_read128<i * 64>(a)); rs[i] = __builtin_bit_cast(f32x4, lds_read128<1024 + i * 64>(a));
            ws[i] = lds_read32<2048 + i * 64>(lds0 + (unsigned)(XLDS + wn * 256 + l15 * 4)); });
        wait_lgkm0(); sched0();
#pragma unroll
        for (int m = 0; m < 4; ++m) {
            const f32x4 r = rs[m] * sgn;
#pragma unroll
            for (int n = 0; n < 4; ++n) acc[m][n] = (acc[m][n] - mean[m] * ws[n]) * r;
        }
#pragma unroll
        for (int n = 0; n < 4; ++n) { const int e = (tn & 7) * 128 + wn * 64 + n * 16 + l15;
#pragma unroll
            for (int mp = 0; mp < 2; ++mp) {
                unsigned x0 = pack_bf16(acc[2 * mp][n][0], acc[2 * mp][n][1]), x1 = pack_bf16(acc[2 * mp][n][2], acc[2 * mp][n][3]);
                unsigned y0 = pack_bf16(acc[2 * mp + 1][n][0], acc[2 * mp + 1][n][1]), y1 = pack_bf16(acc[2 * mp + 1][n][2], acc[2 * mp + 1][n][3]);
                swap16u(x0, y0); swap16u(x1, y1);
                const int s = (tn >> 3) * SEQ + col0 + mp * 32;
                __builtin_amdgcn_raw_buffer_store_b128((u32x4){x0, x1, y0, y1}, ro, (unsigned)(((s >> 3) * ABP + e) * 16), 0, STA);
            } }
    } else if (!recv) {
        send(0);
        wait_vm<0>();
        if (lane == 0) flag_store(flag, ep);
        if constexpr (ID == 2) bar();
    } else {
        bar();
        STAMP(4);
        const unsigned px = (unsigned)(size_t)lds + (unsigned)(XLDS + wid * 8192 + lane * 16);
        constexpr int HH = ID == 4 ? 1 : 2;
        u32x4 pw[4 * HH];
        static_for<0, 4 * HH>([&](auto ic) { pw[decltype(ic)::value] = __builtin_bit_cast(u32x4, lds_read128<decltype(ic)::value * 1024>(px)); });
        wait_lgkm0(); sched0();
#pragma unroll
        for (int m = 0; m < 4; ++m)
#pragma unroll
            for (int h = 0; h < HH; ++h) { const u32x4 w = pw[m * HH + h];
                acc[m][2 * h] += (f32x4){bf_lo(w[0]), bf_hi(w[0]), bf_lo(w[1]), bf_hi(w[1])}; acc[m][2 * h + 1] += (f32x4){bf_lo(w[2]), bf_hi(w[2]), bf_lo(w[3]), bf_hi(w[3])}; }
        const auto ro = RSRC(ID == 2 ? p.h : p.out, ID == 2 ? 136 * SEQ * 16 : SEQ * DM * 2);
#pragma unroll
        for (int n = 0; n < 2 * HH; ++n) {
            unsigned o[4][2];
#pragma unroll
            for (int m = 0; m < 4; ++m) { o[m][0] = pack_bf16(acc[m][n][0], acc[m][n][1]); o[m][1] = pack_bf16(acc[m][n][2], acc[m][n][3]); }
            if constexpr (ID == 2) {
                const f32x4 s4 = (acc[0][n] + acc[1][n]) + (acc[2][n] + acc[3][n]);
                const float mw = qsum((s4[0] + s4[1]) + (s4[2] + s4[3])) * (1.f / 64.f);
                const f32x4 d0 = acc[0][n] - mw, d1 = acc[1][n] - mw, d2 = acc[2][n] - mw, d3 = acc[3][n] - mw, q4 = (d0 * d0 + d1 * d1) + (d2 * d2 + d3 * d3);
                float m2 = (q4[0] + q4[1]) + (q4[2] + q4[3]);
                m2 = qsum(m2);
                if (q == 0) __builtin_amdgcn_raw_buffer_store_b64((u32x2){__float_as_uint(mw), __float_as_uint(m2)}, ro, (unsigned)(((128 + tm) * SEQ + row0 + n * 16) * 16 + wm * 8), 0, SC1);
            }
#pragma unroll
            for (int mp = 0; mp < 2; ++mp) {
                swap16u(o[2 * mp][0], o[2 * mp + 1][0]); swap16u(o[2 * mp][1], o[2 * mp + 1][1]);
                __builtin_amdgcn_raw_buffer_store_b128((u32x4){o[2 * mp][0], o[2 * mp][1], o[2 * mp + 1][0], o[2 * mp + 1][1]}, ro,
                    (unsigned)(ID == 4 ? ((row0 + n * 16) * DM + col0 + mp * 32) * 2 : ((col0 / 8 + mp * 4) * SEQ + row0 + n * 16) * 16), 0, SC1);
            }
        }
        STAMP(5);
        if constexpr (ID == 2) { wait_vm<0>(); bar(); }
    }
    STAMP_END(ID);
}
#endif

#if defined(__HIP_DEVICE_COMPILE__)
template <int GRP>
__device__ __forceinline__ void gemm3_body(const Params& p, LAS unsigned char* lds, const int wid, u64& ep) {
    constexpr int NK = 16, FA = 7, HB0 = 3 * 32768;
    STAMP_BEGIN;
    const int tid = threadIdx.x, lane = tid & 63;
    const int wm = wid & 1, wn = wid >> 1, l15 = lane & 15, q = lane >> 4;
    const int xcd = blockIdx.x & 7, idx = blockIdx.x >> 3, im = idx & 7, in = idx >> 3;
    const int tm = (xcd & 3) * 8 + im, tn = (xcd >> 2) * 4 + in;
    const G3W gw(p, wid, lane);
    unsigned pf = 0;
    u64* words = p.flags + (F_H + tn * 32) * FLAG_STRIDE;
    if (threadIdx.x == 0 && !(xcd & 1)) flag_store(words + tm * FLAG_STRIDE, ep);
    if (wid) __builtin_amdgcn_s_sleep(2);
    gw.early(lds, wid, 0);
    if (W1E) gw.early(lds, wid, 1);
    if ((xcd & 1) && wid == 1) colsum_final(p, lds, ep);
    if (wid == 0) {
        wait_group(words + (lane & 31) * FLAG_STRIDE, (lane & 8) == 0, ep);
        if (blockIdx.x == 0 && lane == 0) flag_store(p.flags, ep);
    }
    bar();
    const unsigned voffB = (unsigned)((wid * SEQ + tn * 256 + lane) * 16);
    struct Src { const void* base; int so; unsigned nrec; };
    auto pin = [](Src x) { asm volatile("" : "+s"(x.base), "+s"(x.so), "+s"(x.nrec)); return x; };
    auto srcW = [&](int t) { return pin(Src{t < NK ? (const void*)gw.ptr : (const void*)(p.csum + GRP * 3584), t < NK ? gw.kstep(t) * 64 * FF * 2 : 0, t < NK ? (unsigned)(DM * FF * 2) : t == NK ? (unsigned)(2 * FF * 2) : 0u}); };
    auto srcH = [&](int t) { Src x{nullptr, (t < NK ? gw.kstep(t) : NK) * 8 * SEQ * 16, 136 * SEQ * 16}; asm volatile("" : "+s"(x.so)); return x; };
    auto stageW = [&](Src x, int slot, int i) { gw.piece(lds, wid, x.base, x.so, x.nrec, slot, i); };
    auto stageH = [&](Src x, int slot, int i) { DMA16_DEV(RSRC(p.h, x.nrec), lds + HB0 + slot * 32768 + wid * 4096 + i * 1024, voffB, x.so + i * 1024); };
#pragma unroll
    for (int t = 0; t < 2; ++t) {
        const Src xh = srcH(t);
#pragma unroll
        for (int i = 0; i < 4; ++i) stageH(xh, t, i);
        if (!W1E && t == 0) gw.early(lds, wid, 1);
    }

    unsigned adA[FA];
    const unsigned lds0 = (unsigned)(size_t)lds;
#pragma unroll
    for (int m = 0; m < FA; ++m) { const int q4 = l15 >> 2, pp = lane & 3, reg = m < 6 ? m & 1 : pp >> 1, ch = m < 6 ? wm * 8 + (m & 6) + (pp >> 1) : 6 + wm;
        adA[m] = lds0 + (unsigned)(reg * 16384 + (8 * q + q4) * 256 + ((ch ^ (((q4 | ((q & 1) << 2)) << 1) | reg)) << 4) + 8 * (pp & 1)); }
    unsigned adB = lds0 + (unsigned)(HB0 + q * 4096 + (wn * 64 + l15) * 16);
    auto rd = [&](auto ic, Frag (&ra)[FA], bf16x8 (&rb)[4], auto kkc) {
        constexpr int kk = decltype(kkc)::value, i = decltype(ic)::value;
        if constexpr (i < 4) rb[i] = lds_read128<kk * 16384 + i * 256>(adB);
        else if constexpr (i % 2 == 0) ra[(i - 4) / 2].lo = tr_read<kk * 8192>(adA[(i - 4) / 2]);
        else ra[(i - 4) / 2].hi = tr_read<kk * 8192 + 1024>(adA[(i - 4) / 2]);
    };
    f32x4 acc[FA][4];
#pragma unroll
    for (int m = 0; m < FA; ++m)
#pragma unroll
        for (int n = 0; n < 4; ++n) acc[m][n] = (f32x4){0.f, 0.f, 0.f, 0.f};
#define DSTR 2
#define LASTP 3
#define PR1 18
#define PR2 6
#define PR3 22

    auto phase = [&](const Frag (&ra)[FA], const bf16x8 (&rb)[4], Frag (&na)[FA], bf16x8 (&nb)[4], int dw, int dh, auto kkc, auto grpc, auto&& dma, auto&& prep) {
        static_for<0, 28>([&](auto sc) {
            constexpr int s = decltype(sc)::value, m = s >> 2, n = s & 3;
            if constexpr (decltype(kkc)::value == 0) { if constexpr (s == 0) __builtin_amdgcn_s_setprio(3); if constexpr (s == PR1) __builtin_amdgcn_s_setprio(2); }
            else { if constexpr (s == PR2) __builtin_amdgcn_s_setprio(1); if constexpr (s == PR3) __builtin_amdgcn_s_setprio(0); }
            acc[m][n] = MFMA(ra[m].v(), rb[n], acc[m][n]);
            sched0();
            if constexpr (s < 18) rd(sc, na, nb, kkc);
            if constexpr (s == 20) prep();
            if constexpr (s % DSTR == 0 && s < 8 * DSTR && decltype(grpc)::value == ((s / DSTR) & 1)) dma(s / (2 * DSTR));
            if constexpr (decltype(kkc)::value == 1 && s >= 18 && s < 26) { if constexpr (s < 25) bump(adA[s - 18], dw); else bump(adB, dh); }
            sched0();
        });
        wait_lgkm0(); sched0();
    };
    Frag a0[FA], a1[FA]; bf16x8 b0[4], b1[4];
    constexpr std::integral_constant<int, 0> K0{}; constexpr std::integral_constant<int, 1> K1{};
    wait_vm<W1E ? 4 : 8>(); bar();
    STAMP(1);
    static_for<0, 18>([&](auto ic) { rd(ic, a0, b0, K0); });
    wait_lgkm0(); sched0();
    const unsigned pfo = (unsigned)(((blockIdx.x * 8 + wid) * 28 + (lane < 28 ? lane : 0)) * 128); const int pft = 1 + idx % 12;
    constexpr std::integral_constant<int, GRP> grpc{};
    Src xw = srcW(2), xh;
    auto phaseA = [&](int t, int sw2) {
        phase(a0, b0, a1, b1, sw2 == 1 ? -65536 : 32768, (t & 1) ? -32768 : 32768, K1, grpc, [&](int i) { stageW(xw, sw2, i);
            if (i == 3) touch(pf, pfo, p.wd, t == pft ? FF * DM * 2 : 0);
        }, [&]() { xh = srcH(t + 2); });
        wait_vm<5>();
    };
    u64 cseen = 0;
    phaseA(0, 2);
    int sw2 = 0;
#pragma nounroll
    for (int t = 0; t < NK - 1; ++t) {
        if (__builtin_expect(t >= 11 && t <= 12 && (wid & 3) == 0, 0)) {
            const int sl = min((tm * 112 >> 6) + (lane & 3), (tm * 112 + 111) >> 6);
            u64* w = p.flags + (F_CS + GRP * 56 + sl) * FLAG_STRIDE;
            if (t == 11) flag_peek(cseen, w); else if (__any(cseen != ep)) wait_group(w, true, ep);
        }
        bar();
        phase(a1, b1, a0, b0, 0, 0, K0, grpc, [&](int i) { stageH(xh, t & 1, i); }, [&]() { xw = srcW(t + 3); });
        phaseA(t + 1, sw2);
        sw2 = sw2 == 2 ? 0 : sw2 + 1;
    }
    bar();
    __builtin_amdgcn_s_setprio(GRP ? 0 : LASTP);
    f32x4 csv[FA], sp[4][2];
    static_for<0, 16>([&](auto sc) {
        constexpr int s = decltype(sc)::value;
        acc[s >> 2][s & 3] = MFMA(a1[s >> 2].v(), b1[s & 3], acc[s >> 2][s & 3]);
        if constexpr (s < 8) sp[s >> 1][s & 1] = __builtin_bit_cast(f32x4, lds_read128<(s & 1) * 16384 + (s >> 1) * 256>(adB));
        else if constexpr (s < 15) { constexpr int m = s - 8; const int reg = m < 6 ? m & 1 : q >> 1, c4 = m < 6 ? wm * 2 + (m & 6) * 2 + q : (wm ? 0 : 12) + (q & 1);
            csv[m] = __builtin_bit_cast(f32x4, lds_read128<32768>(lds0 + (unsigned)(reg * 16384 + wm * 256 + ((c4 ^ ((wm << 1) | reg)) << 4)))); }
        if constexpr (GRP == 1 && s == 2) g128_load<4, 0>(p, lds, wid, ep);
        sched0();
    });
    wait_lgkm0(); wait_vm<GRP ? 4 : 0>(); asm volatile("; pf %0" ::"v"(pf)); sched0();
    float mean[4], c1[4], ve[4];
#pragma unroll
    for (int n = 0; n < 4; ++n) {
        const f32x4 s0 = sp[n][0], s1 = sp[n][1];
        mean[n] = qsum((s0[0] + s0[2]) + (s1[0] + s1[2])) * (1.f / 16.f);
        const float d0 = s0[0] - mean[n], d1 = s0[2] - mean[n], d2 = s1[0] - mean[n], d3 = s1[2] - mean[n];
        const float m2 = qsum((s0[1] + s0[3]) + (s1[1] + s1[3]) + 64.f * ((d0 * d0 + d1 * d1) + (d2 * d2 + d3 * d3)));
        ve[n] = m2 * (1.f / DM) + LN_EPS; c1[n] = -1.44269504f * __builtin_amdgcn_rsqf(ve[n]);
    }
#pragma unroll
    for (int s = 16; s < 28; ++s) acc[s >> 2][s & 3] = MFMA(a1[s >> 2].v(), b1[s & 3], acc[s >> 2][s & 3]);
#pragma unroll
    for (int s = 16; s < 28; ++s) { __builtin_amdgcn_sched_group_barrier(0x008, 1, 0); __builtin_amdgcn_sched_group_barrier(0x002, 10, 0); }
    sched0();
    STAMP(2);
    const auto ro = RSRC(p.act, SEQ * FF * 2);
    auto silu2 = [&](f32x2 g, f32x2 u, int n) {
        const f32x2 x = g * c1[n], d = (f32x2){__builtin_amdgcn_exp2f(x[0]), __builtin_amdgcn_exp2f(x[1])} * ve[n] + ve[n];
        const f32x2 r = g * u * (f32x2){__builtin_amdgcn_rcpf(d[0]), __builtin_amdgcn_rcpf(d[1])};
        return pack_bf16(r[0], r[1]);
    };
#pragma unroll
    for (int j = 0; j < 3; ++j)
#pragma unroll
        for (int np = 0; np < 2; ++np) {
            unsigned o[2][2];
#pragma unroll
            for (int i = 0; i < 2; ++i) { const int n = 2 * np + i; const f32x4 g = acc[2 * j][n] - mean[n] * csv[2 * j], u = acc[2 * j + 1][n] - mean[n] * csv[2 * j + 1];
                o[i][0] = silu2((f32x2){g[0], g[1]}, (f32x2){u[0], u[1]}, n); o[i][1] = silu2((f32x2){g[2], g[3]}, (f32x2){u[2], u[3]}, n); }
            swap16u(o[0][0], o[1][0]); swap16u(o[0][1], o[1][1]);
            __builtin_amdgcn_raw_buffer_store_b128((u32x4){o[0][0], o[0][1], o[1][0], o[1][1]}, ro, (unsigned)(((tm * 14 + wm * 8 + 2 * j + (q >> 1)) * SEQ + tn * 256 + wn * 64 + np * 32 + (lane & 31)) * 16), 0, STA);
        }
    {
        unsigned o[4];
#pragma unroll
        for (int n = 0; n < 4; ++n) {
            const f32x4 v = acc[6][n] - mean[n] * csv[6];
            float g0 = v[0], g1 = v[1], u0 = v[2], u1 = v[3];
            swap32(g0, u0); swap32(g1, u1);
            o[n] = silu2((f32x2){g0, g1}, (f32x2){u0, u1}, n);
        }
        swap16u(o[0], o[1]); swap16u(o[2], o[3]); swap32u(o[0], o[2]); swap32u(o[1], o[3]);
        __builtin_amdgcn_raw_buffer_store_b128((u32x4){o[0], o[2], o[1], o[3]}, ro, (unsigned)(((tm * 14 + 6 + wm) * SEQ + tn * 256 + wn * 64 + lane) * 16), 0, STA);
    }
    STAMP(5);
    __builtin_amdgcn_s_setprio(0);
    if constexpr (GRP == 1) { wait_vm<0>(); bar(); }
    STAMP_END(3);
}
#endif

__global__ __launch_bounds__(512, 2) void fused_k(const Params p) {
#if defined(__HIP_DEVICE_COMPILE__)
    __shared__ __attribute__((aligned(16))) unsigned char lds_raw[5 * 32768];
    LAS unsigned char* lds = (LAS unsigned char*)lds_raw;
    const int wid = __builtin_amdgcn_readfirstlane(threadIdx.x >> 6);
    u64 ep = 0;
    if (wid < 4) { g128_compute<1>(p, lds, wid, ep); g128_compute<2>(p, lds, wid, ep); gemm3_body<0>(p, lds, wid, ep); g128_compute<4>(p, lds, wid, ep); }
    else { g128_load<1, -1>(p, lds, wid, ep); g128_load<2, -1>(p, lds, wid, ep); gemm3_body<1>(p, lds, wid, ep); g128_load<4, -1>(p, lds, wid, ep); }
#endif
}
__global__ void tail_k() {}

extern "C" void kernel_launch(void* const* d_in, const int* in_sizes, int n_in, void* d_out, int out_size, void* d_ws, size_t ws_size, hipStream_t stream) {
    Params p;
    p.x = (const u16*)d_in[0]; p.cre = (const u16*)d_in[1]; p.cim = (const u16*)d_in[2]; p.wre = (const u16*)d_in[3]; p.wim = (const u16*)d_in[4];
    p.wg = (const u16*)d_in[6]; p.wu = (const u16*)d_in[7]; p.wd = (const u16*)d_in[8];
    p.out = (u16*)d_out;
    char* ws = (char*)d_ws;
    p.abT = (u16*)(ws + (ABP == 1024 ? 0u : 48u << 20)); p.h = (u16*)(ws + (8u << 20)); p.act = (u16*)(ws + (16u << 20));
    p.xch = (u16*)(ws + (32u << 20)); p.flags = (unsigned long long*)(ws + (40u << 20)); p.csum = (float*)(ws + (40u << 20) + (512u << 10)); p.ln1 = (float*)(ws + (40u << 20) + (768u << 10));
    fused_k<<<256, 512, 0, stream>>>(p);
}
```

```cpp
#include <hip/hip_runtime.h>
#include <hip/hip_bf16.h>
#include <type_traits>

#define LAS __attribute__((address_space(3)))
typedef unsigned short u16;
typedef __bf16 bf16x8 __attribute__((ext_vector_type(8)));
typedef __bf16 bf16x2 __attribute__((ext_vector_type(2)));
typedef float f32x4 __attribute__((ext_vector_type(4)));
typedef float f32x2 __attribute__((ext_vector_type(2)));
typedef unsigned u32x4 __attribute__((ext_vector_type(4)));
typedef unsigned u32x2 __attribute__((ext_vector_type(2)));

constexpr int SEQ = 2048, DM = 1024, FF = 3584;
constexpr float LN_EPS = 1e-5f;
constexpr int SC1 = 16;

struct Params {
    const u16* x; const u16* cre; const u16* cim; const u16* wre; const u16* wim;
    const u16* wg; const u16* wu; const u16* wd;
    u16* out; u16* abT; u16* h; u16* act; u16* xch; unsigned long long* flags; float* csum; float* ln1;
};

template <int N> __device__ __forceinline__ void wait_vm() { asm volatile("s_waitcnt vmcnt(%0)" ::"n"(N) : "memory"); }
__device__ __forceinline__ void wait_lgkm0() { __builtin_amdgcn_s_waitcnt(0xC07F); asm volatile("s_waitcnt lgkmcnt(0)" ::: "memory"); }
__device__ __forceinline__ void sched0() { __builtin_amdgcn_sched_barrier(0); }
__device__ __forceinline__ void bar() { sched0(); asm volatile("" ::: "memory"); __builtin_amdgcn_s_barrier(); asm volatile("" ::: "memory"); sched0(); }
__device__ __forceinline__ unsigned pack_bf16(float lo, float hi) { bf16x2 c; c[0] = (__bf16)lo; c[1] = (__bf16)hi; return __builtin_bit_cast(unsigned, c); }
__device__ __forceinline__ float bf_lo(unsigned v) { return __uint_as_float(v << 16); }
__device__ __forceinline__ float bf_hi(unsigned v) { return __uint_as_float(v & 0xffff0000u); }
__device__ __forceinline__ void swap16(float& a, float& b) { auto r = __builtin_amdgcn_permlane16_swap(__float_as_uint(a), __float_as_uint(b), false, false); a = __uint_as_float(r[0]); b = __uint_as_float(r[1]); }
__device__ __forceinline__ void swap32(float& a, float& b) { auto r = __builtin_amdgcn_permlane32_swap(__float_as_uint(a), __float_as_uint(b), false, false); a = __uint_as_float(r[0]); b = __uint_as_float(r[1]); }
__device__ __forceinline__ void swap16u(unsigned& a, unsigned& b) { auto r = __builtin_amdgcn_permlane16_swap(a, b, false, false); a = r[0]; b = r[1]; }
__device__ __forceinline__ void swap32u(unsigned& a, unsigned& b) { auto r = __builtin_amdgcn_permlane32_swap(a, b, false, false); a = r[0]; b = r[1]; }
__device__ __forceinline__ float qsum(float v) { float a = v, b = v; swap16(a, b); a += b; b = a; swap32(a, b); return a + b; }

template <int CTRL> __device__ __forceinline__ float dpp_add(float v) { return v + __int_as_float(__builtin_amdgcn_update_dpp(0, __float_as_int(v), CTRL, 0xf, 0xf, false)); }
__device__ __forceinline__ float hsum(float v) { v = dpp_add<0x128>(v); v = dpp_add<0x124>(v); v = dpp_add<0x122>(v); v = dpp_add<0x121>(v); float b = v; swap16(v, b); return v + b; }

struct Frag { u32x2 lo, hi; __device__ __forceinline__ bf16x8 v() const { return __builtin_bit_cast(bf16x8, __builtin_shufflevector(lo, hi, 0, 1, 2, 3)); } };
template <int OFF> __device__ __forceinline__ u32x2 tr_read(unsigned addr) { u32x2 r; asm volatile("ds_read_b64_tr_b16 %0, %1 offset:%2" : "=v"(r) : "v"(addr), "n"(OFF) : "memory"); return r; }
template <int OFF> __device__ __forceinline__ bf16x8 lds_read128(unsigned addr) { bf16x8 r; asm volatile("ds_read_b128 %0, %1 offset:%2" : "=v"(r) : "v"(addr), "n"(OFF) : "memory"); return r; }
template <int OFF> __device__ __forceinline__ float lds_read32(unsigned addr) { float r; asm volatile("ds_read_b32 %0, %1 offset:%2" : "=v"(r) : "v"(addr), "n"(OFF) : "memory"); return r; }
template <int N> __device__ __forceinline__ void wait_lgkm() { asm volatile("s_waitcnt lgkmcnt(%0)" ::"n"(N) : "memory"); }
template <int I, int N, class F> __device__ __forceinline__ void static_for(F&& f) { if constexpr (I < N) { f(std::integral_constant<int, I>{}); static_for<I + 1, N>(f); } }
template <int NA> __device__ __forceinline__ void keep(const Frag (&a)[NA], const bf16x8 (&b)[4]) {
#pragma unroll
    for (int i = 0; i < NA; ++i) asm volatile("" ::"v"(a[i].lo), "v"(a[i].hi));
#pragma unroll
    for (int i = 0; i < 4; ++i) asm volatile("" ::"v"(b[i]));
}
__device__ __forceinline__ void bump(unsigned& a, int d) { asm volatile("v_add_u32 %0, %1, %0" : "+v"(a) : "s"(d)); }
#define MFMA(a, b, c) __builtin_amdgcn_mfma_f32_16x16x32_bf16(a, b, c, 0, 0, 0)
#define RSRC(ptr, bytes) __builtin_amdgcn_make_buffer_rsrc((void*)(ptr), 0, (bytes), 0x00020000)
#define DMA16(rs, ldsp, voff, soff) __builtin_amdgcn_raw_ptr_buffer_load_lds(rs, (LAS void*)(ldsp), 16, voff, soff, 0, 0)
#define DMA16_DEV(rs, ldsp, voff, soff) __builtin_amdgcn_raw_ptr_buffer_load_lds(rs, (LAS void*)(ldsp), 16, voff, soff, 0, SC1)

typedef unsigned long long u64;
constexpr int FLAG_STRIDE = 8, F_PAIR = 1, F_AB = F_PAIR + 1536, F_H = F_AB + 256, F_ACT = F_H + 256, F_CS = F_ACT + 256, F_LN = F_CS + 128;
__device__ __forceinline__ u64 flag_load(u64* w) { return __hip_atomic_load(w, __ATOMIC_RELAXED, __HIP_MEMORY_SCOPE_AGENT); }
__device__ __forceinline__ void flag_store(u64* w, u64 v) { __hip_atomic_store(w, v, __ATOMIC_RELAXED, __HIP_MEMORY_SCOPE_AGENT); }
__device__ __forceinline__ u64 uniform64(u64 v) { return ((u64)(unsigned)__builtin_amdgcn_readfirstlane((unsigned)(v >> 32)) << 32) | (unsigned)__builtin_amdgcn_readfirstlane((unsigned)v); }
__device__ __forceinline__ void wait_group(u64* word, bool need, u64 ep) {
#pragma nounroll
    for (int spin = 0; spin < (1 << 20); ++spin) {
        if (!__any(need && flag_load(word) != ep)) break;
        __builtin_amdgcn_s_sleep(2);
    }
}
#if defined(__HIP_DEVICE_COMPILE__)
__device__ __forceinline__ void touch(unsigned& sink, unsigned voff, const void* base, unsigned bytes) {
    asm volatile("s_nop 4\n\tbuffer_load_dword %0, %1, %2, 0 offen" : "+v"(sink) : "v"(voff), "s"(RSRC(base, bytes)) : "memory");
}
__device__ __forceinline__ void flag_peek(u64& v, const u64* w) { asm volatile("global_load_dwordx2 %0, %1, off sc1" : "+v"(v) : "v"(w) : "memory"); }
struct G3W {
    const u16* ptr; unsigned voff[2]; int phi, koff;
    __device__ __forceinline__ G3W(const Params& p, int wid, int lane) {
        const int xcd = blockIdx.x & 7, idx = blockIdx.x >> 3, tm = (xcd & 3) * 8 + (idx & 7), q = lane >> 4;
        ptr = wid < 4 ? p.wg : p.wu; phi = ((idx & 7) + (idx >> 3)) & 3; koff = xcd * 2;
#pragma unroll
        for (int i = 0; i < 2; ++i) { const int j = (lane & 15) ^ (((q | (i << 2)) << 1) | (wid >> 2));
            voff[i] = j < 14 ? (unsigned)(((wid & 3) * 16 + q) * FF * 2 + tm * 224 + j * 16) : 0x7ffffff0u; }
    }
    __device__ __forceinline__ int kstep(int t) const { return (t + koff + ((t & 3) == phi ? 4 : 0)) & 15; }
    __device__ __forceinline__ void piece(LAS unsigned char* lds, int wid, const void* base, int so, unsigned nrec, int slot, int i) const { DMA16_DEV(RSRC(base, nrec), lds + slot * 32768 + wid * 4096 + i * 1024, voff[i >> 1], so + i * 4 * FF * 2); }
    __device__ __forceinline__ void early(LAS unsigned char* lds, int wid, int t) const {
#pragma unroll
        for (int i = 0; i < 4; ++i) piece(lds, wid, ptr, kstep(t) * 64 * FF * 2, DM * FF * 2, t, i);
    }
};
__device__ __forceinline__ void colsum_partial(const Params& p, LAS unsigned char* lds, int lw) {
    const int lane = threadIdx.x & 63, slab = blockIdx.x >> 1, mat = slab >= 56, c0 = (slab - mat * 56) * 64;
    const auto rw = RSRC(mat ? p.wu : p.wg, slab < 112 ? DM * FF * 2 : 0);
    const unsigned vo = (unsigned)(((lw * 256 + (lane >> 3)) * FF + c0 + (lane & 7) * 8) * 2);
    u32x4 v[8]; f32x4 s0 = {0.f, 0.f, 0.f, 0.f}, s1 = s0;
    auto add = [&](const u32x4 w) { s0 += (f32x4){bf_lo(w[0]), bf_hi(w[0]), bf_lo(w[1]), bf_hi(w[1])}; s1 += (f32x4){bf_lo(w[2]), bf_hi(w[2]), bf_lo(w[3]), bf_hi(w[3])}; };
#pragma unroll
    for (int i = 0; i < 4; ++i) v[i] = __builtin_amdgcn_raw_buffer_load_b128(rw, vo, i * 8 * FF * 2, 0);
#pragma unroll
    for (int i = 0; i < 8; ++i) { add(v[i % 4]); v[i % 4] = __builtin_amdgcn_raw_buffer_load_b128(rw, vo, (4 + i) * 8 * FF * 2, 0); sched0(); }
#pragma unroll
    for (int i = 4; i < 8; ++i) v[i] = __builtin_amdgcn_raw_buffer_load_b128(rw, vo, (8 + i) * 8 * FF * 2, 0);
#pragma nounroll
    for (int j = 16; j < 40; j += 8)
#pragma unroll
        for (int i = 0; i < 8; ++i) { add(v[i]); v[i] = __builtin_amdgcn_raw_buffer_load_b128(rw, j < 32 ? vo : 0x7ffffff0u, (j + i) * 8 * FF * 2, 0); sched0(); }
    LAS float* tab = (LAS float*)(lds + 131072) + (lw * 8 + (lane >> 3)) * 64 + (lane & 7) * 8;
    *(LAS f32x4*)tab = s0; *(LAS f32x4*)(tab + 4) = s1;
    wait_lgkm0();
}
__device__ __forceinline__ void colsum_final(const Params& p, LAS unsigned char* lds, u64 ep) {
    const int lane = threadIdx.x & 63, slab = blockIdx.x >> 1, mat = slab >= 56, c0 = (slab - mat * 56) * 64;
    if (slab >= 112) return;
    const LAS float* tab = (const LAS float*)(lds + 131072);
    float a = 0.f, b = 0.f;
#pragma unroll
    for (int i = 0; i < 32; i += 2) { a += tab[i * 64 + lane]; b += tab[(i + 1) * 64 + lane]; }
    const int f = c0 + lane, t = f / 112, r = f - t * 112, h = r >= 56;
    __hip_atomic_store(p.csum + (mat * 2 + h) * 1792 + t * 56 + r - h * 56, a + b, __ATOMIC_RELAXED, __HIP_MEMORY_SCOPE_AGENT);
    wait_vm<0>();
    if (lane == 0) flag_store(p.flags + (F_CS + slab) * FLAG_STRIDE, ep);
}
#endif

enum { ROW, CHK, TR };
constexpr int NKS2 = 27;
constexpr int RE4 = 1, RO4 = 3;
constexpr int FLAGD = 1;
constexpr int LNT0 = 1, LNT1 = 5;
template <int ID> struct Cfg;
template <> struct Cfg<1> { static constexpr int NKT = 16, NKS = 16, FA = ROW, FB = ROW, LDB = DM; };
template <> struct Cfg<2> { static constexpr int NKT = 64, NKS = NKS2, FA = CHK, FB = ROW, LDB = SEQ; };
template <> struct Cfg<4> { static constexpr int NKT = 56, NKS = 0, FA = TR, FB = CHK, LDB = 0; };

#if defined(__HIP_DEVICE_COMPILE__)
template <int ID, bool PRE>
__device__ __forceinline__ void g128_load(const Params& p, LAS unsigned char* lds, const int wid, u64& ep) {
    using C = Cfg<ID>;
    constexpr int STAGE = 32768, XLDS = 4 * STAGE;
    const int lane = threadIdx.x & 63, l15 = lane & 15, q = lane >> 4;
    const int xcd = blockIdx.x & 7, idx = blockIdx.x >> 3, im = idx & 3, in = idx >> 2;
    constexpr int NH = ID == 4 ? 2 * (RE4 + RO4) : 0;
    const int ks = (xcd & 1) ^ (ID == 2); const bool recv = ID == 4 || (ID == 2 && ks);
    const int tm = (ID == 1 ? (xcd & 3) : ((xcd >> 1) & 1)) * 4 + im, tn = (xcd >> 2) * 8 + in;
    const int k0 = ID != 1 && ks ? C::NKS : 0, nk = ID == 4 ? 28 - 2 * (ks ? RO4 : RE4) : ID == 2 && ks ? C::NKT - C::NKS : C::NKS;
    const int xb = ID == 4 ? 128 + (blockIdx.x ^ (wid >> 2)) : blockIdx.x >> 1;
    u64* const flag = p.flags + (F_PAIR + xb * 4 + (wid & 3)) * FLAG_STRIDE;
    const unsigned xoff = (unsigned)(ID == 4 ? (128 + xb) * 16384 + (wid & 3) * 4096 : xb * 32768 + (wid & 3) * 8192);
    const int lw = wid - 4, phi = (im + in) & 3;
    const int koff = ID == 1 ? xcd * 2 : ((xcd >> (ID == 2 ? 1 : 2)) & 1) * (nk >> 1);
    const unsigned ksw = (unsigned)(((lane & 7) ^ (lane >> 3)) << 4);
    unsigned voffA[2], voffB;
    if constexpr (C::FA == ROW) voffA[0] = (unsigned)((tm * 128 + lw * 32 + (lane >> 3)) * DM * 2) + ksw;
    if constexpr (C::FA == CHK) voffA[0] = (unsigned)((lw * 2 * DM + tm * 128 + lane) * 16);
    if constexpr (C::FA == TR) {
#pragma unroll
        for (int i = 0; i < 2; ++i) voffA[i] = (unsigned)((lw * 16 + q) * DM * 2 + tm * 256 + ((l15 ^ ((q | (i << 2)) << 1)) << 4));
    }
    if constexpr (C::FB == ROW) voffB = (unsigned)((((ID == 1 ? tn & 7 : tn)) * 128 + lw * 32 + (lane >> 3)) * C::LDB * 2) + ksw;
    else voffB = (unsigned)((lw * 2 * SEQ + tn * 128 + lane) * 16);
    const auto ra = ID == 1 ? RSRC(p.x, SEQ * DM * 2) : ID == 2 ? RSRC(p.abT, 2 * SEQ * DM * 2) : RSRC(p.wd, FF * DM * 2);
    auto stage = [&](int t, bool doA, bool doB, bool half = false) {
        int k = (t & 3) != phi ? t : t + 4 < nk ? t + 4 : phi;
        k += koff; k = k >= nk ? k - nk : k;
        if constexpr (ID == 4) k = ks * 14 + (k < (nk >> 1) ? k : k + 28 - (nk >> 1)); else k += k0;
        if (half) { int u = t - nk + phi; u = u >= NH ? u - NH : u; const int h = u >= RE4 + RO4, v = u - h * (RE4 + RO4); k = h * 28 + (v < RE4 ? 14 - RE4 + v : 28 - RO4 - RE4 + v); }
        const auto rb = ID == 1 ? RSRC(tn < 8 ? p.wre : p.wim, DM * DM * 2) : ID == 2 ? RSRC(k < 32 ? p.cre : p.cim, SEQ * SEQ * 2) : RSRC(p.act, SEQ * FF * 2);
        LAS unsigned char* base = lds + (t & 3) * STAGE + lw * 4096;
#pragma unroll
        for (int i = 0; i < 4; ++i) {
            if (doA) {
                if constexpr (C::FA == ROW) DMA16(ra, base + i * 1024, voffA[0], k * 128 + i * 8 * DM * 2);
                if constexpr (C::FA == CHK) DMA16_DEV(ra, base + i * 1024, voffA[0], (k * 8 + (i >> 1)) * DM * 16 + (i & 1) * 1024);
                if constexpr (C::FA == TR) DMA16(ra, base + i * 1024, voffA[i >> 1], (k * 64 + i * 4) * DM * 2);
            }
            if (doB) {
                if constexpr (C::FB == ROW) DMA16(rb, base + 16384 + i * 1024, voffB, (k & 31) * 128 + i * 8 * C::LDB * 2);
                else if (!(half && (i & 1))) DMA16_DEV(rb, base + 16384 + i * 1024, voffB, (k * 8 + (i >> 1)) * SEQ * 16 + ((i & 1) ^ ks) * 1024);
            }
        }
    };
    if constexpr (PRE) { stage(0, ID == 4, ID == 2); return; }
    if constexpr (ID != 1) {
        u64* words = p.flags + (ID == 2 ? F_AB + tm * 32 : F_ACT + (tn >> 1) * 32) * FLAG_STRIDE;
        if (wid == 4 && lane == 0) flag_store(ID == 2 ? p.flags + (F_AB + (tn & 7) * 32 + (tn >> 3) * 16 + (xcd & 3) * 4 + im) * FLAG_STRIDE : words + ((xcd & 3) * 8 + (idx & 7)) * FLAG_STRIDE, ep);
        if (wid != 4) __builtin_amdgcn_s_sleep(2);
        stage(1, ID == 4, ID == 2); stage(2, ID == 4, ID == 2);
        if (wid == 4) { const int j = lane & 31; wait_group(words + j * FLAG_STRIDE, ID == 2 ? 2 * j + 2 > k0 && 2 * j < k0 + nk : ((j >> 3) & 1) == ks, ep); }
        bar();
        stage(0, ID == 2, ID == 4); stage(1, ID == 2, ID == 4); stage(2, ID == 2, ID == 4);
        wait_vm<8>(); bar();
        wait_vm<4>();
    } else {
        stage(0, true, true); stage(1, true, true);
        wait_vm<8>(); bar();
        stage(2, true, true);
        const unsigned a = (unsigned)(size_t)lds + XLDS;
        const float e0 = lds_read32<3072>(a), e1 = lds_read32<3076>(a);
        wait_lgkm0(); sched0();
        ep = uniform64(((u64)__float_as_uint(e1) << 32) | __float_as_uint(e0));
    }
    u64 pseen = 0; u64* const pwords = p.flags + (F_ACT + (tn >> 1) * 32 + (lane & 31)) * FLAG_STRIDE;
#pragma nounroll
    for (int t = 0; t < nk - 3; ++t) {
        wait_vm<8>();
        if constexpr (ID == 4) { if (t == nk - 4 && wid == 4 && __any(pseen != ep)) wait_group(pwords, true, ep); }
        bar();
        if constexpr (ID == 4) { if (t == nk - 5 && wid == 4) flag_peek(pseen, pwords); }
        stage(t + 3, true, true);
        if constexpr (ID == 1) { if (t == LNT1 && wid == 4 && lane == 0) flag_store(p.flags + (F_LN + tm * 16 + tn) * FLAG_STRIDE, ep); }
    }
    if constexpr (NH > 0) {
        wait_vm<8>(); bar(); stage(nk, true, true, true);
#pragma nounroll
        for (int t = nk - 2; t < nk + NH - 3; ++t) { wait_vm<6>(); bar(); stage(t + 3, true, true, true); }
    }
    bool got = !recv;
    auto fetch = [&](u64 seen) {
        if (uniform64(seen) != ep) return false;
        asm volatile("" ::: "memory");
#pragma unroll
        for (int i = 0; i < (ID == 4 ? 4 : 8); ++i) DMA16_DEV(RSRC(p.xch, 256 * 32768), lds + XLDS + lw * 8192 + i * 1024, (unsigned)(lane * 16), xoff + i * 1024);
        return true;
    };
    wait_vm<NH ? 6 : 8>(); bar();
    u64* const lnw = p.flags + (F_LN + (lane & 16 ? l15 * 16 + tn : tm * 16 + l15)) * FLAG_STRIDE;
    u64 seen0 = flag_load(ID == 1 ? lnw : flag);
    wait_vm<0>();
    if (!got) got = fetch(seen0);
    if constexpr (ID == 1) { if (wid == 4) {
        if (__any(seen0 != ep)) wait_group(lnw, true, ep);
        asm volatile("" ::: "memory");
#pragma unroll
        for (int i = 0; i < 3; ++i) DMA16_DEV(RSRC(p.ln1, 3 * SEQ * 4), lds + XLDS + i * 1024, lane < 32 ? (unsigned)(lane * 16) : 0x7ffffff0u, (i * SEQ + (i < 2 ? tm : tn) * 128) * 4);
    } }
    if constexpr (ID == 1) g128_load<2, true>(p, lds, wid, ep);
    bar();
    if constexpr (ID != 1) seen0 = flag_load(flag);
    if constexpr (ID == 2) { if (!got) got = fetch(seen0); }
    bar();
    if (!got) got = fetch(seen0);
    if (recv) {
#pragma nounroll
        for (int spin = 0; !got && spin < (1 << 20); ++spin) { got = fetch(flag_load(flag)); __builtin_amdgcn_s_sleep(1); }
        wait_vm<0>(); bar();
    }
    if constexpr (ID == 2) { if (!recv) colsum_partial(p, lds, lw); }
    if constexpr (ID == 1) { wait_vm<4>(); bar(); }
    if constexpr (ID != 4) bar();
}

template <int ID>
__device__ __forceinline__ void g128_compute(const Params& p, LAS unsigned char* lds, const int wid, u64& ep) {
    using C = Cfg<ID>;
    constexpr int STAGE = 32768, XLDS = 4 * STAGE;
    const int lane = threadIdx.x & 63, l15 = lane & 15, q = lane >> 4;
    const int xcd = blockIdx.x & 7, idx = blockIdx.x >> 3, im = idx & 3, in = idx >> 2;
    constexpr int NH = ID == 4 ? 2 * (RE4 + RO4) : 0;
    const int ks = (xcd & 1) ^ (ID == 2); const bool recv = ID == 4 || (ID == 2 && ks);
    const int tm = (ID == 1 ? (xcd & 3) : ((xcd >> 1) & 1)) * 4 + im, tn = (xcd >> 2) * 8 + in;
    const int k0 = ID != 1 && ks ? C::NKS : 0, nk = ID == 4 ? 28 - 2 * (ks ? RO4 : RE4) : ID == 2 && ks ? C::NKT - C::NKS : C::NKS;
    const int xb = ID == 4 ? 128 + (blockIdx.x ^ (wid >> 2)) : blockIdx.x >> 1;
    u64* const flag = p.flags + (F_PAIR + xb * 4 + (wid & 3)) * FLAG_STRIDE;
    const unsigned xoff = (unsigned)(ID == 4 ? (128 + xb) * 16384 + (wid & 3) * 4096 : xb * 32768 + (wid & 3) * 8192);
    const int wm = wid & 1, wn = wid >> 1;
    const int eo = (q & 1) * 16 + (q >> 1) * 8;
    f32x4 acc[4][4];
#pragma unroll
    for (int m = 0; m < 4; ++m)
#pragma unroll
        for (int n = 0; n < 4; ++n) acc[m][n] = (f32x4){0.f, 0.f, 0.f, 0.f};
    const int row0 = tn * 128 + (ID == 4 ? ks * 64 + wn * 32 : wn * 64) + l15, col0 = tm * 128 + wm * 64 + eo;
    if constexpr (ID != 1) {
        constexpr int NN = ID == 4 ? 2 : 4;
        if (ID == 4 || !recv) {
            if constexpr (ID == 2) { wait_vm<0>(); bar(); __builtin_amdgcn_s_sleep(4); }
            u32x4 r[NN][2];
#pragma unroll
            for (int n = 0; n < NN; ++n)
#pragma unroll
                for (int mp = 0; mp < 2; ++mp)
                    r[n][mp] = __builtin_amdgcn_raw_buffer_load_b128(RSRC(ID == 2 ? p.x : p.h, SEQ * DM * 2), (unsigned)(ID == 2 ? ((row0 + n * 16) * DM + col0 + mp * 32) * 2 : ((col0 / 8 + mp * 4) * SEQ + row0 + n * 16) * 16), 0, ID == 4 ? SC1 : 0);
            if constexpr (ID == 4) { wait_vm<4>(); bar(); }
#pragma unroll
            for (int n = 0; n < NN; ++n)
#pragma unroll
                for (int mp = 0; mp < 2; ++mp) {
                    unsigned r0 = r[n][mp][0], r1 = r[n][mp][1], r2 = r[n][mp][2], r3 = r[n][mp][3];
                    swap16u(r0, r2); swap16u(r1, r3);
                    acc[2 * mp][n] = (f32x4){bf_lo(r0), bf_hi(r0), bf_lo(r1), bf_hi(r1)}; acc[2 * mp + 1][n] = (f32x4){bf_lo(r2), bf_hi(r2), bf_lo(r3), bf_hi(r3)};
                }
        } else { wait_vm<0>(); bar(); }
    }
    unsigned pfc = 0;
    if constexpr (ID == 1) {
        ep = uniform64(flag_load(p.flags)) + 1;
        if (threadIdx.x == 0) *(LAS u64*)(lds + XLDS + 3072) = ep;
        wait_lgkm0();
        if (idx == 0) {
            unsigned long long pc; int lo, hi;
            asm volatile("s_getpc_b64 %0\n s_mov_b32 %1, _Z7fused_k6Params@rel32@lo+4\n s_mov_b32 %2, _Z6tail_kv@rel32@lo+12" : "=s"(pc), "=s"(lo), "=s"(hi));
            touch(pfc, threadIdx.x * 128, (const void*)(pc + (long long)lo), (unsigned)max(hi - lo, 0)); touch(pfc, threadIdx.x * 128 + 32768, (const void*)(pc + (long long)lo), (unsigned)max(hi - lo, 0));
        }
    }
    constexpr int NA = C::FA == CHK ? 1 : C::FA == ROW ? 2 : 4, NB = C::FB == CHK ? 1 : 2;
    unsigned adA[NA], adB[NB];
    const unsigned lds0 = (unsigned)(size_t)lds, row_sw = lds0 + (unsigned)(l15 * 128 + ((q ^ (lane & 7)) << 4)), chk = lds0 + (unsigned)(q * 2048 + l15 * 16);
#pragma unroll
    for (int i = 0; i < NA; ++i) {
        if constexpr (C::FA == ROW) adA[i] = (row_sw ^ (unsigned)(i * 64)) + (unsigned)(wm * 8192);
        if constexpr (C::FA == CHK) adA[i] = chk + (unsigned)(wm * 1024);
        if constexpr (C::FA == TR) { const int q4 = l15 >> 2; adA[i] = lds0 + (unsigned)((8 * q + q4) * 256 + (((wm * 4 + i) ^ (q4 | ((q & 1) << 2))) << 5) + 8 * (lane & 3)); }
    }
#pragma unroll
    for (int i = 0; i < NB; ++i) adB[i] = C::FB == CHK ? 16384u + chk + (unsigned)(wn * 512) : 16384u + (row_sw ^ (unsigned)(i * 64)) + (unsigned)(wn * 8192);
    constexpr int W = C::FA == TR ? 2 : 1, NR = 4 + 4 * W;
    auto rd = [&](auto ic, Frag (&ra)[4], bf16x8 (&rb)[4], auto kkc) {
        constexpr int kk = decltype(kkc)::value, i = decltype(ic)::value;
        if constexpr (i == 0 || (i > W && i <= W + 3)) { constexpr int n = i == 0 ? 0 : i - W;
            if constexpr (C::FB == CHK) rb[n] = lds_read128<kk * 8192 + (n >> 1) * 1024 + (n & 1) * 256>(adB[0]);
            else rb[n] = lds_read128<n * 2048>(adB[kk]); }
        else { constexpr int j = i <= W ? i - 1 : i - 4, m = j / W;
            if constexpr (C::FA != TR) { bf16x8 v;
                if constexpr (C::FA == CHK) v = lds_read128<kk * 8192 + m * 256>(adA[0]); else v = lds_read128<m * 2048>(adA[kk]);
                const u32x4 w = __builtin_bit_cast(u32x4, v); ra[m].lo = (u32x2){w[0], w[1]}; ra[m].hi = (u32x2){w[2], w[3]}; }
            else if constexpr (j % 2 == 0) ra[m].lo = tr_read<kk * 8192>(adA[m]);
            else ra[m].hi = tr_read<kk * 8192 + 1024>(adA[m]); }
    };
    auto phase = [&](const Frag (&ra)[4], const bf16x8 (&rb)[4], Frag (&na)[4], bf16x8 (&nb)[4], int dstep, auto kkc) {
        static_for<0, 16>([&](auto sc) {
            constexpr int s = decltype(sc)::value, m = s >> 2, n = s & 3;
            if constexpr (n == 0) { wait_lgkm<(3 - m) * W + (s < NR ? s : NR)>(); sched0(); }
            acc[m][n] = MFMA(ra[m].v(), rb[n], acc[m][n]);
            if constexpr (s < NR) rd(sc, na, nb, kkc);
            if constexpr (s >= 12 && decltype(kkc)::value == 1) {
                if constexpr (s - 12 < NA) bump(adA[s - 12], dstep);
                if constexpr (s - 12 < NB) bump(adB[s - 12], dstep);
            }
            sched0();
        });
    };
    Frag a0[4], a1[4]; bf16x8 b0[4], b1[4];
    constexpr std::integral_constant<int, 0> K0{}; constexpr std::integral_constant<int, 1> K1{};
    if constexpr (ID != 1) bar();
    bar();
    static_for<0, NR>([&](auto ic) { rd(ic, a0, b0, K0); });
    sched0();
    const int rx = tm * 128 + tn * 8 + wid * 2 + (lane >> 5), rw = tn * 128 + tm * 8 + wid * 2 + (lane >> 5);
    u32x4 xv[4], wv[4];
    if constexpr (ID == 1) {
#pragma unroll
        for (int i = 0; i < 4; ++i) { xv[i] = __builtin_amdgcn_raw_buffer_load_b128(RSRC(p.x, SEQ * DM * 2), (unsigned)(rx * 2048 + (lane & 31) * 16 + i * 512), 0, 0);
                                      wv[i] = __builtin_amdgcn_raw_buffer_load_b128(RSRC(tn < 8 ? p.wre : p.wim, DM * DM * 2), (unsigned)((rw & 1023) * 2048 + (lane & 31) * 16 + i * 512), 0, 0); }
        sched0();
    }
    auto steps = [&](int t0, int t1) {
#pragma nounroll
        for (int t = t0; t < t1; ++t) {
            phase(a0, b0, a1, b1, (t & 3) == 3 ? -3 * STAGE : STAGE, K1);
            if constexpr (ID == 1) { if (t == LNT1) wait_vm<0>(); }
            bar();
            phase(a1, b1, a0, b0, 0, K0);
        }
    };
    if constexpr (ID == 1) {
        steps(0, LNT0);
        wait_lgkm0(); sched0();
        f32x2 sx = {0.f, 0.f}, sw = sx, sq = sx;
#pragma unroll
        for (int i = 0; i < 16; ++i) { sx += (f32x2){bf_lo(xv[i >> 2][i & 3]), bf_hi(xv[i >> 2][i & 3])}; sw += (f32x2){bf_lo(wv[i >> 2][i & 3]), bf_hi(wv[i >> 2][i & 3])}; }
        const float mean = hsum(sx[0] + sx[1]) * (1.f / DM);
#pragma unroll
        for (int i = 0; i < 16; ++i) { const f32x2 d = (f32x2){bf_lo(xv[i >> 2][i & 3]), bf_hi(xv[i >> 2][i & 3])} - mean; sq += d * d; }
        const float rstd = __builtin_amdgcn_rsqf(hsum(sq[0] + sq[1]) * (1.f / DM) + LN_EPS), wsum = hsum(sw[0] + sw[1]);
        const int j = lane & 31;
        __builtin_amdgcn_raw_buffer_store_b32(__float_as_uint(j == 0 ? mean : j == 1 ? rstd : wsum), RSRC(p.ln1, 3 * SEQ * 4), j < 3 ? (unsigned)((j * SEQ + (j < 2 ? rx : rw)) * 4) : 0x7ffffff0u, 0, SC1);
        sched0();
        steps(LNT0, nk);
    } else steps(0, nk);
    wait_lgkm0(); keep(a0, b0); sched0();
    auto send = [&](int h0) {
        const auto rx = RSRC(p.xch, 256 * 32768);
#pragma unroll
        for (int m = 0; m < 4; ++m)
#pragma unroll
            for (int h = h0; h < 2; ++h) {
                const f32x4 u = acc[m][2 * h], v = acc[m][2 * h + 1];
                __builtin_amdgcn_raw_buffer_store_b128((u32x4){pack_bf16(u[0], u[1]), pack_bf16(u[2], u[3]), pack_bf16(v[0], v[1]), pack_bf16(v[2], v[3])}, rx, xoff + (unsigned)((h0 ? m : m * 2 + h) * 1024 + lane * 16), 0, SC1);
            }
    };
    if constexpr (NH > 0) {
        send(1); sched0();
        auto phaseH = [&](const Frag (&ra)[4], const bf16x8 (&rb)[4], Frag (&na)[4], bf16x8 (&nb)[4], int dstep, auto kkc) {
            static_for<0, 8>([&](auto sc) {
                constexpr int s = decltype(sc)::value;
                if constexpr ((s & 1) == 0) { wait_lgkm<6 - s + (s < 5 ? 2 * s : 10)>(); sched0(); }
                acc[s >> 1][s & 1] = MFMA(ra[s >> 1].v(), rb[s & 1], acc[s >> 1][s & 1]);
                if constexpr (s < 5) { rd(std::integral_constant<int, s < 2 ? 2 * s : 2 * s + 2>{}, na, nb, kkc); rd(std::integral_constant<int, s < 2 ? 2 * s + 1 : 2 * s + 3>{}, na, nb, kkc); }
                if constexpr (s >= 5 && decltype(kkc)::value == 1) { if constexpr (s < 7) { bump(adA[2 * (s - 5)], dstep); bump(adA[2 * (s - 5) + 1], dstep); } else bump(adB[0], dstep); }
                sched0();
            });
        };
#pragma nounroll
        for (int t = nk; t < nk + NH; ++t) {
            phaseH(a0, b0, a1, b1, (t & 3) == 3 ? -3 * STAGE : STAGE, K1);
            bar();
            phaseH(a1, b1, a0, b0, 0, K0);
            if (t == nk + FLAGD) { wait_vm<0>(); if (lane == 0) flag_store(flag, ep); }
        }
        wait_lgkm0();
        asm volatile("" ::"v"(b0[0]), "v"(b0[1]));
#pragma unroll
        for (int i = 0; i < 4; ++i) asm volatile("" ::"v"(a0[i].lo), "v"(a0[i].hi));
        sched0();
    }
    if constexpr (ID == 1) {
        wait_vm<0>(); asm volatile("; pfc %0" ::"v"(pfc));
        bar();
        const auto ro = RSRC(p.abT, 2 * SEQ * DM * 2);
        const float sgn = tn < 8 ? 1.f : -1.f;
        f32x4 mean[4], rs[4]; float ws[4];
        static_for<0, 4>([&](auto ic) { constexpr int i = decltype(ic)::value; const unsigned a = lds0 + (unsigned)(XLDS + wm * 256 + q * 16);
            mean[i] = __builtin_bit_cast(f32x4, lds_read128<i * 64>(a)); rs[i] = __builtin_bit_cast(f32x4, lds_read128<1024 + i * 64>(a));
            ws[i] = lds_read32<2048 + i * 64>(lds0 + (unsigned)(XLDS + wn * 256 + l15 * 4)); });
        wait_lgkm0(); sched0();
#pragma unroll
        for (int m = 0; m < 4; ++m) {
            const f32x4 r = rs[m] * sgn;
#pragma unroll
            for (int n = 0; n < 4; ++n) acc[m][n] = (acc[m][n] - mean[m] * ws[n]) * r;
        }
#pragma unroll
        for (int n = 0; n < 4; ++n) { const int e = (tn & 7) * 128 + wn * 64 + n * 16 + l15;
#pragma unroll
            for (int mp = 0; mp < 2; ++mp) {
                unsigned x0 = pack_bf16(acc[2 * mp][n][0], acc[2 * mp][n][1]), x1 = pack_bf16(acc[2 * mp][n][2], acc[2 * mp][n][3]);
                unsigned y0 = pack_bf16(acc[2 * mp + 1][n][0], acc[2 * mp + 1][n][1]), y1 = pack_bf16(acc[2 * mp + 1][n][2], acc[2 * mp + 1][n][3]);
                swap16u(x0, y0); swap16u(x1, y1);
                const int s = (tn >> 3) * SEQ + col0 + mp * 32;
                __builtin_amdgcn_raw_buffer_store_b128((u32x4){x0, x1, y0, y1}, ro, (unsigned)(((s >> 3) * DM + e) * 16), 0, SC1);
            } }
    } else if (!recv) {
        send(0);
        wait_vm<0>();
        if (lane == 0) flag_store(flag, ep);
        if constexpr (ID == 2) bar();
    } else {
        bar();
        const unsigned px = (unsigned)(size_t)lds + (unsigned)(XLDS + wid * 8192 + lane * 16);
        constexpr int HH = ID == 4 ? 1 : 2;
        u32x4 pw[4 * HH];
        static_for<0, 4 * HH>([&](auto ic) { pw[decltype(ic)::value] = __builtin_bit_cast(u32x4, lds_read128<decltype(ic)::value * 1024>(px)); });
        wait_lgkm0(); sched0();
#pragma unroll
        for (int m = 0; m < 4; ++m)
#pragma unroll
            for (int h = 0; h < HH; ++h) { const u32x4 w = pw[m * HH + h];
                acc[m][2 * h] += (f32x4){bf_lo(w[0]), bf_hi(w[0]), bf_lo(w[1]), bf_hi(w[1])}; acc[m][2 * h + 1] += (f32x4){bf_lo(w[2]), bf_hi(w[2]), bf_lo(w[3]), bf_hi(w[3])}; }
        const auto ro = RSRC(ID == 2 ? p.h : p.out, ID == 2 ? 136 * SEQ * 16 : SEQ * DM * 2);
#pragma unroll
        for (int n = 0; n < 2 * HH; ++n) {
            unsigned o[4][2];
#pragma unroll
            for (int m = 0; m < 4; ++m) { o[m][0] = pack_bf16(acc[m][n][0], acc[m][n][1]); o[m][1] = pack_bf16(acc[m][n][2], acc[m][n][3]); }
            if constexpr (ID == 2) {
                const f32x4 s4 = (acc[0][n] + acc[1][n]) + (acc[2][n] + acc[3][n]);
                const float mw = qsum((s4[0] + s4[1]) + (s4[2] + s4[3])) * (1.f / 64.f);
                const f32x4 d0 = acc[0][n] - mw, d1 = acc[1][n] - mw, d2 = acc[2][n] - mw, d3 = acc[3][n] - mw, q4 = (d0 * d0 + d1 * d1) + (d2 * d2 + d3 * d3);
                float m2 = (q4[0] + q4[1]) + (q4[2] + q4[3]);
                m2 = qsum(m2);
                if (q == 0) __builtin_amdgcn_raw_buffer_store_b64((u32x2){__float_as_uint(mw), __float_as_uint(m2)}, ro, (unsigned)(((128 + tm) * SEQ + row0 + n * 16) * 16 + wm * 8), 0, SC1);
            }
#pragma unroll
            for (int mp = 0; mp < 2; ++mp) {
                swap16u(o[2 * mp][0], o[2 * mp + 1][0]); swap16u(o[2 * mp][1], o[2 * mp + 1][1]);
                __builtin_amdgcn_raw_buffer_store_b128((u32x4){o[2 * mp][0], o[2 * mp][1], o[2 * mp + 1][0], o[2 * mp + 1][1]}, ro,
                    (unsigned)(ID == 4 ? ((row0 + n * 16) * DM + col0 + mp * 32) * 2 : ((col0 / 8 + mp * 4) * SEQ + row0 + n * 16) * 16), 0, SC1);
            }
        }
        if constexpr (ID == 2) { wait_vm<0>(); bar(); }
    }
}
#endif

#if defined(__HIP_DEVICE_COMPILE__)
template <int GRP>
__device__ __forceinline__ void gemm3_body(const Params& p, LAS unsigned char* lds, const int wid, u64& ep) {
    constexpr int NK = 16, FA = 7, HB0 = 3 * 32768;
    const int lane = threadIdx.x & 63;
    const int wm = wid & 1, wn = wid >> 1, l15 = lane & 15, q = lane >> 4;
    const int xcd = blockIdx.x & 7, idx = blockIdx.x >> 3, im = idx & 7, in = idx >> 3;
    const int tm = (xcd & 3) * 8 + im, tn = (xcd >> 2) * 4 + in;
    const G3W gw(p, wid, lane);
    unsigned pf = 0;
    u64* words = p.flags + (F_H + tn * 32) * FLAG_STRIDE;
    if (threadIdx.x == 0 && !(xcd & 1)) flag_store(words + tm * FLAG_STRIDE, ep);
    if (wid) __builtin_amdgcn_s_sleep(2);
    gw.early(lds, wid, 0); gw.early(lds, wid, 1);
    if ((xcd & 1) && wid == 1) colsum_final(p, lds, ep);
    if (wid == 0) {
        wait_group(words + (lane & 31) * FLAG_STRIDE, (lane & 8) == 0, ep);
        if (blockIdx.x == 0 && lane == 0) flag_store(p.flags, ep);
    }
    bar();
    const unsigned voffB = (unsigned)((wid * SEQ + tn * 256 + lane) * 16);
    struct Src { const void* base; int so; unsigned nrec; };
    auto pin = [](Src x) { asm volatile("" : "+s"(x.base), "+s"(x.so), "+s"(x.nrec)); return x; };
    auto srcW = [&](int t) { return pin(Src{t < NK ? (const void*)gw.ptr : (const void*)(p.csum + GRP * 3584), t < NK ? gw.kstep(t) * 64 * FF * 2 : 0, t < NK ? (unsigned)(DM * FF * 2) : t == NK ? (unsigned)(2 * FF * 2) : 0u}); };
    auto srcH = [&](int t) { Src x{nullptr, (t < NK ? gw.kstep(t) : NK) * 8 * SEQ * 16, 136 * SEQ * 16}; asm volatile("" : "+s"(x.so)); return x; };
    auto stageW = [&](Src x, int slot, int i) { gw.piece(lds, wid, x.base, x.so, x.nrec, slot, i); };
    auto stageH = [&](Src x, int slot, int i) { DMA16_DEV(RSRC(p.h, x.nrec), lds + HB0 + slot * 32768 + wid * 4096 + i * 1024, voffB, x.so + i * 1024); };
#pragma unroll
    for (int t = 0; t < 2; ++t) {
        const Src xh = srcH(t);
#pragma unroll
        for (int i = 0; i < 4; ++i) stageH(xh, t, i);
    }

    unsigned adA[FA];
    const unsigned lds0 = (unsigned)(size_t)lds;
#pragma unroll
    for (int m = 0; m < FA; ++m) { const int q4 = l15 >> 2, pp = lane & 3, reg = m < 6 ? m & 1 : pp >> 1, ch = m < 6 ? wm * 8 + (m & 6) + (pp >> 1) : 6 + wm;
        adA[m] = lds0 + (unsigned)(reg * 16384 + (8 * q + q4) * 256 + ((ch ^ (((q4 | ((q & 1) << 2)) << 1) | reg)) << 4) + 8 * (pp & 1)); }
    unsigned adB = lds0 + (unsigned)(HB0 + q * 4096 + (wn * 64 + l15) * 16);
    auto rd = [&](auto ic, Frag (&ra)[FA], bf16x8 (&rb)[4], auto kkc) {
        constexpr int kk = decltype(kkc)::value, i = decltype(ic)::value;
        if constexpr (i < 4) rb[i] = lds_read128<kk * 16384 + i * 256>(adB);
        else if constexpr (i % 2 == 0) ra[(i - 4) / 2].lo = tr_read<kk * 8192>(adA[(i - 4) / 2]);
        else ra[(i - 4) / 2].hi = tr_read<kk * 8192 + 1024>(adA[(i - 4) / 2]);
    };
    f32x4 acc[FA][4];
#pragma unroll
    for (int m = 0; m < FA; ++m)
#pragma unroll
        for (int n = 0; n < 4; ++n) acc[m][n] = (f32x4){0.f, 0.f, 0.f, 0.f};
    auto phase = [&](const Frag (&ra)[FA], const bf16x8 (&rb)[4], Frag (&na)[FA], bf16x8 (&nb)[4], int dw, int dh, auto kkc, auto&& dma, auto&& prep) {
        static_for<0, 28>([&](auto sc) {
            constexpr int s = decltype(sc)::value, m = s >> 2, n = s & 3;
            if constexpr (decltype(kkc)::value == 0) { if constexpr (s == 0) __builtin_amdgcn_s_setprio(3); if constexpr (s == 18) __builtin_amdgcn_s_setprio(2); }
            else { if constexpr (s == 6) __builtin_amdgcn_s_setprio(1); if constexpr (s == 22) __builtin_amdgcn_s_setprio(0); }
            acc[m][n] = MFMA(ra[m].v(), rb[n], acc[m][n]);
            sched0();
            if constexpr (s < 18) rd(sc, na, nb, kkc);
            if constexpr (s == 20) prep();
            if constexpr (s % 4 == 2 * GRP && s < 16) dma(s / 4);
            if constexpr (decltype(kkc)::value == 1 && s >= 18 && s < 26) { if constexpr (s < 25) bump(adA[s - 18], dw); else bump(adB, dh); }
            sched0();
        });
        wait_lgkm0(); sched0();
    };
    Frag a0[FA], a1[FA]; bf16x8 b0[4], b1[4];
    constexpr std::integral_constant<int, 0> K0{}; constexpr std::integral_constant<int, 1> K1{};
    wait_vm<4>(); bar();
    static_for<0, 18>([&](auto ic) { rd(ic, a0, b0, K0); });
    wait_lgkm0(); sched0();
    const unsigned pfo = (unsigned)(((blockIdx.x * 8 + wid) * 28 + (lane < 28 ? lane : 0)) * 128); const int pft = 1 + idx % 12;
    Src xw = srcW(2), xh;
    auto phaseA = [&](int t, int sw2) {
        phase(a0, b0, a1, b1, sw2 == 1 ? -65536 : 32768, (t & 1) ? -32768 : 32768, K1, [&](int i) { stageW(xw, sw2, i);
            if (i == 3) touch(pf, pfo, p.wd, t == pft ? FF * DM * 2 : 0);
        }, [&]() { xh = srcH(t + 2); });
        wait_vm<5>();
    };
    u64 cseen = 0;
    phaseA(0, 2);
    int sw2 = 0;
#pragma nounroll
    for (int t = 0; t < NK - 1; ++t) {
        if (__builtin_expect(t >= 11 && t <= 12 && (wid & 3) == 0, 0)) {
            const int sl = min((tm * 112 >> 6) + (lane & 3), (tm * 112 + 111) >> 6);
            u64* w = p.flags + (F_CS + GRP * 56 + sl) * FLAG_STRIDE;
            if (t == 11) flag_peek(cseen, w); else if (__any(cseen != ep)) wait_group(w, true, ep);
        }
        bar();
        phase(a1, b1, a0, b0, 0, 0, K0, [&](int i) { stageH(xh, t & 1, i); }, [&]() { xw = srcW(t + 3); });
        phaseA(t + 1, sw2);
        sw2 = sw2 == 2 ? 0 : sw2 + 1;
    }
    bar();
    __builtin_amdgcn_s_setprio(GRP ? 0 : 3);
    f32x4 csv[FA], sp[4][2];
    static_for<0, 16>([&](auto sc) {
        constexpr int s = decltype(sc)::value;
        acc[s >> 2][s & 3] = MFMA(a1[s >> 2].v(), b1[s & 3], acc[s >> 2][s & 3]);
        if constexpr (s < 8) sp[s >> 1][s & 1] = __builtin_bit_cast(f32x4, lds_read128<(s & 1) * 16384 + (s >> 1) * 256>(adB));
        else if constexpr (s < 15) { constexpr int m = s - 8; const int reg = m < 6 ? m & 1 : q >> 1, c4 = m < 6 ? wm * 2 + (m & 6) * 2 + q : (wm ? 0 : 12) + (q & 1);
            csv[m] = __builtin_bit_cast(f32x4, lds_read128<32768>(lds0 + (unsigned)(reg * 16384 + wm * 256 + ((c4 ^ ((wm << 1) | reg)) << 4)))); }
        if constexpr (GRP == 1 && s == 2) g128_load<4, true>(p, lds, wid, ep);
        sched0();
    });
    wait_lgkm0(); wait_vm<GRP ? 4 : 0>(); asm volatile("; pf %0" ::"v"(pf)); sched0();
    float mean[4], c1[4], ve[4];
#pragma unroll
    for (int n = 0; n < 4; ++n) {
        const f32x4 s0 = sp[n][0], s1 = sp[n][1];
        mean[n] = qsum((s0[0] + s0[2]) + (s1[0] + s1[2])) * (1.f / 16.f);
        const float d0 = s0[0] - mean[n], d1 = s0[2] - mean[n], d2 = s1[0] - mean[n], d3 = s1[2] - mean[n];
        const float m2 = qsum((s0[1] + s0[3]) + (s1[1] + s1[3]) + 64.f * ((d0 * d0 + d1 * d1) + (d2 * d2 + d3 * d3)));
        ve[n] = m2 * (1.f / DM) + LN_EPS; c1[n] = -1.44269504f * __builtin_amdgcn_rsqf(ve[n]);
    }
#pragma unroll
    for (int s = 16; s < 28; ++s) acc[s >> 2][s & 3] = MFMA(a1[s >> 2].v(), b1[s & 3], acc[s >> 2][s & 3]);
#pragma unroll
    for (int s = 16; s < 28; ++s) { __builtin_amdgcn_sched_group_barrier(0x008, 1, 0); __builtin_amdgcn_sched_group_barrier(0x002, 10, 0); }
    sched0();
    const auto ro = RSRC(p.act, SEQ * FF * 2);
    auto silu2 = [&](f32x2 g, f32x2 u, int n) {
        const f32x2 x = g * c1[n], d = (f32x2){__builtin_amdgcn_exp2f(x[0]), __builtin_amdgcn_exp2f(x[1])} * ve[n] + ve[n];
        const f32x2 r = g * u * (f32x2){__builtin_amdgcn_rcpf(d[0]), __builtin_amdgcn_rcpf(d[1])};
        return pack_bf16(r[0], r[1]);
    };
#pragma unroll
    for (int j = 0; j < 3; ++j)
#pragma unroll
        for (int np = 0; np < 2; ++np) {
            unsigned o[2][2];
#pragma unroll
            for (int i = 0; i < 2; ++i) { const int n = 2 * np + i; const f32x4 g = acc[2 * j][n] - mean[n] * csv[2 * j], u = acc[2 * j + 1][n] - mean[n] * csv[2 * j + 1];
                o[i][0] = silu2((f32x2){g[0], g[1]}, (f32x2){u[0], u[1]}, n); o[i][1] = silu2((f32x2){g[2], g[3]}, (f32x2){u[2], u[3]}, n); }
            swap16u(o[0][0], o[1][0]); swap16u(o[0][1], o[1][1]);
            __builtin_amdgcn_raw_buffer_store_b128((u32x4){o[0][0], o[0][1], o[1][0], o[1][1]}, ro, (unsigned)(((tm * 14 + wm * 8 + 2 * j + (q >> 1)) * SEQ + tn * 256 + wn * 64 + np * 32 + (lane & 31)) * 16), 0, SC1);
        }
    {
        unsigned o[4];
#pragma unroll
        for (int n = 0; n < 4; ++n) {
            const f32x4 v = acc[6][n] - mean[n] * csv[6];
            float g0 = v[0], g1 = v[1], u0 = v[2], u1 = v[3];
            swap32(g0, u0); swap32(g1, u1);
            o[n] = silu2((f32x2){g0, g1}, (f32x2){u0, u1}, n);
        }
        swap16u(o[0], o[1]); swap16u(o[2], o[3]); swap32u(o[0], o[2]); swap32u(o[1], o[3]);
        __builtin_amdgcn_raw_buffer_store_b128((u32x4){o[0], o[2], o[1], o[3]}, ro, (unsigned)(((tm * 14 + 6 + wm) * SEQ + tn * 256 + wn * 64 + lane) * 16), 0, SC1);
    }
    __builtin_amdgcn_s_setprio(0);
    if constexpr (GRP == 1) { wait_vm<0>(); bar(); }
}
#endif

__global__ __launch_bounds__(512, 2) void fused_k(const Params p) {
#if defined(__HIP_DEVICE_COMPILE__)
    __shared__ __attribute__((aligned(16))) unsigned char lds_raw[5 * 32768];
    LAS unsigned char* lds = (LAS unsigned char*)lds_raw;
    const int wid = __builtin_amdgcn_readfirstlane(threadIdx.x >> 6);
    u64 ep = 0;
    if (wid < 4) { g128_compute<1>(p, lds, wid, ep); g128_compute<2>(p, lds, wid, ep); gemm3_body<0>(p, lds, wid, ep); g128_compute<4>(p, lds, wid, ep); }
    else { g128_load<1, false>(p, lds, wid, ep); g128_load<2, false>(p, lds, wid, ep); gemm3_body<1>(p, lds, wid, ep); g128_load<4, false>(p, lds, wid, ep); }
#endif
}
__global__ void tail_k() {}

extern "C" void kernel_launch(void* const* d_in, const int* in_sizes, int n_in, void* d_out, int out_size, void* d_ws, size_t ws_size, hipStream_t stream) {
    Params p;
    p.x = (const u16*)d_in[0]; p.cre = (const u16*)d_in[1]; p.cim = (const u16*)d_in[2]; p.wre = (const u16*)d_in[3]; p.wim = (const u16*)d_in[4];
    p.wg = (const u16*)d_in[6]; p.wu = (const u16*)d_in[7]; p.wd = (const u16*)d_in[8];
    p.out = (u16*)d_out;
    char* ws = (char*)d_ws + ((ws_size - (42u  __H0__ )) & ~(size_t)0x1fffff);
    p.abT = (u16*)ws; p.h = (u16*)(ws + (8u << 20)); p.act = (u16*)(ws + (16u << 20));
    p.xch = (u16*)(ws + (32u << 20)); p.flags = (unsigned long long*)(ws + (40u << 20)); p.csum = (float*)(ws + (40u << 20) + (512u << 10)); p.ln1 = (float*)(ws + (40u << 20) + (768u << 10));
    fused_k<<<256, 512, 0, stream>>>(p);
}
```

```cpp
#include <hip/hip_runtime.h>
#include <hip/hip_bf16.h>
#include <type_traits>

#define LAS __attribute__((address_space(3)))
typedef unsigned short u16;
typedef __bf16 bf16x8 __attribute__((ext_vector_type(8)));
typedef __bf16 bf16x2 __attribute__((ext_vector_type(2)));
typedef float f32x4 __attribute__((ext_vector_type(4)));
typedef float f32x2 __attribute__((ext_vector_type(2)));
typedef unsigned u32x4 __attribute__((ext_vector_type(4)));
typedef unsigned u32x2 __attribute__((ext_vector_type(2)));

constexpr int SEQ = 2048, DM = 1024, FF = 3584;
constexpr float LN_EPS = 1e-5f;
constexpr int SC1 = 16;

struct Params {
    const u16* x; const u16* cre; const u16* cim; const u16* wre; const u16* wim;
    const u16* wg; const u16* wu; const u16* wd;
    u16* out; u16* abT; u16* h; u16* act; u16* xch; unsigned long long* flags; float* csum; float* ln1;
};

template <int N> __device__ __forceinline__ void wait_vm() { asm volatile("s_waitcnt vmcnt(%0)" ::"n"(N) : "memory"); }
__device__ __forceinline__ void wait_lgkm0() { __builtin_amdgcn_s_waitcnt(0xC07F); asm volatile("s_waitcnt lgkmcnt(0)" ::: "memory"); }
__device__ __forceinline__ void sched0() { __builtin_amdgcn_sched_barrier(0); }
__device__ __forceinline__ void bar() { sched0(); asm volatile("" ::: "memory"); __builtin_amdgcn_s_barrier(); asm volatile("" ::: "memory"); sched0(); }
__device__ __forceinline__ unsigned pack_bf16(float lo, float hi) { bf16x2 c; c[0] = (__bf16)lo; c[1] = (__bf16)hi; return __builtin_bit_cast(unsigned, c); }
__device__ __forceinline__ float bf_lo(unsigned v) { return __uint_as_float(v << 16); }
__device__ __forceinline__ float bf_hi(unsigned v) { return __uint_as_float(v & 0xffff0000u); }
__device__ __forceinline__ void swap16(float& a, float& b) { auto r = __builtin_amdgcn_permlane16_swap(__float_as_uint(a), __float_as_uint(b), false, false); a = __uint_as_float(r[0]); b = __uint_as_float(r[1]); }
__device__ __forceinline__ void swap32(float& a, float& b) { auto r = __builtin_amdgcn_permlane32_swap(__float_as_uint(a), __float_as_uint(b), false, false); a = __uint_as_float(r[0]); b = __uint_as_float(r[1]); }
__device__ __forceinline__ void swap16u(unsigned& a, unsigned& b) { auto r = __builtin_amdgcn_permlane16_swap(a, b, false, false); a = r[0]; b = r[1]; }
__device__ __forceinline__ void swap32u(unsigned& a, unsigned& b) { auto r = __builtin_amdgcn_permlane32_swap(a, b, false, false); a = r[0]; b = r[1]; }
__device__ __forceinline__ float qsum(float v) { float a = v, b = v; swap16(a, b); a += b; b = a; swap32(a, b); return a + b; }

template <int CTRL> __device__ __forceinline__ float dpp_add(float v) { return v + __int_as_float(__builtin_amdgcn_update_dpp(0, __float_as_int(v), CTRL, 0xf, 0xf, false)); }
__device__ __forceinline__ float hsum(float v) { v = dpp_add<0x128>(v); v = dpp_add<0x124>(v); v = dpp_add<0x122>(v); v = dpp_add<0x121>(v); float b = v; swap16(v, b); return v + b; }

struct Frag { u32x2 lo, hi; __device__ __forceinline__ bf16x8 v() const { return __builtin_bit_cast(bf16x8, __builtin_shufflevector(lo, hi, 0, 1, 2, 3)); } };
template <int OFF> __device__ __forceinline__ u32x2 tr_read(unsigned addr) { u32x2 r; asm volatile("ds_read_b64_tr_b16 %0, %1 offset:%2" : "=v"(r) : "v"(addr), "n"(OFF) : "memory"); return r; }
template <int OFF> __device__ __forceinline__ bf16x8 lds_read128(unsigned addr) { bf16x8 r; asm volatile("ds_read_b128 %0, %1 offset:%2" : "=v"(r) : "v"(addr), "n"(OFF) : "memory"); return r; }
template <int OFF> __device__ __forceinline__ float lds_read32(unsigned addr) { float r; asm volatile("ds_read_b32 %0, %1 offset:%2" : "=v"(r) : "v"(addr), "n"(OFF) : "memory"); return r; }
template <int N> __device__ __forceinline__ void wait_lgkm() { asm volatile("s_waitcnt lgkmcnt(%0)" ::"n"(N) : "memory"); }
template <int I, int N, class F> __device__ __forceinline__ void static_for(F&& f) { if constexpr (I < N) { f(std::integral_constant<int, I>{}); static_for<I + 1, N>(f); } }
template <int NA> __device__ __forceinline__ void keep(const Frag (&a)[NA], const bf16x8 (&b)[4]) {
#pragma unroll
    for (int i = 0; i < NA; ++i) asm volatile("" ::"v"(a[i].lo), "v"(a[i].hi));
#pragma unroll
    for (int i = 0; i < 4; ++i) asm volatile("" ::"v"(b[i]));
}
__device__ __forceinline__ void bump(unsigned& a, int d) { asm volatile("v_add_u32 %0, %1, %0" : "+v"(a) : "s"(d)); }
#define MFMA(a, b, c) __builtin_amdgcn_mfma_f32_16x16x32_bf16(a, b, c, 0, 0, 0)
#define RSRC(ptr, bytes) __builtin_amdgcn_make_buffer_rsrc((void*)(ptr), 0, (bytes), 0x00020000)
#define DMA16(rs, ldsp, voff, soff) __builtin_amdgcn_raw_ptr_buffer_load_lds(rs, (LAS void*)(ldsp), 16, voff, soff, 0, 0)
#define DMA16_DEV(rs, ldsp, voff, soff) __builtin_amdgcn_raw_ptr_buffer_load_lds(rs, (LAS void*)(ldsp), 16, voff, soff, 0, SC1)

typedef unsigned long long u64;
constexpr int FLAG_STRIDE = 8, F_PAIR = 1, F_AB = F_PAIR + 1536, F_H = F_AB + 256, F_ACT = F_H + 256, F_CS = F_ACT + 256, F_LN = F_CS + 128;
__device__ __forceinline__ u64 flag_load(u64* w) { return __hip_atomic_load(w, __ATOMIC_RELAXED, __HIP_MEMORY_SCOPE_AGENT); }
__device__ __forceinline__ void flag_store(u64* w, u64 v) { __hip_atomic_store(w, v, __ATOMIC_RELAXED, __HIP_MEMORY_SCOPE_AGENT); }
__device__ __forceinline__ u64 uniform64(u64 v) { return ((u64)(unsigned)__builtin_amdgcn_readfirstlane((unsigned)(v >> 32)) << 32) | (unsigned)__builtin_amdgcn_readfirstlane((unsigned)v); }
__device__ __forceinline__ void wait_group(u64* word, bool need, u64 ep) {
#pragma nounroll
    for (int spin = 0; spin < (1 << 20); ++spin) {
        if (!__any(need && flag_load(word) != ep)) break;
        __builtin_amdgcn_s_sleep(2);
    }
}
#if defined(__HIP_DEVICE_COMPILE__)
__device__ __forceinline__ void touch(unsigned& sink, unsigned voff, const void* base, unsigned bytes) {
    asm volatile("s_nop 4\n\tbuffer_load_dword %0, %1, %2, 0 offen" : "+v"(sink) : "v"(voff), "s"(RSRC(base, bytes)) : "memory");
}
__device__ __forceinline__ void flag_peek(u64& v, const u64* w) { asm volatile("global_load_dwordx2 %0, %1, off sc1" : "+v"(v) : "v"(w) : "memory"); }
struct G3W {
    const u16* ptr; unsigned voff[2]; int phi, koff;
    __device__ __forceinline__ G3W(const Params& p, int wid, int lane) {
        const int xcd = blockIdx.x & 7, idx = blockIdx.x >> 3, tm = (xcd & 3) * 8 + (idx & 7), q = lane >> 4;
        ptr = wid < 4 ? p.wg : p.wu; phi = ((idx & 7) + (idx >> 3)) & 3; koff = xcd * 2;
#pragma unroll
        for (int i = 0; i < 2; ++i) { const int j = (lane & 15) ^ (((q | (i << 2)) << 1) | (wid >> 2));
            voff[i] = j < 14 ? (unsigned)(((wid & 3) * 16 + q) * FF * 2 + tm * 224 + j * 16) : 0x7ffffff0u; }
    }
    __device__ __forceinline__ int kstep(int t) const { return (t + koff + ((t & 3) == phi ? 4 : 0)) & 15; }
    __device__ __forceinline__ void piece(LAS unsigned char* lds, int wid, const void* base, int so, unsigned nrec, int slot, int i) const { DMA16_DEV(RSRC(base, nrec), lds + slot * 32768 + wid * 4096 + i * 1024, voff[i >> 1], so + i * 4 * FF * 2); }
    __device__ __forceinline__ void early(LAS unsigned char* lds, int wid, int t) const {
#pragma unroll
        for (int i = 0; i < 4; ++i) piece(lds, wid, ptr, kstep(t) * 64 * FF * 2, DM * FF * 2, t, i);
    }
};
__device__ __forceinline__ void colsum_partial(const Params& p, LAS unsigned char* lds, int lw) {
    const int lane = threadIdx.x & 63, slab = blockIdx.x >> 1, mat = slab >= 56, c0 = (slab - mat * 56) * 64;
    const auto rw = RSRC(mat ? p.wu : p.wg, slab < 112 ? DM * FF * 2 : 0);
    const unsigned vo = (unsigned)(((lw * 256 + (lane >> 3)) * FF + c0 + (lane & 7) * 8) * 2);
    u32x4 v[8]; f32x4 s0 = {0.f, 0.f, 0.f, 0.f}, s1 = s0;
    auto add = [&](const u32x4 w) { s0 += (f32x4){bf_lo(w[0]), bf_hi(w[0]), bf_lo(w[1]), bf_hi(w[1])}; s1 += (f32x4){bf_lo(w[2]), bf_hi(w[2]), bf_lo(w[3]), bf_hi(w[3])}; };
#pragma unroll
    for (int i = 0; i < 4; ++i) v[i] = __builtin_amdgcn_raw_buffer_load_b128(rw, vo, i * 8 * FF * 2, 0);
#pragma unroll
    for (int i = 0; i < 8; ++i) { add(v[i % 4]); v[i % 4] = __builtin_amdgcn_raw_buffer_load_b128(rw, vo, (4 + i) * 8 * FF * 2, 0); sched0(); }
#pragma unroll
    for (int i = 4; i < 8; ++i) v[i] = __builtin_amdgcn_raw_buffer_load_b128(rw, vo, (8 + i) * 8 * FF * 2, 0);
#pragma nounroll
    for (int j = 16; j < 40; j += 8)
#pragma unroll
        for (int i = 0; i < 8; ++i) { add(v[i]); v[i] = __builtin_amdgcn_raw_buffer_load_b128(rw, j < 32 ? vo : 0x7ffffff0u, (j + i) * 8 * FF * 2, 0); sched0(); }
    LAS float* tab = (LAS float*)(lds + 131072) + (lw * 8 + (lane >> 3)) * 64 + (lane & 7) * 8;
    *(LAS f32x4*)tab = s0; *(LAS f32x4*)(tab + 4) = s1;
    wait_lgkm0();
}
__device__ __forceinline__ void colsum_final(const Params& p, LAS unsigned char* lds, u64 ep) {
    const int lane = threadIdx.x & 63, slab = blockIdx.x >> 1, mat = slab >= 56, c0 = (slab - mat * 56) * 64;
    if (slab >= 112) return;
    const LAS float* tab = (const LAS float*)(lds + 131072);
    float a = 0.f, b = 0.f;
#pragma unroll
    for (int i = 0; i < 32; i += 2) { a += tab[i * 64 + lane]; b += tab[(i + 1) * 64 + lane]; }
    const int f = c0 + lane, t = f / 112, r = f - t * 112, h = r >= 56;
    __hip_atomic_store(p.csum + (mat * 2 + h) * 1792 + t * 56 + r - h * 56, a + b, __ATOMIC_RELAXED, __HIP_MEMORY_SCOPE_AGENT);
    wait_vm<0>();
    if (lane == 0) flag_store(p.flags + (F_CS + slab) * FLAG_STRIDE, ep);
}
#endif

enum { ROW, CHK, TR };
constexpr int NKS2 = 27;
constexpr int RE4 = 1, RO4 = 3;
constexpr int FLAGD = 1;
constexpr int LNT0 = 1, LNT1 = 5;
template <int ID> struct Cfg;
template <> struct Cfg<1> { static constexpr int NKT = 16, NKS = 16, FA = ROW, FB = ROW, LDB = DM; };
template <> struct Cfg<2> { static constexpr int NKT = 64, NKS = NKS2, FA = CHK, FB = ROW, LDB = SEQ; };
template <> struct Cfg<4> { static constexpr int NKT = 56, NKS = 0, FA = TR, FB = CHK, LDB = 0; };

#if defined(__HIP_DEVICE_COMPILE__)
template <int ID, bool PRE>
__device__ __forceinline__ void g128_load(const Params& p, LAS unsigned char* lds, const int wid, u64& ep) {
    using C = Cfg<ID>;
    constexpr int STAGE = 32768, XLDS = 4 * STAGE;
    const int lane = threadIdx.x & 63, l15 = lane & 15, q = lane >> 4;
    const int xcd = blockIdx.x & 7, idx = blockIdx.x >> 3, im = idx & 3, in = idx >> 2;
    constexpr int NH = ID == 4 ? 2 * (RE4 + RO4) : 0;
    const int ks = (xcd & 1) ^ (ID == 2); const bool recv = ID == 4 || (ID == 2 && ks);
    const int tm = (ID == 1 ? (xcd & 3) : ((xcd >> 1) & 1)) * 4 + im, tn = (xcd >> 2) * 8 + in;
    const int k0 = ID != 1 && ks ? C::NKS : 0, nk = ID == 4 ? 28 - 2 * (ks ? RO4 : RE4) : ID == 2 && ks ? C::NKT - C::NKS : C::NKS;
    const int xb = ID == 4 ? 128 + (blockIdx.x ^ (wid >> 2)) : blockIdx.x >> 1;
    u64* const flag = p.flags + (F_PAIR + xb * 4 + (wid & 3)) * FLAG_STRIDE;
    const unsigned xoff = (unsigned)(ID == 4 ? (128 + xb) * 16384 + (wid & 3) * 4096 : xb * 32768 + (wid & 3) * 8192);
    if constexpr (ID != 1 && !PRE) {
        if (wid == 4 && lane == 0) flag_store(p.flags + (ID == 2 ? F_AB + (tn & 7) * 32 + (tn >> 3) * 16 + (xcd & 3) * 4 + im : F_ACT + (tn >> 1) * 32 + (xcd & 3) * 8 + (idx & 7)) * FLAG_STRIDE, ep);
        sched0();
    }
    const int lw = wid - 4, phi = (im + in) & 3;
    const int koff = ID == 1 ? xcd * 2 : ((xcd >> (ID == 2 ? 1 : 2)) & 1) * (nk >> 1);
    const unsigned ksw = (unsigned)(((lane & 7) ^ (lane >> 3)) << 4);
    unsigned voffA[2], voffB;
    if constexpr (C::FA == ROW) voffA[0] = (unsigned)((tm * 128 + lw * 32 + (lane >> 3)) * DM * 2) + ksw;
    if constexpr (C::FA == CHK) voffA[0] = (unsigned)((lw * 2 * DM + tm * 128 + lane) * 16);
    if constexpr (C::FA == TR) {
#pragma unroll
        for (int i = 0; i < 2; ++i) voffA[i] = (unsigned)((lw * 16 + q) * DM * 2 + tm * 256 + ((l15 ^ ((q | (i << 2)) << 1)) << 4));
    }
    if constexpr (C::FB == ROW) voffB = (unsigned)((((ID == 1 ? tn & 7 : tn)) * 128 + lw * 32 + (lane >> 3)) * C::LDB * 2) + ksw;
    else voffB = (unsigned)((lw * 2 * SEQ + tn * 128 + lane) * 16);
    const auto ra = ID == 1 ? RSRC(p.x, SEQ * DM * 2) : ID == 2 ? RSRC(p.abT, 2 * SEQ * DM * 2) : RSRC(p.wd, FF * DM * 2);
    auto kpos = [&](int t) {
        const int u1 = t - 1, ul = ((nk - 1 - phi) & ~3) + phi;
        int k = ID == 1 ? ((t & 3) != phi ? t : t + 4 < nk ? t + 4 : phi) : t < phi || u1 >= ul ? t : t == phi ? phi + 4 : (u1 & 3) == phi && u1 > phi ? u1 + 4 : u1;
        k += koff; k = k >= nk ? k - nk : k;
        if constexpr (ID == 4) {
            k = ks * 14 + (k < (nk >> 1) ? k : k + 28 - (nk >> 1));
            int u = t - nk + phi; u = u >= NH ? u - NH : u; const int h = u >= RE4 + RO4, v = u - h * (RE4 + RO4);
            return t < nk ? k : h * 28 + (v < RE4 ? 14 - RE4 + v : 28 - RO4 - RE4 + v);
        } else return k + k0;
    };
    const int ktab = kpos(lane);
    struct Pos { int sa, sb, lb; const void* pb; };
    auto prep = [&](int t) {
        const int k = ID == 1 || PRE ? kpos(t) : __builtin_amdgcn_readlane(ktab, t);
        Pos x{C::FA == ROW ? k * 128 : C::FA == CHK ? k * 8 * DM * 16 : k * 64 * DM * 2, C::FB == ROW ? (k & 31) * 128 : k * 8 * SEQ * 16, (t & 3) * STAGE + lw * 4096,
              ID == 1 ? (const void*)(tn < 8 ? p.wre : p.wim) : ID == 2 ? (const void*)(k < 32 ? p.cre : p.cim) : (const void*)p.act};
        asm volatile("" : "+s"(x.sa), "+s"(x.sb), "+s"(x.lb)); if constexpr (ID == 2) asm volatile("" : "+s"(x.pb));
        return x;
    };
    auto issue = [&](const Pos& x, bool doA, bool doB, bool half) {
        const auto rb = RSRC(x.pb, ID == 1 ? DM * DM * 2 : ID == 2 ? SEQ * SEQ * 2 : SEQ * FF * 2);
        LAS unsigned char* base = lds + x.lb;
#pragma unroll
        for (int i = 0; i < 4; ++i) {
            if (doA) {
                if constexpr (C::FA == ROW) DMA16(ra, base + i * 1024, voffA[0], x.sa + i * 8 * DM * 2);
                if constexpr (C::FA == CHK) DMA16_DEV(ra, base + i * 1024, voffA[0], x.sa + (i >> 1) * DM * 16 + (i & 1) * 1024);
                if constexpr (C::FA == TR) DMA16(ra, base + i * 1024, voffA[i >> 1], x.sa + i * 4 * DM * 2);
            }
            if (doB) {
                if constexpr (C::FB == ROW) DMA16(rb, base + 16384 + i * 1024, voffB, x.sb + i * 8 * C::LDB * 2);
                else if (!(half && (i & 1))) DMA16_DEV(rb, base + 16384 + i * 1024, voffB, x.sb + (i >> 1) * SEQ * 16 + ((i & 1) ^ ks) * 1024);
            }
        }
    };
    auto stage = [&](int t, bool doA, bool doB, bool half = false) { issue(prep(t), doA, doB, half); };
    if constexpr (PRE) { stage(0, ID == 4, ID == 2); return; }
    if constexpr (ID != 1) {
        u64* words = p.flags + (ID == 2 ? F_AB + tm * 32 : F_ACT + (tn >> 1) * 32) * FLAG_STRIDE;
        if (wid != 4) __builtin_amdgcn_s_sleep(2);
        stage(1, ID == 4, ID == 2); stage(2, ID == 4, ID == 2);
        if (wid == 4) { const int j = lane & 31; wait_group(words + j * FLAG_STRIDE, ID == 2 ? 2 * j + 2 > k0 && 2 * j < k0 + nk : ((j >> 3) & 1) == ks, ep); }
        bar();
        stage(0, ID == 2, ID == 4); stage(1, ID == 2, ID == 4); stage(2, ID == 2, ID == 4);
        wait_vm<8>(); bar();
        wait_vm<4>();
    } else {
        stage(0, true, true); stage(1, true, true);
        wait_vm<8>(); bar();
        stage(2, true, true);
        const unsigned a = (unsigned)(size_t)lds + XLDS;
        const float e0 = lds_read32<3072>(a), e1 = lds_read32<3076>(a);
        wait_lgkm0(); sched0();
        ep = uniform64(((u64)__float_as_uint(e1) << 32) | __float_as_uint(e0));
    }
    u64 pseen = 0; u64* const pwords = p.flags + (F_ACT + (tn >> 1) * 32 + (lane & 31)) * FLAG_STRIDE;
#pragma nounroll
    for (int t = 0; t < nk - 3; ++t) {
        const Pos x = prep(t + 3);
        wait_vm<8>();
        if constexpr (ID == 4) { if (t == nk - 4 && wid == 4 && __any(pseen != ep)) wait_group(pwords, true, ep); }
        bar();
        issue(x, true, true, false);
        if constexpr (ID == 4) { if (t == nk - 6 && wid == 4) flag_peek(pseen, pwords); }
        if constexpr (ID == 1) { if (t == LNT1 && wid == 4 && lane == 0) flag_store(p.flags + (F_LN + tm * 16 + tn) * FLAG_STRIDE, ep); }
    }
    if constexpr (NH > 0) {
        wait_vm<8>(); bar(); stage(nk, true, true, true);
#pragma nounroll
        for (int t = nk - 2; t < nk + NH - 3; ++t) { const Pos x = prep(t + 3); wait_vm<6>(); bar(); issue(x, true, true, true); }
    }
    bool got = !recv;
    auto fetch = [&](u64 seen) {
        if (uniform64(seen) != ep) return false;
        asm volatile("" ::: "memory");
#pragma unroll
        for (int i = 0; i < (ID == 4 ? 4 : 8); ++i) DMA16_DEV(RSRC(p.xch, 256 * 32768), lds + XLDS + lw * 8192 + i * 1024, (unsigned)(lane * 16), xoff + i * 1024);
        return true;
    };
    wait_vm<NH ? 6 : 8>(); bar();
    u64* const lnw = p.flags + (F_LN + (lane & 16 ? l15 * 16 + tn : tm * 16 + l15)) * FLAG_STRIDE;
    u64 seen0 = flag_load(ID == 1 ? lnw : flag);
    wait_vm<0>();
    if (!got) got = fetch(seen0);
    if constexpr (ID == 1) { if (wid == 4) {
        if (__any(seen0 != ep)) wait_group(lnw, true, ep);
        asm volatile("" ::: "memory");
#pragma unroll
        for (int i = 0; i < 3; ++i) DMA16_DEV(RSRC(p.ln1, 3 * SEQ * 4), lds + XLDS + i * 1024, lane < 32 ? (unsigned)(lane * 16) : 0x7ffffff0u, (i * SEQ + (i < 2 ? tm : tn) * 128) * 4);
    } }
    if constexpr (ID == 1) g128_load<2, true>(p, lds, wid, ep);
    bar();
    if constexpr (ID != 1) seen0 = flag_load(flag);
    if constexpr (ID == 2) { if (!got) got = fetch(seen0); }
    bar();
    if (!got) got = fetch(seen0);
    if (recv) {
#pragma nounroll
        for (int spin = 0; !got && spin < (1 << 20); ++spin) { got = fetch(flag_load(flag)); __builtin_amdgcn_s_sleep(1); }
        wait_vm<0>(); bar();
    }
    if constexpr (ID == 2) { if (!recv) colsum_partial(p, lds, lw); }
    if constexpr (ID == 1) { wait_vm<4>(); bar(); }
    if constexpr (ID != 4) bar();
}

template <int ID>
__device__ __forceinline__ void g128_compute(const Params& p, LAS unsigned char* lds, const int wid, u64& ep) {
    using C = Cfg<ID>;
    constexpr int STAGE = 32768, XLDS = 4 * STAGE;
    const int lane = threadIdx.x & 63, l15 = lane & 15, q = lane >> 4;
    const int xcd = blockIdx.x & 7, idx = blockIdx.x >> 3, im = idx & 3, in = idx >> 2;
    constexpr int NH = ID == 4 ? 2 * (RE4 + RO4) : 0;
    const int ks = (xcd & 1) ^ (ID == 2); const bool recv = ID == 4 || (ID == 2 && ks);
    const int tm = (ID == 1 ? (xcd & 3) : ((xcd >> 1) & 1)) * 4 + im, tn = (xcd >> 2) * 8 + in;
    const int k0 = ID != 1 && ks ? C::NKS : 0, nk = ID == 4 ? 28 - 2 * (ks ? RO4 : RE4) : ID == 2 && ks ? C::NKT - C::NKS : C::NKS;
    const int xb = ID == 4 ? 128 + (blockIdx.x ^ (wid >> 2)) : blockIdx.x >> 1;
    u64* const flag = p.flags + (F_PAIR + xb * 4 + (wid & 3)) * FLAG_STRIDE;
    const unsigned xoff = (unsigned)(ID == 4 ? (128 + xb) * 16384 + (wid & 3) * 4096 : xb * 32768 + (wid & 3) * 8192);
    const int wm = wid & 1, wn = wid >> 1;
    const int eo = (q & 1) * 16 + (q >> 1) * 8;
    f32x4 acc[4][4];
#pragma unroll
    for (int m = 0; m < 4; ++m)
#pragma unroll
        for (int n = 0; n < 4; ++n) acc[m][n] = (f32x4){0.f, 0.f, 0.f, 0.f};
    const int row0 = tn * 128 + (ID == 4 ? ks * 64 + wn * 32 : wn * 64) + l15, col0 = tm * 128 + wm * 64 + eo;
    if constexpr (ID != 1) {
        constexpr int NN = ID == 4 ? 2 : 4;
        if (ID == 4 || !recv) {
            if constexpr (ID == 2) { wait_vm<0>(); bar(); __builtin_amdgcn_s_sleep(4); }
            u32x4 r[NN][2];
#pragma unroll
            for (int n = 0; n < NN; ++n)
#pragma unroll
                for (int mp = 0; mp < 2; ++mp)
                    r[n][mp] = __builtin_amdgcn_raw_buffer_load_b128(RSRC(ID == 2 ? p.x : p.h, SEQ * DM * 2), (unsigned)(ID == 2 ? ((row0 + n * 16) * DM + col0 + mp * 32) * 2 : ((col0 / 8 + mp * 4) * SEQ + row0 + n * 16) * 16), 0, ID == 4 ? SC1 : 0);
            if constexpr (ID == 4) { wait_vm<0>(); bar(); }
#pragma unroll
            for (int n = 0; n < NN; ++n)
#pragma unroll
                for (int mp = 0; mp < 2; ++mp) {
                    unsigned r0 = r[n][mp][0], r1 = r[n][mp][1], r2 = r[n][mp][2], r3 = r[n][mp][3];
                    swap16u(r0, r2); swap16u(r1, r3);
                    acc[2 * mp][n] = (f32x4){bf_lo(r0), bf_hi(r0), bf_lo(r1), bf_hi(r1)}; acc[2 * mp + 1][n] = (f32x4){bf_lo(r2), bf_hi(r2), bf_lo(r3), bf_hi(r3)};
                }
        } else { wait_vm<0>(); bar(); }
    }
    unsigned pfc = 0;
    if constexpr (ID == 1) {
        ep = uniform64(flag_load(p.flags)) + 1;
        if (threadIdx.x == 0) *(LAS u64*)(lds + XLDS + 3072) = ep;
        wait_lgkm0();
        if (idx == 0) {
            unsigned long long pc; int lo, hi;
            asm volatile("s_getpc_b64 %0\n s_mov_b32 %1, _Z7fused_k6Params@rel32@lo+4\n s_mov_b32 %2, _Z6tail_kv@rel32@lo+12" : "=s"(pc), "=s"(lo), "=s"(hi));
            touch(pfc, threadIdx.x * 128, (const void*)(pc + (long long)lo), (unsigned)max(hi - lo, 0)); touch(pfc, threadIdx.x * 128 + 32768, (const void*)(pc + (long long)lo), (unsigned)max(hi - lo, 0));
        }
    }
    constexpr int NA = C::FA == CHK ? 1 : C::FA == ROW ? 2 : 4, NB = C::FB == CHK ? 1 : 2;
    unsigned adA[NA], adB[NB];
    const unsigned lds0 = (unsigned)(size_t)lds, row_sw = lds0 + (unsigned)(l15 * 128 + ((q ^ (lane & 7)) << 4)), chk = lds0 + (unsigned)(q * 2048 + l15 * 16);
#pragma unroll
    for (int i = 0; i < NA; ++i) {
        if constexpr (C::FA == ROW) adA[i] = (row_sw ^ (unsigned)(i * 64)) + (unsigned)(wm * 8192);
        if constexpr (C::FA == CHK) adA[i] = chk + (unsigned)(wm * 1024);
        if constexpr (C::FA == TR) { const int q4 = l15 >> 2; adA[i] = lds0 + (unsigned)((8 * q + q4) * 256 + (((wm * 4 + i) ^ (q4 | ((q & 1) << 2))) << 5) + 8 * (lane & 3)); }
    }
#pragma unroll
    for (int i = 0; i < NB; ++i) adB[i] = C::FB == CHK ? 16384u + chk + (unsigned)(wn * 512) : 16384u + (row_sw ^ (unsigned)(i * 64)) + (unsigned)(wn * 8192);
    constexpr int W = C::FA == TR ? 2 : 1, NR = 4 + 4 * W;
    auto rd = [&](auto ic, Frag (&ra)[4], bf16x8 (&rb)[4], auto kkc) {
        constexpr int kk = decltype(kkc)::value, i = decltype(ic)::value;
        if constexpr (i == 0 || (i > W && i <= W + 3)) { constexpr int n = i == 0 ? 0 : i - W;
            if constexpr (C::FB == CHK) rb[n] = lds_read128<kk * 8192 + (n >> 1) * 1024 + (n & 1) * 256>(adB[0]);
            else rb[n] = lds_read128<n * 2048>(adB[kk]); }
        else { constexpr int j = i <= W ? i - 1 : i - 4, m = j / W;
            if constexpr (C::FA != TR) { bf16x8 v;
                if constexpr (C::FA == CHK) v = lds_read128<kk * 8192 + m * 256>(adA[0]); else v = lds_read128<m * 2048>(adA[kk]);
                const u32x4 w = __builtin_bit_cast(u32x4, v); ra[m].lo = (u32x2){w[0], w[1]}; ra[m].hi = (u32x2){w[2], w[3]}; }
            else if constexpr (j % 2 == 0) ra[m].lo = tr_read<kk * 8192>(adA[m]);
            else ra[m].hi = tr_read<kk * 8192 + 1024>(adA[m]); }
    };
    auto phase = [&](const Frag (&ra)[4], const bf16x8 (&rb)[4], Frag (&na)[4], bf16x8 (&nb)[4], int dstep, auto kkc) {
        static_for<0, 16>([&](auto sc) {
            constexpr int s = decltype(sc)::value, m = s >> 2, n = s & 3;
            if constexpr (n == 0) { wait_lgkm<(3 - m) * W + (s < NR ? s : NR)>(); sched0(); }
            acc[m][n] = MFMA(ra[m].v(), rb[n], acc[m][n]);
            if constexpr (s < NR) rd(sc, na, nb, kkc);
            if constexpr (s >= 12 && decltype(kkc)::value == 1) {
                if constexpr (s - 12 < NA) bump(adA[s - 12], dstep);
                if constexpr (s - 12 < NB) bump(adB[s - 12], dstep);
            }
            sched0();
        });
    };
    Frag a0[4], a1[4]; bf16x8 b0[4], b1[4];
    constexpr std::integral_constant<int, 0> K0{}; constexpr std::integral_constant<int, 1> K1{};
    if constexpr (ID != 1) bar();
    bar();
    static_for<0, NR>([&](auto ic) { rd(ic, a0, b0, K0); });
    sched0();
    const int rx = tm * 128 + tn * 8 + wid * 2 + (lane >> 5), rw = tn * 128 + tm * 8 + wid * 2 + (lane >> 5);
    u32x4 xv[4], wv[4];
    if constexpr (ID == 1) {
#pragma unroll
        for (int i = 0; i < 4; ++i) { xv[i] = __builtin_amdgcn_raw_buffer_load_b128(RSRC(p.x, SEQ * DM * 2), (unsigned)(rx * 2048 + (lane & 31) * 16 + i * 512), 0, 0);
                                      wv[i] = __builtin_amdgcn_raw_buffer_load_b128(RSRC(tn < 8 ? p.wre : p.wim, DM * DM * 2), (unsigned)((rw & 1023) * 2048 + (lane & 31) * 16 + i * 512), 0, 0); }
        sched0();
    }
    auto steps = [&](int t0, int t1) {
#pragma nounroll
        for (int t = t0; t < t1; ++t) {
            phase(a0, b0, a1, b1, (t & 3) == 3 ? -3 * STAGE : STAGE, K1);
            if constexpr (ID == 1) { if (t == LNT1) wait_vm<0>(); }
            bar();
            phase(a1, b1, a0, b0, 0, K0);
        }
    };
    if constexpr (ID == 1) {
        steps(0, LNT0);
        wait_lgkm0(); sched0();
        f32x2 sx = {0.f, 0.f}, sw = sx, sq = sx;
#pragma unroll
        for (int i = 0; i < 16; ++i) { sx += (f32x2){bf_lo(xv[i >> 2][i & 3]), bf_hi(xv[i >> 2][i & 3])}; sw += (f32x2){bf_lo(wv[i >> 2][i & 3]), bf_hi(wv[i >> 2][i & 3])}; }
        const float mean = hsum(sx[0] + sx[1]) * (1.f / DM);
#pragma unroll
        for (int i = 0; i < 16; ++i) { const f32x2 d = (f32x2){bf_lo(xv[i >> 2][i & 3]), bf_hi(xv[i >> 2][i & 3])} - mean; sq += d * d; }
        const float rstd = __builtin_amdgcn_rsqf(hsum(sq[0] + sq[1]) * (1.f / DM) + LN_EPS), wsum = hsum(sw[0] + sw[1]);
        const int j = lane & 31;
        __builtin_amdgcn_raw_buffer_store_b32(__float_as_uint(j == 0 ? mean : j == 1 ? rstd : wsum), RSRC(p.ln1, 3 * SEQ * 4), j < 3 ? (unsigned)((j * SEQ + (j < 2 ? rx : rw)) * 4) : 0x7ffffff0u, 0, SC1);
        sched0();
        steps(LNT0, nk);
    } else steps(0, nk);
    wait_lgkm0(); keep(a0, b0); sched0();
    auto send = [&](int h0) {
        const auto rx = RSRC(p.xch, 256 * 32768);
#pragma unroll
        for (int m = 0; m < 4; ++m)
#pragma unroll
            for (int h = h0; h < 2; ++h) {
                const f32x4 u = acc[m][2 * h], v = acc[m][2 * h + 1];
                __builtin_amdgcn_raw_buffer_store_b128((u32x4){pack_bf16(u[0], u[1]), pack_bf16(u[2], u[3]), pack_bf16(v[0], v[1]), pack_bf16(v[2], v[3])}, rx, xoff + (unsigned)((h0 ? m : m * 2 + h) * 1024 + lane * 16), 0, SC1);
            }
    };
    if constexpr (NH > 0) {
        send(1); sched0();
        auto phaseH = [&](const Frag (&ra)[4], const bf16x8 (&rb)[4], Frag (&na)[4], bf16x8 (&nb)[4], int dstep, auto kkc) {
            static_for<0, 8>([&](auto sc) {
                constexpr int s = decltype(sc)::value;
                if constexpr ((s & 1) == 0) { wait_lgkm<6 - s + (s < 5 ? 2 * s : 10)>(); sched0(); }
                acc[s >> 1][s & 1] = MFMA(ra[s >> 1].v(), rb[s & 1], acc[s >> 1][s & 1]);
                if constexpr (s < 5) { rd(std::integral_constant<int, s < 2 ? 2 * s : 2 * s + 2>{}, na, nb, kkc); rd(std::integral_constant<int, s < 2 ? 2 * s + 1 : 2 * s + 3>{}, na, nb, kkc); }
                if constexpr (s >= 5 && decltype(kkc)::value == 1) { if constexpr (s < 7) { bump(adA[2 * (s - 5)], dstep); bump(adA[2 * (s - 5) + 1], dstep); } else bump(adB[0], dstep); }
                sched0();
            });
        };
#pragma nounroll
        for (int t = nk; t < nk + NH; ++t) {
            phaseH(a0, b0, a1, b1, (t & 3) == 3 ? -3 * STAGE : STAGE, K1);
            bar();
            phaseH(a1, b1, a0, b0, 0, K0);
            if (t == nk + FLAGD) { wait_vm<0>(); if (lane == 0) flag_store(flag, ep); }
        }
        wait_lgkm0();
        asm volatile("" ::"v"(b0[0]), "v"(b0[1]));
#pragma unroll
        for (int i = 0; i < 4; ++i) asm volatile("" ::"v"(a0[i].lo), "v"(a0[i].hi));
        sched0();
    }
    if constexpr (ID == 1) {
        wait_vm<0>(); asm volatile("; pfc %0" ::"v"(pfc));
        bar();
        const auto ro = RSRC(p.abT, 2 * SEQ * DM * 2);
        const float sgn = tn < 8 ? 1.f : -1.f;
        f32x4 mean[4], rs[4]; float ws[4];
        static_for<0, 4>([&](auto ic) { constexpr int i = decltype(ic)::value; const unsigned a = lds0 + (unsigned)(XLDS + wm * 256 + q * 16);
            mean[i] = __builtin_bit_cast(f32x4, lds_read128<i * 64>(a)); rs[i] = __builtin_bit_cast(f32x4, lds_read128<1024 + i * 64>(a));
            ws[i] = lds_read32<2048 + i * 64>(lds0 + (unsigned)(XLDS + wn * 256 + l15 * 4)); });
        wait_lgkm0(); sched0();
#pragma unroll
        for (int m = 0; m < 4; ++m) {
            const f32x4 r = rs[m] * sgn;
#pragma unroll
            for (int n = 0; n < 4; ++n) acc[m][n] = (acc[m][n] - mean[m] * ws[n]) * r;
        }
#pragma unroll
        for (int n = 0; n < 4; ++n) { const int e = (tn & 7) * 128 + wn * 64 + n * 16 + l15;
#pragma unroll
            for (int mp = 0; mp < 2; ++mp) {
                unsigned x0 = pack_bf16(acc[2 * mp][n][0], acc[2 * mp][n][1]), x1 = pack_bf16(acc[2 * mp][n][2], acc[2 * mp][n][3]);
                unsigned y0 = pack_bf16(acc[2 * mp + 1][n][0], acc[2 * mp + 1][n][1]), y1 = pack_bf16(acc[2 * mp + 1][n][2], acc[2 * mp + 1][n][3]);
                swap16u(x0, y0); swap16u(x1, y1);
                const int s = (tn >> 3) * SEQ + col0 + mp * 32;
                __builtin_amdgcn_raw_buffer_store_b128((u32x4){x0, x1, y0, y1}, ro, (unsigned)(((s >> 3) * DM + e) * 16), 0, SC1);
            } }
    } else if (!recv) {
        send(0);
        wait_vm<0>();
        if (lane == 0) flag_store(flag, ep);
        if constexpr (ID == 2) bar();
    } else {
        bar();
        const unsigned px = (unsigned)(size_t)lds + (unsigned)(XLDS + wid * 8192 + lane * 16);
        constexpr int HH = ID == 4 ? 1 : 2;
        u32x4 pw[4 * HH];
        static_for<0, 4 * HH>([&](auto ic) { pw[decltype(ic)::value] = __builtin_bit_cast(u32x4, lds_read128<decltype(ic)::value * 1024>(px)); });
        wait_lgkm0(); sched0();
#pragma unroll
        for (int m = 0; m < 4; ++m)
#pragma unroll
            for (int h = 0; h < HH; ++h) { const u32x4 w = pw[m * HH + h];
                acc[m][2 * h] += (f32x4){bf_lo(w[0]), bf_hi(w[0]), bf_lo(w[1]), bf_hi(w[1])}; acc[m][2 * h + 1] += (f32x4){bf_lo(w[2]), bf_hi(w[2]), bf_lo(w[3]), bf_hi(w[3])}; }
        const auto ro = RSRC(ID == 2 ? p.h : p.out, ID == 2 ? 136 * SEQ * 16 : SEQ * DM * 2);
#pragma unroll
        for (int n = 0; n < 2 * HH; ++n) {
            unsigned o[4][2];
#pragma unroll
            for (int m = 0; m < 4; ++m) { o[m][0] = pack_bf16(acc[m][n][0], acc[m][n][1]); o[m][1] = pack_bf16(acc[m][n][2], acc[m][n][3]); }
            if constexpr (ID == 2) {
                const f32x4 s4 = (acc[0][n] + acc[1][n]) + (acc[2][n] + acc[3][n]);
                const float mw = qsum((s4[0] + s4[1]) + (s4[2] + s4[3])) * (1.f / 64.f);
                const f32x4 d0 = acc[0][n] - mw, d1 = acc[1][n] - mw, d2 = acc[2][n] - mw, d3 = acc[3][n] - mw, q4 = (d0 * d0 + d1 * d1) + (d2 * d2 + d3 * d3);
                float m2 = (q4[0] + q4[1]) + (q4[2] + q4[3]);
                m2 = qsum(m2);
                if (q == 0) __builtin_amdgcn_raw_buffer_store_b64((u32x2){__float_as_uint(mw), __float_as_uint(m2)}, ro, (unsigned)(((128 + tm) * SEQ + row0 + n * 16) * 16 + wm * 8), 0, SC1);
            }
#pragma unroll
            for (int mp = 0; mp < 2; ++mp) {
                swap16u(o[2 * mp][0], o[2 * mp + 1][0]); swap16u(o[2 * mp][1], o[2 * mp + 1][1]);
                __builtin_amdgcn_raw_buffer_store_b128((u32x4){o[2 * mp][0], o[2 * mp][1], o[2 * mp + 1][0], o[2 * mp + 1][1]}, ro,
                    (unsigned)(ID == 4 ? ((row0 + n * 16) * DM + col0 + mp * 32) * 2 : ((col0 / 8 + mp * 4) * SEQ + row0 + n * 16) * 16), 0, SC1);
            }
        }
        if constexpr (ID == 2) { wait_vm<0>(); bar(); }
    }
}
#endif

#if defined(__HIP_DEVICE_COMPILE__)
template <int GRP>
__device__ __forceinline__ void gemm3_body(const Params& p, LAS unsigned char* lds, const int wid, u64& ep) {
    constexpr int NK = 16, FA = 7, HB0 = 3 * 32768;
    const int lane = threadIdx.x & 63;
    const int wm = wid & 1, wn = wid >> 1, l15 = lane & 15, q = lane >> 4;
    const int xcd = blockIdx.x & 7, idx = blockIdx.x >> 3, im = idx & 7, in = idx >> 3;
    const int tm = (xcd & 3) * 8 + im, tn = (xcd >> 2) * 4 + in;
    const G3W gw(p, wid, lane);
    unsigned pf = 0;
    u64* words = p.flags + (F_H + tn * 32) * FLAG_STRIDE;
    if (threadIdx.x == 0 && !(xcd & 1)) flag_store(words + tm * FLAG_STRIDE, ep);
    if (wid) __builtin_amdgcn_s_sleep(2);
    gw.early(lds, wid, 0); gw.early(lds, wid, 1);
    if ((xcd & 1) && wid == 1) colsum_final(p, lds, ep);
    if (wid == 0) {
        wait_group(words + (lane & 31) * FLAG_STRIDE, (lane & 8) == 0, ep);
        if (blockIdx.x == 0 && lane == 0) flag_store(p.flags, ep);
    }
    bar();
    const unsigned voffB = (unsigned)((wid * SEQ + tn * 256 + lane) * 16);
    struct Src { const void* base; int so; unsigned nrec; };
    auto pin = [](Src x) { asm volatile("" : "+s"(x.base), "+s"(x.so), "+s"(x.nrec)); return x; };
    auto srcW = [&](int t) { return pin(Src{t < NK ? (const void*)gw.ptr : (const void*)(p.csum + GRP * 3584), t < NK ? gw.kstep(t) * 64 * FF * 2 : 0, t < NK ? (unsigned)(DM * FF * 2) : t == NK ? (unsigned)(2 * FF * 2) : 0u}); };
    auto srcH = [&](int t) { Src x{nullptr, (t < NK ? gw.kstep(t) : NK) * 8 * SEQ * 16, 136 * SEQ * 16}; asm volatile("" : "+s"(x.so)); return x; };
    auto stageW = [&](Src x, int slot, int i) { gw.piece(lds, wid, x.base, x.so, x.nrec, slot, i); };
    auto stageH = [&](Src x, int slot, int i) { DMA16_DEV(RSRC(p.h, x.nrec), lds + HB0 + slot * 32768 + wid * 4096 + i * 1024, voffB, x.so + i * 1024); };
#pragma unroll
    for (int t = 0; t < 2; ++t) {
        const Src xh = srcH(t);
#pragma unroll
        for (int i = 0; i < 4; ++i) stageH(xh, t, i);
    }

    unsigned adA[FA];
    const unsigned lds0 = (unsigned)(size_t)lds;
#pragma unroll
    for (int m = 0; m < FA; ++m) { const int q4 = l15 >> 2, pp = lane & 3, reg = m < 6 ? m & 1 : pp >> 1, ch = m < 6 ? wm * 8 + (m & 6) + (pp >> 1) : 6 + wm;
        adA[m] = lds0 + (unsigned)(reg * 16384 + (8 * q + q4) * 256 + ((ch ^ (((q4 | ((q & 1) << 2)) << 1) | reg)) << 4) + 8 * (pp & 1)); }
    unsigned adB = lds0 + (unsigned)(HB0 + q * 4096 + (wn * 64 + l15) * 16);
    auto rd = [&](auto ic, Frag (&ra)[FA], bf16x8 (&rb)[4], auto kkc) {
        constexpr int kk = decltype(kkc)::value, i = decltype(ic)::value;
        if constexpr (i < 4) rb[i] = lds_read128<kk * 16384 + i * 256>(adB);
        else if constexpr (i % 2 == 0) ra[(i - 4) / 2].lo = tr_read<kk * 8192>(adA[(i - 4) / 2]);
        else ra[(i - 4) / 2].hi = tr_read<kk * 8192 + 1024>(adA[(i - 4) / 2]);
    };
    f32x4 acc[FA][4];
#pragma unroll
    for (int m = 0; m < FA; ++m)
#pragma unroll
        for (int n = 0; n < 4; ++n) acc[m][n] = (f32x4){0.f, 0.f, 0.f, 0.f};
    auto phase = [&](const Frag (&ra)[FA], const bf16x8 (&rb)[4], Frag (&na)[FA], bf16x8 (&nb)[4], int dw, int dh, auto kkc, auto&& dma, auto&& prep) {
        static_for<0, 28>([&](auto sc) {
            constexpr int s = decltype(sc)::value, m = s >> 2, n = s & 3;
            if constexpr (decltype(kkc)::value == 0) { if constexpr (s == 0) __builtin_amdgcn_s_setprio(3); if constexpr (s == 18) __builtin_amdgcn_s_setprio(2); }
            else { if constexpr (s == 6) __builtin_amdgcn_s_setprio(1); if constexpr (s == 22) __builtin_amdgcn_s_setprio(0); }
            acc[m][n] = MFMA(ra[m].v(), rb[n], acc[m][n]);
            sched0();
            if constexpr (s < 18) rd(sc, na, nb, kkc);
            if constexpr (s == 20) prep();
            if constexpr (s % 4 == 2 * GRP && s < 16) dma(s / 4);
            if constexpr (decltype(kkc)::value == 1 && s >= 18 && s < 26) { if constexpr (s < 25) bump(adA[s - 18], dw); else bump(adB, dh); }
            sched0();
        });
        wait_lgkm0(); sched0();
    };
    Frag a0[FA], a1[FA]; bf16x8 b0[4], b1[4];
    constexpr std::integral_constant<int, 0> K0{}; constexpr std::integral_constant<int, 1> K1{};
    wait_vm<4>(); bar();
    static_for<0, 18>([&](auto ic) { rd(ic, a0, b0, K0); });
    wait_lgkm0(); sched0();
    const unsigned pfo = (unsigned)(((blockIdx.x * 8 + wid) * 28 + (lane < 28 ? lane : 0)) * 128); const int pft = 1 + idx % 12;
    Src xw = srcW(2), xh;
    auto phaseA = [&](int t, int sw2) {
        phase(a0, b0, a1, b1, sw2 == 1 ? -65536 : 32768, (t & 1) ? -32768 : 32768, K1, [&](int i) { stageW(xw, sw2, i);
            if (i == 3) touch(pf, pfo, p.wd, t == pft ? FF * DM * 2 : 0);
        }, [&]() { xh = srcH(t + 2); });
        wait_vm<5>();
    };
    u64 cseen = 0;
    phaseA(0, 2);
    int sw2 = 0;
#pragma nounroll
    for (int t = 0; t < NK - 1; ++t) {
        if (__builtin_expect(t >= 11 && t <= 12 && (wid & 3) == 0, 0)) {
            const int sl = min((tm * 112 >> 6) + (lane & 3), (tm * 112 + 111) >> 6);
            u64* w = p.flags + (F_CS + GRP * 56 + sl) * FLAG_STRIDE;
            if (t == 11) flag_peek(cseen, w); else if (__any(cseen != ep)) wait_group(w, true, ep);
        }
        bar();
        phase(a1, b1, a0, b0, 0, 0, K0, [&](int i) { stageH(xh, t & 1, i); }, [&]() { xw = srcW(t + 3); });
        phaseA(t + 1, sw2);
        sw2 = sw2 == 2 ? 0 : sw2 + 1;
    }
    bar();
    __builtin_amdgcn_s_setprio(GRP ? 0 : 3);
    f32x4 csv[FA], sp[4][2];
    static_for<0, 16>([&](auto sc) {
        constexpr int s = decltype(sc)::value;
        acc[s >> 2][s & 3] = MFMA(a1[s >> 2].v(), b1[s & 3], acc[s >> 2][s & 3]);
        if constexpr (s < 8) sp[s >> 1][s & 1] = __builtin_bit_cast(f32x4, lds_read128<(s & 1) * 16384 + (s >> 1) * 256>(adB));
        else if constexpr (s < 15) { constexpr int m = s - 8; const int reg = m < 6 ? m & 1 : q >> 1, c4 = m < 6 ? wm * 2 + (m & 6) * 2 + q : (wm ? 0 : 12) + (q & 1);
            csv[m] = __builtin_bit_cast(f32x4, lds_read128<32768>(lds0 + (unsigned)(reg * 16384 + wm * 256 + ((c4 ^ ((wm << 1) | reg)) << 4)))); }
        if constexpr (GRP == 1 && s == 2) g128_load<4, true>(p, lds, wid, ep);
        sched0();
    });
    wait_lgkm0(); wait_vm<GRP ? 4 : 0>(); asm volatile("; pf %0" ::"v"(pf)); sched0();
    float mean[4], c1[4], ve[4];
#pragma unroll
    for (int n = 0; n < 4; ++n) {
        const f32x4 s0 = sp[n][0], s1 = sp[n][1];
        mean[n] = qsum((s0[0] + s0[2]) + (s1[0] + s1[2])) * (1.f / 16.f);
        const float d0 = s0[0] - mean[n], d1 = s0[2] - mean[n], d2 = s1[0] - mean[n], d3 = s1[2] - mean[n];
        const float m2 = qsum((s0[1] + s0[3]) + (s1[1] + s1[3]) + 64.f * ((d0 * d0 + d1 * d1) + (d2 * d2 + d3 * d3)));
        ve[n] = m2 * (1.f / DM) + LN_EPS; c1[n] = -1.44269504f * __builtin_amdgcn_rsqf(ve[n]);
    }
#pragma unroll
    for (int s = 16; s < 28; ++s) acc[s >> 2][s & 3] = MFMA(a1[s >> 2].v(), b1[s & 3], acc[s >> 2][s & 3]);
#pragma unroll
    for (int s = 16; s < 28; ++s) { __builtin_amdgcn_sched_group_barrier(0x008, 1, 0); __builtin_amdgcn_sched_group_barrier(0x002, 10, 0); }
    sched0();
    const auto ro = RSRC(p.act, SEQ * FF * 2);
    auto silu2 = [&](f32x2 g, f32x2 u, int n) {
        const f32x2 x = g * c1[n], d = (f32x2){__builtin_amdgcn_exp2f(x[0]), __builtin_amdgcn_exp2f(x[1])} * ve[n] + ve[n];
        const f32x2 r = g * u * (f32x2){__builtin_amdgcn_rcpf(d[0]), __builtin_amdgcn_rcpf(d[1])};
        return pack_bf16(r[0], r[1]);
    };
#pragma unroll
    for (int j = 0; j < 3; ++j)
#pragma unroll
        for (int np = 0; np < 2; ++np) {
            unsigned o[2][2];
#pragma unroll
            for (int i = 0; i < 2; ++i) { const int n = 2 * np + i; const f32x4 g = acc[2 * j][n] - mean[n] * csv[2 * j], u = acc[2 * j + 1][n] - mean[n] * csv[2 * j + 1];
                o[i][0] = silu2((f32x2){g[0], g[1]}, (f32x2){u[0], u[1]}, n); o[i][1] = silu2((f32x2){g[2], g[3]}, (f32x2){u[2], u[3]}, n); }
            swap16u(o[0][0], o[1][0]); swap16u(o[0][1], o[1][1]);
            __builtin_amdgcn_raw_buffer_store_b128((u32x4){o[0][0], o[0][1], o[1][0], o[1][1]}, ro, (unsigned)(((tm * 14 + wm * 8 + 2 * j + (q >> 1)) * SEQ + tn * 256 + wn * 64 + np * 32 + (lane & 31)) * 16), 0, SC1);
        }
    {
        unsigned o[4];
#pragma unroll
        for (int n = 0; n < 4; ++n) {
            const f32x4 v = acc[6][n] - mean[n] * csv[6];
            float g0 = v[0], g1 = v[1], u0 = v[2], u1 = v[3];
            swap32(g0, u0); swap32(g1, u1);
            o[n] = silu2((f32x2){g0, g1}, (f32x2){u0, u1}, n);
        }
        swap16u(o[0], o[1]); swap16u(o[2], o[3]); swap32u(o[0], o[2]); swap32u(o[1], o[3]);
        __builtin_amdgcn_raw_buffer_store_b128((u32x4){o[0], o[2], o[1], o[3]}, ro, (unsigned)(((tm * 14 + 6 + wm) * SEQ + tn * 256 + wn * 64 + lane) * 16), 0, SC1);
    }
    __builtin_amdgcn_s_setprio(0);
    if constexpr (GRP == 1) { wait_vm<0>(); bar(); }
}
#endif

__global__ __launch_bounds__(512, 2) void fused_k(const Params p) {
#if defined(__HIP_DEVICE_COMPILE__)
    __shared__ __attribute__((aligned(16))) unsigned char lds_raw[5 * 32768];
    LAS unsigned char* lds = (LAS unsigned char*)lds_raw;
    const int wid = __builtin_amdgcn_readfirstlane(threadIdx.x >> 6);
    u64 ep = 0;
    if (wid < 4) { g128_compute<1>(p, lds, wid, ep); g128_compute<2>(p, lds, wid, ep); gemm3_body<0>(p, lds, wid, ep); g128_compute<4>(p, lds, wid, ep); }
    else { g128_load<1, false>(p, lds, wid, ep); g128_load<2, false>(p, lds, wid, ep); gemm3_body<1>(p, lds, wid, ep); g128_load<4, false>(p, lds, wid, ep); }
#endif
}
__global__ void tail_k() {}

extern "C" void kernel_launch(void* const* d_in, const int* in_sizes, int n_in, void* d_out, int out_size, void* d_ws, size_t ws_size, hipStream_t stream) {
    Params p;
    p.x = (const u16*)d_in[0]; p.cre = (const u16*)d_in[1]; p.cim = (const u16*)d_in[2]; p.wre = (const u16*)d_in[3]; p.wim = (const u16*)d_in[4];
    p.wg = (const u16*)d_in[6]; p.wu = (const u16*)d_in[7]; p.wd = (const u16*)d_in[8];
    p.out = (u16*)d_out;
    char* ws = (char*)d_ws;
    p.abT = (u16*)ws; p.h = (u16*)(ws + (8u << 20)); p.act = (u16*)(ws + (16u << 20));
    p.xch = (u16*)(ws + (32u << 20)); p.flags = (unsigned long long*)(ws + (40u << 20)); p.csum = (float*)(ws + (40u << 20) + (512u << 10)); p.ln1 = (float*)(ws + (40u << 20) + (768u << 10));
    fused_k<<<256, 512, 0, stream>>>(p);
}
```

```cpp
#include <hip/hip_runtime.h>
#include <hip/hip_bf16.h>
#include <type_traits>

#define LAS __attribute__((address_space(3)))
typedef unsigned short u16;
typedef __bf16 bf16x8 __attribute__((ext_vector_type(8)));
typedef __bf16 bf16x2 __attribute__((ext_vector_type(2)));
typedef float f32x4 __attribute__((ext_vector_type(4)));
typedef float f32x2 __attribute__((ext_vector_type(2)));
typedef unsigned u32x4 __attribute__((ext_vector_type(4)));
typedef unsigned u32x2 __attribute__((ext_vector_type(2)));

constexpr int SEQ = 2048, DM = 1024, FF = 3584;
#define ABP 1024
constexpr int ABT_BYTES = 512 * ABP * 16;
#define STA 16
constexpr float LN_EPS = 1e-5f;
constexpr int SC1 = 16;

struct Params {
    const u16* x; const u16* cre; const u16* cim; const u16* wre; const u16* wim;
    const u16* wg; const u16* wu; const u16* wd;
    u16* out; u16* abT; u16* h; u16* act; u16* xch; unsigned long long* flags; float* csum; float* ln1;
};

template <int N> __device__ __forceinline__ void wait_vm() { asm volatile("s_waitcnt vmcnt(%0)" ::"n"(N) : "memory"); }
__device__ __forceinline__ void wait_lgkm0() { __builtin_amdgcn_s_waitcnt(0xC07F); asm volatile("s_waitcnt lgkmcnt(0)" ::: "memory"); }
__device__ __forceinline__ void sched0() { __builtin_amdgcn_sched_barrier(0); }
__device__ __forceinline__ void bar() { sched0(); asm volatile("" ::: "memory"); __builtin_amdgcn_s_barrier(); asm volatile("" ::: "memory"); sched0(); }
__device__ __forceinline__ unsigned pack_bf16(float lo, float hi) { bf16x2 c; c[0] = (__bf16)lo; c[1] = (__bf16)hi; return __builtin_bit_cast(unsigned, c); }
__device__ __forceinline__ float bf_lo(unsigned v) { return __uint_as_float(v << 16); }
__device__ __forceinline__ float bf_hi(unsigned v) { return __uint_as_float(v & 0xffff0000u); }
__device__ __forceinline__ void swap16(float& a, float& b) { auto r = __builtin_amdgcn_permlane16_swap(__float_as_uint(a), __float_as_uint(b), false, false); a = __uint_as_float(r[0]); b = __uint_as_float(r[1]); }
__device__ __forceinline__ void swap32(float& a, float& b) { auto r = __builtin_amdgcn_permlane32_swap(__float_as_uint(a), __float_as_uint(b), false, false); a = __uint_as_float(r[0]); b = __uint_as_float(r[1]); }
__device__ __forceinline__ void swap16u(unsigned& a, unsigned& b) { auto r = __builtin_amdgcn_permlane16_swap(a, b, false, false); a = r[0]; b = r[1]; }
__device__ __forceinline__ void swap32u(unsigned& a, unsigned& b) { auto r = __builtin_amdgcn_permlane32_swap(a, b, false, false); a = r[0]; b = r[1]; }
__device__ __forceinline__ float qsum(float v) { float a = v, b = v; swap16(a, b); a += b; b = a; swap32(a, b); return a + b; }

template <int CTRL> __device__ __forceinline__ float dpp_add(float v) { return v + __int_as_float(__builtin_amdgcn_update_dpp(0, __float_as_int(v), CTRL, 0xf, 0xf, false)); }
__device__ __forceinline__ float hsum(float v) { v = dpp_add<0x128>(v); v = dpp_add<0x124>(v); v = dpp_add<0x122>(v); v = dpp_add<0x121>(v); float b = v; swap16(v, b); return v + b; }

struct Frag { u32x2 lo, hi; __device__ __forceinline__ bf16x8 v() const { return __builtin_bit_cast(bf16x8, __builtin_shufflevector(lo, hi, 0, 1, 2, 3)); } };
template <int OFF> __device__ __forceinline__ u32x2 tr_read(unsigned addr) { u32x2 r; asm volatile("ds_read_b64_tr_b16 %0, %1 offset:%2" : "=v"(r) : "v"(addr), "n"(OFF) : "memory"); return r; }
template <int OFF> __device__ __forceinline__ bf16x8 lds_read128(unsigned addr) { bf16x8 r; asm volatile("ds_read_b128 %0, %1 offset:%2" : "=v"(r) : "v"(addr), "n"(OFF) : "memory"); return r; }
template <int OFF> __device__ __forceinline__ float lds_read32(unsigned addr) { float r; asm volatile("ds_read_b32 %0, %1 offset:%2" : "=v"(r) : "v"(addr), "n"(OFF) : "memory"); return r; }
template <int N> __device__ __forceinline__ void wait_lgkm() { asm volatile("s_waitcnt lgkmcnt(%0)" ::"n"(N) : "memory"); }
template <int I, int N, class F> __device__ __forceinline__ void static_for(F&& f) { if constexpr (I < N) { f(std::integral_constant<int, I>{}); static_for<I + 1, N>(f); } }
template <int NA> __device__ __forceinline__ void keep(const Frag (&a)[NA], const bf16x8 (&b)[4]) {
#pragma unroll
    for (int i = 0; i < NA; ++i) asm volatile("" ::"v"(a[i].lo), "v"(a[i].hi));
#pragma unroll
    for (int i = 0; i < 4; ++i) asm volatile("" ::"v"(b[i]));
}
__device__ __forceinline__ void bump(unsigned& a, int d) { asm volatile("v_add_u32 %0, %1, %0" : "+v"(a) : "s"(d)); }
#define MFMA(a, b, c) __builtin_amdgcn_mfma_f32_16x16x32_bf16(a, b, c, 0, 0, 0)
#define RSRC(ptr, bytes) __builtin_amdgcn_make_buffer_rsrc((void*)(ptr), 0, (bytes), 0x00020000)
#define DMA16(rs, ldsp, voff, soff) __builtin_amdgcn_raw_ptr_buffer_load_lds(rs, (LAS void*)(ldsp), 16, voff, soff, 0, 0)
#define DMA16_DEV(rs, ldsp, voff, soff) __builtin_amdgcn_raw_ptr_buffer_load_lds(rs, (LAS void*)(ldsp), 16, voff, soff, 0, SC1)

#define STAMP(i)
#define STAMP_BEGIN
#define STAMP_END(ID)

typedef unsigned long long u64;
#define FSTR 8
constexpr int FLAG_STRIDE = FSTR, F_PAIR = 1, F_AB = F_PAIR + 1536, F_H = F_AB + 256, F_ACT = F_H + 256, F_CS = F_ACT + 256, F_LN = F_CS + 128, F_ST = F_LN + 256;
#define PSCOPE __HIP_MEMORY_SCOPE_AGENT
#define PSLEEP 2
__device__ __forceinline__ u64 flag_load(u64* w) { return __hip_atomic_load(w, __ATOMIC_RELAXED, PSCOPE); }
__device__ __forceinline__ void flag_store(u64* w, u64 v) { __hip_atomic_store(w, v, __ATOMIC_RELAXED, PSCOPE); }
__device__ __forceinline__ u64 uniform64(u64 v) { return ((u64)(unsigned)__builtin_amdgcn_readfirstlane((unsigned)(v >> 32)) << 32) | (unsigned)__builtin_amdgcn_readfirstlane((unsigned)v); }
__device__ __forceinline__ void wait_group(u64* word, bool need, u64 ep) {
#pragma nounroll
    for (int spin = 0; spin < (1 << 20); ++spin) {
        if (!__any(need && flag_load(word) != ep)) break;
        __builtin_amdgcn_s_sleep(PSLEEP);
    }
}
#if defined(__HIP_DEVICE_COMPILE__)
__device__ __forceinline__ void touch(unsigned& sink, unsigned voff, const void* base, unsigned bytes) {
    asm volatile("s_nop 4\n\tbuffer_load_dword %0, %1, %2, 0 offen" : "+v"(sink) : "v"(voff), "s"(RSRC(base, bytes)) : "memory");
}
__device__ __forceinline__ void flag_peek(u64& v, const u64* w) { asm volatile("global_load_dwordx2 %0, %1, off sc1" : "+v"(v) : "v"(w) : "memory"); }
struct G3W {
    const u16* ptr; unsigned voff[2]; int phi, koff;
    __device__ __forceinline__ G3W(const Params& p, int wid, int lane) {
        const int xcd = blockIdx.x & 7, idx = blockIdx.x >> 3, tm = (xcd & 3) * 8 + (idx & 7), q = lane >> 4;
        ptr = wid < 4 ? p.wg : p.wu; phi = ((idx & 7) + (idx >> 3)) & 3; koff = xcd * 2;
#pragma unroll
        for (int i = 0; i < 2; ++i) { const int j = (lane & 15) ^ (((q | (i << 2)) << 1) | (wid >> 2));
            voff[i] = j < 14 ? (unsigned)(((wid & 3) * 16 + q) * FF * 2 + tm * 224 + j * 16) : 0x7ffffff0u; }
    }
    __device__ __forceinline__ int kstep(int t) const { return (t + koff + ((t & 3) == phi ? 4 : 0)) & 15; }
    __device__ __forceinline__ void piece(LAS unsigned char* lds, int wid, const void* base, int so, unsigned nrec, int slot, int i) const { DMA16_DEV(RSRC(base, nrec), lds + slot * 32768 + wid * 4096 + i * 1024, voff[i >> 1], so + i * 4 * FF * 2); }
    __device__ __forceinline__ void early(LAS unsigned char* lds, int wid, int t) const {
#pragma unroll
        for (int i = 0; i < 4; ++i) piece(lds, wid, ptr, kstep(t) * 64 * FF * 2, DM * FF * 2, t, i);
    }
};
#define CSW 4
#define CSD 0
__device__ __forceinline__ void colsum_partial(const Params& p, LAS unsigned char* lds, int lw) {
    const int lane = threadIdx.x & 63, slab = blockIdx.x >> 1, mat = slab >= 56, c0 = (slab - mat * 56) * 64;
    const auto rw = RSRC(mat ? p.wu : p.wg, slab < 112 ? DM * FF * 2 : 0);
    const unsigned vo = (unsigned)(((lw * 256 + (lane >> 3)) * FF + c0 + (lane & 7) * 8) * 2);
    u32x4 v[8]; f32x4 s0 = {0.f, 0.f, 0.f, 0.f}, s1 = s0;
    auto add = [&](const u32x4 w) { s0 += (f32x4){bf_lo(w[0]), bf_hi(w[0]), bf_lo(w[1]), bf_hi(w[1])}; s1 += (f32x4){bf_lo(w[2]), bf_hi(w[2]), bf_lo(w[3]), bf_hi(w[3])}; };
    if (CSD) __builtin_amdgcn_s_sleep(CSD);
#pragma unroll
    for (int i = 0; i < CSW; ++i) v[i] = __builtin_amdgcn_raw_buffer_load_b128(rw, vo, i * 8 * FF * 2, 0);
#pragma unroll
    for (int i = 0; i < 8; ++i) { add(v[i % CSW]); v[i % CSW] = __builtin_amdgcn_raw_buffer_load_b128(rw, vo, (CSW + i) * 8 * FF * 2, 0); sched0(); }
#pragma unroll
    for (int i = CSW; i < 8; ++i) v[i] = __builtin_amdgcn_raw_buffer_load_b128(rw, vo, (8 + i) * 8 * FF * 2, 0);
#pragma nounroll
    for (int j = 16; j < 40; j += 8)
#pragma unroll
        for (int i = 0; i < 8; ++i) { add(v[i]); v[i] = __builtin_amdgcn_raw_buffer_load_b128(rw, j < 32 ? vo : 0x7ffffff0u, (j + i) * 8 * FF * 2, 0); sched0(); }
    LAS float* tab = (LAS float*)(lds + 131072) + (lw * 8 + (lane >> 3)) * 64 + (lane & 7) * 8;
    *(LAS f32x4*)tab = s0; *(LAS f32x4*)(tab + 4) = s1;
    wait_lgkm0();
}
__device__ __forceinline__ void colsum_final(const Params& p, LAS unsigned char* lds, u64 ep) {
    const int lane = threadIdx.x & 63, slab = blockIdx.x >> 1, mat = slab >= 56, c0 = (slab - mat * 56) * 64;
    if (slab >= 112) return;
    const LAS float* tab = (const LAS float*)(lds + 131072);
    float a = 0.f, b = 0.f;
#pragma unroll
    for (int i = 0; i < 32; i += 2) { a += tab[i * 64 + lane]; b += tab[(i + 1) * 64 + lane]; }
    const int f = c0 + lane, t = f / 112, r = f - t * 112, h = r >= 56;
    __hip_atomic_store(p.csum + (mat * 2 + h) * 1792 + t * 56 + r - h * 56, a + b, __ATOMIC_RELAXED, __HIP_MEMORY_SCOPE_AGENT);
    wait_vm<0>();
    if (lane == 0) flag_store(p.flags + (F_CS + slab) * FLAG_STRIDE, ep);
}
#endif

enum { ROW, CHK, TR };
#define NKS2 27
#define FLAGD 1
#define W1E 1
#define LNT0 1
#define LNT1 5
#define NPRE2 1
#define G1PRE 1
#define RE4 1
#define RO4 3
template <int ID> struct Cfg;
template <> struct Cfg<1> { static constexpr int NKT = 16, NKS = 16, FA = ROW, FB = ROW, LDB = DM; };
template <> struct Cfg<2> { static constexpr int NKT = 64, NKS = NKS2, FA = CHK, FB = ROW, LDB = SEQ; };
template <> struct Cfg<4> { static constexpr int NKT = 56, NKS = 0, FA = TR, FB = CHK, LDB = 0; };

#if defined(__HIP_DEVICE_COMPILE__)
template <int ID, int PRE>
__device__ __forceinline__ void g128_load(const Params& p, LAS unsigned char* lds, const int wid, u64& ep) {
    using C = Cfg<ID>;
    constexpr int STAGE = 32768, XLDS = 4 * STAGE;
    const int lane = threadIdx.x & 63, l15 = lane & 15, q = lane >> 4;
    const int xcd = blockIdx.x & 7, idx = blockIdx.x >> 3, im = idx & 3, in = idx >> 2;
    constexpr int NH = ID == 4 ? 2 * (RE4 + RO4) : 0;
    const int ks = (xcd & 1) ^ (ID == 2); const bool recv = ID == 4 || (ID == 2 && ks);
    const int tm = (ID == 1 ? (xcd & 3) : ((xcd >> 1) & 1)) * 4 + im, tn = (xcd >> 2) * 8 + in;
    const int k0 = ID != 1 && ks ? C::NKS : 0, nk = ID == 4 ? 28 - 2 * (ks ? RO4 : RE4) : ID == 2 && ks ? C::NKT - C::NKS : C::NKS;
    const int xb = ID == 4 ? 128 + (blockIdx.x ^ (wid >> 2)) : blockIdx.x >> 1;
    u64* const flag = p.flags + (F_PAIR + xb * 4 + (wid & 3)) * FLAG_STRIDE;
    const unsigned xoff = (unsigned)(ID == 4 ? (128 + xb) * 16384 + (wid & 3) * 4096 : xb * 32768 + (wid & 3) * 8192);
    if constexpr (ID != 1 && PRE < 0) {
        if (wid == 4 && lane == 0) flag_store(p.flags + (ID == 2 ? F_AB + (tn & 7) * 32 + (tn >> 3) * 16 + (xcd & 3) * 4 + im : F_ACT + (tn >> 1) * 32 + (xcd & 3) * 8 + (idx & 7)) * FLAG_STRIDE, ep);
        sched0();
    }
    const int lw = wid - 4, phi = (im + in) & 3;
    const int koff = ID == 1 ? xcd * 2 : ((xcd >> (ID == 2 ? 1 : 2)) & 1) * (nk >> 1);
    const unsigned ksw = (unsigned)(((lane & 7) ^ (lane >> 3)) << 4);
    unsigned voffA[2], voffB;
    if constexpr (C::FA == ROW) voffA[0] = (unsigned)((tm * 128 + lw * 32 + (lane >> 3)) * DM * 2) + ksw;
    if constexpr (C::FA == CHK) voffA[0] = (unsigned)((lw * 2 * ABP + tm * 128 + lane) * 16);
    if constexpr (C::FA == TR) {
#pragma unroll
        for (int i = 0; i < 2; ++i) voffA[i] = (unsigned)((lw * 16 + q) * DM * 2 + tm * 256 + ((l15 ^ ((q | (i << 2)) << 1)) << 4));
    }
    if constexpr (C::FB == ROW) voffB = (unsigned)((((ID == 1 ? tn & 7 : tn)) * 128 + lw * 32 + (lane >> 3)) * C::LDB * 2) + ksw;
    else voffB = (unsigned)((lw * 2 * SEQ + tn * 128 + lane) * 16);
    const auto ra = ID == 1 ? RSRC(p.x, SEQ * DM * 2) : ID == 2 ? RSRC(p.abT, ABT_BYTES) : RSRC(p.wd, FF * DM * 2);
    auto kpos = [&](int t) {
        const int u1 = t - 1, ul = ((nk - 1 - phi) & ~3) + phi;
        int k = ID == 1 ? ((t & 3) != phi ? t : t + 4 < nk ? t + 4 : phi) : t < phi || u1 >= ul ? t : t == phi ? phi + 4 : (u1 & 3) == phi && u1 > phi ? u1 + 4 : u1;
        k += koff; k = k >= nk ? k - nk : k;
        if constexpr (ID == 4) {
            k = ks * 14 + (k < (nk >> 1) ? k : k + 28 - (nk >> 1));
            int u = t - nk + phi; u = u >= NH ? u - NH : u; const int h = u >= RE4 + RO4, v = u - h * (RE4 + RO4);
            return t < nk ? k : h * 28 + (v < RE4 ? 14 - RE4 + v : 28 - RO4 - RE4 + v);
        } else return k + k0;
    };
    const int ktab = kpos(lane);
    struct Pos { int sa, sb, lb; const void* pb; };
    auto prep = [&](int t) {
        const int k = ID == 1 || PRE >= 0 ? kpos(t) : __builtin_amdgcn_readlane(ktab, t);
        Pos x{C::FA == ROW ? k * 128 : C::FA == CHK ? k * 8 * ABP * 16 : k * 64 * DM * 2, C::FB == ROW ? (k & 31) * 128 : k * 8 * SEQ * 16, (t & 3) * STAGE + lw * 4096,
              ID == 1 ? (const void*)(tn < 8 ? p.wre : p.wim) : ID == 2 ? (const void*)(k < 32 ? p.cre : p.cim) : (const void*)p.act};
        asm volatile("" : "+s"(x.sa), "+s"(x.sb), "+s"(x.lb)); if constexpr (ID == 2) asm volatile("" : "+s"(x.pb));
        return x;
    };
    auto issue = [&](const Pos& x, bool doA, bool doB, bool half) {
        const auto rb = RSRC(x.pb, ID == 1 ? DM * DM * 2 : ID == 2 ? SEQ * SEQ * 2 : SEQ * FF * 2);
        LAS unsigned char* base = lds + x.lb;
#pragma unroll
        for (int i = 0; i < 4; ++i) {
            if (doA) {
                if constexpr (C::FA == ROW) DMA16(ra, base + i * 1024, voffA[0], x.sa + i * 8 * DM * 2);
                if constexpr (C::FA == CHK) DMA16_DEV(ra, base + i * 1024, voffA[0], x.sa + (i >> 1) * ABP * 16 + (i & 1) * 1024);
                if constexpr (C::FA == TR) DMA16(ra, base + i * 1024, voffA[i >> 1], x.sa + i * 4 * DM * 2);
            }
            if (doB) {
                if constexpr (C::FB == ROW) DMA16(rb, base + 16384 + i * 1024, voffB, x.sb + i * 8 * C::LDB * 2);
                else if (!(half && (i & 1))) DMA16_DEV(rb, base + 16384 + i * 1024, voffB, x.sb + (i >> 1) * SEQ * 16 + ((i & 1) ^ ks) * 1024);
            }
        }
    };
    auto stage = [&](int t, bool doA, bool doB, bool half = false) { issue(prep(t), doA, doB, half); };
    if constexpr (PRE >= 0) { stage(PRE, ID == 4, ID == 2); return; }
    if constexpr (ID != 1) {
        u64* words = p.flags + (ID == 2 ? F_AB + tm * 32 : F_ACT + (tn >> 1) * 32) * FLAG_STRIDE;
        if constexpr (ID == 4) { if (wid != 4) __builtin_amdgcn_s_sleep(2); stage(1, true, false); stage(2, true, false); }
        if constexpr (ID == 2 && NPRE2 < 3) { if (wid != 4) __builtin_amdgcn_s_sleep(2); if (NPRE2 < 2) stage(1, false, true); stage(2, false, true); }
        if (wid == 4) { const int j = lane & 31; wait_group(words + j * FLAG_STRIDE, ID == 2 ? 2 * j + 2 > k0 && 2 * j < k0 + nk : ((j >> 3) & 1) == ks, ep); }
        bar();
        stage(0, ID == 2, ID == 4); stage(1, ID == 2, ID == 4); stage(2, ID == 2, ID == 4);
        wait_vm<8>(); bar();
        wait_vm<4>();
    } else {
        stage(0, true, true); stage(1, true, true);
        wait_vm<8>(); bar();
        stage(2, true, true);
        const unsigned a = (unsigned)(size_t)lds + XLDS;
        const float e0 = lds_read32<3072>(a), e1 = lds_read32<3076>(a);
        wait_lgkm0(); sched0();
        ep = uniform64(((u64)__float_as_uint(e1) << 32) | __float_as_uint(e0));
    }
    u64 pseen = 0; u64* const pwords = p.flags + (F_ACT + (tn >> 1) * 32 + (lane & 31)) * FLAG_STRIDE;
#pragma nounroll
    for (int t = 0; t < nk - 3; ++t) {
        Pos x; if constexpr (ID != 1 || G1PRE) x = prep(t + 3);
        wait_vm<8>();
        if constexpr (ID == 4) { if (t == nk - 4 && wid == 4 && __any(pseen != ep)) wait_group(pwords, true, ep); }
        bar();
        if constexpr (ID == 1 && !G1PRE) x = prep(t + 3);
        issue(x, true, true, false);
        if constexpr (ID == 4) { if (t == nk - 6 && wid == 4) flag_peek(pseen, pwords); }
        if constexpr (ID == 1) { if (t == LNT1 && wid == 4 && lane == 0) flag_store(p.flags + (F_LN + tm * 16 + tn) * FLAG_STRIDE, ep); }
    }
    if constexpr (NH > 0) {
        wait_vm<8>(); bar(); stage(nk, true, true, true);
#pragma nounroll
        for (int t = nk - 2; t < nk + NH - 3; ++t) { const Pos x = prep(t + 3); wait_vm<6>(); bar(); issue(x, true, true, true); }
    }
    bool got = !recv;
    auto fetch = [&](u64 seen) {
        if (uniform64(seen) != ep) return false;
        asm volatile("" ::: "memory");
#pragma unroll
        for (int i = 0; i < (ID == 4 ? 4 : 8); ++i) DMA16_DEV(RSRC(p.xch, 256 * 32768), lds + XLDS + lw * 8192 + i * 1024, (unsigned)(lane * 16), xoff + i * 1024);
        return true;
    };
    wait_vm<NH ? 6 : 8>(); bar();
    u64* const lnw = p.flags + (F_LN + (lane & 16 ? l15 * 16 + tn : tm * 16 + l15)) * FLAG_STRIDE;
    u64 seen0 = flag_load(ID == 1 ? lnw : flag);
    wait_vm<0>();
    if (!got) got = fetch(seen0);
    if constexpr (ID == 1) { if (wid == 4) {
        if (__any(seen0 != ep)) wait_group(lnw, true, ep);
        asm volatile("" ::: "memory");
#pragma unroll
        for (int i = 0; i < 3; ++i) DMA16_DEV(RSRC(p.ln1, 3 * SEQ * 4), lds + XLDS + i * 1024, lane < 32 ? (unsigned)(lane * 16) : 0x7ffffff0u, (i * SEQ + (i < 2 ? tm : tn) * 128) * 4);
    } }
    if constexpr (ID == 1) g128_load<2, 0>(p, lds, wid, ep);
    bar();
    if constexpr (ID != 1) seen0 = flag_load(flag);
    if constexpr (ID == 2) { if (!got) got = fetch(seen0); }
    if constexpr (ID == 1 && NPRE2 > 1) g128_load<2, 1>(p, lds, wid, ep);
    bar();
    if (!got) got = fetch(seen0);
    if constexpr (ID == 1 && NPRE2 > 2) g128_load<2, 2>(p, lds, wid, ep);
    if (recv) {
#pragma nounroll
        for (int spin = 0; !got && spin < (1 << 20); ++spin) { got = fetch(flag_load(flag)); __builtin_amdgcn_s_sleep(1); }
        wait_vm<0>(); bar();
    }
    if constexpr (ID == 2) { if (!recv) colsum_partial(p, lds, lw); }
    if constexpr (ID == 1) { wait_vm<4 * NPRE2>(); bar(); }
    if constexpr (ID != 4) bar();
}

template <int ID>
__device__ __forceinline__ void g128_compute(const Params& p, LAS unsigned char* lds, const int wid, u64& ep, u32x2& stv, unsigned& sto) {
    using C = Cfg<ID>;
    constexpr int STAGE = 32768, XLDS = 4 * STAGE;
    const int lane = threadIdx.x & 63, l15 = lane & 15, q = lane >> 4;
    const int xcd = blockIdx.x & 7, idx = blockIdx.x >> 3, im = idx & 3, in = idx >> 2;
    constexpr int NH = ID == 4 ? 2 * (RE4 + RO4) : 0;
    const int ks = (xcd & 1) ^ (ID == 2); const bool recv = ID == 4 || (ID == 2 && ks);
    const int tm = (ID == 1 ? (xcd & 3) : ((xcd >> 1) & 1)) * 4 + im, tn = (xcd >> 2) * 8 + in;
    const int k0 = ID != 1 && ks ? C::NKS : 0, nk = ID == 4 ? 28 - 2 * (ks ? RO4 : RE4) : ID == 2 && ks ? C::NKT - C::NKS : C::NKS;
    const int xb = ID == 4 ? 128 + (blockIdx.x ^ (wid >> 2)) : blockIdx.x >> 1;
    u64* const flag = p.flags + (F_PAIR + xb * 4 + (wid & 3)) * FLAG_STRIDE;
    const unsigned xoff = (unsigned)(ID == 4 ? (128 + xb) * 16384 + (wid & 3) * 4096 : xb * 32768 + (wid & 3) * 8192);
    STAMP_BEGIN;
    const int wm = wid & 1, wn = wid >> 1;
    const int eo = (q & 1) * 16 + (q >> 1) * 8;
    f32x4 acc[4][4];
#pragma unroll
    for (int m = 0; m < 4; ++m)
#pragma unroll
        for (int n = 0; n < 4; ++n) acc[m][n] = (f32x4){0.f, 0.f, 0.f, 0.f};
    const int row0 = tn * 128 + (ID == 4 ? ks * 64 + wn * 32 : wn * 64) + l15, col0 = tm * 128 + wm * 64 + eo;
    if constexpr (ID != 1) {
        constexpr int NN = ID == 4 ? 2 : 4;
        if (ID == 4 || !recv) {
            if constexpr (ID == 2) { wait_vm<0>(); bar(); __builtin_amdgcn_s_sleep(4); }
            u32x4 r[NN][2];
#pragma unroll
            for (int n = 0; n < NN; ++n)
#pragma unroll
                for (int mp = 0; mp < 2; ++mp)
                    r[n][mp] = __builtin_amdgcn_raw_buffer_load_b128(RSRC(ID == 2 ? p.x : p.h, SEQ * DM * 2), (unsigned)(ID == 2 ? ((row0 + n * 16) * DM + col0 + mp * 32) * 2 : ((col0 / 8 + mp * 4) * SEQ + row0 + n * 16) * 16), 0, ID == 4 ? SC1 : 0);
            if constexpr (ID == 4) { wait_vm<0>(); bar(); }
#pragma unroll
            for (int n = 0; n < NN; ++n)
#pragma unroll
                for (int mp = 0; mp < 2; ++mp) {
                    unsigned r0 = r[n][mp][0], r1 = r[n][mp][1], r2 = r[n][mp][2], r3 = r[n][mp][3];
                    swap16u(r0, r2); swap16u(r1, r3);
                    acc[2 * mp][n] = (f32x4){bf_lo(r0), bf_hi(r0), bf_lo(r1), bf_hi(r1)}; acc[2 * mp + 1][n] = (f32x4){bf_lo(r2), bf_hi(r2), bf_lo(r3), bf_hi(r3)};
                }
        } else { wait_vm<0>(); bar(); }
    }
    unsigned pfc = 0;
    if constexpr (ID == 1) {
        ep = uniform64(flag_load(p.flags)) + 1;
        if (threadIdx.x == 0) *(LAS u64*)(lds + XLDS + 3072) = ep;
        wait_lgkm0();
        if (idx == 0) {
            unsigned long long pc; int lo, hi;
            asm volatile("s_getpc_b64 %0\n s_mov_b32 %1, _Z7fused_k6Params@rel32@lo+4\n s_mov_b32 %2, _Z6tail_kv@rel32@lo+12" : "=s"(pc), "=s"(lo), "=s"(hi));
            touch(pfc, threadIdx.x * 128, (const void*)(pc + (long long)lo), (unsigned)max(hi - lo, 0)); touch(pfc, threadIdx.x * 128 + 32768, (const void*)(pc + (long long)lo), (unsigned)max(hi - lo, 0));
        }
    }
    constexpr int NA = C::FA == CHK ? 1 : C::FA == ROW ? 2 : 4, NB = C::FB == CHK ? 1 : 2;
    unsigned adA[NA], adB[NB];
    const unsigned lds0 = (unsigned)(size_t)lds, row_sw = lds0 + (unsigned)(l15 * 128 + ((q ^ (lane & 7)) << 4)), chk = lds0 + (unsigned)(q * 2048 + l15 * 16);
#pragma unroll
    for (int i = 0; i < NA; ++i) {
        if constexpr (C::FA == ROW) adA[i] = (row_sw ^ (unsigned)(i * 64)) + (unsigned)(wm * 8192);
        if constexpr (C::FA == CHK) adA[i] = chk + (unsigned)(wm * 1024);
        if constexpr (C::FA == TR) { const int q4 = l15 >> 2; adA[i] = lds0 + (unsigned)((8 * q + q4) * 256 + (((wm * 4 + i) ^ (q4 | ((q & 1) << 2))) << 5) + 8 * (lane & 3)); }
    }
#pragma unroll
    for (int i = 0; i < NB; ++i) adB[i] = C::FB == CHK ? 16384u + chk + (unsigned)(wn * 512) : 16384u + (row_sw ^ (unsigned)(i * 64)) + (unsigned)(wn * 8192);
    constexpr int W = C::FA == TR ? 2 : 1, NR = 4 + 4 * W;
    auto rd = [&](auto ic, Frag (&ra)[4], bf16x8 (&rb)[4], auto kkc) {
        constexpr int kk = decltype(kkc)::value, i = decltype(ic)::value;
        if constexpr (i == 0 || (i > W && i <= W + 3)) { constexpr int n = i == 0 ? 0 : i - W;
            if constexpr (C::FB == CHK) rb[n] = lds_read128<kk * 8192 + (n >> 1) * 1024 + (n & 1) * 256>(adB[0]);
            else rb[n] = lds_read128<n * 2048>(adB[kk]); }
        else { constexpr int j = i <= W ? i - 1 : i - 4, m = j / W;
            if constexpr (C::FA != TR) { bf16x8 v;
                if constexpr (C::FA == CHK) v = lds_read128<kk * 8192 + m * 256>(adA[0]); else v = lds_read128<m * 2048>(adA[kk]);
                const u32x4 w = __builtin_bit_cast(u32x4, v); ra[m].lo = (u32x2){w[0], w[1]}; ra[m].hi = (u32x2){w[2], w[3]}; }
            else if constexpr (j % 2 == 0) ra[m].lo = tr_read<kk * 8192>(adA[m]);
            else ra[m].hi = tr_read<kk * 8192 + 1024>(adA[m]); }
    };
    auto phase = [&](const Frag (&ra)[4], const bf16x8 (&rb)[4], Frag (&na)[4], bf16x8 (&nb)[4], int dstep, auto kkc) {
        static_for<0, 16>([&](auto sc) {
            constexpr int s = decltype(sc)::value, m = s >> 2, n = s & 3;
            if constexpr (n == 0) { wait_lgkm<(3 - m) * W + (s < NR ? s : NR)>(); sched0(); }
            acc[m][n] = MFMA(ra[m].v(), rb[n], acc[m][n]);
            if constexpr (s < NR) rd(sc, na, nb, kkc);
            if constexpr (s >= 12 && decltype(kkc)::value == 1) {
                if constexpr (s - 12 < NA) bump(adA[s - 12], dstep);
                if constexpr (s - 12 < NB) bump(adB[s - 12], dstep);
            }
            sched0();
        });
    };
    Frag a0[4], a1[4]; bf16x8 b0[4], b1[4];
    constexpr std::integral_constant<int, 0> K0{}; constexpr std::integral_constant<int, 1> K1{};
    if constexpr (ID != 1) bar();
    bar();
    STAMP(1);
    static_for<0, NR>([&](auto ic) { rd(ic, a0, b0, K0); });
    sched0();
    const int rx = tm * 128 + tn * 8 + wid * 2 + (lane >> 5), rw = tn * 128 + tm * 8 + wid * 2 + (lane >> 5);
    u32x4 xv[4], wv[4];
    if constexpr (ID == 1) {
#pragma unroll
        for (int i = 0; i < 4; ++i) { xv[i] = __builtin_amdgcn_raw_buffer_load_b128(RSRC(p.x, SEQ * DM * 2), (unsigned)(rx * 2048 + (lane & 31) * 16 + i * 512), 0, 0);
                                      wv[i] = __builtin_amdgcn_raw_buffer_load_b128(RSRC(tn < 8 ? p.wre : p.wim, DM * DM * 2), (unsigned)((rw & 1023) * 2048 + (lane & 31) * 16 + i * 512), 0, 0); }
        sched0();
    }
    auto steps = [&](int t0, int t1) {
#pragma nounroll
        for (int t = t0; t < t1; ++t) {
            phase(a0, b0, a1, b1, (t & 3) == 3 ? -3 * STAGE : STAGE, K1);
            if constexpr (ID == 1) { if (t == LNT1) wait_vm<0>(); }
            bar();
            phase(a1, b1, a0, b0, 0, K0);
        }
    };
    if constexpr (ID == 1) {
        steps(0, LNT0);
        wait_lgkm0(); sched0();
        f32x2 sx = {0.f, 0.f}, sw = sx, sq = sx;
#pragma unroll
        for (int i = 0; i < 16; ++i) { sx += (f32x2){bf_lo(xv[i >> 2][i & 3]), bf_hi(xv[i >> 2][i & 3])}; sw += (f32x2){bf_lo(wv[i >> 2][i & 3]), bf_hi(wv[i >> 2][i & 3])}; }
        const float mean = hsum(sx[0] + sx[1]) * (1.f / DM);
#pragma unroll
        for (int i = 0; i < 16; ++i) { const f32x2 d = (f32x2){bf_lo(xv[i >> 2][i & 3]), bf_hi(xv[i >> 2][i & 3])} - mean; sq += d * d; }
        const float rstd = __builtin_amdgcn_rsqf(hsum(sq[0] + sq[1]) * (1.f / DM) + LN_EPS), wsum = hsum(sw[0] + sw[1]);
        const int j = lane & 31;
        __builtin_amdgcn_raw_buffer_store_b32(__float_as_uint(j == 0 ? mean : j == 1 ? rstd : wsum), RSRC(p.ln1, 3 * SEQ * 4), j < 3 ? (unsigned)((j * SEQ + (j < 2 ? rx : rw)) * 4) : 0x7ffffff0u, 0, SC1);
        sched0();
        steps(LNT0, nk);
    } else steps(0, nk);
    wait_lgkm0(); keep(a0, b0); sched0();
    auto send = [&](int h0) {
        const auto rx = RSRC(p.xch, 256 * 32768);
#pragma unroll
        for (int m = 0; m < 4; ++m)
#pragma unroll
            for (int h = h0; h < 2; ++h) {
                const f32x4 u = acc[m][2 * h], v = acc[m][2 * h + 1];
                __builtin_amdgcn_raw_buffer_store_b128((u32x4){pack_bf16(u[0], u[1]), pack_bf16(u[2], u[3]), pack_bf16(v[0], v[1]), pack_bf16(v[2], v[3])}, rx, xoff + (unsigned)((h0 ? m : m * 2 + h) * 1024 + lane * 16), 0, SC1);
            }
    };
    if constexpr (NH > 0) {
        send(1); sched0();
        auto phaseH = [&](const Frag (&ra)[4], const bf16x8 (&rb)[4], Frag (&na)[4], bf16x8 (&nb)[4], int dstep, auto kkc) {
            static_for<0, 8>([&](auto sc) {
                constexpr int s = decltype(sc)::value;
                if constexpr ((s & 1) == 0) { wait_lgkm<6 - s + (s < 5 ? 2 * s : 10)>(); sched0(); }
                acc[s >> 1][s & 1] = MFMA(ra[s >> 1].v(), rb[s & 1], acc[s >> 1][s & 1]);
                if constexpr (s < 5) { rd(std::integral_constant<int, s < 2 ? 2 * s : 2 * s + 2>{}, na, nb, kkc); rd(std::integral_constant<int, s < 2 ? 2 * s + 1 : 2 * s + 3>{}, na, nb, kkc); }
                if constexpr (s >= 5 && decltype(kkc)::value == 1) { if constexpr (s < 7) { bump(adA[2 * (s - 5)], dstep); bump(adA[2 * (s - 5) + 1], dstep); } else bump(adB[0], dstep); }
                sched0();
            });
        };
#pragma nounroll
        for (int t = nk; t < nk + NH; ++t) {
            phaseH(a0, b0, a1, b1, (t & 3) == 3 ? -3 * STAGE : STAGE, K1);
            bar();
            phaseH(a1, b1, a0, b0, 0, K0);
            if (t == nk + FLAGD) { wait_vm<0>(); if (lane == 0) flag_store(flag, ep); }
        }
        wait_lgkm0();
        asm volatile("" ::"v"(b0[0]), "v"(b0[1]));
#pragma unroll
        for (int i = 0; i < 4; ++i) asm volatile("" ::"v"(a0[i].lo), "v"(a0[i].hi));
        sched0();
    }
    STAMP(2);
    if constexpr (ID == 1) {
        wait_vm<0>(); asm volatile("; pfc %0" ::"v"(pfc));
        bar();
        const auto ro = RSRC(p.abT, ABT_BYTES);
        const float sgn = tn < 8 ? 1.f : -1.f;
        f32x4 mean[4], rs[4]; float ws[4];
        static_for<0, 4>([&](auto ic) { constexpr int i = decltype(ic)::value; const unsigned a = lds0 + (unsigned)(XLDS + wm * 256 + q * 16);
            mean[i] = __builtin_bit_cast(f32x4, lds_read128<i * 64>(a)); rs[i] = __builtin_bit_cast(f32x4, lds_read128<1024 + i * 64>(a));
            ws[i] = lds_read32<2048 + i * 64>(lds0 + (unsigned)(XLDS + wn * 256 + l15 * 4)); });
        wait_lgkm0(); sched0();
#pragma unroll
        for (int m = 0; m < 4; ++m) {
            const f32x4 r = rs[m] * sgn;
#pragma unroll
            for (int n = 0; n < 4; ++n) acc[m][n] = (acc[m][n] - mean[m] * ws[n]) * r;
        }
#pragma unroll
        for (int n = 0; n < 4; ++n) { const int e = (tn & 7) * 128 + wn * 64 + n * 16 + l15;
#pragma unroll
            for (int mp = 0; mp < 2; ++mp) {
                unsigned x0 = pack_bf16(acc[2 * mp][n][0], acc[2 * mp][n][1]), x1 = pack_bf16(acc[2 * mp][n][2], acc[2 * mp][n][3]);
                unsigned y0 = pack_bf16(acc[2 * mp + 1][n][0], acc[2 * mp + 1][n][1]), y1 = pack_bf16(acc[2 * mp + 1][n][2], acc[2 * mp + 1][n][3]);
                swap16u(x0, y0); swap16u(x1, y1);
                const int s = (tn >> 3) * SEQ + col0 + mp * 32;
                __builtin_amdgcn_raw_buffer_store_b128((u32x4){x0, x1, y0, y1}, ro, (unsigned)(((s >> 3) * ABP + e) * 16), 0, STA);
            } }
    } else if (!recv) {
        send(0);
        wait_vm<0>();
        if (lane == 0) flag_store(flag, ep);
        if constexpr (ID == 2) bar();
    } else {
        bar();
        STAMP(4);
        const unsigned px = (unsigned)(size_t)lds + (unsigned)(XLDS + wid * 8192 + lane * 16);
        constexpr int HH = ID == 4 ? 1 : 2;
        u32x4 pw[4 * HH];
        static_for<0, 4 * HH>([&](auto ic) { pw[decltype(ic)::value] = __builtin_bit_cast(u32x4, lds_read128<decltype(ic)::value * 1024>(px)); });
        wait_lgkm0(); sched0();
#pragma unroll
        for (int m = 0; m < 4; ++m)
#pragma unroll
            for (int h = 0; h < HH; ++h) { const u32x4 w = pw[m * HH + h];
                acc[m][2 * h] += (f32x4){bf_lo(w[0]), bf_hi(w[0]), bf_lo(w[1]), bf_hi(w[1])}; acc[m][2 * h + 1] += (f32x4){bf_lo(w[2]), bf_hi(w[2]), bf_lo(w[3]), bf_hi(w[3])}; }
        const auto ro = RSRC(ID == 2 ? p.h : p.out, ID == 2 ? 136 * SEQ * 16 : SEQ * DM * 2);
#pragma unroll
        for (int n = 0; n < 2 * HH; ++n) {
            unsigned o[4][2];
#pragma unroll
            for (int m = 0; m < 4; ++m) { o[m][0] = pack_bf16(acc[m][n][0], acc[m][n][1]); o[m][1] = pack_bf16(acc[m][n][2], acc[m][n][3]); }
#pragma unroll
            for (int mp = 0; mp < 2; ++mp) {
                swap16u(o[2 * mp][0], o[2 * mp + 1][0]); swap16u(o[2 * mp][1], o[2 * mp + 1][1]);
                __builtin_amdgcn_raw_buffer_store_b128((u32x4){o[2 * mp][0], o[2 * mp][1], o[2 * mp + 1][0], o[2 * mp + 1][1]}, ro,
                    (unsigned)(ID == 4 ? ((row0 + n * 16) * DM + col0 + mp * 32) * 2 : ((col0 / 8 + mp * 4) * SEQ + row0 + n * 16) * 16), 0, SC1);
            }
        }
        STAMP(5);
        if constexpr (ID == 2) {
            wait_vm<0>(); bar();
            sto = (unsigned)(((128 + tm) * SEQ + row0 + q * 16) * 16 + wm * 8);
#pragma unroll
            for (int n = 0; n < 4; ++n) {
                const f32x4 s4 = (acc[0][n] + acc[1][n]) + (acc[2][n] + acc[3][n]);
                const float mw = qsum((s4[0] + s4[1]) + (s4[2] + s4[3])) * (1.f / 64.f);
                const f32x4 d0 = acc[0][n] - mw, d1 = acc[1][n] - mw, d2 = acc[2][n] - mw, d3 = acc[3][n] - mw, q4 = (d0 * d0 + d1 * d1) + (d2 * d2 + d3 * d3);
                float m2 = (q4[0] + q4[1]) + (q4[2] + q4[3]);
                m2 = qsum(m2);
                if (q == n) stv = (u32x2){__float_as_uint(mw), __float_as_uint(m2)};
            }
        }
    }
    STAMP_END(ID);
}
#endif

#if defined(__HIP_DEVICE_COMPILE__)
template <int GRP>
__device__ __forceinline__ void gemm3_body(const Params& p, LAS unsigned char* lds, const int wid, u64& ep, u32x2 stv, unsigned sto) {
    constexpr int NK = 16, FA = 7, HB0 = 3 * 32768;
    STAMP_BEGIN;
    const int tid = threadIdx.x, lane = tid & 63;
    const int wm = wid & 1, wn = wid >> 1, l15 = lane & 15, q = lane >> 4;
    const int xcd = blockIdx.x & 7, idx = blockIdx.x >> 3, im = idx & 7, in = idx >> 3;
    const int tm = (xcd & 3) * 8 + im, tn = (xcd >> 2) * 4 + in;
    const G3W gw(p, wid, lane);
    unsigned pf = 0;
    u64* words = p.flags + (F_H + tn * 32) * FLAG_STRIDE;
    if (threadIdx.x == 256 && !(xcd & 1)) flag_store(words + tm * FLAG_STRIDE, ep);
    if (wid != 4) __builtin_amdgcn_s_sleep(2);
    if constexpr (GRP == 1) { const G3W g0(p, wid - 4, lane); g0.early(lds, wid - 4, 0); gw.early(lds, wid, 0); g0.early(lds, wid - 4, 1); gw.early(lds, wid, 1); }
    if ((xcd & 1) && wid == 1) colsum_final(p, lds, ep);
    if (wid == 4) {
        wait_group(words + (lane & 31) * FLAG_STRIDE, (lane & 8) == 0, ep);
        if (blockIdx.x == 0 && lane == 0) flag_store(p.flags, ep);
    }
    bar();
    const unsigned voffB = (unsigned)((wid * SEQ + tn * 256 + lane) * 16);
    struct Src { const void* base; int so; unsigned nrec; };
    auto pin = [](Src x) { asm volatile("" : "+s"(x.base), "+s"(x.so), "+s"(x.nrec)); return x; };
    auto srcW = [&](int t) { return pin(Src{t < NK ? (const void*)gw.ptr : (const void*)(p.csum + GRP * 3584), t < NK ? gw.kstep(t) * 64 * FF * 2 : 0, t < NK ? (unsigned)(DM * FF * 2) : t == NK ? (unsigned)(2 * FF * 2) : 0u}); };
    auto srcH = [&](int t) { Src x{nullptr, (t < NK ? gw.kstep(t) : NK) * 8 * SEQ * 16, 136 * SEQ * 16}; asm volatile("" : "+s"(x.so)); return x; };
    auto stageW = [&](Src x, int slot, int i) { gw.piece(lds, wid, x.base, x.so, x.nrec, slot, i); };
    auto stageH = [&](Src x, int slot, int i) { DMA16_DEV(RSRC(p.h, x.nrec), lds + HB0 + slot * 32768 + wid * 4096 + i * 1024, voffB, x.so + i * 1024); };
#pragma unroll
    for (int t = 0; t < 2; ++t) {
        const Src xh = srcH(t);
#pragma unroll
        for (int i = 0; i < 4; ++i) stageH(xh, t, i);
        if (!W1E && t == 0) gw.early(lds, wid, 1);
    }

    unsigned adA[FA];
    const unsigned lds0 = (unsigned)(size_t)lds;
#pragma unroll
    for (int m = 0; m < FA; ++m) { const int q4 = l15 >> 2, pp = lane & 3, reg = m < 6 ? m & 1 : pp >> 1, ch = m < 6 ? wm * 8 + (m & 6) + (pp >> 1) : 6 + wm;
        adA[m] = lds0 + (unsigned)(reg * 16384 + (8 * q + q4) * 256 + ((ch ^ (((q4 | ((q & 1) << 2)) << 1) | reg)) << 4) + 8 * (pp & 1)); }
    unsigned adB = lds0 + (unsigned)(HB0 + q * 4096 + (wn * 64 + l15) * 16);
    auto rd = [&](auto ic, Frag (&ra)[FA], bf16x8 (&rb)[4], auto kkc) {
        constexpr int kk = decltype(kkc)::value, i = decltype(ic)::value;
        if constexpr (i < 4) rb[i] = lds_read128<kk * 16384 + i * 256>(adB);
        else if constexpr (i % 2 == 0) ra[(i - 4) / 2].lo = tr_read<kk * 8192>(adA[(i - 4) / 2]);
        else ra[(i - 4) / 2].hi = tr_read<kk * 8192 + 1024>(adA[(i - 4) / 2]);
    };
    f32x4 acc[FA][4];
#pragma unroll
    for (int m = 0; m < FA; ++m)
#pragma unroll
        for (int n = 0; n < 4; ++n) acc[m][n] = (f32x4){0.f, 0.f, 0.f, 0.f};
#define DSTR 2
#define LASTP 3
#define PR1 18
#define PR2 6
#define PR3 22

    auto phase = [&](const Frag (&ra)[FA], const bf16x8 (&rb)[4], Frag (&na)[FA], bf16x8 (&nb)[4], int dw, int dh, auto kkc, auto grpc, auto&& dma, auto&& prep) {
        static_for<0, 28>([&](auto sc) {
            constexpr int s = decltype(sc)::value, m = s >> 2, n = s & 3;
            if constexpr (decltype(kkc)::value == 0) { if constexpr (s == 0) __builtin_amdgcn_s_setprio(3); if constexpr (s == PR1) __builtin_amdgcn_s_setprio(2); }
            else { if constexpr (s == PR2) __builtin_amdgcn_s_setprio(1); if constexpr (s == PR3) __builtin_amdgcn_s_setprio(0); }
            acc[m][n] = MFMA(ra[m].v(), rb[n], acc[m][n]);
            sched0();
            if constexpr (s < 18) rd(sc, na, nb, kkc);
            if constexpr (s == 20) prep();
            if constexpr (s % DSTR == 0 && s < 8 * DSTR && decltype(grpc)::value == ((s / DSTR) & 1)) dma(s / (2 * DSTR));
            if constexpr (decltype(kkc)::value == 1 && s >= 18 && s < 26) { if constexpr (s < 25) bump(adA[s - 18], dw); else bump(adB, dh); }
            sched0();
        });
        wait_lgkm0(); sched0();
    };
    Frag a0[FA], a1[FA]; bf16x8 b0[4], b1[4];
    constexpr std::integral_constant<int, 0> K0{}; constexpr std::integral_constant<int, 1> K1{};
    wait_vm<W1E ? 4 : 8>(); bar();
    if constexpr (GRP == 0) __builtin_amdgcn_raw_buffer_store_b64(stv, RSRC(p.h, 136 * SEQ * 16), sto, 0, SC1);
    STAMP(1);
    static_for<0, 18>([&](auto ic) { rd(ic, a0, b0, K0); });
    wait_lgkm0(); sched0();
    const unsigned pfo = (unsigned)(((blockIdx.x * 8 + wid) * 28 + (lane < 28 ? lane : 0)) * 128); const int pft = 1 + idx % 12;
    constexpr std::integral_constant<int, GRP> grpc{};
    Src xw = srcW(2), xh;
    auto phaseA = [&](int t, int sw2) {
        phase(a0, b0, a1, b1, sw2 == 1 ? -65536 : 32768, (t & 1) ? -32768 : 32768, K1, grpc, [&](int i) { stageW(xw, sw2, i);
            if (i == 3) touch(pf, pfo, p.wd, t == pft ? FF * DM * 2 : 0);
        }, [&]() { xh = srcH(t + 2); });
        wait_vm<5>();
    };
    u64 cseen = 0;
    phaseA(0, 2);
    int sw2 = 0;
#pragma nounroll
    for (int t = 0; t < NK - 1; ++t) {
        if (__builtin_expect(t == 1 && threadIdx.x == 0 && !(xcd & 1), 0)) flag_store(p.flags + (F_ST + tn * 32 + tm) * FLAG_STRIDE, ep);
        if (__builtin_expect(t >= 11 && t <= 12 && (wid & 3) == 0, 0)) {
            const int sl = min((tm * 112 >> 6) + (lane & 3), (tm * 112 + 111) >> 6);
            u64* w = p.flags + (lane < 32 ? F_CS + GRP * 56 + sl : F_ST + tn * 32 + (lane & 23)) * FLAG_STRIDE;
            if (t == 11) flag_peek(cseen, w); else if (__any(cseen != ep)) wait_group(w, true, ep);
        }
        bar();
        phase(a1, b1, a0, b0, 0, 0, K0, grpc, [&](int i) { stageH(xh, t & 1, i); }, [&]() { xw = srcW(t + 3); });
        phaseA(t + 1, sw2);
        sw2 = sw2 == 2 ? 0 : sw2 + 1;
    }
    bar();
    __builtin_amdgcn_s_setprio(GRP ? 0 : LASTP);
    f32x4 csv[FA], sp[4][2];
    static_for<0, 16>([&](auto sc) {
        constexpr int s = decltype(sc)::value;
        acc[s >> 2][s & 3] = MFMA(a1[s >> 2].v(), b1[s & 3], acc[s >> 2][s & 3]);
        if constexpr (s < 8) sp[s >> 1][s & 1] = __builtin_bit_cast(f32x4, lds_read128<(s & 1) * 16384 + (s >> 1) * 256>(adB));
        else if constexpr (s < 15) { constexpr int m = s - 8; const int reg = m < 6 ? m & 1 : q >> 1, c4 = m < 6 ? wm * 2 + (m & 6) * 2 + q : (wm ? 0 : 12) + (q & 1);
            csv[m] = __builtin_bit_cast(f32x4, lds_read128<32768>(lds0 + (unsigned)(reg * 16384 + wm * 256 + ((c4 ^ ((wm << 1) | reg)) << 4)))); }
        if constexpr (GRP == 1 && s == 2) g128_load<4, 0>(p, lds, wid, ep);
        sched0();
    });
    wait_lgkm0(); wait_vm<GRP ? 4 : 0>(); asm volatile("; pf %0" ::"v"(pf)); sched0();
    float mean[4], c1[4], ve[4];
#pragma unroll
    for (int n = 0; n < 4; ++n) {
        const f32x4 s0 = sp[n][0], s1 = sp[n][1];
        mean[n] = qsum((s0[0] + s0[2]) + (s1[0] + s1[2])) * (1.f / 16.f);
        const float d0 = s0[0] - mean[n], d1 = s0[2] - mean[n], d2 = s1[0] - mean[n], d3 = s1[2] - mean[n];
        const float m2 = qsum((s0[1] + s0[3]) + (s1[1] + s1[3]) + 64.f * ((d0 * d0 + d1 * d1) + (d2 * d2 + d3 * d3)));
        ve[n] = m2 * (1.f / DM) + LN_EPS; c1[n] = -1.44269504f * __builtin_amdgcn_rsqf(ve[n]);
    }
#pragma unroll
    for (int s = 16; s < 28; ++s) acc[s >> 2][s & 3] = MFMA(a1[s >> 2].v(), b1[s & 3], acc[s >> 2][s & 3]);
#pragma unroll
    for (int s = 16; s < 28; ++s) { __builtin_amdgcn_sched_group_barrier(0x008, 1, 0); __builtin_amdgcn_sched_group_barrier(0x002, 10, 0); }
    sched0();
    STAMP(2);
    const auto ro = RSRC(p.act, SEQ * FF * 2);
    auto silu2 = [&](f32x2 g, f32x2 u, int n) {
        const f32x2 x = g * c1[n], d = (f32x2){__builtin_amdgcn_exp2f(x[0]), __builtin_amdgcn_exp2f(x[1])} * ve[n] + ve[n];
        const f32x2 r = g * u * (f32x2){__builtin_amdgcn_rcpf(d[0]), __builtin_amdgcn_rcpf(d[1])};
        return pack_bf16(r[0], r[1]);
    };
#pragma unroll
    for (int j = 0; j < 3; ++j)
#pragma unroll
        for (int np = 0; np < 2; ++np) {
            unsigned o[2][2];
#pragma unroll
            for (int i = 0; i < 2; ++i) { const int n = 2 * np + i; const f32x4 g = acc[2 * j][n] - mean[n] * csv[2 * j], u = acc[2 * j + 1][n] - mean[n] * csv[2 * j + 1];
                o[i][0] = silu2((f32x2){g[0], g[1]}, (f32x2){u[0], u[1]}, n); o[i][1] = silu2((f32x2){g[2], g[3]}, (f32x2){u[2], u[3]}, n); }
            swap16u(o[0][0], o[1][0]); swap16u(o[0][1], o[1][1]);
            __builtin_amdgcn_raw_buffer_store_b128((u32x4){o[0][0], o[0][1], o[1][0], o[1][1]}, ro, (unsigned)(((tm * 14 + wm * 8 + 2 * j + (q >> 1)) * SEQ + tn * 256 + wn * 64 + np * 32 + (lane & 31)) * 16), 0, STA);
        }
    {
        unsigned o[4];
#pragma unroll
        for (int n = 0; n < 4; ++n) {
            const f32x4 v = acc[6][n] - mean[n] * csv[6];
            float g0 = v[0], g1 = v[1], u0 = v[2], u1 = v[3];
            swap32(g0, u0); swap32(g1, u1);
            o[n] = silu2((f32x2){g0, g1}, (f32x2){u0, u1}, n);
        }
        swap16u(o[0], o[1]); swap16u(o[2], o[3]); swap32u(o[0], o[2]); swap32u(o[1], o[3]);
        __builtin_amdgcn_raw_buffer_store_b128((u32x4){o[0], o[2], o[1], o[3]}, ro, (unsigned)(((tm * 14 + 6 + wm) * SEQ + tn * 256 + wn * 64 + lane) * 16), 0, STA);
    }
    STAMP(5);
    __builtin_amdgcn_s_setprio(0);
    if constexpr (GRP == 1) { wait_vm<0>(); bar(); }
    STAMP_END(3);
}
#endif

__global__ __launch_bounds__(512, 2) void fused_k(const Params p) {
#if defined(__HIP_DEVICE_COMPILE__)
    __shared__ __attribute__((aligned(16))) unsigned char lds_raw[5 * 32768];
    LAS unsigned char* lds = (LAS unsigned char*)lds_raw;
    const int wid = __builtin_amdgcn_readfirstlane(threadIdx.x >> 6);
    u64 ep = 0;
    u32x2 stv = {0u, 0u}; unsigned sto = 0x7ffffff0u;
    if (wid < 4) { g128_compute<1>(p, lds, wid, ep, stv, sto); g128_compute<2>(p, lds, wid, ep, stv, sto); gemm3_body<0>(p, lds, wid, ep, stv, sto); g128_compute<4>(p, lds, wid, ep, stv, sto); }
    else { g128_load<1, -1>(p, lds, wid, ep); g128_load<2, -1>(p, lds, wid, ep); gemm3_body<1>(p, lds, wid, ep, stv, sto); g128_load<4, -1>(p, lds, wid, ep); }
#endif
}
__global__ void tail_k() {}

extern "C" void kernel_launch(void* const* d_in, const int* in_sizes, int n_in, void* d_out, int out_size, void* d_ws, size_t ws_size, hipStream_t stream) {
    Params p;
    p.x = (const u16*)d_in[0]; p.cre = (const u16*)d_in[1]; p.cim = (const u16*)d_in[2]; p.wre = (const u16*)d_in[3]; p.wim = (const u16*)d_in[4];
    p.wg = (const u16*)d_in[6]; p.wu = (const u16*)d_in[7]; p.wd = (const u16*)d_in[8];
    p.out = (u16*)d_out;
    char* ws = (char*)d_ws;
    p.abT = (u16*)(ws + (ABP == 1024 ? 0u : 48u << 20)); p.h = (u16*)(ws + (8u << 20)); p.act = (u16*)(ws + (16u << 20));
    p.xch = (u16*)(ws + (32u << 20)); p.flags = (unsigned long long*)(ws + (40u << 20)); p.csum = (float*)(ws + (40u << 20) + (512u << 10)); p.ln1 = (float*)(ws + (40u << 20) + (768u << 10));
    fused_k<<<256, 512, 0, stream>>>(p);
}
```

```cpp
#include <hip/hip_runtime.h>
#include <hip/hip_bf16.h>
#include <type_traits>

#define LAS __attribute__((address_space(3)))
typedef unsigned short u16;
typedef __bf16 bf16x8 __attribute__((ext_vector_type(8)));
typedef __bf16 bf16x2 __attribute__((ext_vector_type(2)));
typedef float f32x4 __attribute__((ext_vector_type(4)));
typedef float f32x2 __attribute__((ext_vector_type(2)));
typedef unsigned u32x4 __attribute__((ext_vector_type(4)));
typedef unsigned u32x2 __attribute__((ext_vector_type(2)));

constexpr int SEQ = 2048, DM = 1024, FF = 3584;
constexpr float LN_EPS = 1e-5f;
constexpr int SC1 = 16;

struct Params {
    const u16* x; const u16* cre; const u16* cim; const u16* wre; const u16* wim;
    const u16* wg; const u16* wu; const u16* wd;
    u16* out; u16* abT; u16* h; u16* act; u16* xch; unsigned long long* flags; float* csum; float* ln1;
};

template <int N> __device__ __forceinline__ void wait_vm() { asm volatile("s_waitcnt vmcnt(%0)" ::"n"(N) : "memory"); }
__device__ __forceinline__ void wait_lgkm0() { __builtin_amdgcn_s_waitcnt(0xC07F); asm volatile("s_waitcnt lgkmcnt(0)" ::: "memory"); }
__device__ __forceinline__ void sched0() { __builtin_amdgcn_sched_barrier(0); }
__device__ __forceinline__ void bar() { sched0(); asm volatile("" ::: "memory"); __builtin_amdgcn_s_barrier(); asm volatile("" ::: "memory"); sched0(); }
__device__ __forceinline__ unsigned pack_bf16(float lo, float hi) { bf16x2 c; c[0] = (__bf16)lo; c[1] = (__bf16)hi; return __builtin_bit_cast(unsigned, c); }
__device__ __forceinline__ float bf_lo(unsigned v) { return __uint_as_float(v << 16); }
__device__ __forceinline__ float bf_hi(unsigned v) { return __uint_as_float(v & 0xffff0000u); }
__device__ __forceinline__ void swap16(float& a, float& b) { auto r = __builtin_amdgcn_permlane16_swap(__float_as_uint(a), __float_as_uint(b), false, false); a = __uint_as_float(r[0]); b = __uint_as_float(r[1]); }
__device__ __forceinline__ void swap32(float& a, float& b) { auto r = __builtin_amdgcn_permlane32_swap(__float_as_uint(a), __float_as_uint(b), false, false); a = __uint_as_float(r[0]); b = __uint_as_float(r[1]); }
__device__ __forceinline__ void swap16u(unsigned& a, unsigned& b) { auto r = __builtin_amdgcn_permlane16_swap(a, b, false, false); a = r[0]; b = r[1]; }
__device__ __forceinline__ void swap32u(unsigned& a, unsigned& b) { auto r = __builtin_amdgcn_permlane32_swap(a, b, false, false); a = r[0]; b = r[1]; }
__device__ __forceinline__ float qsum(float v) { float a = v, b = v; swap16(a, b); a += b; b = a; swap32(a, b); return a + b; }

template <int CTRL> __device__ __forceinline__ float dpp_add(float v) { return v + __int_as_float(__builtin_amdgcn_update_dpp(0, __float_as_int(v), CTRL, 0xf, 0xf, false)); }
__device__ __forceinline__ float hsum(float v) { v = dpp_add<0x128>(v); v = dpp_add<0x124>(v); v = dpp_add<0x122>(v); v = dpp_add<0x121>(v); float b = v; swap16(v, b); return v + b; }

struct Frag { u32x2 lo, hi; __device__ __forceinline__ bf16x8 v() const { return __builtin_bit_cast(bf16x8, __builtin_shufflevector(lo, hi, 0, 1, 2, 3)); } };
template <int OFF> __device__ __forceinline__ u32x2 tr_read(unsigned addr) { u32x2 r; asm volatile("ds_read_b64_tr_b16 %0, %1 offset:%2" : "=v"(r) : "v"(addr), "n"(OFF) : "memory"); return r; }
template <int OFF> __device__ __forceinline__ bf16x8 lds_read128(unsigned addr) { bf16x8 r; asm volatile("ds_read_b128 %0, %1 offset:%2" : "=v"(r) : "v"(addr), "n"(OFF) : "memory"); return r; }
template <int OFF> __device__ __forceinline__ float lds_read32(unsigned addr) { float r; asm volatile("ds_read_b32 %0, %1 offset:%2" : "=v"(r) : "v"(addr), "n"(OFF) : "memory"); return r; }
template <int N> __device__ __forceinline__ void wait_lgkm() { asm volatile("s_waitcnt lgkmcnt(%0)" ::"n"(N) : "memory"); }
template <int I, int N, class F> __device__ __forceinline__ void static_for(F&& f) { if constexpr (I < N) { f(std::integral_constant<int, I>{}); static_for<I + 1, N>(f); } }
template <int NA> __device__ __forceinline__ void keep(const Frag (&a)[NA], const bf16x8 (&b)[4]) {
#pragma unroll
    for (int i = 0; i < NA; ++i) asm volatile("" ::"v"(a[i].lo), "v"(a[i].hi));
#pragma unroll
    for (int i = 0; i < 4; ++i) asm volatile("" ::"v"(b[i]));
}
__device__ __forceinline__ void bump(unsigned& a, int d) { asm volatile("v_add_u32 %0, %1, %0" : "+v"(a) : "s"(d)); }
#define MFMA(a, b, c) __builtin_amdgcn_mfma_f32_16x16x32_bf16(a, b, c, 0, 0, 0)
#define RSRC(ptr, bytes) __builtin_amdgcn_make_buffer_rsrc((void*)(ptr), 0, (bytes), 0x00020000)
#define DMA16(rs, ldsp, voff, soff) __builtin_amdgcn_raw_ptr_buffer_load_lds(rs, (LAS void*)(ldsp), 16, voff, soff, 0, 0)
#define DMA16_DEV(rs, ldsp, voff, soff) __builtin_amdgcn_raw_ptr_buffer_load_lds(rs, (LAS void*)(ldsp), 16, voff, soff, 0, SC1)

typedef unsigned long long u64;
constexpr int FLAG_STRIDE = 8, F_PAIR = 1, F_AB = F_PAIR + 1536, F_H = F_AB + 256, F_ACT = F_H + 256, F_CS = F_ACT + 256, F_LN = F_CS + 128;
__device__ __forceinline__ u64 flag_load(u64* w) { return __hip_atomic_load(w, __ATOMIC_RELAXED, __HIP_MEMORY_SCOPE_AGENT); }
__device__ __forceinline__ void flag_store(u64* w, u64 v) { __hip_atomic_store(w, v, __ATOMIC_RELAXED, __HIP_MEMORY_SCOPE_AGENT); }
__device__ __forceinline__ u64 uniform64(u64 v) { return ((u64)(unsigned)__builtin_amdgcn_readfirstlane((unsigned)(v >> 32)) << 32) | (unsigned)__builtin_amdgcn_readfirstlane((unsigned)v); }
__device__ __forceinline__ void wait_group(u64* word, bool need, u64 ep) {
#pragma nounroll
    for (int spin = 0; spin < (1 << 20); ++spin) {
        if (!__any(need && flag_load(word) != ep)) break;
        __builtin_amdgcn_s_sleep(2);
    }
}
#if defined(__HIP_DEVICE_COMPILE__)
__device__ __forceinline__ void touch(unsigned& sink, unsigned voff, const void* base, unsigned bytes) {
    asm volatile("s_nop 4\n\tbuffer_load_dword %0, %1, %2, 0 offen" : "+v"(sink) : "v"(voff), "s"(RSRC(base, bytes)) : "memory");
}
__device__ __forceinline__ void flag_peek(u64& v, const u64* w) { asm volatile("global_load_dwordx2 %0, %1, off sc1" : "+v"(v) : "v"(w) : "memory"); }
struct G3W {
    const u16* ptr; unsigned voff[2]; int phi, koff;
    __device__ __forceinline__ G3W(const Params& p, int wid, int lane) {
        const int xcd = blockIdx.x & 7, idx = blockIdx.x >> 3, tm = (xcd & 3) * 8 + (idx & 7), q = lane >> 4;
        ptr = wid < 4 ? p.wg : p.wu; phi = ((idx & 7) + (idx >> 3)) & 3; koff = xcd * 2;
#pragma unroll
        for (int i = 0; i < 2; ++i) { const int j = (lane & 15) ^ (((q | (i << 2)) << 1) | (wid >> 2));
            voff[i] = j < 14 ? (unsigned)(((wid & 3) * 16 + q) * FF * 2 + tm * 224 + j * 16) : 0x7ffffff0u; }
    }
    __device__ __forceinline__ int kstep(int t) const { return (t + koff + ((t & 3) == phi ? 4 : 0)) & 15; }
    __device__ __forceinline__ void piece(LAS unsigned char* lds, int wid, const void* base, int so, unsigned nrec, int slot, int i) const { DMA16_DEV(RSRC(base, nrec), lds + slot * 32768 + wid * 4096 + i * 1024, voff[i >> 1], so + i * 4 * FF * 2); }
    __device__ __forceinline__ void early(LAS unsigned char* lds, int wid, int t) const {
#pragma unroll
        for (int i = 0; i < 4; ++i) piece(lds, wid, ptr, kstep(t) * 64 * FF * 2, DM * FF * 2, t, i);
    }
};
__device__ __forceinline__ void colsum_partial(const Params& p, LAS unsigned char* lds, int lw) {
    const int lane = threadIdx.x & 63, slab = blockIdx.x >> 1, mat = slab >= 56, c0 = (slab - mat * 56) * 64;
    const auto rw = RSRC(mat ? p.wu : p.wg, slab < 112 ? DM * FF * 2 : 0);
    const unsigned vo = (unsigned)(((lw * 256 + (lane >> 3)) * FF + c0 + (lane & 7) * 8) * 2);
    u32x4 v[8]; f32x4 s0 = {0.f, 0.f, 0.f, 0.f}, s1 = s0;
    auto add = [&](const u32x4 w) { s0 += (f32x4){bf_lo(w[0]), bf_hi(w[0]), bf_lo(w[1]), bf_hi(w[1])}; s1 += (f32x4){bf_lo(w[2]), bf_hi(w[2]), bf_lo(w[3]), bf_hi(w[3])}; };
#pragma unroll
    for (int i = 0; i < 4; ++i) v[i] = __builtin_amdgcn_raw_buffer_load_b128(rw, vo, i * 8 * FF * 2, 0);
#pragma unroll
    for (int i = 0; i < 8; ++i) { add(v[i % 4]); v[i % 4] = __builtin_amdgcn_raw_buffer_load_b128(rw, vo, (4 + i) * 8 * FF * 2, 0); sched0(); }
#pragma unroll
    for (int i = 4; i < 8; ++i) v[i] = __builtin_amdgcn_raw_buffer_load_b128(rw, vo, (8 + i) * 8 * FF * 2, 0);
#pragma nounroll
    for (int j = 16; j < 40; j += 8)
#pragma unroll
        for (int i = 0; i < 8; ++i) { add(v[i]); v[i] = __builtin_amdgcn_raw_buffer_load_b128(rw, j < 32 ? vo : 0x7ffffff0u, (j + i) * 8 * FF * 2, 0); sched0(); }
    LAS float* tab = (LAS float*)(lds + 131072) + (lw * 8 + (lane >> 3)) * 64 + (lane & 7) * 8;
    *(LAS f32x4*)tab = s0; *(LAS f32x4*)(tab + 4) = s1;
    wait_lgkm0();
}
__device__ __forceinline__ void colsum_final(const Params& p, LAS unsigned char* lds, u64 ep) {
    const int lane = threadIdx.x & 63, slab = blockIdx.x >> 1, mat = slab >= 56, c0 = (slab - mat * 56) * 64;
    if (slab >= 112) return;
    const LAS float* tab = (const LAS float*)(lds + 131072);
    float a = 0.f, b = 0.f;
#pragma unroll
    for (int i = 0; i < 32; i += 2) { a += tab[i * 64 + lane]; b += tab[(i + 1) * 64 + lane]; }
    const int f = c0 + lane, t = f / 112, r = f - t * 112, h = r >= 56;
    __hip_atomic_store(p.csum + (mat * 2 + h) * 1792 + t * 56 + r - h * 56, a + b, __ATOMIC_RELAXED, __HIP_MEMORY_SCOPE_AGENT);
    wait_vm<0>();
    if (lane == 0) flag_store(p.flags + (F_CS + slab) * FLAG_STRIDE, ep);
}
#endif

enum { ROW, CHK, TR };
constexpr int NKS2 = 27;
constexpr int RE4 = 1, RO4 = 3;
constexpr int FLAGD = 1;
constexpr int LNT0 = 1, LNT1 = 5;
template <int ID> struct Cfg;
template <> struct Cfg<1> { static constexpr int NKT = 16, NKS = 16, FA = ROW, FB = ROW, LDB = DM; };
template <> struct Cfg<2> { static constexpr int NKT = 64, NKS = NKS2, FA = CHK, FB = ROW, LDB = SEQ; };
template <> struct Cfg<4> { static constexpr int NKT = 56, NKS = 0, FA = TR, FB = CHK, LDB = 0; };

#if defined(__HIP_DEVICE_COMPILE__)
template <int ID, bool PRE>
__device__ __forceinline__ void g128_load(const Params& p, LAS unsigned char* lds, const int wid, u64& ep) {
    using C = Cfg<ID>;
    constexpr int STAGE = 32768, XLDS = 4 * STAGE;
    const int lane = threadIdx.x & 63, l15 = lane & 15, q = lane >> 4;
    const int xcd = blockIdx.x & 7, idx = blockIdx.x >> 3, im = idx & 3, in = idx >> 2;
    constexpr int NH = ID == 4 ? 2 * (RE4 + RO4) : 0;
    const int ks = (xcd & 1) ^ (ID == 2); const bool recv = ID == 4 || (ID == 2 && ks);
    const int tm = (ID == 1 ? (xcd & 3) : ((xcd >> 1) & 1)) * 4 + im, tn = (xcd >> 2) * 8 + in;
    const int k0 = ID != 1 && ks ? C::NKS : 0, nk = ID == 4 ? 28 - 2 * (ks ? RO4 : RE4) : ID == 2 && ks ? C::NKT - C::NKS : C::NKS;
    const int xb = ID == 4 ? 128 + (blockIdx.x ^ (wid >> 2)) : blockIdx.x >> 1;
    u64* const flag = p.flags + (F_PAIR + xb * 4 + (wid & 3)) * FLAG_STRIDE;
    const unsigned xoff = (unsigned)(ID == 4 ? (128 + xb) * 16384 + (wid & 3) * 4096 : xb * 32768 + (wid & 3) * 8192);
    if constexpr (ID != 1 && !PRE) {
        if (wid == 4 && lane == 0) flag_store(p.flags + (ID == 2 ? F_AB + (tn & 7) * 32 + (tn >> 3) * 16 + (xcd & 3) * 4 + im : F_ACT + (tn >> 1) * 32 + (xcd & 3) * 8 + (idx & 7)) * FLAG_STRIDE, ep);
        sched0();
    }
    const int lw = wid - 4, phi = (im + in) & 3;
    const int koff = ID == 1 ? xcd * 2 : ((xcd >> (ID == 2 ? 1 : 2)) & 1) * (nk >> 1);
    const unsigned ksw = (unsigned)(((lane & 7) ^ (lane >> 3)) << 4);
    unsigned voffA[2], voffB;
    if constexpr (C::FA == ROW) voffA[0] = (unsigned)((tm * 128 + lw * 32 + (lane >> 3)) * DM * 2) + ksw;
    if constexpr (C::FA == CHK) voffA[0] = (unsigned)((lw * 2 * DM + tm * 128 + lane) * 16);
    if constexpr (C::FA == TR) {
#pragma unroll
        for (int i = 0; i < 2; ++i) voffA[i] = (unsigned)((lw * 16 + q) * DM * 2 + tm * 256 + ((l15 ^ ((q | (i << 2)) << 1)) << 4));
    }
    if constexpr (C::FB == ROW) voffB = (unsigned)((((ID == 1 ? tn & 7 : tn)) * 128 + lw * 32 + (lane >> 3)) * C::LDB * 2) + ksw;
    else voffB = (unsigned)((lw * 2 * SEQ + tn * 128 + lane) * 16);
    const auto ra = ID == 1 ? RSRC(p.x, SEQ * DM * 2) : ID == 2 ? RSRC(p.abT, 2 * SEQ * DM * 2) : RSRC(p.wd, FF * DM * 2);
    auto kpos = [&](int t) {
        const int u1 = t - 1, ul = ((nk - 1 - phi) & ~3) + phi;
        int k = ID == 1 ? ((t & 3) != phi ? t : t + 4 < nk ? t + 4 : phi) : t < phi || u1 >= ul ? t : t == phi ? phi + 4 : (u1 & 3) == phi && u1 > phi ? u1 + 4 : u1;
        k += koff; k = k >= nk ? k - nk : k;
        if constexpr (ID == 4) {
            k = ks * 14 + (k < (nk >> 1) ? k : k + 28 - (nk >> 1));
            int u = t - nk + phi; u = u >= NH ? u - NH : u; const int h = u >= RE4 + RO4, v = u - h * (RE4 + RO4);
            return t < nk ? k : h * 28 + (v < RE4 ? 14 - RE4 + v : 28 - RO4 - RE4 + v);
        } else return k + k0;
    };
    const int ktab = kpos(lane);
    struct Pos { int sa, sb, lb; const void* pb; };
    auto prep = [&](int t) {
        const int k = ID == 1 || PRE ? kpos(t) : __builtin_amdgcn_readlane(ktab, t);
        Pos x{C::FA == ROW ? k * 128 : C::FA == CHK ? k * 8 * DM * 16 : k * 64 * DM * 2, C::FB == ROW ? (k & 31) * 128 : k * 8 * SEQ * 16, (t & 3) * STAGE + lw * 4096,
              ID == 1 ? (const void*)(tn < 8 ? p.wre : p.wim) : ID == 2 ? (const void*)(k < 32 ? p.cre : p.cim) : (const void*)p.act};
        asm volatile("" : "+s"(x.sa), "+s"(x.sb), "+s"(x.lb)); if constexpr (ID == 2) asm volatile("" : "+s"(x.pb));
        return x;
    };
    auto issue = [&](const Pos& x, bool doA, bool doB, bool half) {
        const auto rb = RSRC(x.pb, ID == 1 ? DM * DM * 2 : ID == 2 ? SEQ * SEQ * 2 : SEQ * FF * 2);
        LAS unsigned char* base = lds + x.lb;
#pragma unroll
        for (int i = 0; i < 4; ++i) {
            if (doA) {
                if constexpr (C::FA == ROW) DMA16(ra, base + i * 1024, voffA[0], x.sa + i * 8 * DM * 2);
                if constexpr (C::FA == CHK) DMA16_DEV(ra, base + i * 1024, voffA[0], x.sa + (i >> 1) * DM * 16 + (i & 1) * 1024);
                if constexpr (C::FA == TR) DMA16(ra, base + i * 1024, voffA[i >> 1], x.sa + i * 4 * DM * 2);
            }
            if (doB) {
                if constexpr (C::FB == ROW) DMA16(rb, base + 16384 + i * 1024, voffB, x.sb + i * 8 * C::LDB * 2);
                else if (!(half && (i & 1))) DMA16_DEV(rb, base + 16384 + i * 1024, voffB, x.sb + (i >> 1) * SEQ * 16 + ((i & 1) ^ ks) * 1024);
            }
        }
    };
    auto stage = [&](int t, bool doA, bool doB, bool half = false) { issue(prep(t), doA, doB, half); };
    if constexpr (PRE) { stage(0, ID == 4, ID == 2); return; }
    if constexpr (ID != 1) {
        u64* words = p.flags + (ID == 2 ? F_AB + tm * 32 : F_ACT + (tn >> 1) * 32) * FLAG_STRIDE;
        if (wid != 4) __builtin_amdgcn_s_sleep(2);
        stage(1, ID == 4, ID == 2); stage(2, ID == 4, ID == 2);
        if (wid == 4) { const int j = lane & 31; wait_group(words + j * FLAG_STRIDE, ID == 2 ? 2 * j + 2 > k0 && 2 * j < k0 + nk : ((j >> 3) & 1) == ks, ep); }
        bar();
        stage(0, ID == 2, ID == 4); stage(1, ID == 2, ID == 4); stage(2, ID == 2, ID == 4);
        wait_vm<8>(); bar();
        wait_vm<4>();
    } else {
        stage(0, true, true); stage(1, true, true);
        wait_vm<8>(); bar();
        stage(2, true, true);
        const unsigned a = (unsigned)(size_t)lds + XLDS;
        const float e0 = lds_read32<3072>(a), e1 = lds_read32<3076>(a);
        wait_lgkm0(); sched0();
        ep = uniform64(((u64)__float_as_uint(e1) << 32) | __float_as_uint(e0));
    }
    u64 pseen = 0; u64* const pwords = p.flags + (F_ACT + (tn >> 1) * 32 + (lane & 31)) * FLAG_STRIDE;
#pragma nounroll
    for (int t = 0; t < nk - 3; ++t) {
        const Pos x = prep(t + 3);
        wait_vm<8>();
        if constexpr (ID == 4) { if (t == nk - 4 && wid == 4 && __any(pseen != ep)) wait_group(pwords, true, ep); }
        bar();
        issue(x, true, true, false);
        if constexpr (ID == 4) { if (t == nk - 6 && wid == 4) flag_peek(pseen, pwords); }
        if constexpr (ID == 1) { if (t == LNT1 && wid == 4 && lane == 0) flag_store(p.flags + (F_LN + tm * 16 + tn) * FLAG_STRIDE, ep); }
    }
    if constexpr (NH > 0) {
        wait_vm<8>(); bar(); stage(nk, true, true, true);
#pragma nounroll
        for (int t = nk - 2; t < nk + NH - 3; ++t) { const Pos x = prep(t + 3); wait_vm<6>(); bar(); issue(x, true, true, true); }
    }
    bool got = !recv;
    auto fetch = [&](u64 seen) {
        if (uniform64(seen) != ep) return false;
        asm volatile("" ::: "memory");
#pragma unroll
        for (int i = 0; i < (ID == 4 ? 4 : 8); ++i) DMA16_DEV(RSRC(p.xch, 256 * 32768), lds + XLDS + lw * 8192 + i * 1024, (unsigned)(lane * 16), xoff + i * 1024);
        return true;
    };
    wait_vm<NH ? 6 : 8>(); bar();
    u64* const lnw = p.flags + (F_LN + (lane & 16 ? l15 * 16 + tn : tm * 16 + l15)) * FLAG_STRIDE;
    u64 seen0 = flag_load(ID == 1 ? lnw : flag);
    wait_vm<0>();
    if (!got) got = fetch(seen0);
    if constexpr (ID == 1) { if (wid == 4) {
        if (__any(seen0 != ep)) wait_group(lnw, true, ep);
        asm volatile("" ::: "memory");
#pragma unroll
        for (int i = 0; i < 3; ++i) DMA16_DEV(RSRC(p.ln1, 3 * SEQ * 4), lds + XLDS + i * 1024, lane < 32 ? (unsigned)(lane * 16) : 0x7ffffff0u, (i * SEQ + (i < 2 ? tm : tn) * 128) * 4);
    } }
    if constexpr (ID == 1) g128_load<2, true>(p, lds, wid, ep);
    bar();
    if constexpr (ID != 1) seen0 = flag_load(flag);
    if constexpr (ID == 2) { if (!got) got = fetch(seen0); }
    bar();
    if (!got) got = fetch(seen0);
    if (recv) {
#pragma nounroll
        for (int spin = 0; !got && spin < (1 << 20); ++spin) { got = fetch(flag_load(flag)); __builtin_amdgcn_s_sleep(1); }
        wait_vm<0>(); bar();
    }
    if constexpr (ID == 2) { if (!recv) colsum_partial(p, lds, lw); }
    if constexpr (ID == 1) { wait_vm<4>(); bar(); }
    if constexpr (ID != 4) bar();
}

template <int ID>
__device__ __forceinline__ void g128_compute(const Params& p, LAS unsigned char* lds, const int wid, u64& ep) {
    using C = Cfg<ID>;
    constexpr int STAGE = 32768, XLDS = 4 * STAGE;
    const int lane = threadIdx.x & 63, l15 = lane & 15, q = lane >> 4;
    const int xcd = blockIdx.x & 7, idx = blockIdx.x >> 3, im = idx & 3, in = idx >> 2;
    constexpr int NH = ID == 4 ? 2 * (RE4 + RO4) : 0;
    const int ks = (xcd & 1) ^ (ID == 2); const bool recv = ID == 4 || (ID == 2 && ks);
    const int tm = (ID == 1 ? (xcd & 3) : ((xcd >> 1) & 1)) * 4 + im, tn = (xcd >> 2) * 8 + in;
    const int k0 = ID != 1 && ks ? C::NKS : 0, nk = ID == 4 ? 28 - 2 * (ks ? RO4 : RE4) : ID == 2 && ks ? C::NKT - C::NKS : C::NKS;
    const int xb = ID == 4 ? 128 + (blockIdx.x ^ (wid >> 2)) : blockIdx.x >> 1;
    u64* const flag = p.flags + (F_PAIR + xb * 4 + (wid & 3)) * FLAG_STRIDE;
    const unsigned xoff = (unsigned)(ID == 4 ? (128 + xb) * 16384 + (wid & 3) * 4096 : xb * 32768 + (wid & 3) * 8192);
    const int wm = wid & 1, wn = wid >> 1;
    const int eo = (q & 1) * 16 + (q >> 1) * 8;
    f32x4 acc[4][4];
#pragma unroll
    for (int m = 0; m < 4; ++m)
#pragma unroll
        for (int n = 0; n < 4; ++n) acc[m][n] = (f32x4){0.f, 0.f, 0.f, 0.f};
    const int row0 = tn * 128 + (ID == 4 ? ks * 64 + wn * 32 : wn * 64) + l15, col0 = tm * 128 + wm * 64 + eo;
    if constexpr (ID != 1) {
        constexpr int NN = ID == 4 ? 2 : 4;
        if (ID == 4 || !recv) {
            if constexpr (ID == 2) { wait_vm<0>(); bar(); __builtin_amdgcn_s_sleep(4); }
            u32x4 r[NN][2];
#pragma unroll
            for (int n = 0; n < NN; ++n)
#pragma unroll
                for (int mp = 0; mp < 2; ++mp)
                    r[n][mp] = __builtin_amdgcn_raw_buffer_load_b128(RSRC(ID == 2 ? p.x : p.h, SEQ * DM * 2), (unsigned)(ID == 2 ? ((row0 + n * 16) * DM + col0 + mp * 32) * 2 : ((col0 / 8 + mp * 4) * SEQ + row0 + n * 16) * 16), 0, ID == 4 ? SC1 : 0);
            if constexpr (ID == 4) { wait_vm<0>(); bar(); }
#pragma unroll
            for (int n = 0; n < NN; ++n)
#pragma unroll
                for (int mp = 0; mp < 2; ++mp) {
                    unsigned r0 = r[n][mp][0], r1 = r[n][mp][1], r2 = r[n][mp][2], r3 = r[n][mp][3];
                    swap16u(r0, r2); swap16u(r1, r3);
                    acc[2 * mp][n] = (f32x4){bf_lo(r0), bf_hi(r0), bf_lo(r1), bf_hi(r1)}; acc[2 * mp + 1][n] = (f32x4){bf_lo(r2), bf_hi(r2), bf_lo(r3), bf_hi(r3)};
                }
        } else { wait_vm<0>(); bar(); }
    }
    unsigned pfc = 0;
    if constexpr (ID == 1) {
        ep = uniform64(flag_load(p.flags)) + 1;
        if (threadIdx.x == 0) *(LAS u64*)(lds + XLDS + 3072) = ep;
        wait_lgkm0();
        if (idx == 0) {
            unsigned long long pc; int lo, hi;
            asm volatile("s_getpc_b64 %0\n s_mov_b32 %1, _Z7fused_k6Params@rel32@lo+4\n s_mov_b32 %2, _Z6tail_kv@rel32@lo+12" : "=s"(pc), "=s"(lo), "=s"(hi));
            touch(pfc, threadIdx.x * 128, (const void*)(pc + (long long)lo), (unsigned)max(hi - lo, 0)); touch(pfc, threadIdx.x * 128 + 32768, (const void*)(pc + (long long)lo), (unsigned)max(hi - lo, 0));
        }
    }
    constexpr int NA = C::FA == CHK ? 1 : C::FA == ROW ? 2 : 4, NB = C::FB == CHK ? 1 : 2;
    unsigned adA[NA], adB[NB];
    const unsigned lds0 = (unsigned)(size_t)lds, row_sw = lds0 + (unsigned)(l15 * 128 + ((q ^ (lane & 7)) << 4)), chk = lds0 + (unsigned)(q * 2048 + l15 * 16);
#pragma unroll
    for (int i = 0; i < NA; ++i) {
        if constexpr (C::FA == ROW) adA[i] = (row_sw ^ (unsigned)(i * 64)) + (unsigned)(wm * 8192);
        if constexpr (C::FA == CHK) adA[i] = chk + (unsigned)(wm * 1024);
        if constexpr (C::FA == TR) { const int q4 = l15 >> 2; adA[i] = lds0 + (unsigned)((8 * q + q4) * 256 + (((wm * 4 + i) ^ (q4 | ((q & 1) << 2))) << 5) + 8 * (lane & 3)); }
    }
#pragma unroll
    for (int i = 0; i < NB; ++i) adB[i] = C::FB == CHK ? 16384u + chk + (unsigned)(wn * 512) : 16384u + (row_sw ^ (unsigned)(i * 64)) + (unsigned)(wn * 8192);
    constexpr int W = C::FA == TR ? 2 : 1, NR = 4 + 4 * W;
    auto rd = [&](auto ic, Frag (&ra)[4], bf16x8 (&rb)[4], auto kkc) {
        constexpr int kk = decltype(kkc)::value, i = decltype(ic)::value;
        if constexpr (i == 0 || (i > W && i <= W + 3)) { constexpr int n = i == 0 ? 0 : i - W;
            if constexpr (C::FB == CHK) rb[n] = lds_read128<kk * 8192 + (n >> 1) * 1024 + (n & 1) * 256>(adB[0]);
            else rb[n] = lds_read128<n * 2048>(adB[kk]); }
        else { constexpr int j = i <= W ? i - 1 : i - 4, m = j / W;
            if constexpr (C::FA != TR) { bf16x8 v;
                if constexpr (C::FA == CHK) v = lds_read128<kk * 8192 + m * 256>(adA[0]); else v = lds_read128<m * 2048>(adA[kk]);
                const u32x4 w = __builtin_bit_cast(u32x4, v); ra[m].lo = (u32x2){w[0], w[1]}; ra[m].hi = (u32x2){w[2], w[3]}; }
            else if constexpr (j % 2 == 0) ra[m].lo = tr_read<kk * 8192>(adA[m]);
            else ra[m].hi = tr_read<kk * 8192 + 1024>(adA[m]); }
    };
    auto phase = [&](const Frag (&ra)[4], const bf16x8 (&rb)[4], Frag (&na)[4], bf16x8 (&nb)[4], int dstep, auto kkc) {
        static_for<0, 16>([&](auto sc) {
            constexpr int s = decltype(sc)::value, m = s >> 2, n = s & 3;
            if constexpr (n == 0) { wait_lgkm<(3 - m) * W + (s < NR ? s : NR)>(); sched0(); }
            acc[m][n] = MFMA(ra[m].v(), rb[n], acc[m][n]);
            if constexpr (s < NR) rd(sc, na, nb, kkc);
            if constexpr (s >= 12 && decltype(kkc)::value == 1) {
                if constexpr (s - 12 < NA) bump(adA[s - 12], dstep);
                if constexpr (s - 12 < NB) bump(adB[s - 12], dstep);
            }
            sched0();
        });
    };
    Frag a0[4], a1[4]; bf16x8 b0[4], b1[4];
    constexpr std::integral_constant<int, 0> K0{}; constexpr std::integral_constant<int, 1> K1{};
    if constexpr (ID != 1) bar();
    bar();
    static_for<0, NR>([&](auto ic) { rd(ic, a0, b0, K0); });
    sched0();
    const int rx = tm * 128 + tn * 8 + wid * 2 + (lane >> 5), rw = tn * 128 + tm * 8 + wid * 2 + (lane >> 5);
    u32x4 xv[4], wv[4];
    if constexpr (ID == 1) {
#pragma unroll
        for (int i = 0; i < 4; ++i) { xv[i] = __builtin_amdgcn_raw_buffer_load_b128(RSRC(p.x, SEQ * DM * 2), (unsigned)(rx * 2048 + (lane & 31) * 16 + i * 512), 0, 0);
                                      wv[i] = __builtin_amdgcn_raw_buffer_load_b128(RSRC(tn < 8 ? p.wre : p.wim, DM * DM * 2), (unsigned)((rw & 1023) * 2048 + (lane & 31) * 16 + i * 512), 0, 0); }
        sched0();
    }
    auto steps = [&](int t0, int t1) {
#pragma nounroll
        for (int t = t0; t < t1; ++t) {
            phase(a0, b0, a1, b1, (t & 3) == 3 ? -3 * STAGE : STAGE, K1);
            if constexpr (ID == 1) { if (t == LNT1) wait_vm<0>(); }
            bar();
            phase(a1, b1, a0, b0, 0, K0);
        }
    };
    if constexpr (ID == 1) {
        steps(0, LNT0);
        wait_lgkm0(); sched0();
        f32x2 sx = {0.f, 0.f}, sw = sx, sq = sx;
#pragma unroll
        for (int i = 0; i < 16; ++i) { sx += (f32x2){bf_lo(xv[i >> 2][i & 3]), bf_hi(xv[i >> 2][i & 3])}; sw += (f32x2){bf_lo(wv[i >> 2][i & 3]), bf_hi(wv[i >> 2][i & 3])}; }
        const float mean = hsum(sx[0] + sx[1]) * (1.f / DM);
#pragma unroll
        for (int i = 0; i < 16; ++i) { const f32x2 d = (f32x2){bf_lo(xv[i >> 2][i & 3]), bf_hi(xv[i >> 2][i & 3])} - mean; sq += d * d; }
        const float rstd = __builtin_amdgcn_rsqf(hsum(sq[0] + sq[1]) * (1.f / DM) + LN_EPS), wsum = hsum(sw[0] + sw[1]);
        const int j = lane & 31;
        __builtin_amdgcn_raw_buffer_store_b32(__float_as_uint(j == 0 ? mean : j == 1 ? rstd : wsum), RSRC(p.ln1, 3 * SEQ * 4), j < 3 ? (unsigned)((j * SEQ + (j < 2 ? rx : rw)) * 4) : 0x7ffffff0u, 0, SC1);
        sched0();
        steps(LNT0, nk);
    } else steps(0, nk);
    wait_lgkm0(); keep(a0, b0); sched0();
    auto send = [&](int h0) {
        const auto rx = RSRC(p.xch, 256 * 32768);
#pragma unroll
        for (int m = 0; m < 4; ++m)
#pragma unroll
            for (int h = h0; h < 2; ++h) {
                const f32x4 u = acc[m][2 * h], v = acc[m][2 * h + 1];
                __builtin_amdgcn_raw_buffer_store_b128((u32x4){pack_bf16(u[0], u[1]), pack_bf16(u[2], u[3]), pack_bf16(v[0], v[1]), pack_bf16(v[2], v[3])}, rx, xoff + (unsigned)((h0 ? m : m * 2 + h) * 1024 + lane * 16), 0, SC1);
            }
    };
    if constexpr (NH > 0) {
        send(1); sched0();
        auto phaseH = [&](const Frag (&ra)[4], const bf16x8 (&rb)[4], Frag (&na)[4], bf16x8 (&nb)[4], int dstep, auto kkc) {
            static_for<0, 8>([&](auto sc) {
                constexpr int s = decltype(sc)::value;
                if constexpr ((s & 1) == 0) { wait_lgkm<6 - s + (s < 5 ? 2 * s : 10)>(); sched0(); }
                acc[s >> 1][s & 1] = MFMA(ra[s >> 1].v(), rb[s & 1], acc[s >> 1][s & 1]);
                if constexpr (s < 5) { rd(std::integral_constant<int, s < 2 ? 2 * s : 2 * s + 2>{}, na, nb, kkc); rd(std::integral_constant<int, s < 2 ? 2 * s + 1 : 2 * s + 3>{}, na, nb, kkc); }
                if constexpr (s >= 5 && decltype(kkc)::value == 1) { if constexpr (s < 7) { bump(adA[2 * (s - 5)], dstep); bump(adA[2 * (s - 5) + 1], dstep); } else bump(adB[0], dstep); }
                sched0();
            });
        };
#pragma nounroll
        for (int t = nk; t < nk + NH; ++t) {
            phaseH(a0, b0, a1, b1, (t & 3) == 3 ? -3 * STAGE : STAGE, K1);
            bar();
            phaseH(a1, b1, a0, b0, 0, K0);
            if (t == nk + FLAGD) { wait_vm<0>(); if (lane == 0) flag_store(flag, ep); }
        }
        wait_lgkm0();
        asm volatile("" ::"v"(b0[0]), "v"(b0[1]));
#pragma unroll
        for (int i = 0; i < 4; ++i) asm volatile("" ::"v"(a0[i].lo), "v"(a0[i].hi));
        sched0();
    }
    if constexpr (ID == 1) {
        wait_vm<0>(); asm volatile("; pfc %0" ::"v"(pfc));
        bar();
        const auto ro = RSRC(p.abT, 2 * SEQ * DM * 2);
        const float sgn = tn < 8 ? 1.f : -1.f;
        f32x4 mean[4], rs[4]; float ws[4];
        static_for<0, 4>([&](auto ic) { constexpr int i = decltype(ic)::value; const unsigned a = lds0 + (unsigned)(XLDS + wm * 256 + q * 16);
            mean[i] = __builtin_bit_cast(f32x4, lds_read128<i * 64>(a)); rs[i] = __builtin_bit_cast(f32x4, lds_read128<1024 + i * 64>(a));
            ws[i] = lds_read32<2048 + i * 64>(lds0 + (unsigned)(XLDS + wn * 256 + l15 * 4)); });
        wait_lgkm0(); sched0();
#pragma unroll
        for (int m = 0; m < 4; ++m) {
            const f32x4 r = rs[m] * sgn;
#pragma unroll
            for (int n = 0; n < 4; ++n) acc[m][n] = (acc[m][n] - mean[m] * ws[n]) * r;
        }
#pragma unroll
        for (int n = 0; n < 4; ++n) { const int e = (tn & 7) * 128 + wn * 64 + n * 16 + l15;
#pragma unroll
            for (int mp = 0; mp < 2; ++mp) {
                unsigned x0 = pack_bf16(acc[2 * mp][n][0], acc[2 * mp][n][1]), x1 = pack_bf16(acc[2 * mp][n][2], acc[2 * mp][n][3]);
                unsigned y0 = pack_bf16(acc[2 * mp + 1][n][0], acc[2 * mp + 1][n][1]), y1 = pack_bf16(acc[2 * mp + 1][n][2], acc[2 * mp + 1][n][3]);
                swap16u(x0, y0); swap16u(x1, y1);
                const int s = (tn >> 3) * SEQ + col0 + mp * 32;
                __builtin_amdgcn_raw_buffer_store_b128((u32x4){x0, x1, y0, y1}, ro, (unsigned)(((s >> 3) * DM + e) * 16), 0, SC1);
            } }
    } else if (!recv) {
        send(0);
        wait_vm<0>();
        if (lane == 0) flag_store(flag, ep);
        if constexpr (ID == 2) bar();
    } else {
        bar();
        const unsigned px = (unsigned)(size_t)lds + (unsigned)(XLDS + wid * 8192 + lane * 16);
        constexpr int HH = ID == 4 ? 1 : 2;
        u32x4 pw[4 * HH];
        static_for<0, 4 * HH>([&](auto ic) { pw[decltype(ic)::value] = __builtin_bit_cast(u32x4, lds_read128<decltype(ic)::value * 1024>(px)); });
        wait_lgkm0(); sched0();
#pragma unroll
        for (int m = 0; m < 4; ++m)
#pragma unroll
            for (int h = 0; h < HH; ++h) { const u32x4 w = pw[m * HH + h];
                acc[m][2 * h] += (f32x4){bf_lo(w[0]), bf_hi(w[0]), bf_lo(w[1]), bf_hi(w[1])}; acc[m][2 * h + 1] += (f32x4){bf_lo(w[2]), bf_hi(w[2]), bf_lo(w[3]), bf_hi(w[3])}; }
        const auto ro = RSRC(ID == 2 ? p.h : p.out, ID == 2 ? 136 * SEQ * 16 : SEQ * DM * 2);
#pragma unroll
        for (int n = 0; n < 2 * HH; ++n) {
            unsigned o[4][2];
#pragma unroll
            for (int m = 0; m < 4; ++m) { o[m][0] = pack_bf16(acc[m][n][0], acc[m][n][1]); o[m][1] = pack_bf16(acc[m][n][2], acc[m][n][3]); }
            if constexpr (ID == 2) {
                const f32x4 s4 = (acc[0][n] + acc[1][n]) + (acc[2][n] + acc[3][n]);
                const float mw = qsum((s4[0] + s4[1]) + (s4[2] + s4[3])) * (1.f / 64.f);
                const f32x4 d0 = acc[0][n] - mw, d1 = acc[1][n] - mw, d2 = acc[2][n] - mw, d3 = acc[3][n] - mw, q4 = (d0 * d0 + d1 * d1) + (d2 * d2 + d3 * d3);
                float m2 = (q4[0] + q4[1]) + (q4[2] + q4[3]);
                m2 = qsum(m2);
                if (q == 0) __builtin_amdgcn_raw_buffer_store_b64((u32x2){__float_as_uint(mw), __float_as_uint(m2)}, ro, (unsigned)(((128 + tm) * SEQ + row0 + n * 16) * 16 + wm * 8), 0, SC1);
            }
#pragma unroll
            for (int mp = 0; mp < 2; ++mp) {
                swap16u(o[2 * mp][0], o[2 * mp + 1][0]); swap16u(o[2 * mp][1], o[2 * mp + 1][1]);
                __builtin_amdgcn_raw_buffer_store_b128((u32x4){o[2 * mp][0], o[2 * mp][1], o[2 * mp + 1][0], o[2 * mp + 1][1]}, ro,
                    (unsigned)(ID == 4 ? ((row0 + n * 16) * DM + col0 + mp * 32) * 2 : ((col0 / 8 + mp * 4) * SEQ + row0 + n * 16) * 16), 0, SC1);
            }
        }
        if constexpr (ID == 2) { wait_vm<0>(); bar(); }
    }
}
#endif

#if defined(__HIP_DEVICE_COMPILE__)
template <int GRP>
__device__ __forceinline__ void gemm3_body(const Params& p, LAS unsigned char* lds, const int wid, u64& ep) {
    constexpr int NK = 16, FA = 7, HB0 = 3 * 32768;
    const int lane = threadIdx.x & 63;
    const int wm = wid & 1, wn = wid >> 1, l15 = lane & 15, q = lane >> 4;
    const int xcd = blockIdx.x & 7, idx = blockIdx.x >> 3, im = idx & 7, in = idx >> 3;
    const int tm = (xcd & 3) * 8 + im, tn = (xcd >> 2) * 4 + in;
    const G3W gw(p, wid, lane);
    unsigned pf = 0;
    u64* words = p.flags + (F_H + tn * 32) * FLAG_STRIDE;
    if (threadIdx.x == 0 && !(xcd & 1)) flag_store(words + tm * FLAG_STRIDE, ep);
    if (wid) __builtin_amdgcn_s_sleep(2);
    gw.early(lds, wid, 0); gw.early(lds, wid, 1);
    if ((xcd & 1) && wid == 1) colsum_final(p, lds, ep);
    if (wid == 0) {
        wait_group(words + (lane & 31) * FLAG_STRIDE, (lane & 8) == 0, ep);
        if (blockIdx.x == 0 && lane == 0) flag_store(p.flags, ep);
    }
    bar();
    const unsigned voffB = (unsigned)((wid * SEQ + tn * 256 + lane) * 16);
    struct Src { const void* base; int so; unsigned nrec; };
    auto pin = [](Src x) { asm volatile("" : "+s"(x.base), "+s"(x.so), "+s"(x.nrec)); return x; };
    auto srcW = [&](int t) { return pin(Src{t < NK ? (const void*)gw.ptr : (const void*)(p.csum + GRP * 3584), t < NK ? gw.kstep(t) * 64 * FF * 2 : 0, t < NK ? (unsigned)(DM * FF * 2) : t == NK ? (unsigned)(2 * FF * 2) : 0u}); };
    auto srcH = [&](int t) { Src x{nullptr, (t < NK ? gw.kstep(t) : NK) * 8 * SEQ * 16, 136 * SEQ * 16}; asm volatile("" : "+s"(x.so)); return x; };
    auto stageW = [&](Src x, int slot, int i) { gw.piece(lds, wid, x.base, x.so, x.nrec, slot, i); };
    auto stageH = [&](Src x, int slot, int i) { DMA16_DEV(RSRC(p.h, x.nrec), lds + HB0 + slot * 32768 + wid * 4096 + i * 1024, voffB, x.so + i * 1024); };
#pragma unroll
    for (int t = 0; t < 2; ++t) {
        const Src xh = srcH(t);
#pragma unroll
        for (int i = 0; i < 4; ++i) stageH(xh, t, i);
    }

    unsigned adA[FA];
    const unsigned lds0 = (unsigned)(size_t)lds;
#pragma unroll
    for (int m = 0; m < FA; ++m) { const int q4 = l15 >> 2, pp = lane & 3, reg = m < 6 ? m & 1 : pp >> 1, ch = m < 6 ? wm * 8 + (m & 6) + (pp >> 1) : 6 + wm;
        adA[m] = lds0 + (unsigned)(reg * 16384 + (8 * q + q4) * 256 + ((ch ^ (((q4 | ((q & 1) << 2)) << 1) | reg)) << 4) + 8 * (pp & 1)); }
    unsigned adB = lds0 + (unsigned)(HB0 + q * 4096 + (wn * 64 + l15) * 16);
    auto rd = [&](auto ic, Frag (&ra)[FA], bf16x8 (&rb)[4], auto kkc) {
        constexpr int kk = decltype(kkc)::value, i = decltype(ic)::value;
        if constexpr (i < 4) rb[i] = lds_read128<kk * 16384 + i * 256>(adB);
        else if constexpr (i % 2 == 0) ra[(i - 4) / 2].lo = tr_read<kk * 8192>(adA[(i - 4) / 2]);
        else ra[(i - 4) / 2].hi = tr_read<kk * 8192 + 1024>(adA[(i - 4) / 2]);
    };
    f32x4 acc[FA][4];
#pragma unroll
    for (int m = 0; m < FA; ++m)
#pragma unroll
        for (int n = 0; n < 4; ++n) acc[m][n] = (f32x4){0.f, 0.f, 0.f, 0.f};
    auto phase = [&](const Frag (&ra)[FA], const bf16x8 (&rb)[4], Frag (&na)[FA], bf16x8 (&nb)[4], int dw, int dh, auto kkc, auto&& dma, auto&& prep) {
        static_for<0, 28>([&](auto sc) {
            constexpr int s = decltype(sc)::value, m = s >> 2, n = s & 3;
            if constexpr (decltype(kkc)::value == 0) { if constexpr (s == 0) __builtin_amdgcn_s_setprio(3); if constexpr (s == 18) __builtin_amdgcn_s_setprio(2); }
            else { if constexpr (s == 6) __builtin_amdgcn_s_setprio(1); if constexpr (s == 22) __builtin_amdgcn_s_setprio(0); }
            acc[m][n] = MFMA(ra[m].v(), rb[n], acc[m][n]);
            sched0();
            if constexpr (s < 18) rd(sc, na, nb, kkc);
            if constexpr (s == 20) prep();
            if constexpr (s % 4 == 2 * GRP && s < 16) dma(s / 4);
            if constexpr (decltype(kkc)::value == 1 && s >= 18 && s < 26) { if constexpr (s < 25) bump(adA[s - 18], dw); else bump(adB, dh); }
            sched0();
        });
        wait_lgkm0(); sched0();
    };
    Frag a0[FA], a1[FA]; bf16x8 b0[4], b1[4];
    constexpr std::integral_constant<int, 0> K0{}; constexpr std::integral_constant<int, 1> K1{};
    wait_vm<4>(); bar();
    static_for<0, 18>([&](auto ic) { rd(ic, a0, b0, K0); });
    wait_lgkm0(); sched0();
    const unsigned pfo = (unsigned)(((blockIdx.x * 8 + wid) * 28 + (lane < 28 ? lane : 0)) * 128); const int pft = 1 + idx % 12;
    Src xw = srcW(2), xh;
    auto phaseA = [&](int t, int sw2) {
        phase(a0, b0, a1, b1, sw2 == 1 ? -65536 : 32768, (t & 1) ? -32768 : 32768, K1, [&](int i) { stageW(xw, sw2, i);
            if (i == 3) touch(pf, pfo, p.wd, t == pft ? FF * DM * 2 : 0);
        }, [&]() { xh = srcH(t + 2); });
        wait_vm<5>();
    };
    u64 cseen = 0;
    phaseA(0, 2);
    int sw2 = 0;
#pragma nounroll
    for (int t = 0; t < NK - 1; ++t) {
        if (__builtin_expect(t >= 11 && t <= 12 && (wid & 3) == 0, 0)) {
            const int sl = min((tm * 112 >> 6) + (lane & 3), (tm * 112 + 111) >> 6);
            u64* w = p.flags + (F_CS + GRP * 56 + sl) * FLAG_STRIDE;
            if (t == 11) flag_peek(cseen, w); else if (__any(cseen != ep)) wait_group(w, true, ep);
        }
        bar();
        phase(a1, b1, a0, b0, 0, 0, K0, [&](int i) { stageH(xh, t & 1, i); }, [&]() { xw = srcW(t + 3); });
        phaseA(t + 1, sw2);
        sw2 = sw2 == 2 ? 0 : sw2 + 1;
    }
    bar();
    __builtin_amdgcn_s_setprio(GRP ? 0 : 3);
    f32x4 csv[FA], sp[4][2];
    static_for<0, 16>([&](auto sc) {
        constexpr int s = decltype(sc)::value;
        acc[s >> 2][s & 3] = MFMA(a1[s >> 2].v(), b1[s & 3], acc[s >> 2][s & 3]);
        if constexpr (s < 8) sp[s >> 1][s & 1] = __builtin_bit_cast(f32x4, lds_read128<(s & 1) * 16384 + (s >> 1) * 256>(adB));
        else if constexpr (s < 15) { constexpr int m = s - 8; const int reg = m < 6 ? m & 1 : q >> 1, c4 = m < 6 ? wm * 2 + (m & 6) * 2 + q : (wm ? 0 : 12) + (q & 1);
            csv[m] = __builtin_bit_cast(f32x4, lds_read128<32768>(lds0 + (unsigned)(reg * 16384 + wm * 256 + ((c4 ^ ((wm << 1) | reg)) << 4)))); }
        if constexpr (GRP == 1 && s == 2) g128_load<4, true>(p, lds, wid, ep);
        sched0();
    });
    wait_lgkm0(); wait_vm<GRP ? 4 : 0>(); asm volatile("; pf %0" ::"v"(pf)); sched0();
    float mean[4], c1[4], ve[4];
#pragma unroll
    for (int n = 0; n < 4; ++n) {
        const f32x4 s0 = sp[n][0], s1 = sp[n][1];
        mean[n] = qsum((s0[0] + s0[2]) + (s1[0] + s1[2])) * (1.f / 16.f);
        const float d0 = s0[0] - mean[n], d1 = s0[2] - mean[n], d2 = s1[0] - mean[n], d3 = s1[2] - mean[n];
        const float m2 = qsum((s0[1] + s0[3]) + (s1[1] + s1[3]) + 64.f * ((d0 * d0 + d1 * d1) + (d2 * d2 + d3 * d3)));
        ve[n] = m2 * (1.f / DM) + LN_EPS; c1[n] = -1.44269504f * __builtin_amdgcn_rsqf(ve[n]);
    }
#pragma unroll
    for (int s = 16; s < 28; ++s) acc[s >> 2][s & 3] = MFMA(a1[s >> 2].v(), b1[s & 3], acc[s >> 2][s & 3]);
#pragma unroll
    for (int s = 16; s < 28; ++s) { __builtin_amdgcn_sched_group_barrier(0x008, 1, 0); __builtin_amdgcn_sched_group_barrier(0x002, 10, 0); }
    sched0();
    const auto ro = RSRC(p.act, SEQ * FF * 2);
    auto silu2 = [&](f32x2 g, f32x2 u, int n) {
        const f32x2 x = g * c1[n], d = (f32x2){__builtin_amdgcn_exp2f(x[0]), __builtin_amdgcn_exp2f(x[1])} * ve[n] + ve[n];
        const f32x2 r = g * u * (f32x2){__builtin_amdgcn_rcpf(d[0]), __builtin_amdgcn_rcpf(d[1])};
        return pack_bf16(r[0], r[1]);
    };
#pragma unroll
    for (int j = 0; j < 3; ++j)
#pragma unroll
        for (int np = 0; np < 2; ++np) {
            unsigned o[2][2];
#pragma unroll
            for (int i = 0; i < 2; ++i) { const int n = 2 * np + i; const f32x4 g = acc[2 * j][n] - mean[n] * csv[2 * j], u = acc[2 * j + 1][n] - mean[n] * csv[2 * j + 1];
                o[i][0] = silu2((f32x2){g[0], g[1]}, (f32x2){u[0], u[1]}, n); o[i][1] = silu2((f32x2){g[2], g[3]}, (f32x2){u[2], u[3]}, n); }
            swap16u(o[0][0], o[1][0]); swap16u(o[0][1], o[1][1]);
            __builtin_amdgcn_raw_buffer_store_b128((u32x4){o[0][0], o[0][1], o[1][0], o[1][1]}, ro, (unsigned)(((tm * 14 + wm * 8 + 2 * j + (q >> 1)) * SEQ + tn * 256 + wn * 64 + np * 32 + (lane & 31)) * 16), 0, SC1);
        }
    {
        unsigned o[4];
#pragma unroll
        for (int n = 0; n < 4; ++n) {
            const f32x4 v = acc[6][n] - mean[n] * csv[6];
            float g0 = v[0], g1 = v[1], u0 = v[2], u1 = v[3];
            swap32(g0, u0); swap32(g1, u1);
            o[n] = silu2((f32x2){g0, g1}, (f32x2){u0, u1}, n);
        }
        swap16u(o[0], o[1]); swap16u(o[2], o[3]); swap32u(o[0], o[2]); swap32u(o[1], o[3]);
        __builtin_amdgcn_raw_buffer_store_b128((u32x4){o[0], o[2], o[1], o[3]}, ro, (unsigned)(((tm * 14 + 6 + wm) * SEQ + tn * 256 + wn * 64 + lane) * 16), 0, SC1);
    }
    __builtin_amdgcn_s_setprio(0);
    if constexpr (GRP == 1) { wait_vm<0>(); bar(); }
}
#endif

__global__ __launch_bounds__(512, 2) void fused_k(const Params p) {
#if defined(__HIP_DEVICE_COMPILE__)
    __shared__ __attribute__((aligned(16))) unsigned char lds_raw[5 * 32768];
    LAS unsigned char* lds = (LAS unsigned char*)lds_raw;
    const int wid = __builtin_amdgcn_readfirstlane(threadIdx.x >> 6);
    u64 ep = 0;
    if (wid < 4) { g128_compute<1>(p, lds, wid, ep); g128_compute<2>(p, lds, wid, ep); gemm3_body<0>(p, lds, wid, ep); g128_compute<4>(p, lds, wid, ep); }
    else { g128_load<1, false>(p, lds, wid, ep); g128_load<2, false>(p, lds, wid, ep); gemm3_body<1>(p, lds, wid, ep); g128_load<4, false>(p, lds, wid, ep); }
#endif
}
__global__ void tail_k() {}

template <int MODE> __global__ __launch_bounds__(512) void pre_k(const Params p) {
#if defined(__HIP_DEVICE_COMPILE__)
    const unsigned long long t0 = __builtin_amdgcn_s_memrealtime();
    unsigned a = 0;
    const u16* ptr[13] = {p.x, p.wre, p.wim, p.cre, p.cim, p.wg, p.wu, p.wd, p.abT, p.h, p.act, p.xch, p.out};
    const int nl[13] = {32768, 16384, 16384, 65536, 65536, 57344, 57344, 57344, 65536, 36864, 114688, 73728, 32768};
    const int grp[13] = {1, 1, 1, 2, 2, 4, 4, 4, 8, 8, 8, 8, 8};
    const int gg = blockIdx.x * 512 + threadIdx.x;
    unsigned v[13];
#pragma unroll
    for (int i = 0; i < 13; ++i) v[i] = (MODE & grp[i]) ? *(const unsigned*)(ptr[i] + (size_t)(gg < nl[i] ? gg : 0) * 64) : 0u;
#pragma unroll
    for (int i = 0; i < 13; ++i) a += v[i];
    asm volatile("s_waitcnt vmcnt(0)" : "+v"(a) :: "memory");
    while (__builtin_amdgcn_s_memrealtime() - t0 < 3000) __builtin_amdgcn_s_sleep(2);
    if (a == 0x12345678u && p.x == nullptr) p.out[0] = 0;
#endif
}
extern "C" void kernel_launch(void* const* d_in, const int* in_sizes, int n_in, void* d_out, int out_size, void* d_ws, size_t ws_size, hipStream_t stream) {
    Params p;
    p.x = (const u16*)d_in[0]; p.cre = (const u16*)d_in[1]; p.cim = (const u16*)d_in[2]; p.wre = (const u16*)d_in[3]; p.wim = (const u16*)d_in[4];
    p.wg = (const u16*)d_in[6]; p.wu = (const u16*)d_in[7]; p.wd = (const u16*)d_in[8];
    p.out = (u16*)d_out;
    char* ws = (char*)d_ws;
    p.abT = (u16*)ws; p.h = (u16*)(ws + (8u << 20)); p.act = (u16*)(ws + (16u << 20));
    p.xch = (u16*)(ws + (32u << 20)); p.flags = (unsigned long long*)(ws + (40u << 20)); p.csum = (float*)(ws + (40u << 20) + (512u << 10)); p.ln1 = (float*)(ws + (40u << 20) + (768u << 10));
    pre_k<15><<<256, 512, 0, stream>>>(p);
    fused_k<<<256, 512, 0, stream>>>(p);
}
```

```cpp
#include <hip/hip_runtime.h>
#include <hip/hip_bf16.h>
#include <type_traits>

#define LAS __attribute__((address_space(3)))
typedef unsigned short u16;
typedef __bf16 bf16x8 __attribute__((ext_vector_type(8)));
typedef __bf16 bf16x2 __attribute__((ext_vector_type(2)));
typedef float f32x4 __attribute__((ext_vector_type(4)));
typedef float f32x2 __attribute__((ext_vector_type(2)));
typedef unsigned u32x4 __attribute__((ext_vector_type(4)));
typedef unsigned u32x2 __attribute__((ext_vector_type(2)));

constexpr int SEQ = 2048, DM = 1024, FF = 3584;
constexpr float LN_EPS = 1e-5f;
constexpr int SC1 = 16;

struct Params {
    const u16* x; const u16* cre; const u16* cim; const u16* wre; const u16* wim;
    const u16* wg; const u16* wu; const u16* wd;
    u16* out; u16* abT; u16* h; u16* act; u16* xch; unsigned long long* flags; float* csum; float* ln1;
};

template <int N> __device__ __forceinline__ void wait_vm() { asm volatile("s_waitcnt vmcnt(%0)" ::"n"(N) : "memory"); }
__device__ __forceinline__ void wait_lgkm0() { __builtin_amdgcn_s_waitcnt(0xC07F); asm volatile("s_waitcnt lgkmcnt(0)" ::: "memory"); }
__device__ __forceinline__ void sched0() { __builtin_amdgcn_sched_barrier(0); }
__device__ __forceinline__ void bar() { sched0(); asm volatile("" ::: "memory"); __builtin_amdgcn_s_barrier(); asm volatile("" ::: "memory"); sched0(); }
__device__ __forceinline__ unsigned pack_bf16(float lo, float hi) { bf16x2 c; c[0] = (__bf16)lo; c[1] = (__bf16)hi; return __builtin_bit_cast(unsigned, c); }
__device__ __forceinline__ float bf_lo(unsigned v) { return __uint_as_float(v << 16); }
__device__ __forceinline__ float bf_hi(unsigned v) { return __uint_as_float(v & 0xffff0000u); }
__device__ __forceinline__ void swap16(float& a, float& b) { auto r = __builtin_amdgcn_permlane16_swap(__float_as_uint(a), __float_as_uint(b), false, false); a = __uint_as_float(r[0]); b = __uint_as_float(r[1]); }
__device__ __forceinline__ void swap32(float& a, float& b) { auto r = __builtin_amdgcn_permlane32_swap(__float_as_uint(a), __float_as_uint(b), false, false); a = __uint_as_float(r[0]); b = __uint_as_float(r[1]); }
__device__ __forceinline__ void swap16u(unsigned& a, unsigned& b) { auto r = __builtin_amdgcn_permlane16_swap(a, b, false, false); a = r[0]; b = r[1]; }
__device__ __forceinline__ void swap32u(unsigned& a, unsigned& b) { auto r = __builtin_amdgcn_permlane32_swap(a, b, false, false); a = r[0]; b = r[1]; }
__device__ __forceinline__ float qsum(float v) { float a = v, b = v; swap16(a, b); a += b; b = a; swap32(a, b); return a + b; }

template <int CTRL> __device__ __forceinline__ float dpp_add(float v) { return v + __int_as_float(__builtin_amdgcn_update_dpp(0, __float_as_int(v), CTRL, 0xf, 0xf, false)); }
__device__ __forceinline__ float hsum(float v) { v = dpp_add<0x128>(v); v = dpp_add<0x124>(v); v = dpp_add<0x122>(v); v = dpp_add<0x121>(v); float b = v; swap16(v, b); return v + b; }

struct Frag { u32x2 lo, hi; __device__ __forceinline__ bf16x8 v() const { return __builtin_bit_cast(bf16x8, __builtin_shufflevector(lo, hi, 0, 1, 2, 3)); } };
template <int OFF> __device__ __forceinline__ u32x2 tr_read(unsigned addr) { u32x2 r; asm volatile("ds_read_b64_tr_b16 %0, %1 offset:%2" : "=v"(r) : "v"(addr), "n"(OFF) : "memory"); return r; }
template <int OFF> __device__ __forceinline__ bf16x8 lds_read128(unsigned addr) { bf16x8 r; asm volatile("ds_read_b128 %0, %1 offset:%2" : "=v"(r) : "v"(addr), "n"(OFF) : "memory"); return r; }
template <int OFF> __device__ __forceinline__ float lds_read32(unsigned addr) { float r; asm volatile("ds_read_b32 %0, %1 offset:%2" : "=v"(r) : "v"(addr), "n"(OFF) : "memory"); return r; }
template <int N> __device__ __forceinline__ void wait_lgkm() { asm volatile("s_waitcnt lgkmcnt(%0)" ::"n"(N) : "memory"); }
template <int I, int N, class F> __device__ __forceinline__ void static_for(F&& f) { if constexpr (I < N) { f(std::integral_constant<int, I>{}); static_for<I + 1, N>(f); } }
template <int NA> __device__ __forceinline__ void keep(const Frag (&a)[NA], const bf16x8 (&b)[4]) {
#pragma unroll
    for (int i = 0; i < NA; ++i) asm volatile("" ::"v"(a[i].lo), "v"(a[i].hi));
#pragma unroll
    for (int i = 0; i < 4; ++i) asm volatile("" ::"v"(b[i]));
}
__device__ __forceinline__ void bump(unsigned& a, int d) { asm volatile("v_add_u32 %0, %1, %0" : "+v"(a) : "s"(d)); }
#define MFMA(a, b, c) __builtin_amdgcn_mfma_f32_16x16x32_bf16(a, b, c, 0, 0, 0)
#define RSRC(ptr, bytes) __builtin_amdgcn_make_buffer_rsrc((void*)(ptr), 0, (bytes), 0x00020000)
#define DMA16(rs, ldsp, voff, soff) __builtin_amdgcn_raw_ptr_buffer_load_lds(rs, (LAS void*)(ldsp), 16, voff, soff, 0, 0)
#define DMA16_DEV(rs, ldsp, voff, soff) __builtin_amdgcn_raw_ptr_buffer_load_lds(rs, (LAS void*)(ldsp), 16, voff, soff, 0, SC1)

typedef unsigned long long u64;
constexpr int FLAG_STRIDE = 8, F_PAIR = 1, F_AB = F_PAIR + 1536, F_H = F_AB + 256, F_ACT = F_H + 256, F_CS = F_ACT + 256, F_LN = F_CS + 128, F_ST = F_LN + 256;
__device__ __forceinline__ u64 flag_load(u64* w) { return __hip_atomic_load(w, __ATOMIC_RELAXED, __HIP_MEMORY_SCOPE_AGENT); }
__device__ __forceinline__ void flag_store(u64* w, u64 v) { __hip_atomic_store(w, v, __ATOMIC_RELAXED, __HIP_MEMORY_SCOPE_AGENT); }
__device__ __forceinline__ u64 uniform64(u64 v) { return ((u64)(unsigned)__builtin_amdgcn_readfirstlane((unsigned)(v >> 32)) << 32) | (unsigned)__builtin_amdgcn_readfirstlane((unsigned)v); }
__device__ __forceinline__ void wait_group(u64* word, bool need, u64 ep) {
#pragma nounroll
    for (int spin = 0; spin < (1 << 20); ++spin) {
        if (!__any(need && flag_load(word) != ep)) break;
        __builtin_amdgcn_s_sleep(2);
    }
}
#if defined(__HIP_DEVICE_COMPILE__)
__device__ __forceinline__ void touch(unsigned& sink, unsigned voff, const void* base, unsigned bytes) {
    asm volatile("s_nop 4\n\tbuffer_load_dword %0, %1, %2, 0 offen" : "+v"(sink) : "v"(voff), "s"(RSRC(base, bytes)) : "memory");
}
__device__ __forceinline__ void flag_peek(u64& v, const u64* w) { asm volatile("global_load_dwordx2 %0, %1, off sc1" : "+v"(v) : "v"(w) : "memory"); }
struct G3W {
    const u16* ptr; unsigned voff[2]; int phi, koff;
    __device__ __forceinline__ G3W(const Params& p, int wid, int lane) {
        const int xcd = blockIdx.x & 7, idx = blockIdx.x >> 3, tm = (xcd & 3) * 8 + (idx & 7), q = lane >> 4;
        ptr = wid < 4 ? p.wg : p.wu; phi = ((idx & 7) + (idx >> 3)) & 3; koff = xcd * 2;
#pragma unroll
        for (int i = 0; i < 2; ++i) { const int j = (lane & 15) ^ (((q | (i << 2)) << 1) | (wid >> 2));
            voff[i] = j < 14 ? (unsigned)(((wid & 3) * 16 + q) * FF * 2 + tm * 224 + j * 16) : 0x7ffffff0u; }
    }
    __device__ __forceinline__ int kstep(int t) const { return (t + koff + ((t & 3) == phi ? 4 : 0)) & 15; }
    __device__ __forceinline__ void piece(LAS unsigned char* lds, int wid, const void* base, int so, unsigned nrec, int slot, int i) const { DMA16_DEV(RSRC(base, nrec), lds + slot * 32768 + wid * 4096 + i * 1024, voff[i >> 1], so + i * 4 * FF * 2); }
    __device__ __forceinline__ void early(LAS unsigned char* lds, int wid, int t) const {
#pragma unroll
        for (int i = 0; i < 4; ++i) piece(lds, wid, ptr, kstep(t) * 64 * FF * 2, DM * FF * 2, t, i);
    }
};
__device__ __forceinline__ void colsum_partial(const Params& p, LAS unsigned char* lds, int lw) {
    const int lane = threadIdx.x & 63, slab = blockIdx.x >> 1, mat = slab >= 56, c0 = (slab - mat * 56) * 64;
    const auto rw = RSRC(mat ? p.wu : p.wg, slab < 112 ? DM * FF * 2 : 0);
    const unsigned vo = (unsigned)(((lw * 256 + (lane >> 3)) * FF + c0 + (lane & 7) * 8) * 2);
    u32x4 v[8]; f32x4 s0 = {0.f, 0.f, 0.f, 0.f}, s1 = s0;
    auto add = [&](const u32x4 w) { s0 += (f32x4){bf_lo(w[0]), bf_hi(w[0]), bf_lo(w[1]), bf_hi(w[1])}; s1 += (f32x4){bf_lo(w[2]), bf_hi(w[2]), bf_lo(w[3]), bf_hi(w[3])}; };
#pragma unroll
    for (int i = 0; i < 4; ++i) v[i] = __builtin_amdgcn_raw_buffer_load_b128(rw, vo, i * 8 * FF * 2, 0);
#pragma unroll
    for (int i = 0; i < 8; ++i) { add(v[i % 4]); v[i % 4] = __builtin_amdgcn_raw_buffer_load_b128(rw, vo, (4 + i) * 8 * FF * 2, 0); sched0(); }
#pragma unroll
    for (int i = 4; i < 8; ++i) v[i] = __builtin_amdgcn_raw_buffer_load_b128(rw, vo, (8 + i) * 8 * FF * 2, 0);
#pragma nounroll
    for (int j = 16; j < 40; j += 8)
#pragma unroll
        for (int i = 0; i < 8; ++i) { add(v[i]); v[i] = __builtin_amdgcn_raw_buffer_load_b128(rw, j < 32 ? vo : 0x7ffffff0u, (j + i) * 8 * FF * 2, 0); sched0(); }
    LAS float* tab = (LAS float*)(lds + 131072) + (lw * 8 + (lane >> 3)) * 64 + (lane & 7) * 8;
    *(LAS f32x4*)tab = s0; *(LAS f32x4*)(tab + 4) = s1;
    wait_lgkm0();
}
__device__ __forceinline__ void colsum_final(const Params& p, LAS unsigned char* lds, u64 ep) {
    const int lane = threadIdx.x & 63, slab = blockIdx.x >> 1, mat = slab >= 56, c0 = (slab - mat * 56) * 64;
    if (slab >= 112) return;
    const LAS float* tab = (const LAS float*)(lds + 131072);
    float a = 0.f, b = 0.f;
#pragma unroll
    for (int i = 0; i < 32; i += 2) { a += tab[i * 64 + lane]; b += tab[(i + 1) * 64 + lane]; }
    const int f = c0 + lane, t = f / 112, r = f - t * 112, h = r >= 56;
    __hip_atomic_store(p.csum + (mat * 2 + h) * 1792 + t * 56 + r - h * 56, a + b, __ATOMIC_RELAXED, __HIP_MEMORY_SCOPE_AGENT);
    wait_vm<0>();
    if (lane == 0) flag_store(p.flags + (F_CS + slab) * FLAG_STRIDE, ep);
}
#endif

enum { ROW, CHK, TR };
constexpr int NKS2 = 27;
constexpr int RE4 = 1, RO4 = 3;
constexpr int FLAGD = 1;
constexpr int LNT0 = 1, LNT1 = 5;
template <int ID> struct Cfg;
template <> struct Cfg<1> { static constexpr int NKT = 16, NKS = 16, FA = ROW, FB = ROW, LDB = DM; };
template <> struct Cfg<2> { static constexpr int NKT = 64, NKS = NKS2, FA = CHK, FB = ROW, LDB = SEQ; };
template <> struct Cfg<4> { static constexpr int NKT = 56, NKS = 0, FA = TR, FB = CHK, LDB = 0; };

#if defined(__HIP_DEVICE_COMPILE__)
template <int ID, bool PRE>
__device__ __forceinline__ void g128_load(const Params& p, LAS unsigned char* lds, const int wid, u64& ep) {
    using C = Cfg<ID>;
    constexpr int STAGE = 32768, XLDS = 4 * STAGE;
    const int lane = threadIdx.x & 63, l15 = lane & 15, q = lane >> 4;
    const int xcd = blockIdx.x & 7, idx = blockIdx.x >> 3, im = idx & 3, in = idx >> 2;
    constexpr int NH = ID == 4 ? 2 * (RE4 + RO4) : 0;
    const int ks = (xcd & 1) ^ (ID == 2); const bool recv = ID == 4 || (ID == 2 && ks);
    const int tm = (ID == 1 ? (xcd & 3) : ((xcd >> 1) & 1)) * 4 + im, tn = (xcd >> 2) * 8 + in;
    const int k0 = ID != 1 && ks ? C::NKS : 0, nk = ID == 4 ? 28 - 2 * (ks ? RO4 : RE4) : ID == 2 && ks ? C::NKT - C::NKS : C::NKS;
    const int xb = ID == 4 ? 128 + (blockIdx.x ^ (wid >> 2)) : blockIdx.x >> 1;
    u64* const flag = p.flags + (F_PAIR + xb * 4 + (wid & 3)) * FLAG_STRIDE;
    const unsigned xoff = (unsigned)(ID == 4 ? (128 + xb) * 16384 + (wid & 3) * 4096 : xb * 32768 + (wid & 3) * 8192);
    if constexpr (ID != 1 && !PRE) {
        if (wid == 4 && lane == 0) flag_store(p.flags + (ID == 2 ? F_AB + (tn & 7) * 32 + (tn >> 3) * 16 + (xcd & 3) * 4 + im : F_ACT + (tn >> 1) * 32 + (xcd & 3) * 8 + (idx & 7)) * FLAG_STRIDE, ep);
        sched0();
    }
    const int lw = wid - 4, phi = (im + in) & 3;
    const int koff = ID == 1 ? xcd * 2 : ((xcd >> (ID == 2 ? 1 : 2)) & 1) * (nk >> 1);
    const unsigned ksw = (unsigned)(((lane & 7) ^ (lane >> 3)) << 4);
    unsigned voffA[2], voffB;
    if constexpr (C::FA == ROW) voffA[0] = (unsigned)((tm * 128 + lw * 32 + (lane >> 3)) * DM * 2) + ksw;
    if constexpr (C::FA == CHK) voffA[0] = (unsigned)((lw * 2 * DM + tm * 128 + lane) * 16);
    if constexpr (C::FA == TR) {
#pragma unroll
        for (int i = 0; i < 2; ++i) voffA[i] = (unsigned)((lw * 16 + q) * DM * 2 + tm * 256 + ((l15 ^ ((q | (i << 2)) << 1)) << 4));
    }
    if constexpr (C::FB == ROW) voffB = (unsigned)((((ID == 1 ? tn & 7 : tn)) * 128 + lw * 32 + (lane >> 3)) * C::LDB * 2) + ksw;
    else voffB = (unsigned)((lw * 2 * SEQ + tn * 128 + lane) * 16);
    const auto ra = ID == 1 ? RSRC(p.x, SEQ * DM * 2) : ID == 2 ? RSRC(p.abT, 2 * SEQ * DM * 2) : RSRC(p.wd, FF * DM * 2);
    auto kpos = [&](int t) {
        const int u1 = t - 1, ul = ((nk - 1 - phi) & ~3) + phi;
        int k = ID == 1 ? ((t & 3) != phi ? t : t + 4 < nk ? t + 4 : phi) : t < phi || u1 >= ul ? t : t == phi ? phi + 4 : (u1 & 3) == phi && u1 > phi ? u1 + 4 : u1;
        k += koff; k = k >= nk ? k - nk : k;
        if constexpr (ID == 4) {
            k = ks * 14 + (k < (nk >> 1) ? k : k + 28 - (nk >> 1));
            int u = t - nk + phi; u = u >= NH ? u - NH : u; const int h = u >= RE4 + RO4, v = u - h * (RE4 + RO4);
            return t < nk ? k : h * 28 + (v < RE4 ? 14 - RE4 + v : 28 - RO4 - RE4 + v);
        } else return k + k0;
    };
    const int ktab = kpos(lane);
    struct Pos { int sa, sb, lb; const void* pb; };
    auto prep = [&](int t) {
        const int k = ID == 1 || PRE ? kpos(t) : __builtin_amdgcn_readlane(ktab, t);
        Pos x{C::FA == ROW ? k * 128 : C::FA == CHK ? k * 8 * DM * 16 : k * 64 * DM * 2, C::FB == ROW ? (k & 31) * 128 : k * 8 * SEQ * 16, (t & 3) * STAGE + lw * 4096,
              ID == 1 ? (const void*)(tn < 8 ? p.wre : p.wim) : ID == 2 ? (const void*)(k < 32 ? p.cre : p.cim) : (const void*)p.act};
        asm volatile("" : "+s"(x.sa), "+s"(x.sb), "+s"(x.lb)); if constexpr (ID == 2) asm volatile("" : "+s"(x.pb));
        return x;
    };
    auto issue = [&](const Pos& x, bool doA, bool doB, bool half) {
        const auto rb = RSRC(x.pb, ID == 1 ? DM * DM * 2 : ID == 2 ? SEQ * SEQ * 2 : SEQ * FF * 2);
        LAS unsigned char* base = lds + x.lb;
#pragma unroll
        for (int i = 0; i < 4; ++i) {
            if (doA) {
                if constexpr (C::FA == ROW) DMA16(ra, base + i * 1024, voffA[0], x.sa + i * 8 * DM * 2);
                if constexpr (C::FA == CHK) DMA16_DEV(ra, base + i * 1024, voffA[0], x.sa + (i >> 1) * DM * 16 + (i & 1) * 1024);
                if constexpr (C::FA == TR) DMA16(ra, base + i * 1024, voffA[i >> 1], x.sa + i * 4 * DM * 2);
            }
            if (doB) {
                if constexpr (C::FB == ROW) DMA16(rb, base + 16384 + i * 1024, voffB, x.sb + i * 8 * C::LDB * 2);
                else if (!(half && (i & 1))) DMA16_DEV(rb, base + 16384 + i * 1024, voffB, x.sb + (i >> 1) * SEQ * 16 + ((i & 1) ^ ks) * 1024);
            }
        }
    };
    auto stage = [&](int t, bool doA, bool doB, bool half = false) { issue(prep(t), doA, doB, half); };
    if constexpr (PRE) { stage(0, ID == 4, ID == 2); return; }
    if constexpr (ID != 1) {
        u64* words = p.flags + (ID == 2 ? F_AB + tm * 32 : F_ACT + (tn >> 1) * 32) * FLAG_STRIDE;
        if (wid != 4) __builtin_amdgcn_s_sleep(2);
        stage(1, ID == 4, ID == 2); stage(2, ID == 4, ID == 2);
        if (wid == 4) { const int j = lane & 31; wait_group(words + j * FLAG_STRIDE, ID == 2 ? 2 * j + 2 > k0 && 2 * j < k0 + nk : ((j >> 3) & 1) == ks, ep); }
        bar();
        stage(0, ID == 2, ID == 4); stage(1, ID == 2, ID == 4); stage(2, ID == 2, ID == 4);
        wait_vm<8>(); bar();
        wait_vm<4>();
    } else {
        stage(0, true, true); stage(1, true, true);
        wait_vm<8>(); bar();
        stage(2, true, true);
        const unsigned a = (unsigned)(size_t)lds + XLDS;
        const float e0 = lds_read32<3072>(a), e1 = lds_read32<3076>(a);
        wait_lgkm0(); sched0();
        ep = uniform64(((u64)__float_as_uint(e1) << 32) | __float_as_uint(e0));
    }
    u64 pseen = 0; u64* const pwords = p.flags + (F_ACT + (tn >> 1) * 32 + (lane & 31)) * FLAG_STRIDE;
#pragma nounroll
    for (int t = 0; t < nk - 3; ++t) {
        const Pos x = prep(t + 3);
        wait_vm<8>();
        if constexpr (ID == 4) { if (t == nk - 4 && wid == 4 && __any(pseen != ep)) wait_group(pwords, true, ep); }
        bar();
        issue(x, true, true, false);
        if constexpr (ID == 4) { if (t == nk - 6 && wid == 4) flag_peek(pseen, pwords); }
        if constexpr (ID == 1) { if (t == LNT1 && wid == 4 && lane == 0) flag_store(p.flags + (F_LN + tm * 16 + tn) * FLAG_STRIDE, ep); }
    }
    if constexpr (NH > 0) {
        wait_vm<8>(); bar(); stage(nk, true, true, true);
#pragma nounroll
        for (int t = nk - 2; t < nk + NH - 3; ++t) { const Pos x = prep(t + 3); wait_vm<6>(); bar(); issue(x, true, true, true); }
    }
    bool got = !recv;
    auto fetch = [&](u64 seen) {
        if (uniform64(seen) != ep) return false;
        asm volatile("" ::: "memory");
#pragma unroll
        for (int i = 0; i < (ID == 4 ? 4 : 8); ++i) DMA16_DEV(RSRC(p.xch, 256 * 32768), lds + XLDS + lw * 8192 + i * 1024, (unsigned)(lane * 16), xoff + i * 1024);
        return true;
    };
    wait_vm<NH ? 6 : 8>(); bar();
    u64* const lnw = p.flags + (F_LN + (lane & 16 ? l15 * 16 + tn : tm * 16 + l15)) * FLAG_STRIDE;
    u64 seen0 = flag_load(ID == 1 ? lnw : flag);
    wait_vm<0>();
    if (!got) got = fetch(seen0);
    if constexpr (ID == 1) { if (wid == 4) {
        if (__any(seen0 != ep)) wait_group(lnw, true, ep);
        asm volatile("" ::: "memory");
#pragma unroll
        for (int i = 0; i < 3; ++i) DMA16_DEV(RSRC(p.ln1, 3 * SEQ * 4), lds + XLDS + i * 1024, lane < 32 ? (unsigned)(lane * 16) : 0x7ffffff0u, (i * SEQ + (i < 2 ? tm : tn) * 128) * 4);
    } }
    if constexpr (ID == 1) g128_load<2, true>(p, lds, wid, ep);
    bar();
    if constexpr (ID != 1) seen0 = flag_load(flag);
    if constexpr (ID == 2) { if (!got) got = fetch(seen0); }
    bar();
    if (!got) got = fetch(seen0);
    if (recv) {
#pragma nounroll
        for (int spin = 0; !got && spin < (1 << 20); ++spin) { got = fetch(flag_load(flag)); __builtin_amdgcn_s_sleep(1); }
        wait_vm<0>(); bar();
    }
    if constexpr (ID == 2) { if (!recv) colsum_partial(p, lds, lw); }
    if constexpr (ID == 1) { wait_vm<4>(); bar(); }
    if constexpr (ID != 4) bar();
}

template <int ID>
__device__ __forceinline__ void g128_compute(const Params& p, LAS unsigned char* lds, const int wid, u64& ep, u32x2& stv, unsigned& sto) {
    using C = Cfg<ID>;
    constexpr int STAGE = 32768, XLDS = 4 * STAGE;
    const int lane = threadIdx.x & 63, l15 = lane & 15, q = lane >> 4;
    const int xcd = blockIdx.x & 7, idx = blockIdx.x >> 3, im = idx & 3, in = idx >> 2;
    constexpr int NH = ID == 4 ? 2 * (RE4 + RO4) : 0;
    const int ks = (xcd & 1) ^ (ID == 2); const bool recv = ID == 4 || (ID == 2 && ks);
    const int tm = (ID == 1 ? (xcd & 3) : ((xcd >> 1) & 1)) * 4 + im, tn = (xcd >> 2) * 8 + in;
    const int k0 = ID != 1 && ks ? C::NKS : 0, nk = ID == 4 ? 28 - 2 * (ks ? RO4 : RE4) : ID == 2 && ks ? C::NKT - C::NKS : C::NKS;
    const int xb = ID == 4 ? 128 + (blockIdx.x ^ (wid >> 2)) : blockIdx.x >> 1;
    u64* const flag = p.flags + (F_PAIR + xb * 4 + (wid & 3)) * FLAG_STRIDE;
    const unsigned xoff = (unsigned)(ID == 4 ? (128 + xb) * 16384 + (wid & 3) * 4096 : xb * 32768 + (wid & 3) * 8192);
    const int wm = wid & 1, wn = wid >> 1;
    const int eo = (q & 1) * 16 + (q >> 1) * 8;
    f32x4 acc[4][4];
#pragma unroll
    for (int m = 0; m < 4; ++m)
#pragma unroll
        for (int n = 0; n < 4; ++n) acc[m][n] = (f32x4){0.f, 0.f, 0.f, 0.f};
    const int row0 = tn * 128 + (ID == 4 ? ks * 64 + wn * 32 : wn * 64) + l15, col0 = tm * 128 + wm * 64 + eo;
    if constexpr (ID != 1) {
        constexpr int NN = ID == 4 ? 2 : 4;
        if (ID == 4 || !recv) {
            if constexpr (ID == 2) { wait_vm<0>(); bar(); __builtin_amdgcn_s_sleep(4); }
            u32x4 r[NN][2];
#pragma unroll
            for (int n = 0; n < NN; ++n)
#pragma unroll
                for (int mp = 0; mp < 2; ++mp)
                    r[n][mp] = __builtin_amdgcn_raw_buffer_load_b128(RSRC(ID == 2 ? p.x : p.h, SEQ * DM * 2), (unsigned)(ID == 2 ? ((row0 + n * 16) * DM + col0 + mp * 32) * 2 : ((col0 / 8 + mp * 4) * SEQ + row0 + n * 16) * 16), 0, ID == 4 ? SC1 : 0);
            if constexpr (ID == 4) { wait_vm<0>(); bar(); }
#pragma unroll
            for (int n = 0; n < NN; ++n)
#pragma unroll
                for (int mp = 0; mp < 2; ++mp) {
                    unsigned r0 = r[n][mp][0], r1 = r[n][mp][1], r2 = r[n][mp][2], r3 = r[n][mp][3];
                    swap16u(r0, r2); swap16u(r1, r3);
                    acc[2 * mp][n] = (f32x4){bf_lo(r0), bf_hi(r0), bf_lo(r1), bf_hi(r1)}; acc[2 * mp + 1][n] = (f32x4){bf_lo(r2), bf_hi(r2), bf_lo(r3), bf_hi(r3)};
                }
        } else { wait_vm<0>(); bar(); }
    }
    unsigned pfc = 0;
    if constexpr (ID == 1) {
        ep = uniform64(flag_load(p.flags)) + 1;
        if (threadIdx.x == 0) *(LAS u64*)(lds + XLDS + 3072) = ep;
        wait_lgkm0();
        if (idx == 0) {
            unsigned long long pc; int lo, hi;
            asm volatile("s_getpc_b64 %0\n s_mov_b32 %1, _Z7fused_k6Params@rel32@lo+4\n s_mov_b32 %2, _Z6tail_kv@rel32@lo+12" : "=s"(pc), "=s"(lo), "=s"(hi));
            touch(pfc, threadIdx.x * 128, (const void*)(pc + (long long)lo), (unsigned)max(hi - lo, 0)); touch(pfc, threadIdx.x * 128 + 32768, (const void*)(pc + (long long)lo), (unsigned)max(hi - lo, 0));
        }
    }
    constexpr int NA = C::FA == CHK ? 1 : C::FA == ROW ? 2 : 4, NB = C::FB == CHK ? 1 : 2;
    unsigned adA[NA], adB[NB];
    const unsigned lds0 = (unsigned)(size_t)lds, row_sw = lds0 + (unsigned)(l15 * 128 + ((q ^ (lane & 7)) << 4)), chk = lds0 + (unsigned)(q * 2048 + l15 * 16);
#pragma unroll
    for (int i = 0; i < NA; ++i) {
        if constexpr (C::FA == ROW) adA[i] = (row_sw ^ (unsigned)(i * 64)) + (unsigned)(wm * 8192);
        if constexpr (C::FA == CHK) adA[i] = chk + (unsigned)(wm * 1024);
        if constexpr (C::FA == TR) { const int q4 = l15 >> 2; adA[i] = lds0 + (unsigned)((8 * q + q4) * 256 + (((wm * 4 + i) ^ (q4 | ((q & 1) << 2))) << 5) + 8 * (lane & 3)); }
    }
#pragma unroll
    for (int i = 0; i < NB; ++i) adB[i] = C::FB == CHK ? 16384u + chk + (unsigned)(wn * 512) : 16384u + (row_sw ^ (unsigned)(i * 64)) + (unsigned)(wn * 8192);
    constexpr int W = C::FA == TR ? 2 : 1, NR = 4 + 4 * W;
    auto rd = [&](auto ic, Frag (&ra)[4], bf16x8 (&rb)[4], auto kkc) {
        constexpr int kk = decltype(kkc)::value, i = decltype(ic)::value;
        if constexpr (i == 0 || (i > W && i <= W + 3)) { constexpr int n = i == 0 ? 0 : i - W;
            if constexpr (C::FB == CHK) rb[n] = lds_read128<kk * 8192 + (n >> 1) * 1024 + (n & 1) * 256>(adB[0]);
            else rb[n] = lds_read128<n * 2048>(adB[kk]); }
        else { constexpr int j = i <= W ? i - 1 : i - 4, m = j / W;
            if constexpr (C::FA != TR) { bf16x8 v;
                if constexpr (C::FA == CHK) v = lds_read128<kk * 8192 + m * 256>(adA[0]); else v = lds_read128<m * 2048>(adA[kk]);
                const u32x4 w = __builtin_bit_cast(u32x4, v); ra[m].lo = (u32x2){w[0], w[1]}; ra[m].hi = (u32x2){w[2], w[3]}; }
            else if constexpr (j % 2 == 0) ra[m].lo = tr_read<kk * 8192>(adA[m]);
            else ra[m].hi = tr_read<kk * 8192 + 1024>(adA[m]); }
    };
    auto phase = [&](const Frag (&ra)[4], const bf16x8 (&rb)[4], Frag (&na)[4], bf16x8 (&nb)[4], int dstep, auto kkc) {
        static_for<0, 16>([&](auto sc) {
            constexpr int s = decltype(sc)::value, m = s >> 2, n = s & 3;
            if constexpr (n == 0) { wait_lgkm<(3 - m) * W + (s < NR ? s : NR)>(); sched0(); }
            acc[m][n] = MFMA(ra[m].v(), rb[n], acc[m][n]);
            if constexpr (s < NR) rd(sc, na, nb, kkc);
            if constexpr (s >= 12 && decltype(kkc)::value == 1) {
                if constexpr (s - 12 < NA) bump(adA[s - 12], dstep);
                if constexpr (s - 12 < NB) bump(adB[s - 12], dstep);
            }
            sched0();
        });
    };
    Frag a0[4], a1[4]; bf16x8 b0[4], b1[4];
    constexpr std::integral_constant<int, 0> K0{}; constexpr std::integral_constant<int, 1> K1{};
    if constexpr (ID != 1) bar();
    bar();
    static_for<0, NR>([&](auto ic) { rd(ic, a0, b0, K0); });
    sched0();
    const int rx = tm * 128 + tn * 8 + wid * 2 + (lane >> 5), rw = tn * 128 + tm * 8 + wid * 2 + (lane >> 5);
    u32x4 xv[4], wv[4];
    if constexpr (ID == 1) {
#pragma unroll
        for (int i = 0; i < 4; ++i) { xv[i] = __builtin_amdgcn_raw_buffer_load_b128(RSRC(p.x, SEQ * DM * 2), (unsigned)(rx * 2048 + (lane & 31) * 16 + i * 512), 0, 0);
                                      wv[i] = __builtin_amdgcn_raw_buffer_load_b128(RSRC(tn < 8 ? p.wre : p.wim, DM * DM * 2), (unsigned)((rw & 1023) * 2048 + (lane & 31) * 16 + i * 512), 0, 0); }
        sched0();
    }
    auto steps = [&](int t0, int t1) {
#pragma nounroll
        for (int t = t0; t < t1; ++t) {
            phase(a0, b0, a1, b1, (t & 3) == 3 ? -3 * STAGE : STAGE, K1);
            if constexpr (ID == 1) { if (t == LNT1) wait_vm<0>(); }
            bar();
            phase(a1, b1, a0, b0, 0, K0);
        }
    };
    if constexpr (ID == 1) {
        steps(0, LNT0);
        wait_lgkm0(); sched0();
        f32x2 sx = {0.f, 0.f}, sw = sx, sq = sx;
#pragma unroll
        for (int i = 0; i < 16; ++i) { sx += (f32x2){bf_lo(xv[i >> 2][i & 3]), bf_hi(xv[i >> 2][i & 3])}; sw += (f32x2){bf_lo(wv[i >> 2][i & 3]), bf_hi(wv[i >> 2][i & 3])}; }
        const float mean = hsum(sx[0] + sx[1]) * (1.f / DM);
#pragma unroll
        for (int i = 0; i < 16; ++i) { const f32x2 d = (f32x2){bf_lo(xv[i >> 2][i & 3]), bf_hi(xv[i >> 2][i & 3])} - mean; sq += d * d; }
        const float rstd = __builtin_amdgcn_rsqf(hsum(sq[0] + sq[1]) * (1.f / DM) + LN_EPS), wsum = hsum(sw[0] + sw[1]);
        const int j = lane & 31;
        __builtin_amdgcn_raw_buffer_store_b32(__float_as_uint(j == 0 ? mean : j == 1 ? rstd : wsum), RSRC(p.ln1, 3 * SEQ * 4), j < 3 ? (unsigned)((j * SEQ + (j < 2 ? rx : rw)) * 4) : 0x7ffffff0u, 0, SC1);
        sched0();
        steps(LNT0, nk);
    } else steps(0, nk);
    wait_lgkm0(); keep(a0, b0); sched0();
    auto send = [&](int h0) {
        const auto rx = RSRC(p.xch, 256 * 32768);
#pragma unroll
        for (int m = 0; m < 4; ++m)
#pragma unroll
            for (int h = h0; h < 2; ++h) {
                const f32x4 u = acc[m][2 * h], v = acc[m][2 * h + 1];
                __builtin_amdgcn_raw_buffer_store_b128((u32x4){pack_bf16(u[0], u[1]), pack_bf16(u[2], u[3]), pack_bf16(v[0], v[1]), pack_bf16(v[2], v[3])}, rx, xoff + (unsigned)((h0 ? m : m * 2 + h) * 1024 + lane * 16), 0, SC1);
            }
    };
    if constexpr (NH > 0) {
        send(1); sched0();
        auto phaseH = [&](const Frag (&ra)[4], const bf16x8 (&rb)[4], Frag (&na)[4], bf16x8 (&nb)[4], int dstep, auto kkc) {
            static_for<0, 8>([&](auto sc) {
                constexpr int s = decltype(sc)::value;
                if constexpr ((s & 1) == 0) { wait_lgkm<6 - s + (s < 5 ? 2 * s : 10)>(); sched0(); }
                acc[s >> 1][s & 1] = MFMA(ra[s >> 1].v(), rb[s & 1], acc[s >> 1][s & 1]);
                if constexpr (s < 5) { rd(std::integral_constant<int, s < 2 ? 2 * s : 2 * s + 2>{}, na, nb, kkc); rd(std::integral_constant<int, s < 2 ? 2 * s + 1 : 2 * s + 3>{}, na, nb, kkc); }
                if constexpr (s >= 5 && decltype(kkc)::value == 1) { if constexpr (s < 7) { bump(adA[2 * (s - 5)], dstep); bump(adA[2 * (s - 5) + 1], dstep); } else bump(adB[0], dstep); }
                sched0();
            });
        };
#pragma nounroll
        for (int t = nk; t < nk + NH; ++t) {
            phaseH(a0, b0, a1, b1, (t & 3) == 3 ? -3 * STAGE : STAGE, K1);
            bar();
            phaseH(a1, b1, a0, b0, 0, K0);
            if (t == nk + FLAGD) { wait_vm<0>(); if (lane == 0) flag_store(flag, ep); }
        }
        wait_lgkm0();
        asm volatile("" ::"v"(b0[0]), "v"(b0[1]));
#pragma unroll
        for (int i = 0; i < 4; ++i) asm volatile("" ::"v"(a0[i].lo), "v"(a0[i].hi));
        sched0();
    }
    if constexpr (ID == 1) {
        wait_vm<0>(); asm volatile("; pfc %0" ::"v"(pfc));
        bar();
        const auto ro = RSRC(p.abT, 2 * SEQ * DM * 2);
        const float sgn = tn < 8 ? 1.f : -1.f;
        f32x4 mean[4], rs[4]; float ws[4];
        static_for<0, 4>([&](auto ic) { constexpr int i = decltype(ic)::value; const unsigned a = lds0 + (unsigned)(XLDS + wm * 256 + q * 16);
            mean[i] = __builtin_bit_cast(f32x4, lds_read128<i * 64>(a)); rs[i] = __builtin_bit_cast(f32x4, lds_read128<1024 + i * 64>(a));
            ws[i] = lds_read32<2048 + i * 64>(lds0 + (unsigned)(XLDS + wn * 256 + l15 * 4)); });
        wait_lgkm0(); sched0();
#pragma unroll
        for (int m = 0; m < 4; ++m) {
            const f32x4 r = rs[m] * sgn;
#pragma unroll
            for (int n = 0; n < 4; ++n) acc[m][n] = (acc[m][n] - mean[m] * ws[n]) * r;
        }
#pragma unroll
        for (int n = 0; n < 4; ++n) { const int e = (tn & 7) * 128 + wn * 64 + n * 16 + l15;
#pragma unroll
            for (int mp = 0; mp < 2; ++mp) {
                unsigned x0 = pack_bf16(acc[2 * mp][n][0], acc[2 * mp][n][1]), x1 = pack_bf16(acc[2 * mp][n][2], acc[2 * mp][n][3]);
                unsigned y0 = pack_bf16(acc[2 * mp + 1][n][0], acc[2 * mp + 1][n][1]), y1 = pack_bf16(acc[2 * mp + 1][n][2], acc[2 * mp + 1][n][3]);
                swap16u(x0, y0); swap16u(x1, y1);
                const int s = (tn >> 3) * SEQ + col0 + mp * 32;
                __builtin_amdgcn_raw_buffer_store_b128((u32x4){x0, x1, y0, y1}, ro, (unsigned)(((s >> 3) * DM + e) * 16), 0, SC1);
            } }
    } else if (!recv) {
        send(0);
        wait_vm<0>();
        if (lane == 0) flag_store(flag, ep);
        if constexpr (ID == 2) bar();
    } else {
        bar();
        const unsigned px = (unsigned)(size_t)lds + (unsigned)(XLDS + wid * 8192 + lane * 16);
        constexpr int HH = ID == 4 ? 1 : 2;
        u32x4 pw[4 * HH];
        static_for<0, 4 * HH>([&](auto ic) { pw[decltype(ic)::value] = __builtin_bit_cast(u32x4, lds_read128<decltype(ic)::value * 1024>(px)); });
        wait_lgkm0(); sched0();
#pragma unroll
        for (int m = 0; m < 4; ++m)
#pragma unroll
            for (int h = 0; h < HH; ++h) { const u32x4 w = pw[m * HH + h];
                acc[m][2 * h] += (f32x4){bf_lo(w[0]), bf_hi(w[0]), bf_lo(w[1]), bf_hi(w[1])}; acc[m][2 * h + 1] += (f32x4){bf_lo(w[2]), bf_hi(w[2]), bf_lo(w[3]), bf_hi(w[3])}; }
        const auto ro = RSRC(ID == 2 ? p.h : p.out, ID == 2 ? 136 * SEQ * 16 : SEQ * DM * 2);
#pragma unroll
        for (int n = 0; n < 2 * HH; ++n) {
            unsigned o[4][2];
#pragma unroll
            for (int m = 0; m < 4; ++m) { o[m][0] = pack_bf16(acc[m][n][0], acc[m][n][1]); o[m][1] = pack_bf16(acc[m][n][2], acc[m][n][3]); }
#pragma unroll
            for (int mp = 0; mp < 2; ++mp) {
                swap16u(o[2 * mp][0], o[2 * mp + 1][0]); swap16u(o[2 * mp][1], o[2 * mp + 1][1]);
                __builtin_amdgcn_raw_buffer_store_b128((u32x4){o[2 * mp][0], o[2 * mp][1], o[2 * mp + 1][0], o[2 * mp + 1][1]}, ro,
                    (unsigned)(ID == 4 ? ((row0 + n * 16) * DM + col0 + mp * 32) * 2 : ((col0 / 8 + mp * 4) * SEQ + row0 + n * 16) * 16), 0, SC1);
            }
        }
        if constexpr (ID == 2) {
            wait_vm<0>(); bar();
            sto = (unsigned)(((128 + tm) * SEQ + row0 + q * 16) * 16 + wm * 8);
#pragma unroll
            for (int n = 0; n < 4; ++n) {
                const f32x4 s4 = (acc[0][n] + acc[1][n]) + (acc[2][n] + acc[3][n]);
                const float mw = qsum((s4[0] + s4[1]) + (s4[2] + s4[3])) * (1.f / 64.f);
                const f32x4 d0 = acc[0][n] - mw, d1 = acc[1][n] - mw, d2 = acc[2][n] - mw, d3 = acc[3][n] - mw, q4 = (d0 * d0 + d1 * d1) + (d2 * d2 + d3 * d3);
                float m2 = (q4[0] + q4[1]) + (q4[2] + q4[3]);
                m2 = qsum(m2);
                if (q == n) stv = (u32x2){__float_as_uint(mw), __float_as_uint(m2)};
            }
        }
    }
}
#endif

#if defined(__HIP_DEVICE_COMPILE__)
template <int GRP>
__device__ __forceinline__ void gemm3_body(const Params& p, LAS unsigned char* lds, const int wid, u64& ep, u32x2 stv, unsigned sto) {
    constexpr int NK = 16, FA = 7, HB0 = 3 * 32768;
    const int lane = threadIdx.x & 63;
    const int wm = wid & 1, wn = wid >> 1, l15 = lane & 15, q = lane >> 4;
    const int xcd = blockIdx.x & 7, idx = blockIdx.x >> 3, im = idx & 7, in = idx >> 3;
    const int tm = (xcd & 3) * 8 + im, tn = (xcd >> 2) * 4 + in;
    const G3W gw(p, wid, lane);
    unsigned pf = 0;
    u64* words = p.flags + (F_H + tn * 32) * FLAG_STRIDE;
    if (threadIdx.x == 256 && !(xcd & 1)) flag_store(words + tm * FLAG_STRIDE, ep);
    if (wid != 4) __builtin_amdgcn_s_sleep(2);
    if constexpr (GRP == 1) { const G3W g0(p, wid - 4, lane); g0.early(lds, wid - 4, 0); gw.early(lds, wid, 0); g0.early(lds, wid - 4, 1); gw.early(lds, wid, 1); }
    if ((xcd & 1) && wid == 1) colsum_final(p, lds, ep);
    if (wid == 4) {
        wait_group(words + (lane & 31) * FLAG_STRIDE, (lane & 8) == 0, ep);
        if (blockIdx.x == 0 && lane == 0) flag_store(p.flags, ep);
    }
    bar();
    const unsigned voffB = (unsigned)((wid * SEQ + tn * 256 + lane) * 16);
    struct Src { const void* base; int so; unsigned nrec; };
    auto pin = [](Src x) { asm volatile("" : "+s"(x.base), "+s"(x.so), "+s"(x.nrec)); return x; };
    auto srcW = [&](int t) { return pin(Src{t < NK ? (const void*)gw.ptr : (const void*)(p.csum + GRP * 3584), t < NK ? gw.kstep(t) * 64 * FF * 2 : 0, t < NK ? (unsigned)(DM * FF * 2) : t == NK ? (unsigned)(2 * FF * 2) : 0u}); };
    auto srcH = [&](int t) { Src x{nullptr, (t < NK ? gw.kstep(t) : NK) * 8 * SEQ * 16, 136 * SEQ * 16}; asm volatile("" : "+s"(x.so)); return x; };
    auto stageW = [&](Src x, int slot, int i) { gw.piece(lds, wid, x.base, x.so, x.nrec, slot, i); };
    auto stageH = [&](Src x, int slot, int i) { DMA16_DEV(RSRC(p.h, x.nrec), lds + HB0 + slot * 32768 + wid * 4096 + i * 1024, voffB, x.so + i * 1024); };
#pragma unroll
    for (int t = 0; t < 2; ++t) {
        const Src xh = srcH(t);
#pragma unroll
        for (int i = 0; i < 4; ++i) stageH(xh, t, i);
    }

    unsigned adA[FA];
    const unsigned lds0 = (unsigned)(size_t)lds;
#pragma unroll
    for (int m = 0; m < FA; ++m) { const int q4 = l15 >> 2, pp = lane & 3, reg = m < 6 ? m & 1 : pp >> 1, ch = m < 6 ? wm * 8 + (m & 6) + (pp >> 1) : 6 + wm;
        adA[m] = lds0 + (unsigned)(reg * 16384 + (8 * q + q4) * 256 + ((ch ^ (((q4 | ((q & 1) << 2)) << 1) | reg)) << 4) + 8 * (pp & 1)); }
    unsigned adB = lds0 + (unsigned)(HB0 + q * 4096 + (wn * 64 + l15) * 16);
    auto rd = [&](auto ic, Frag (&ra)[FA], bf16x8 (&rb)[4], auto kkc) {
        constexpr int kk = decltype(kkc)::value, i = decltype(ic)::value;
        if constexpr (i < 4) rb[i] = lds_read128<kk * 16384 + i * 256>(adB);
        else if constexpr (i % 2 == 0) ra[(i - 4) / 2].lo = tr_read<kk * 8192>(adA[(i - 4) / 2]);
        else ra[(i - 4) / 2].hi = tr_read<kk * 8192 + 1024>(adA[(i - 4) / 2]);
    };
    f32x4 acc[FA][4];
#pragma unroll
    for (int m = 0; m < FA; ++m)
#pragma unroll
        for (int n = 0; n < 4; ++n) acc[m][n] = (f32x4){0.f, 0.f, 0.f, 0.f};
    auto phase = [&](const Frag (&ra)[FA], const bf16x8 (&rb)[4], Frag (&na)[FA], bf16x8 (&nb)[4], int dw, int dh, auto kkc, auto&& dma, auto&& prep) {
        static_for<0, 28>([&](auto sc) {
            constexpr int s = decltype(sc)::value, m = s >> 2, n = s & 3;
            if constexpr (decltype(kkc)::value == 0) { if constexpr (s == 0) __builtin_amdgcn_s_setprio(3); if constexpr (s == 18) __builtin_amdgcn_s_setprio(2); }
            else { if constexpr (s == 6) __builtin_amdgcn_s_setprio(1); if constexpr (s == 22) __builtin_amdgcn_s_setprio(0); }
            acc[m][n] = MFMA(ra[m].v(), rb[n], acc[m][n]);
            sched0();
            if constexpr (s < 18) rd(sc, na, nb, kkc);
            if constexpr (s == 20) prep();
            if constexpr (s % 4 == 2 * GRP && s < 16) dma(s / 4);
            if constexpr (decltype(kkc)::value == 1 && s >= 18 && s < 26) { if constexpr (s < 25) bump(adA[s - 18], dw); else bump(adB, dh); }
            sched0();
        });
        wait_lgkm0(); sched0();
    };
    Frag a0[FA], a1[FA]; bf16x8 b0[4], b1[4];
    constexpr std::integral_constant<int, 0> K0{}; constexpr std::integral_constant<int, 1> K1{};
    wait_vm<4>(); bar();
    if constexpr (GRP == 0) __builtin_amdgcn_raw_buffer_store_b64(stv, RSRC(p.h, 136 * SEQ * 16), sto, 0, SC1);
    static_for<0, 18>([&](auto ic) { rd(ic, a0, b0, K0); });
    wait_lgkm0(); sched0();
    const unsigned pfo = (unsigned)(((blockIdx.x * 8 + wid) * 28 + (lane < 28 ? lane : 0)) * 128); const int pft = 1 + idx % 12;
    Src xw = srcW(2), xh;
    auto phaseA = [&](int t, int sw2) {
        phase(a0, b0, a1, b1, sw2 == 1 ? -65536 : 32768, (t & 1) ? -32768 : 32768, K1, [&](int i) { stageW(xw, sw2, i);
            if (i == 3) touch(pf, pfo, p.wd, t == pft ? FF * DM * 2 : 0);
        }, [&]() { xh = srcH(t + 2); });
        wait_vm<5>();
    };
    u64 cseen = 0;
    phaseA(0, 2);
    int sw2 = 0;
#pragma nounroll
    for (int t = 0; t < NK - 1; ++t) {
        if (__builtin_expect(t == 1 && threadIdx.x == 0 && !(xcd & 1), 0)) flag_store(p.flags + (F_ST + tn * 32 + tm) * FLAG_STRIDE, ep);
        if (__builtin_expect(t >= 11 && t <= 12 && (wid & 3) == 0, 0)) {
            const int sl = min((tm * 112 >> 6) + (lane & 3), (tm * 112 + 111) >> 6);
            u64* w = p.flags + (lane < 32 ? F_CS + GRP * 56 + sl : F_ST + tn * 32 + (lane & 23)) * FLAG_STRIDE;
            if (t == 11) flag_peek(cseen, w); else if (__any(cseen != ep)) wait_group(w, true, ep);
        }
        bar();
        phase(a1, b1, a0, b0, 0, 0, K0, [&](int i) { stageH(xh, t & 1, i); }, [&]() { xw = srcW(t + 3); });
        phaseA(t + 1, sw2);
        sw2 = sw2 == 2 ? 0 : sw2 + 1;
    }
    bar();
    __builtin_amdgcn_s_setprio(GRP ? 0 : 3);
    f32x4 csv[FA], sp[4][2];
    static_for<0, 16>([&](auto sc) {
        constexpr int s = decltype(sc)::value;
        acc[s >> 2][s & 3] = MFMA(a1[s >> 2].v(), b1[s & 3], acc[s >> 2][s & 3]);
        if constexpr (s < 8) sp[s >> 1][s & 1] = __builtin_bit_cast(f32x4, lds_read128<(s & 1) * 16384 + (s >> 1) * 256>(adB));
        else if constexpr (s < 15) { constexpr int m = s - 8; const int reg = m < 6 ? m & 1 : q >> 1, c4 = m < 6 ? wm * 2 + (m & 6) * 2 + q : (wm ? 0 : 12) + (q & 1);
            csv[m] = __builtin_bit_cast(f32x4, lds_read128<32768>(lds0 + (unsigned)(reg * 16384 + wm * 256 + ((c4 ^ ((wm << 1) | reg)) << 4)))); }
        if constexpr (GRP == 1 && s == 2) g128_load<4, true>(p, lds, wid, ep);
        sched0();
    });
    wait_lgkm0(); wait_vm<GRP ? 4 : 0>(); asm volatile("; pf %0" ::"v"(pf)); sched0();
    float mean[4], c1[4], ve[4];
#pragma unroll
    for (int n = 0; n < 4; ++n) {
        const f32x4 s0 = sp[n][0], s1 = sp[n][1];
        mean[n] = qsum((s0[0] + s0[2]) + (s1[0] + s1[2])) * (1.f / 16.f);
        const float d0 = s0[0] - mean[n], d1 = s0[2] - mean[n], d2 = s1[0] - mean[n], d3 = s1[2] - mean[n];
        const float m2 = qsum((s0[1] + s0[3]) + (s1[1] + s1[3]) + 64.f * ((d0 * d0 + d1 * d1) + (d2 * d2 + d3 * d3)));
        ve[n] = m2 * (1.f / DM) + LN_EPS; c1[n] = -1.44269504f * __builtin_amdgcn_rsqf(ve[n]);
    }
#pragma unroll
    for (int s = 16; s < 28; ++s) acc[s >> 2][s & 3] = MFMA(a1[s >> 2].v(), b1[s & 3], acc[s >> 2][s & 3]);
#pragma unroll
    for (int s = 16; s < 28; ++s) { __builtin_amdgcn_sched_group_barrier(0x008, 1, 0); __builtin_amdgcn_sched_group_barrier(0x002, 10, 0); }
    sched0();
    const auto ro = RSRC(p.act, SEQ * FF * 2);
    auto silu2 = [&](f32x2 g, f32x2 u, int n) {
        const f32x2 x = g * c1[n], d = (f32x2){__builtin_amdgcn_exp2f(x[0]), __builtin_amdgcn_exp2f(x[1])} * ve[n] + ve[n];
        const f32x2 r = g * u * (f32x2){__builtin_amdgcn_rcpf(d[0]), __builtin_amdgcn_rcpf(d[1])};
        return pack_bf16(r[0], r[1]);
    };
#pragma unroll
    for (int j = 0; j < 3; ++j)
#pragma unroll
        for (int np = 0; np < 2; ++np) {
            unsigned o[2][2];
#pragma unroll
            for (int i = 0; i < 2; ++i) { const int n = 2 * np + i; const f32x4 g = acc[2 * j][n] - mean[n] * csv[2 * j], u = acc[2 * j + 1][n] - mean[n] * csv[2 * j + 1];
                o[i][0] = silu2((f32x2){g[0], g[1]}, (f32x2){u[0], u[1]}, n); o[i][1] = silu2((f32x2){g[2], g[3]}, (f32x2){u[2], u[3]}, n); }
            swap16u(o[0][0], o[1][0]); swap16u(o[0][1], o[1][1]);
            __builtin_amdgcn_raw_buffer_store_b128((u32x4){o[0][0], o[0][1], o[1][0], o[1][1]}, ro, (unsigned)(((tm * 14 + wm * 8 + 2 * j + (q >> 1)) * SEQ + tn * 256 + wn * 64 + np * 32 + (lane & 31)) * 16), 0, SC1);
        }
    {
        unsigned o[4];
#pragma unroll
        for (int n = 0; n < 4; ++n) {
            const f32x4 v = acc[6][n] - mean[n] * csv[6];
            float g0 = v[0], g1 = v[1], u0 = v[2], u1 = v[3];
            swap32(g0, u0); swap32(g1, u1);
            o[n] = silu2((f32x2){g0, g1}, (f32x2){u0, u1}, n);
        }
        swap16u(o[0], o[1]); swap16u(o[2], o[3]); swap32u(o[0], o[2]); swap32u(o[1], o[3]);
        __builtin_amdgcn_raw_buffer_store_b128((u32x4){o[0], o[2], o[1], o[3]}, ro, (unsigned)(((tm * 14 + 6 + wm) * SEQ + tn * 256 + wn * 64 + lane) * 16), 0, SC1);
    }
    __builtin_amdgcn_s_setprio(0);
    if constexpr (GRP == 1) { wait_vm<0>(); bar(); }
}
#endif

__global__ __launch_bounds__(512, 2) void fused_k(const Params p) {
#if defined(__HIP_DEVICE_COMPILE__)
    __shared__ __attribute__((aligned(16))) unsigned char lds_raw[5 * 32768];
    LAS unsigned char* lds = (LAS unsigned char*)lds_raw;
    const int wid = __builtin_amdgcn_readfirstlane(threadIdx.x >> 6);
    u64 ep = 0;
    u32x2 stv = {0u, 0u}; unsigned sto = 0x7ffffff0u;
    if (wid < 4) { g128_compute<1>(p, lds, wid, ep, stv, sto); g128_compute<2>(p, lds, wid, ep, stv, sto); gemm3_body<0>(p, lds, wid, ep, stv, sto); g128_compute<4>(p, lds, wid, ep, stv, sto); }
    else { g128_load<1, false>(p, lds, wid, ep); g128_load<2, false>(p, lds, wid, ep); gemm3_body<1>(p, lds, wid, ep, stv, sto); g128_load<4, false>(p, lds, wid, ep); }
#endif
}
__global__ void tail_k() {}

extern "C" void kernel_launch(void* const* d_in, const int* in_sizes, int n_in, void* d_out, int out_size, void* d_ws, size_t ws_size, hipStream_t stream) {
    Params p;
    p.x = (const u16*)d_in[0]; p.cre = (const u16*)d_in[1]; p.cim = (const u16*)d_in[2]; p.wre = (const u16*)d_in[3]; p.wim = (const u16*)d_in[4];
    p.wg = (const u16*)d_in[6]; p.wu = (const u16*)d_in[7]; p.wd = (const u16*)d_in[8];
    p.out = (u16*)d_out;
    char* ws = (char*)d_ws;
    p.abT = (u16*)ws; p.h = (u16*)(ws + (8u << 20)); p.act = (u16*)(ws + (16u << 20));
    p.xch = (u16*)(ws + (32u << 20)); p.flags = (unsigned long long*)(ws + (40u << 20)); p.csum = (float*)(ws + (40u << 20) + (512u << 10)); p.ln1 = (float*)(ws + (40u << 20) + (768u << 10));
    fused_k<<<256, 512, 0, stream>>>(p);
}
```

```cpp
#include <hip/hip_runtime.h>
#include <hip/hip_bf16.h>
#include <type_traits>
#define STAMPS

#define LAS __attribute__((address_space(3)))
typedef unsigned short u16;
typedef __bf16 bf16x8 __attribute__((ext_vector_type(8)));
typedef __bf16 bf16x2 __attribute__((ext_vector_type(2)));
typedef float f32x4 __attribute__((ext_vector_type(4)));
typedef float f32x2 __attribute__((ext_vector_type(2)));
typedef unsigned u32x4 __attribute__((ext_vector_type(4)));
typedef unsigned u32x2 __attribute__((ext_vector_type(2)));

constexpr int SEQ = 2048, DM = 1024, FF = 3584;
#define ABP 1024
constexpr int ABT_BYTES = 512 * ABP * 16;
#define STA 16
constexpr float LN_EPS = 1e-5f;
constexpr int SC1 = 16;

struct Params {
    const u16* x; const u16* cre; const u16* cim; const u16* wre; const u16* wim;
    const u16* wg; const u16* wu; const u16* wd;
    u16* out; u16* abT; u16* h; u16* act; u16* xch; unsigned long long* flags; float* csum; float* ln1;
};

template <int N> __device__ __forceinline__ void wait_vm() { asm volatile("s_waitcnt vmcnt(%0)" ::"n"(N) : "memory"); }
__device__ __forceinline__ void wait_lgkm0() { __builtin_amdgcn_s_waitcnt(0xC07F); asm volatile("s_waitcnt lgkmcnt(0)" ::: "memory"); }
__device__ __forceinline__ void sched0() { __builtin_amdgcn_sched_barrier(0); }
__device__ __forceinline__ void bar() { sched0(); asm volatile("" ::: "memory"); __builtin_amdgcn_s_barrier(); asm volatile("" ::: "memory"); sched0(); }
__device__ __forceinline__ unsigned pack_bf16(float lo, float hi) { bf16x2 c; c[0] = (__bf16)lo; c[1] = (__bf16)hi; return __builtin_bit_cast(unsigned, c); }
__device__ __forceinline__ float bf_lo(unsigned v) { return __uint_as_float(v << 16); }
__device__ __forceinline__ float bf_hi(unsigned v) { return __uint_as_float(v & 0xffff0000u); }
__device__ __forceinline__ void swap16(float& a, float& b) { auto r = __builtin_amdgcn_permlane16_swap(__float_as_uint(a), __float_as_uint(b), false, false); a = __uint_as_float(r[0]); b = __uint_as_float(r[1]); }
__device__ __forceinline__ void swap32(float& a, float& b) { auto r = __builtin_amdgcn_permlane32_swap(__float_as_uint(a), __float_as_uint(b), false, false); a = __uint_as_float(r[0]); b = __uint_as_float(r[1]); }
__device__ __forceinline__ void swap16u(unsigned& a, unsigned& b) { auto r = __builtin_amdgcn_permlane16_swap(a, b, false, false); a = r[0]; b = r[1]; }
__device__ __forceinline__ void swap32u(unsigned& a, unsigned& b) { auto r = __builtin_amdgcn_permlane32_swap(a, b, false, false); a = r[0]; b = r[1]; }
__device__ __forceinline__ float qsum(float v) { float a = v, b = v; swap16(a, b); a += b; b = a; swap32(a, b); return a + b; }

template <int CTRL> __device__ __forceinline__ float dpp_add(float v) { return v + __int_as_float(__builtin_amdgcn_update_dpp(0, __float_as_int(v), CTRL, 0xf, 0xf, false)); }
__device__ __forceinline__ float hsum(float v) { v = dpp_add<0x128>(v); v = dpp_add<0x124>(v); v = dpp_add<0x122>(v); v = dpp_add<0x121>(v); float b = v; swap16(v, b); return v + b; }

struct Frag { u32x2 lo, hi; __device__ __forceinline__ bf16x8 v() const { return __builtin_bit_cast(bf16x8, __builtin_shufflevector(lo, hi, 0, 1, 2, 3)); } };
template <int OFF> __device__ __forceinline__ u32x2 tr_read(unsigned addr) { u32x2 r; asm volatile("ds_read_b64_tr_b16 %0, %1 offset:%2" : "=v"(r) : "v"(addr), "n"(OFF) : "memory"); return r; }
template <int OFF> __device__ __forceinline__ bf16x8 lds_read128(unsigned addr) { bf16x8 r; asm volatile("ds_read_b128 %0, %1 offset:%2" : "=v"(r) : "v"(addr), "n"(OFF) : "memory"); return r; }
template <int OFF> __device__ __forceinline__ float lds_read32(unsigned addr) { float r; asm volatile("ds_read_b32 %0, %1 offset:%2" : "=v"(r) : "v"(addr), "n"(OFF) : "memory"); return r; }
template <int N> __device__ __forceinline__ void wait_lgkm() { asm volatile("s_waitcnt lgkmcnt(%0)" ::"n"(N) : "memory"); }
template <int I, int N, class F> __device__ __forceinline__ void static_for(F&& f) { if constexpr (I < N) { f(std::integral_constant<int, I>{}); static_for<I + 1, N>(f); } }
template <int NA> __device__ __forceinline__ void keep(const Frag (&a)[NA], const bf16x8 (&b)[4]) {
#pragma unroll
    for (int i = 0; i < NA; ++i) asm volatile("" ::"v"(a[i].lo), "v"(a[i].hi));
#pragma unroll
    for (int i = 0; i < 4; ++i) asm volatile("" ::"v"(b[i]));
}
__device__ __forceinline__ void bump(unsigned& a, int d) { asm volatile("v_add_u32 %0, %1, %0" : "+v"(a) : "s"(d)); }
#define MFMA(a, b, c) __builtin_amdgcn_mfma_f32_16x16x32_bf16(a, b, c, 0, 0, 0)
#define RSRC(ptr, bytes) __builtin_amdgcn_make_buffer_rsrc((void*)(ptr), 0, (bytes), 0x00020000)
#define DMA16(rs, ldsp, voff, soff) __builtin_amdgcn_raw_ptr_buffer_load_lds(rs, (LAS void*)(ldsp), 16, voff, soff, 0, 0)
#define DMA16_DEV(rs, ldsp, voff, soff) __builtin_amdgcn_raw_ptr_buffer_load_lds(rs, (LAS void*)(ldsp), 16, voff, soff, 0, SC1)

#define STW 7
#define STAMP(i) do { sched0(); ts[i] = __builtin_amdgcn_s_memtime(); rt[i] = __builtin_amdgcn_s_memrealtime(); __builtin_amdgcn_s_waitcnt(0xC07F); sched0(); } while (0)
#define STAMP_BEGIN unsigned long long ts[6] = {}, rt[6] = {}; STAMP(0)
#define STAMP_END(ID) do { if (ID == 4) wait_vm<0>(); STAMP(3); if (threadIdx.x == (ID == 3 ? STW * 64 : (STW & 3) * 64)) { unsigned long long* o = (unsigned long long*)((char*)p.xch + (9u << 20)) + ((ID - 1) * 256 + blockIdx.x) * 16; \
        for (int i_ = 0; i_ < 6; ++i_) { o[i_] = ts[i_]; o[8 + i_] = rt[i_]; } } } while (0)

typedef unsigned long long u64;
#define FSTR 8
constexpr int FLAG_STRIDE = FSTR, F_PAIR = 1, F_AB = F_PAIR + 1536, F_H = F_AB + 256, F_ACT = F_H + 256, F_CS = F_ACT + 256, F_LN = F_CS + 128, F_ST = F_LN + 256;
#define PSCOPE __HIP_MEMORY_SCOPE_AGENT
#define PSLEEP 2
__device__ __forceinline__ u64 flag_load(u64* w) { return __hip_atomic_load(w, __ATOMIC_RELAXED, PSCOPE); }
__device__ __forceinline__ void flag_store(u64* w, u64 v) { __hip_atomic_store(w, v, __ATOMIC_RELAXED, PSCOPE); }
__device__ __forceinline__ u64 uniform64(u64 v) { return ((u64)(unsigned)__builtin_amdgcn_readfirstlane((unsigned)(v >> 32)) << 32) | (unsigned)__builtin_amdgcn_readfirstlane((unsigned)v); }
__device__ __forceinline__ void wait_group(u64* word, bool need, u64 ep) {
#pragma nounroll
    for (int spin = 0; spin < (1 << 20); ++spin) {
        if (!__any(need && flag_load(word) != ep)) break;
        __builtin_amdgcn_s_sleep(PSLEEP);
    }
}
#if defined(__HIP_DEVICE_COMPILE__)
__device__ __forceinline__ void touch(unsigned& sink, unsigned voff, const void* base, unsigned bytes) {
    asm volatile("s_nop 4\n\tbuffer_load_dword %0, %1, %2, 0 offen" : "+v"(sink) : "v"(voff), "s"(RSRC(base, bytes)) : "memory");
}
__device__ __forceinline__ void flag_peek(u64& v, const u64* w) { asm volatile("global_load_dwordx2 %0, %1, off sc1" : "+v"(v) : "v"(w) : "memory"); }
struct G3W {
    const u16* ptr; unsigned voff[2]; int phi, koff;
    __device__ __forceinline__ G3W(const Params& p, int wid, int lane) {
        const int xcd = blockIdx.x & 7, idx = blockIdx.x >> 3, tm = (xcd & 3) * 8 + (idx & 7), q = lane >> 4;
        ptr = wid < 4 ? p.wg : p.wu; phi = ((idx & 7) + (idx >> 3)) & 3; koff = xcd * 2;
#pragma unroll
        for (int i = 0; i < 2; ++i) { const int j = (lane & 15) ^ (((q | (i << 2)) << 1) | (wid >> 2));
            voff[i] = j < 14 ? (unsigned)(((wid & 3) * 16 + q) * FF * 2 + tm * 224 + j * 16) : 0x7ffffff0u; }
    }
    __device__ __forceinline__ int kstep(int t) const { return (t + koff + ((t & 3) == phi ? 4 : 0)) & 15; }
    __device__ __forceinline__ void piece(LAS unsigned char* lds, int wid, const void* base, int so, unsigned nrec, int slot, int i) const { DMA16_DEV(RSRC(base, nrec), lds + slot * 32768 + wid * 4096 + i * 1024, voff[i >> 1], so + i * 4 * FF * 2); }
    __device__ __forceinline__ void early(LAS unsigned char* lds, int wid, int t) const {
#pragma unroll
        for (int i = 0; i < 4; ++i) piece(lds, wid, ptr, kstep(t) * 64 * FF * 2, DM * FF * 2, t, i);
    }
};
#define CSW 4
#define CSD 0
__device__ __forceinline__ void colsum_partial(const Params& p, LAS unsigned char* lds, int lw) {
    const int lane = threadIdx.x & 63, slab = blockIdx.x >> 1, mat = slab >= 56, c0 = (slab - mat * 56) * 64;
    const auto rw = RSRC(mat ? p.wu : p.wg, slab < 112 ? DM * FF * 2 : 0);
    const unsigned vo = (unsigned)(((lw * 256 + (lane >> 3)) * FF + c0 + (lane & 7) * 8) * 2);
    u32x4 v[8]; f32x4 s0 = {0.f, 0.f, 0.f, 0.f}, s1 = s0;
    auto add = [&](const u32x4 w) { s0 += (f32x4){bf_lo(w[0]), bf_hi(w[0]), bf_lo(w[1]), bf_hi(w[1])}; s1 += (f32x4){bf_lo(w[2]), bf_hi(w[2]), bf_lo(w[3]), bf_hi(w[3])}; };
    if (CSD) __builtin_amdgcn_s_sleep(CSD);
#pragma unroll
    for (int i = 0; i < CSW; ++i) v[i] = __builtin_amdgcn_raw_buffer_load_b128(rw, vo, i * 8 * FF * 2, 0);
#pragma unroll
    for (int i = 0; i < 8; ++i) { add(v[i % CSW]); v[i % CSW] = __builtin_amdgcn_raw_buffer_load_b128(rw, vo, (CSW + i) * 8 * FF * 2, 0); sched0(); }
#pragma unroll
    for (int i = CSW; i < 8; ++i) v[i] = __builtin_amdgcn_raw_buffer_load_b128(rw, vo, (8 + i) * 8 * FF * 2, 0);
#pragma nounroll
    for (int j = 16; j < 40; j += 8)
#pragma unroll
        for (int i = 0; i < 8; ++i) { add(v[i]); v[i] = __builtin_amdgcn_raw_buffer_load_b128(rw, j < 32 ? vo : 0x7ffffff0u, (j + i) * 8 * FF * 2, 0); sched0(); }
    LAS float* tab = (LAS float*)(lds + 131072) + (lw * 8 + (lane >> 3)) * 64 + (lane & 7) * 8;
    *(LAS f32x4*)tab = s0; *(LAS f32x4*)(tab + 4) = s1;
    wait_lgkm0();
}
__device__ __forceinline__ void colsum_final(const Params& p, LAS unsigned char* lds, u64 ep) {
    const int lane = threadIdx.x & 63, slab = blockIdx.x >> 1, mat = slab >= 56, c0 = (slab - mat * 56) * 64;
    if (slab >= 112) return;
    const LAS float* tab = (const LAS float*)(lds + 131072);
    float a = 0.f, b = 0.f;
#pragma unroll
    for (int i = 0; i < 32; i += 2) { a += tab[i * 64 + lane]; b += tab[(i + 1) * 64 + lane]; }
    const int f = c0 + lane, t = f / 112, r = f - t * 112, h = r >= 56;
    __hip_atomic_store(p.csum + (mat * 2 + h) * 1792 + t * 56 + r - h * 56, a + b, __ATOMIC_RELAXED, __HIP_MEMORY_SCOPE_AGENT);
    wait_vm<0>();
    if (lane == 0) flag_store(p.flags + (F_CS + slab) * FLAG_STRIDE, ep);
}
#endif

enum { ROW, CHK, TR };
#define NKS2 27
#define FLAGD 1
#define W1E 1
#define LNT0 1
#define LNT1 5
#define NPRE2 1
#define G1PRE 1
#define RE4 1
#define RO4 3
template <int ID> struct Cfg;
template <> struct Cfg<1> { static constexpr int NKT = 16, NKS = 16, FA = ROW, FB = ROW, LDB = DM; };
template <> struct Cfg<2> { static constexpr int NKT = 64, NKS = NKS2, FA = CHK, FB = ROW, LDB = SEQ; };
template <> struct Cfg<4> { static constexpr int NKT = 56, NKS = 0, FA = TR, FB = CHK, LDB = 0; };

#if defined(__HIP_DEVICE_COMPILE__)
template <int ID, int PRE>
__device__ __forceinline__ void g128_load(const Params& p, LAS unsigned char* lds, const int wid, u64& ep) {
    using C = Cfg<ID>;
    constexpr int STAGE = 32768, XLDS = 4 * STAGE;
    const int lane = threadIdx.x & 63, l15 = lane & 15, q = lane >> 4;
    const int xcd = blockIdx.x & 7, idx = blockIdx.x >> 3, im = idx & 3, in = idx >> 2;
    constexpr int NH = ID == 4 ? 2 * (RE4 + RO4) : 0;
    const int ks = (xcd & 1) ^ (ID == 2); const bool recv = ID == 4 || (ID == 2 && ks);
    const int tm = (ID == 1 ? (xcd & 3) : ((xcd >> 1) & 1)) * 4 + im, tn = (xcd >> 2) * 8 + in;
    const int k0 = ID != 1 && ks ? C::NKS : 0, nk = ID == 4 ? 28 - 2 * (ks ? RO4 : RE4) : ID == 2 && ks ? C::NKT - C::NKS : C::NKS;
    const int xb = ID == 4 ? 128 + (blockIdx.x ^ (wid >> 2)) : blockIdx.x >> 1;
    u64* const flag = p.flags + (F_PAIR + xb * 4 + (wid & 3)) * FLAG_STRIDE;
    const unsigned xoff = (unsigned)(ID == 4 ? (128 + xb) * 16384 + (wid & 3) * 4096 : xb * 32768 + (wid & 3) * 8192);
    if constexpr (ID != 1 && PRE < 0) {
        if (wid == 4 && lane == 0) flag_store(p.flags + (ID == 2 ? F_AB + (tn & 7) * 32 + (tn >> 3) * 16 + (xcd & 3) * 4 + im : F_ACT + (tn >> 1) * 32 + (xcd & 3) * 8 + (idx & 7)) * FLAG_STRIDE, ep);
        sched0();
    }
    const int lw = wid - 4, phi = (im + in) & 3;
    const int koff = ID == 1 ? xcd * 2 : ((xcd >> (ID == 2 ? 1 : 2)) & 1) * (nk >> 1);
    const unsigned ksw = (unsigned)(((lane & 7) ^ (lane >> 3)) << 4);
    unsigned voffA[2], voffB;
    if constexpr (C::FA == ROW) voffA[0] = (unsigned)((tm * 128 + lw * 32 + (lane >> 3)) * DM * 2) + ksw;
    if constexpr (C::FA == CHK) voffA[0] = (unsigned)((lw * 2 * ABP + tm * 128 + lane) * 16);
    if constexpr (C::FA == TR) {
#pragma unroll
        for (int i = 0; i < 2; ++i) voffA[i] = (unsigned)((lw * 16 + q) * DM * 2 + tm * 256 + ((l15 ^ ((q | (i << 2)) << 1)) << 4));
    }
    if constexpr (C::FB == ROW) voffB = (unsigned)((((ID == 1 ? tn & 7 : tn)) * 128 + lw * 32 + (lane >> 3)) * C::LDB * 2) + ksw;
    else voffB = (unsigned)((lw * 2 * SEQ + tn * 128 + lane) * 16);
    const auto ra = ID == 1 ? RSRC(p.x, SEQ * DM * 2) : ID == 2 ? RSRC(p.abT, ABT_BYTES) : RSRC(p.wd, FF * DM * 2);
    auto kpos = [&](int t) {
        const int u1 = t - 1, ul = ((nk - 1 - phi) & ~3) + phi;
        int k = ID == 1 ? ((t & 3) != phi ? t : t + 4 < nk ? t + 4 : phi) : t < phi || u1 >= ul ? t : t == phi ? phi + 4 : (u1 & 3) == phi && u1 > phi ? u1 + 4 : u1;
        k += koff; k = k >= nk ? k - nk : k;
        if constexpr (ID == 4) {
            k = ks * 14 + (k < (nk >> 1) ? k : k + 28 - (nk >> 1));
            int u = t - nk + phi; u = u >= NH ? u - NH : u; const int h = u >= RE4 + RO4, v = u - h * (RE4 + RO4);
            return t < nk ? k : h * 28 + (v < RE4 ? 14 - RE4 + v : 28 - RO4 - RE4 + v);
        } else return k + k0;
    };
    const int ktab = kpos(lane);
    struct Pos { int sa, sb, lb; const void* pb; };
    auto prep = [&](int t) {
        const int k = ID == 1 || PRE >= 0 ? kpos(t) : __builtin_amdgcn_readlane(ktab, t);
        Pos x{C::FA == ROW ? k * 128 : C::FA == CHK ? k * 8 * ABP * 16 : k * 64 * DM * 2, C::FB == ROW ? (k & 31) * 128 : k * 8 * SEQ * 16, (t & 3) * STAGE + lw * 4096,
              ID == 1 ? (const void*)(tn < 8 ? p.wre : p.wim) : ID == 2 ? (const void*)(k < 32 ? p.cre : p.cim) : (const void*)p.act};
        asm volatile("" : "+s"(x.sa), "+s"(x.sb), "+s"(x.lb)); if constexpr (ID == 2) asm volatile("" : "+s"(x.pb));
        return x;
    };
    auto issue = [&](const Pos& x, bool doA, bool doB, bool half) {
        const auto rb = RSRC(x.pb, ID == 1 ? DM * DM * 2 : ID == 2 ? SEQ * SEQ * 2 : SEQ * FF * 2);
        LAS unsigned char* base = lds + x.lb;
#pragma unroll
        for (int i = 0; i < 4; ++i) {
            if (doA) {
                if constexpr (C::FA == ROW) DMA16(ra, base + i * 1024, voffA[0], x.sa + i * 8 * DM * 2);
                if constexpr (C::FA == CHK) DMA16_DEV(ra, base + i * 1024, voffA[0], x.sa + (i >> 1) * ABP * 16 + (i & 1) * 1024);
                if constexpr (C::FA == TR) DMA16(ra, base + i * 1024, voffA[i >> 1], x.sa + i * 4 * DM * 2);
            }
            if (doB) {
                if constexpr (C::FB == ROW) DMA16(rb, base + 16384 + i * 1024, voffB, x.sb + i * 8 * C::LDB * 2);
                else if (!(half && (i & 1))) DMA16_DEV(rb, base + 16384 + i * 1024, voffB, x.sb + (i >> 1) * SEQ * 16 + ((i & 1) ^ ks) * 1024);
            }
        }
    };
    auto stage = [&](int t, bool doA, bool doB, bool half = false) { issue(prep(t), doA, doB, half); };
    if constexpr (PRE >= 0) { stage(PRE, ID == 4, ID == 2); return; }
    if constexpr (ID != 1) {
        u64* words = p.flags + (ID == 2 ? F_AB + tm * 32 : F_ACT + (tn >> 1) * 32) * FLAG_STRIDE;
        if constexpr (ID == 4) { if (wid != 4) __builtin_amdgcn_s_sleep(2); stage(1, true, false); stage(2, true, false); }
        if constexpr (ID == 2 && NPRE2 < 3) { if (wid != 4) __builtin_amdgcn_s_sleep(2); if (NPRE2 < 2) stage(1, false, true); stage(2, false, true); }
        if (wid == 4) { const int j = lane & 31; wait_group(words + j * FLAG_STRIDE, ID == 2 ? 2 * j + 2 > k0 && 2 * j < k0 + nk : ((j >> 3) & 1) == ks, ep); }
        bar();
        stage(0, ID == 2, ID == 4); stage(1, ID == 2, ID == 4); stage(2, ID == 2, ID == 4);
        wait_vm<8>(); bar();
        wait_vm<4>();
    } else {
        stage(0, true, true); stage(1, true, true);
        wait_vm<8>(); bar();
        stage(2, true, true);
        const unsigned a = (unsigned)(size_t)lds + XLDS;
        const float e0 = lds_read32<3072>(a), e1 = lds_read32<3076>(a);
        wait_lgkm0(); sched0();
        ep = uniform64(((u64)__float_as_uint(e1) << 32) | __float_as_uint(e0));
    }
    u64 pseen = 0; u64* const pwords = p.flags + (F_ACT + (tn >> 1) * 32 + (lane & 31)) * FLAG_STRIDE;
#pragma nounroll
    for (int t = 0; t < nk - 3; ++t) {
        Pos x; if constexpr (ID != 1 || G1PRE) x = prep(t + 3);
        wait_vm<8>();
        if constexpr (ID == 4) { if (t == nk - 4 && wid == 4 && __any(pseen != ep)) wait_group(pwords, true, ep); }
        bar();
        if constexpr (ID == 1 && !G1PRE) x = prep(t + 3);
        issue(x, true, true, false);
        if constexpr (ID == 4) { if (t == nk - 6 && wid == 4) flag_peek(pseen, pwords); }
        if constexpr (ID == 1) { if (t == LNT1 && wid == 4 && lane == 0) flag_store(p.flags + (F_LN + tm * 16 + tn) * FLAG_STRIDE, ep); }
    }
    if constexpr (NH > 0) {
        wait_vm<8>(); bar(); stage(nk, true, true, true);
#pragma nounroll
        for (int t = nk - 2; t < nk + NH - 3; ++t) { const Pos x = prep(t + 3); wait_vm<6>(); bar(); issue(x, true, true, true); }
    }
    bool got = !recv;
    auto fetch = [&](u64 seen) {
        if (uniform64(seen) != ep) return false;
        asm volatile("" ::: "memory");
#pragma unroll
        for (int i = 0; i < (ID == 4 ? 4 : 8); ++i) DMA16_DEV(RSRC(p.xch, 256 * 32768), lds + XLDS + lw * 8192 + i * 1024, (unsigned)(lane * 16), xoff + i * 1024);
        return true;
    };
    wait_vm<NH ? 6 : 8>(); bar();
    u64* const lnw = p.flags + (F_LN + (lane & 16 ? l15 * 16 + tn : tm * 16 + l15)) * FLAG_STRIDE;
    u64 seen0 = flag_load(ID == 1 ? lnw : flag);
    wait_vm<0>();
    if (!got) got = fetch(seen0);
    if constexpr (ID == 1) { if (wid == 4) {
        if (__any(seen0 != ep)) wait_group(lnw, true, ep);
        asm volatile("" ::: "memory");
#pragma unroll
        for (int i = 0; i < 3; ++i) DMA16_DEV(RSRC(p.ln1, 3 * SEQ * 4), lds + XLDS + i * 1024, lane < 32 ? (unsigned)(lane * 16) : 0x7ffffff0u, (i * SEQ + (i < 2 ? tm : tn) * 128) * 4);
    } }
    if constexpr (ID == 1) g128_load<2, 0>(p, lds, wid, ep);
    bar();
    if constexpr (ID != 1) seen0 = flag_load(flag);
    if constexpr (ID == 2) { if (!got) got = fetch(seen0); }
    if constexpr (ID == 1 && NPRE2 > 1) g128_load<2, 1>(p, lds, wid, ep);
    bar();
    if (!got) got = fetch(seen0);
    if constexpr (ID == 1 && NPRE2 > 2) g128_load<2, 2>(p, lds, wid, ep);
    if (recv) {
#pragma nounroll
        for (int spin = 0; !got && spin < (1 << 20); ++spin) { got = fetch(flag_load(flag)); __builtin_amdgcn_s_sleep(1); }
        wait_vm<0>(); bar();
    }
    if constexpr (ID == 2) { if (!recv) colsum_partial(p, lds, lw); }
    if constexpr (ID == 1) { wait_vm<4 * NPRE2>(); bar(); }
    if constexpr (ID != 4) bar();
}

template <int ID>
__device__ __forceinline__ void g128_compute(const Params& p, LAS unsigned char* lds, const int wid, u64& ep, u32x2& stv, unsigned& sto) {
    using C = Cfg<ID>;
    constexpr int STAGE = 32768, XLDS = 4 * STAGE;
    const int lane = threadIdx.x & 63, l15 = lane & 15, q = lane >> 4;
    const int xcd = blockIdx.x & 7, idx = blockIdx.x >> 3, im = idx & 3, in = idx >> 2;
    constexpr int NH = ID == 4 ? 2 * (RE4 + RO4) : 0;
    const int ks = (xcd & 1) ^ (ID == 2); const bool recv = ID == 4 || (ID == 2 && ks);
    const int tm = (ID == 1 ? (xcd & 3) : ((xcd >> 1) & 1)) * 4 + im, tn = (xcd >> 2) * 8 + in;
    const int k0 = ID != 1 && ks ? C::NKS : 0, nk = ID == 4 ? 28 - 2 * (ks ? RO4 : RE4) : ID == 2 && ks ? C::NKT - C::NKS : C::NKS;
    const int xb = ID == 4 ? 128 + (blockIdx.x ^ (wid >> 2)) : blockIdx.x >> 1;
    u64* const flag = p.flags + (F_PAIR + xb * 4 + (wid & 3)) * FLAG_STRIDE;
    const unsigned xoff = (unsigned)(ID == 4 ? (128 + xb) * 16384 + (wid & 3) * 4096 : xb * 32768 + (wid & 3) * 8192);
    STAMP_BEGIN;
    const int wm = wid & 1, wn = wid >> 1;
    const int eo = (q & 1) * 16 + (q >> 1) * 8;
    f32x4 acc[4][4];
#pragma unroll
    for (int m = 0; m < 4; ++m)
#pragma unroll
        for (int n = 0; n < 4; ++n) acc[m][n] = (f32x4){0.f, 0.f, 0.f, 0.f};
    const int row0 = tn * 128 + (ID == 4 ? ks * 64 + wn * 32 : wn * 64) + l15, col0 = tm * 128 + wm * 64 + eo;
    if constexpr (ID != 1) {
        constexpr int NN = ID == 4 ? 2 : 4;
        if (ID == 4 || !recv) {
            if constexpr (ID == 2) { wait_vm<0>(); bar(); __builtin_amdgcn_s_sleep(4); }
            u32x4 r[NN][2];
#pragma unroll
            for (int n = 0; n < NN; ++n)
#pragma unroll
                for (int mp = 0; mp < 2; ++mp)
                    r[n][mp] = __builtin_amdgcn_raw_buffer_load_b128(RSRC(ID == 2 ? p.x : p.h, SEQ * DM * 2), (unsigned)(ID == 2 ? ((row0 + n * 16) * DM + col0 + mp * 32) * 2 : ((col0 / 8 + mp * 4) * SEQ + row0 + n * 16) * 16), 0, ID == 4 ? SC1 : 0);
            if constexpr (ID == 4) { wait_vm<0>(); bar(); }
#pragma unroll
            for (int n = 0; n < NN; ++n)
#pragma unroll
                for (int mp = 0; mp < 2; ++mp) {
                    unsigned r0 = r[n][mp][0], r1 = r[n][mp][1], r2 = r[n][mp][2], r3 = r[n][mp][3];
                    swap16u(r0, r2); swap16u(r1, r3);
                    acc[2 * mp][n] = (f32x4){bf_lo(r0), bf_hi(r0), bf_lo(r1), bf_hi(r1)}; acc[2 * mp + 1][n] = (f32x4){bf_lo(r2), bf_hi(r2), bf_lo(r3), bf_hi(r3)};
                }
        } else { wait_vm<0>(); bar(); }
    }
    unsigned pfc = 0;
    if constexpr (ID == 1) {
        ep = uniform64(flag_load(p.flags)) + 1;
        if (threadIdx.x == 0) *(LAS u64*)(lds + XLDS + 3072) = ep;
        wait_lgkm0();
        if (idx == 0) {
            unsigned long long pc; int lo, hi;
            asm volatile("s_getpc_b64 %0\n s_mov_b32 %1, _Z7fused_k6Params@rel32@lo+4\n s_mov_b32 %2, _Z6tail_kv@rel32@lo+12" : "=s"(pc), "=s"(lo), "=s"(hi));
            touch(pfc, threadIdx.x * 128, (const void*)(pc + (long long)lo), (unsigned)max(hi - lo, 0)); touch(pfc, threadIdx.x * 128 + 32768, (const void*)(pc + (long long)lo), (unsigned)max(hi - lo, 0));
        }
    }
    constexpr int NA = C::FA == CHK ? 1 : C::FA == ROW ? 2 : 4, NB = C::FB == CHK ? 1 : 2;
    unsigned adA[NA], adB[NB];
    const unsigned lds0 = (unsigned)(size_t)lds, row_sw = lds0 + (unsigned)(l15 * 128 + ((q ^ (lane & 7)) << 4)), chk = lds0 + (unsigned)(q * 2048 + l15 * 16);
#pragma unroll
    for (int i = 0; i < NA; ++i) {
        if constexpr (C::FA == ROW) adA[i] = (row_sw ^ (unsigned)(i * 64)) + (unsigned)(wm * 8192);
        if constexpr (C::FA == CHK) adA[i] = chk + (unsigned)(wm * 1024);
        if constexpr (C::FA == TR) { const int q4 = l15 >> 2; adA[i] = lds0 + (unsigned)((8 * q + q4) * 256 + (((wm * 4 + i) ^ (q4 | ((q & 1) << 2))) << 5) + 8 * (lane & 3)); }
    }
#pragma unroll
    for (int i = 0; i < NB; ++i) adB[i] = C::FB == CHK ? 16384u + chk + (unsigned)(wn * 512) : 16384u + (row_sw ^ (unsigned)(i * 64)) + (unsigned)(wn * 8192);
    constexpr int W = C::FA == TR ? 2 : 1, NR = 4 + 4 * W;
    auto rd = [&](auto ic, Frag (&ra)[4], bf16x8 (&rb)[4], auto kkc) {
        constexpr int kk = decltype(kkc)::value, i = decltype(ic)::value;
        if constexpr (i == 0 || (i > W && i <= W + 3)) { constexpr int n = i == 0 ? 0 : i - W;
            if constexpr (C::FB == CHK) rb[n] = lds_read128<kk * 8192 + (n >> 1) * 1024 + (n & 1) * 256>(adB[0]);
            else rb[n] = lds_read128<n * 2048>(adB[kk]); }
        else { constexpr int j = i <= W ? i - 1 : i - 4, m = j / W;
            if constexpr (C::FA != TR) { bf16x8 v;
                if constexpr (C::FA == CHK) v = lds_read128<kk * 8192 + m * 256>(adA[0]); else v = lds_read128<m * 2048>(adA[kk]);
                const u32x4 w = __builtin_bit_cast(u32x4, v); ra[m].lo = (u32x2){w[0], w[1]}; ra[m].hi = (u32x2){w[2], w[3]}; }
            else if constexpr (j % 2 == 0) ra[m].lo = tr_read<kk * 8192>(adA[m]);
            else ra[m].hi = tr_read<kk * 8192 + 1024>(adA[m]); }
    };
    auto phase = [&](const Frag (&ra)[4], const bf16x8 (&rb)[4], Frag (&na)[4], bf16x8 (&nb)[4], int dstep, auto kkc) {
        static_for<0, 16>([&](auto sc) {
            constexpr int s = decltype(sc)::value, m = s >> 2, n = s & 3;
            if constexpr (n == 0) { wait_lgkm<(3 - m) * W + (s < NR ? s : NR)>(); sched0(); }
            acc[m][n] = MFMA(ra[m].v(), rb[n], acc[m][n]);
            if constexpr (s < NR) rd(sc, na, nb, kkc);
            if constexpr (s >= 12 && decltype(kkc)::value == 1) {
                if constexpr (s - 12 < NA) bump(adA[s - 12], dstep);
                if constexpr (s - 12 < NB) bump(adB[s - 12], dstep);
            }
            sched0();
        });
    };
    Frag a0[4], a1[4]; bf16x8 b0[4], b1[4];
    constexpr std::integral_constant<int, 0> K0{}; constexpr std::integral_constant<int, 1> K1{};
    if constexpr (ID != 1) bar();
    bar();
    STAMP(1);
    static_for<0, NR>([&](auto ic) { rd(ic, a0, b0, K0); });
    sched0();
    const int rx = tm * 128 + tn * 8 + wid * 2 + (lane >> 5), rw = tn * 128 + tm * 8 + wid * 2 + (lane >> 5);
    u32x4 xv[4], wv[4];
    if constexpr (ID == 1) {
#pragma unroll
        for (int i = 0; i < 4; ++i) { xv[i] = __builtin_amdgcn_raw_buffer_load_b128(RSRC(p.x, SEQ * DM * 2), (unsigned)(rx * 2048 + (lane & 31) * 16 + i * 512), 0, 0);
                                      wv[i] = __builtin_amdgcn_raw_buffer_load_b128(RSRC(tn < 8 ? p.wre : p.wim, DM * DM * 2), (unsigned)((rw & 1023) * 2048 + (lane & 31) * 16 + i * 512), 0, 0); }
        sched0();
    }
    auto steps = [&](int t0, int t1) {
#pragma nounroll
        for (int t = t0; t < t1; ++t) {
            phase(a0, b0, a1, b1, (t & 3) == 3 ? -3 * STAGE : STAGE, K1);
            if constexpr (ID == 1) { if (t == LNT1) wait_vm<0>(); }
            bar();
            phase(a1, b1, a0, b0, 0, K0);
        }
    };
    if constexpr (ID == 1) {
        steps(0, LNT0);
        wait_lgkm0(); sched0();
        f32x2 sx = {0.f, 0.f}, sw = sx, sq = sx;
#pragma unroll
        for (int i = 0; i < 16; ++i) { sx += (f32x2){bf_lo(xv[i >> 2][i & 3]), bf_hi(xv[i >> 2][i & 3])}; sw += (f32x2){bf_lo(wv[i >> 2][i & 3]), bf_hi(wv[i >> 2][i & 3])}; }
        const float mean = hsum(sx[0] + sx[1]) * (1.f / DM);
#pragma unroll
        for (int i = 0; i < 16; ++i) { const f32x2 d = (f32x2){bf_lo(xv[i >> 2][i & 3]), bf_hi(xv[i >> 2][i & 3])} - mean; sq += d * d; }
        const float rstd = __builtin_amdgcn_rsqf(hsum(sq[0] + sq[1]) * (1.f / DM) + LN_EPS), wsum = hsum(sw[0] + sw[1]);
        const int j = lane & 31;
        __builtin_amdgcn_raw_buffer_store_b32(__float_as_uint(j == 0 ? mean : j == 1 ? rstd : wsum), RSRC(p.ln1, 3 * SEQ * 4), j < 3 ? (unsigned)((j * SEQ + (j < 2 ? rx : rw)) * 4) : 0x7ffffff0u, 0, SC1);
        sched0();
        steps(LNT0, nk);
    } else steps(0, nk);
    wait_lgkm0(); keep(a0, b0); sched0();
    auto send = [&](int h0) {
        const auto rx = RSRC(p.xch, 256 * 32768);
#pragma unroll
        for (int m = 0; m < 4; ++m)
#pragma unroll
            for (int h = h0; h < 2; ++h) {
                const f32x4 u = acc[m][2 * h], v = acc[m][2 * h + 1];
                __builtin_amdgcn_raw_buffer_store_b128((u32x4){pack_bf16(u[0], u[1]), pack_bf16(u[2], u[3]), pack_bf16(v[0], v[1]), pack_bf16(v[2], v[3])}, rx, xoff + (unsigned)((h0 ? m : m * 2 + h) * 1024 + lane * 16), 0, SC1);
            }
    };
    if constexpr (NH > 0) {
        send(1); sched0();
        auto phaseH = [&](const Frag (&ra)[4], const bf16x8 (&rb)[4], Frag (&na)[4], bf16x8 (&nb)[4], int dstep, auto kkc) {
            static_for<0, 8>([&](auto sc) {
                constexpr int s = decltype(sc)::value;
                if constexpr ((s & 1) == 0) { wait_lgkm<6 - s + (s < 5 ? 2 * s : 10)>(); sched0(); }
                acc[s >> 1][s & 1] = MFMA(ra[s >> 1].v(), rb[s & 1], acc[s >> 1][s & 1]);
                if constexpr (s < 5) { rd(std::integral_constant<int, s < 2 ? 2 * s : 2 * s + 2>{}, na, nb, kkc); rd(std::integral_constant<int, s < 2 ? 2 * s + 1 : 2 * s + 3>{}, na, nb, kkc); }
                if constexpr (s >= 5 && decltype(kkc)::value == 1) { if constexpr (s < 7) { bump(adA[2 * (s - 5)], dstep); bump(adA[2 * (s - 5) + 1], dstep); } else bump(adB[0], dstep); }
                sched0();
            });
        };
#pragma nounroll
        for (int t = nk; t < nk + NH; ++t) {
            phaseH(a0, b0, a1, b1, (t & 3) == 3 ? -3 * STAGE : STAGE, K1);
            bar();
            phaseH(a1, b1, a0, b0, 0, K0);
            if (t == nk + FLAGD) { wait_vm<0>(); if (lane == 0) flag_store(flag, ep); }
        }
        wait_lgkm0();
        asm volatile("" ::"v"(b0[0]), "v"(b0[1]));
#pragma unroll
        for (int i = 0; i < 4; ++i) asm volatile("" ::"v"(a0[i].lo), "v"(a0[i].hi));
        sched0();
    }
    STAMP(2);
    if constexpr (ID == 1) {
        wait_vm<0>(); asm volatile("; pfc %0" ::"v"(pfc));
        bar();
        const auto ro = RSRC(p.abT, ABT_BYTES);
        const float sgn = tn < 8 ? 1.f : -1.f;
        f32x4 mean[4], rs[4]; float ws[4];
        static_for<0, 4>([&](auto ic) { constexpr int i = decltype(ic)::value; const unsigned a = lds0 + (unsigned)(XLDS + wm * 256 + q * 16);
            mean[i] = __builtin_bit_cast(f32x4, lds_read128<i * 64>(a)); rs[i] = __builtin_bit_cast(f32x4, lds_read128<1024 + i * 64>(a));
            ws[i] = lds_read32<2048 + i * 64>(lds0 + (unsigned)(XLDS + wn * 256 + l15 * 4)); });
        wait_lgkm0(); sched0();
#pragma unroll
        for (int m = 0; m < 4; ++m) {
            const f32x4 r = rs[m] * sgn;
#pragma unroll
            for (int n = 0; n < 4; ++n) acc[m][n] = (acc[m][n] - mean[m] * ws[n]) * r;
        }
#pragma unroll
        for (int n = 0; n < 4; ++n) { const int e = (tn & 7) * 128 + wn * 64 + n * 16 + l15;
#pragma unroll
            for (int mp = 0; mp < 2; ++mp) {
                unsigned x0 = pack_bf16(acc[2 * mp][n][0], acc[2 * mp][n][1]), x1 = pack_bf16(acc[2 * mp][n][2], acc[2 * mp][n][3]);
                unsigned y0 = pack_bf16(acc[2 * mp + 1][n][0], acc[2 * mp + 1][n][1]), y1 = pack_bf16(acc[2 * mp + 1][n][2], acc[2 * mp + 1][n][3]);
                swap16u(x0, y0); swap16u(x1, y1);
                const int s = (tn >> 3) * SEQ + col0 + mp * 32;
                __builtin_amdgcn_raw_buffer_store_b128((u32x4){x0, x1, y0, y1}, ro, (unsigned)(((s >> 3) * ABP + e) * 16), 0, STA);
            } }
    } else if (!recv) {
        send(0);
        wait_vm<0>();
        if (lane == 0) flag_store(flag, ep);
        if constexpr (ID == 2) bar();
    } else {
        bar();
        STAMP(4);
        const unsigned px = (unsigned)(size_t)lds + (unsigned)(XLDS + wid * 8192 + lane * 16);
        constexpr int HH = ID == 4 ? 1 : 2;
        u32x4 pw[4 * HH];
        static_for<0, 4 * HH>([&](auto ic) { pw[decltype(ic)::value] = __builtin_bit_cast(u32x4, lds_read128<decltype(ic)::value * 1024>(px)); });
        wait_lgkm0(); sched0();
#pragma unroll
        for (int m = 0; m < 4; ++m)
#pragma unroll
            for (int h = 0; h < HH; ++h) { const u32x4 w = pw[m * HH + h];
                acc[m][2 * h] += (f32x4){bf_lo(w[0]), bf_hi(w[0]), bf_lo(w[1]), bf_hi(w[1])}; acc[m][2 * h + 1] += (f32x4){bf_lo(w[2]), bf_hi(w[2]), bf_lo(w[3]), bf_hi(w[3])}; }
        const auto ro = RSRC(ID == 2 ? p.h : p.out, ID == 2 ? 136 * SEQ * 16 : SEQ * DM * 2);
#pragma unroll
        for (int n = 0; n < 2 * HH; ++n) {
            unsigned o[4][2];
#pragma unroll
            for (int m = 0; m < 4; ++m) { o[m][0] = pack_bf16(acc[m][n][0], acc[m][n][1]); o[m][1] = pack_bf16(acc[m][n][2], acc[m][n][3]); }
#pragma unroll
            for (int mp = 0; mp < 2; ++mp) {
                swap16u(o[2 * mp][0], o[2 * mp + 1][0]); swap16u(o[2 * mp][1], o[2 * mp + 1][1]);
                __builtin_amdgcn_raw_buffer_store_b128((u32x4){o[2 * mp][0], o[2 * mp][1], o[2 * mp + 1][0], o[2 * mp + 1][1]}, ro,
                    (unsigned)(ID == 4 ? ((row0 + n * 16) * DM + col0 + mp * 32) * 2 : ((col0 / 8 + mp * 4) * SEQ + row0 + n * 16) * 16), 0, SC1);
            }
        }
        STAMP(5);
        if constexpr (ID == 2) {
            wait_vm<0>(); bar();
            sto = (unsigned)(((128 + tm) * SEQ + row0 + q * 16) * 16 + wm * 8);
#pragma unroll
            for (int n = 0; n < 4; ++n) {
                const f32x4 s4 = (acc[0][n] + acc[1][n]) + (acc[2][n] + acc[3][n]);
                const float mw = qsum((s4[0] + s4[1]) + (s4[2] + s4[3])) * (1.f / 64.f);
                const f32x4 d0 = acc[0][n] - mw, d1 = acc[1][n] - mw, d2 = acc[2][n] - mw, d3 = acc[3][n] - mw, q4 = (d0 * d0 + d1 * d1) + (d2 * d2 + d3 * d3);
                float m2 = (q4[0] + q4[1]) + (q4[2] + q4[3]);
                m2 = qsum(m2);
                if (q == n) stv = (u32x2){__float_as_uint(mw), __float_as_uint(m2)};
            }
        }
    }
    STAMP_END(ID);
}
#endif

#if defined(__HIP_DEVICE_COMPILE__)
template <int GRP>
__device__ __forceinline__ void gemm3_body(const Params& p, LAS unsigned char* lds, const int wid, u64& ep, u32x2 stv, unsigned sto) {
    constexpr int NK = 16, FA = 7, HB0 = 3 * 32768;
    STAMP_BEGIN;
    const int tid = threadIdx.x, lane = tid & 63;
    const int wm = wid & 1, wn = wid >> 1, l15 = lane & 15, q = lane >> 4;
    const int xcd = blockIdx.x & 7, idx = blockIdx.x >> 3, im = idx & 7, in = idx >> 3;
    const int tm = (xcd & 3) * 8 + im, tn = (xcd >> 2) * 4 + in;
    const G3W gw(p, wid, lane);
    unsigned pf = 0;
    u64* words = p.flags + (F_H + tn * 32) * FLAG_STRIDE;
    if (threadIdx.x == 256 && !(xcd & 1)) flag_store(words + tm * FLAG_STRIDE, ep);
    if (wid != 4) __builtin_amdgcn_s_sleep(2);
    if constexpr (GRP == 1) { const G3W g0(p, wid - 4, lane); g0.early(lds, wid - 4, 0); gw.early(lds, wid, 0); g0.early(lds, wid - 4, 1); gw.early(lds, wid, 1); }
    if ((xcd & 1) && wid == 1) colsum_final(p, lds, ep);
    if (wid == 4) {
        wait_group(words + (lane & 31) * FLAG_STRIDE, (lane & 8) == 0, ep);
        if (blockIdx.x == 0 && lane == 0) flag_store(p.flags, ep);
    }
    bar();
    const unsigned voffB = (unsigned)((wid * SEQ + tn * 256 + lane) * 16);
    struct Src { const void* base; int so; unsigned nrec; };
    auto pin = [](Src x) { asm volatile("" : "+s"(x.base), "+s"(x.so), "+s"(x.nrec)); return x; };
    auto srcW = [&](int t) { return pin(Src{t < NK ? (const void*)gw.ptr : (const void*)(p.csum + GRP * 3584), t < NK ? gw.kstep(t) * 64 * FF * 2 : 0, t < NK ? (unsigned)(DM * FF * 2) : t == NK ? (unsigned)(2 * FF * 2) : 0u}); };
    auto srcH = [&](int t) { Src x{nullptr, (t < NK ? gw.kstep(t) : NK) * 8 * SEQ * 16, 136 * SEQ * 16}; asm volatile("" : "+s"(x.so)); return x; };
    auto stageW = [&](Src x, int slot, int i) { gw.piece(lds, wid, x.base, x.so, x.nrec, slot, i); };
    auto stageH = [&](Src x, int slot, int i) { DMA16_DEV(RSRC(p.h, x.nrec), lds + HB0 + slot * 32768 + wid * 4096 + i * 1024, voffB, x.so + i * 1024); };
#pragma unroll
    for (int t = 0; t < 2; ++t) {
        const Src xh = srcH(t);
#pragma unroll
        for (int i = 0; i < 4; ++i) stageH(xh, t, i);
        if (!W1E && t == 0) gw.early(lds, wid, 1);
    }

    unsigned adA[FA];
    const unsigned lds0 = (unsigned)(size_t)lds;
#pragma unroll
    for (int m = 0; m < FA; ++m) { const int q4 = l15 >> 2, pp = lane & 3, reg = m < 6 ? m & 1 : pp >> 1, ch = m < 6 ? wm * 8 + (m & 6) + (pp >> 1) : 6 + wm;
        adA[m] = lds0 + (unsigned)(reg * 16384 + (8 * q + q4) * 256 + ((ch ^ (((q4 | ((q & 1) << 2)) << 1) | reg)) << 4) + 8 * (pp & 1)); }
    unsigned adB = lds0 + (unsigned)(HB0 + q * 4096 + (wn * 64 + l15) * 16);
    auto rd = [&](auto ic, Frag (&ra)[FA], bf16x8 (&rb)[4], auto kkc) {
        constexpr int kk = decltype(kkc)::value, i = decltype(ic)::value;
        if constexpr (i < 4) rb[i] = lds_read128<kk * 16384 + i * 256>(adB);
        else if constexpr (i % 2 == 0) ra[(i - 4) / 2].lo = tr_read<kk * 8192>(adA[(i - 4) / 2]);
        else ra[(i - 4) / 2].hi = tr_read<kk * 8192 + 1024>(adA[(i - 4) / 2]);
    };
    f32x4 acc[FA][4];
#pragma unroll
    for (int m = 0; m < FA; ++m)
#pragma unroll
        for (int n = 0; n < 4; ++n) acc[m][n] = (f32x4){0.f, 0.f, 0.f, 0.f};
#define DSTR 2
#define LASTP 3
#define PR1 18
#define PR2 6
#define PR3 22

    auto phase = [&](const Frag (&ra)[FA], const bf16x8 (&rb)[4], Frag (&na)[FA], bf16x8 (&nb)[4], int dw, int dh, auto kkc, auto grpc, auto&& dma, auto&& prep) {
        static_for<0, 28>([&](auto sc) {
            constexpr int s = decltype(sc)::value, m = s >> 2, n = s & 3;
            if constexpr (decltype(kkc)::value == 0) { if constexpr (s == 0) __builtin_amdgcn_s_setprio(3); if constexpr (s == PR1) __builtin_amdgcn_s_setprio(2); }
            else { if constexpr (s == PR2) __builtin_amdgcn_s_setprio(1); if constexpr (s == PR3) __builtin_amdgcn_s_setprio(0); }
            acc[m][n] = MFMA(ra[m].v(), rb[n], acc[m][n]);
            sched0();
            if constexpr (s < 18) rd(sc, na, nb, kkc);
            if constexpr (s == 20) prep();
            if constexpr (s % DSTR == 0 && s < 8 * DSTR && decltype(grpc)::value == ((s / DSTR) & 1)) dma(s / (2 * DSTR));
            if constexpr (decltype(kkc)::value == 1 && s >= 18 && s < 26) { if constexpr (s < 25) bump(adA[s - 18], dw); else bump(adB, dh); }
            sched0();
        });
        wait_lgkm0(); sched0();
    };
    Frag a0[FA], a1[FA]; bf16x8 b0[4], b1[4];
    constexpr std::integral_constant<int, 0> K0{}; constexpr std::integral_constant<int, 1> K1{};
    wait_vm<W1E ? 4 : 8>(); bar();
    if constexpr (GRP == 0) __builtin_amdgcn_raw_buffer_store_b64(stv, RSRC(p.h, 136 * SEQ * 16), sto, 0, SC1);
    STAMP(1);
    static_for<0, 18>([&](auto ic) { rd(ic, a0, b0, K0); });
    wait_lgkm0(); sched0();
    const unsigned pfo = (unsigned)(((blockIdx.x * 8 + wid) * 28 + (lane < 28 ? lane : 0)) * 128); const int pft = 1 + idx % 12;
    constexpr std::integral_constant<int, GRP> grpc{};
    Src xw = srcW(2), xh;
    auto phaseA = [&](int t, int sw2) {
        phase(a0, b0, a1, b1, sw2 == 1 ? -65536 : 32768, (t & 1) ? -32768 : 32768, K1, grpc, [&](int i) { stageW(xw, sw2, i);
            if (i == 3) touch(pf, pfo, p.wd, t == pft ? FF * DM * 2 : 0);
        }, [&]() { xh = srcH(t + 2); });
        wait_vm<5>();
    };
    u64 cseen = 0;
    phaseA(0, 2);
    int sw2 = 0;
#pragma nounroll
    for (int t = 0; t < NK - 1; ++t) {
        if (__builtin_expect(t == 1 && threadIdx.x == 0 && !(xcd & 1), 0)) flag_store(p.flags + (F_ST + tn * 32 + tm) * FLAG_STRIDE, ep);
        if (__builtin_expect(t >= 11 && t <= 12 && (wid & 3) == 0, 0)) {
            const int sl = min((tm * 112 >> 6) + (lane & 3), (tm * 112 + 111) >> 6);
            u64* w = p.flags + (lane < 32 ? F_CS + GRP * 56 + sl : F_ST + tn * 32 + (lane & 23)) * FLAG_STRIDE;
            if (t == 11) flag_peek(cseen, w); else if (__any(cseen != ep)) wait_group(w, true, ep);
        }
        bar();
        phase(a1, b1, a0, b0, 0, 0, K0, grpc, [&](int i) { stageH(xh, t & 1, i); }, [&]() { xw = srcW(t + 3); });
        phaseA(t + 1, sw2);
        sw2 = sw2 == 2 ? 0 : sw2 + 1;
    }
    bar();
    __builtin_amdgcn_s_setprio(GRP ? 0 : LASTP);
    f32x4 csv[FA], sp[4][2];
    static_for<0, 16>([&](auto sc) {
        constexpr int s = decltype(sc)::value;
        acc[s >> 2][s & 3] = MFMA(a1[s >> 2].v(), b1[s & 3], acc[s >> 2][s & 3]);
        if constexpr (s < 8) sp[s >> 1][s & 1] = __builtin_bit_cast(f32x4, lds_read128<(s & 1) * 16384 + (s >> 1) * 256>(adB));
        else if constexpr (s < 15) { constexpr int m = s - 8; const int reg = m < 6 ? m & 1 : q >> 1, c4 = m < 6 ? wm * 2 + (m & 6) * 2 + q : (wm ? 0 : 12) + (q & 1);
            csv[m] = __builtin_bit_cast(f32x4, lds_read128<32768>(lds0 + (unsigned)(reg * 16384 + wm * 256 + ((c4 ^ ((wm << 1) | reg)) << 4)))); }
        if constexpr (GRP == 1 && s == 2) g128_load<4, 0>(p, lds, wid, ep);
        sched0();
    });
    wait_lgkm0(); wait_vm<GRP ? 4 : 0>(); asm volatile("; pf %0" ::"v"(pf)); sched0();
    float mean[4], c1[4], ve[4];
#pragma unroll
    for (int n = 0; n < 4; ++n) {
        const f32x4 s0 = sp[n][0], s1 = sp[n][1];
        mean[n] = qsum((s0[0] + s0[2]) + (s1[0] + s1[2])) * (1.f / 16.f);
        const float d0 = s0[0] - mean[n], d1 = s0[2] - mean[n], d2 = s1[0] - mean[n], d3 = s1[2] - mean[n];
        const float m2 = qsum((s0[1] + s0[3]) + (s1[1] + s1[3]) + 64.f * ((d0 * d0 + d1 * d1) + (d2 * d2 + d3 * d3)));
        ve[n] = m2 * (1.f / DM) + LN_EPS; c1[n] = -1.44269504f * __builtin_amdgcn_rsqf(ve[n]);
    }
#pragma unroll
    for (int s = 16; s < 28; ++s) acc[s >> 2][s & 3] = MFMA(a1[s >> 2].v(), b1[s & 3], acc[s >> 2][s & 3]);
#pragma unroll
    for (int s = 16; s < 28; ++s) { __builtin_amdgcn_sched_group_barrier(0x008, 1, 0); __builtin_amdgcn_sched_group_barrier(0x002, 10, 0); }
    sched0();
    STAMP(2);
    const auto ro = RSRC(p.act, SEQ * FF * 2);
    auto silu2 = [&](f32x2 g, f32x2 u, int n) {
        const f32x2 x = g * c1[n], d = (f32x2){__builtin_amdgcn_exp2f(x[0]), __builtin_amdgcn_exp2f(x[1])} * ve[n] + ve[n];
        const f32x2 r = g * u * (f32x2){__builtin_amdgcn_rcpf(d[0]), __builtin_amdgcn_rcpf(d[1])};
        return pack_bf16(r[0], r[1]);
    };
#pragma unroll
    for (int j = 0; j < 3; ++j)
#pragma unroll
        for (int np = 0; np < 2; ++np) {
            unsigned o[2][2];
#pragma unroll
            for (int i = 0; i < 2; ++i) { const int n = 2 * np + i; const f32x4 g = acc[2 * j][n] - mean[n] * csv[2 * j], u = acc[2 * j + 1][n] - mean[n] * csv[2 * j + 1];
                o[i][0] = silu2((f32x2){g[0], g[1]}, (f32x2){u[0], u[1]}, n); o[i][1] = silu2((f32x2){g[2], g[3]}, (f32x2){u[2], u[3]}, n); }
            swap16u(o[0][0], o[1][0]); swap16u(o[0][1], o[1][1]);
            __builtin_amdgcn_raw_buffer_store_b128((u32x4){o[0][0], o[0][1], o[1][0], o[1][1]}, ro, (unsigned)(((tm * 14 + wm * 8 + 2 * j + (q >> 1)) * SEQ + tn * 256 + wn * 64 + np * 32 + (lane & 31)) * 16), 0, STA);
        }
    {
        unsigned o[4];
#pragma unroll
        for (int n = 0; n < 4; ++n) {
            const f32x4 v = acc[6][n] - mean[n] * csv[6];
            float g0 = v[0], g1 = v[1], u0 = v[2], u1 = v[3];
            swap32(g0, u0); swap32(g1, u1);
            o[n] = silu2((f32x2){g0, g1}, (f32x2){u0, u1}, n);
        }
        swap16u(o[0], o[1]); swap16u(o[2], o[3]); swap32u(o[0], o[2]); swap32u(o[1], o[3]);
        __builtin_amdgcn_raw_buffer_store_b128((u32x4){o[0], o[2], o[1], o[3]}, ro, (unsigned)(((tm * 14 + 6 + wm) * SEQ + tn * 256 + wn * 64 + lane) * 16), 0, STA);
    }
    STAMP(5);
    __builtin_amdgcn_s_setprio(0);
    if constexpr (GRP == 1) { wait_vm<0>(); bar(); }
    STAMP_END(3);
}
#endif

__global__ __launch_bounds__(512, 2) void fused_k(const Params p) {
#if defined(__HIP_DEVICE_COMPILE__)
    __shared__ __attribute__((aligned(16))) unsigned char lds_raw[5 * 32768];
    LAS unsigned char* lds = (LAS unsigned char*)lds_raw;
    const int wid = __builtin_amdgcn_readfirstlane(threadIdx.x >> 6);
    u64 ep = 0;
    u32x2 stv = {0u, 0u}; unsigned sto = 0x7ffffff0u;
    if (wid < 4) { g128_compute<1>(p, lds, wid, ep, stv, sto); g128_compute<2>(p, lds, wid, ep, stv, sto); gemm3_body<0>(p, lds, wid, ep, stv, sto); g128_compute<4>(p, lds, wid, ep, stv, sto); }
    else { g128_load<1, -1>(p, lds, wid, ep); g128_load<2, -1>(p, lds, wid, ep); gemm3_body<1>(p, lds, wid, ep, stv, sto); g128_load<4, -1>(p, lds, wid, ep); }
#endif
}
__global__ void tail_k() {}

template <int ID, int I> __global__ void echo_k(const Params p) {
#if defined(__HIP_DEVICE_COMPILE__)
    const unsigned long long* o = (const unsigned long long*)((char*)p.xch + (9u << 20));
    const unsigned long long t0 = __builtin_amdgcn_s_memrealtime();
    unsigned long long d = ID ? o[((ID - 1) * 256 + 66) * 16 + 8 + I] - o[66 * 16 + 8] : 0;
    if (d > 20000) d = 20000;
    while (__builtin_amdgcn_s_memrealtime() - t0 < d) __builtin_amdgcn_s_sleep(1);
#endif
}
extern "C" void kernel_launch(void* const* d_in, const int* in_sizes, int n_in, void* d_out, int out_size, void* d_ws, size_t ws_size, hipStream_t stream) {
    Params p;
    p.x = (const u16*)d_in[0]; p.cre = (const u16*)d_in[1]; p.cim = (const u16*)d_in[2]; p.wre = (const u16*)d_in[3]; p.wim = (const u16*)d_in[4];
    p.wg = (const u16*)d_in[6]; p.wu = (const u16*)d_in[7]; p.wd = (const u16*)d_in[8];
    p.out = (u16*)d_out;
    char* ws = (char*)d_ws;
    p.abT = (u16*)(ws + (ABP == 1024 ? 0u : 48u << 20)); p.h = (u16*)(ws + (8u << 20)); p.act = (u16*)(ws + (16u << 20));
    p.xch = (u16*)(ws + (32u << 20)); p.flags = (unsigned long long*)(ws + (40u << 20)); p.csum = (float*)(ws + (40u << 20) + (512u << 10)); p.ln1 = (float*)(ws + (40u << 20) + (768u << 10));
    fused_k<<<256, 512, 0, stream>>>(p);
    echo_k<0, 0><<<1, 64, 0, stream>>>(p);
}
```

```cpp
#include <hip/hip_runtime.h>
#include <hip/hip_bf16.h>
#include <type_traits>
#define STAMPS

#define LAS __attribute__((address_space(3)))
typedef unsigned short u16;
typedef __bf16 bf16x8 __attribute__((ext_vector_type(8)));
typedef __bf16 bf16x2 __attribute__((ext_vector_type(2)));
typedef float f32x4 __attribute__((ext_vector_type(4)));
typedef float f32x2 __attribute__((ext_vector_type(2)));
typedef unsigned u32x4 __attribute__((ext_vector_type(4)));
typedef unsigned u32x2 __attribute__((ext_vector_type(2)));

constexpr int SEQ = 2048, DM = 1024, FF = 3584;
#define ABP 1024
constexpr int ABT_BYTES = 512 * ABP * 16;
#define STA 16
constexpr float LN_EPS = 1e-5f;
constexpr int SC1 = 16;

struct Params {
    const u16* x; const u16* cre; const u16* cim; const u16* wre; const u16* wim;
    const u16* wg; const u16* wu; const u16* wd;
    u16* out; u16* abT; u16* h; u16* act; u16* xch; unsigned long long* flags; float* csum; float* ln1;
};

template <int N> __device__ __forceinline__ void wait_vm() { asm volatile("s_waitcnt vmcnt(%0)" ::"n"(N) : "memory"); }
__device__ __forceinline__ void wait_lgkm0() { __builtin_amdgcn_s_waitcnt(0xC07F); asm volatile("s_waitcnt lgkmcnt(0)" ::: "memory"); }
__device__ __forceinline__ void sched0() { __builtin_amdgcn_sched_barrier(0); }
__device__ __forceinline__ void bar() { sched0(); asm volatile("" ::: "memory"); __builtin_amdgcn_s_barrier(); asm volatile("" ::: "memory"); sched0(); }
__device__ __forceinline__ unsigned pack_bf16(float lo, float hi) { bf16x2 c; c[0] = (__bf16)lo; c[1] = (__bf16)hi; return __builtin_bit_cast(unsigned, c); }
__device__ __forceinline__ float bf_lo(unsigned v) { return __uint_as_float(v << 16); }
__device__ __forceinline__ float bf_hi(unsigned v) { return __uint_as_float(v & 0xffff0000u); }
__device__ __forceinline__ void swap16(float& a, float& b) { auto r = __builtin_amdgcn_permlane16_swap(__float_as_uint(a), __float_as_uint(b), false, false); a = __uint_as_float(r[0]); b = __uint_as_float(r[1]); }
__device__ __forceinline__ void swap32(float& a, float& b) { auto r = __builtin_amdgcn_permlane32_swap(__float_as_uint(a), __float_as_uint(b), false, false); a = __uint_as_float(r[0]); b = __uint_as_float(r[1]); }
__device__ __forceinline__ void swap16u(unsigned& a, unsigned& b) { auto r = __builtin_amdgcn_permlane16_swap(a, b, false, false); a = r[0]; b = r[1]; }
__device__ __forceinline__ void swap32u(unsigned& a, unsigned& b) { auto r = __builtin_amdgcn_permlane32_swap(a, b, false, false); a = r[0]; b = r[1]; }
__device__ __forceinline__ float qsum(float v) { float a = v, b = v; swap16(a, b); a += b; b = a; swap32(a, b); return a + b; }

template <int CTRL> __device__ __forceinline__ float dpp_add(float v) { return v + __int_as_float(__builtin_amdgcn_update_dpp(0, __float_as_int(v), CTRL, 0xf, 0xf, false)); }
__device__ __forceinline__ float hsum(float v) { v = dpp_add<0x128>(v); v = dpp_add<0x124>(v); v = dpp_add<0x122>(v); v = dpp_add<0x121>(v); float b = v; swap16(v, b); return v + b; }

struct Frag { u32x2 lo, hi; __device__ __forceinline__ bf16x8 v() const { return __builtin_bit_cast(bf16x8, __builtin_shufflevector(lo, hi, 0, 1, 2, 3)); } };
template <int OFF> __device__ __forceinline__ u32x2 tr_read(unsigned addr) { u32x2 r; asm volatile("ds_read_b64_tr_b16 %0, %1 offset:%2" : "=v"(r) : "v"(addr), "n"(OFF) : "memory"); return r; }
template <int OFF> __device__ __forceinline__ bf16x8 lds_read128(unsigned addr) { bf16x8 r; asm volatile("ds_read_b128 %0, %1 offset:%2" : "=v"(r) : "v"(addr), "n"(OFF) : "memory"); return r; }
template <int OFF> __device__ __forceinline__ float lds_read32(unsigned addr) { float r; asm volatile("ds_read_b32 %0, %1 offset:%2" : "=v"(r) : "v"(addr), "n"(OFF) : "memory"); return r; }
template <int N> __device__ __forceinline__ void wait_lgkm() { asm volatile("s_waitcnt lgkmcnt(%0)" ::"n"(N) : "memory"); }
template <int I, int N, class F> __device__ __forceinline__ void static_for(F&& f) { if constexpr (I < N) { f(std::integral_constant<int, I>{}); static_for<I + 1, N>(f); } }
template <int NA> __device__ __forceinline__ void keep(const Frag (&a)[NA], const bf16x8 (&b)[4]) {
#pragma unroll
    for (int i = 0; i < NA; ++i) asm volatile("" ::"v"(a[i].lo), "v"(a[i].hi));
#pragma unroll
    for (int i = 0; i < 4; ++i) asm volatile("" ::"v"(b[i]));
}
__device__ __forceinline__ void bump(unsigned& a, int d) { asm volatile("v_add_u32 %0, %1, %0" : "+v"(a) : "s"(d)); }
#define MFMA(a, b, c) __builtin_amdgcn_mfma_f32_16x16x32_bf16(a, b, c, 0, 0, 0)
#define RSRC(ptr, bytes) __builtin_amdgcn_make_buffer_rsrc((void*)(ptr), 0, (bytes), 0x00020000)
#define DMA16(rs, ldsp, voff, soff) __builtin_amdgcn_raw_ptr_buffer_load_lds(rs, (LAS void*)(ldsp), 16, voff, soff, 0, 0)
#define DMA16_DEV(rs, ldsp, voff, soff) __builtin_amdgcn_raw_ptr_buffer_load_lds(rs, (LAS void*)(ldsp), 16, voff, soff, 0, SC1)

#define STW 7
#define STAMP(i) do { sched0(); ts[i] = __builtin_amdgcn_s_memtime(); rt[i] = __builtin_amdgcn_s_memrealtime(); __builtin_amdgcn_s_waitcnt(0xC07F); sched0(); } while (0)
#define STAMP_BEGIN unsigned long long ts[6] = {}, rt[6] = {}; STAMP(0)
#define STAMP_END(ID) do { if (ID == 4) wait_vm<0>(); STAMP(3); if (threadIdx.x == (ID == 3 ? STW * 64 : (STW & 3) * 64)) { unsigned long long* o = (unsigned long long*)((char*)p.xch + (9u << 20)) + ((ID - 1) * 256 + blockIdx.x) * 16; \
        for (int i_ = 0; i_ < 6; ++i_) { o[i_] = ts[i_]; o[8 + i_] = rt[i_]; } } } while (0)

typedef unsigned long long u64;
#define FSTR 8
constexpr int FLAG_STRIDE = FSTR, F_PAIR = 1, F_AB = F_PAIR + 1536, F_H = F_AB + 256, F_ACT = F_H + 256, F_CS = F_ACT + 256, F_LN = F_CS + 128, F_ST = F_LN + 256;
#define PSCOPE __HIP_MEMORY_SCOPE_AGENT
#define PSLEEP 2
__device__ __forceinline__ u64 flag_load(u64* w) { return __hip_atomic_load(w, __ATOMIC_RELAXED, PSCOPE); }
__device__ __forceinline__ void flag_store(u64* w, u64 v) { __hip_atomic_store(w, v, __ATOMIC_RELAXED, PSCOPE); }
__device__ __forceinline__ u64 uniform64(u64 v) { return ((u64)(unsigned)__builtin_amdgcn_readfirstlane((unsigned)(v >> 32)) << 32) | (unsigned)__builtin_amdgcn_readfirstlane((unsigned)v); }
__device__ __forceinline__ void wait_group(u64* word, bool need, u64 ep) {
#pragma nounroll
    for (int spin = 0; spin < (1 << 20); ++spin) {
        if (!__any(need && flag_load(word) != ep)) break;
        __builtin_amdgcn_s_sleep(PSLEEP);
    }
}
#if defined(__HIP_DEVICE_COMPILE__)
__device__ __forceinline__ void touch(unsigned& sink, unsigned voff, const void* base, unsigned bytes) {
    asm volatile("s_nop 4\n\tbuffer_load_dword %0, %1, %2, 0 offen" : "+v"(sink) : "v"(voff), "s"(RSRC(base, bytes)) : "memory");
}
__device__ __forceinline__ void flag_peek(u64& v, const u64* w) { asm volatile("global_load_dwordx2 %0, %1, off sc1" : "+v"(v) : "v"(w) : "memory"); }
struct G3W {
    const u16* ptr; unsigned voff[2]; int phi, koff;
    __device__ __forceinline__ G3W(const Params& p, int wid, int lane) {
        const int xcd = blockIdx.x & 7, idx = blockIdx.x >> 3, tm = (xcd & 3) * 8 + (idx & 7), q = lane >> 4;
        ptr = wid < 4 ? p.wg : p.wu; phi = ((idx & 7) + (idx >> 3)) & 3; koff = xcd * 2;
#pragma unroll
        for (int i = 0; i < 2; ++i) { const int j = (lane & 15) ^ (((q | (i << 2)) << 1) | (wid >> 2));
            voff[i] = j < 14 ? (unsigned)(((wid & 3) * 16 + q) * FF * 2 + tm * 224 + j * 16) : 0x7ffffff0u; }
    }
    __device__ __forceinline__ int kstep(int t) const { return (t + koff + ((t & 3) == phi ? 4 : 0)) & 15; }
    __device__ __forceinline__ void piece(LAS unsigned char* lds, int wid, const void* base, int so, unsigned nrec, int slot, int i) const { DMA16_DEV(RSRC(base, nrec), lds + slot * 32768 + wid * 4096 + i * 1024, voff[i >> 1], so + i * 4 * FF * 2); }
    __device__ __forceinline__ void early(LAS unsigned char* lds, int wid, int t) const {
#pragma unroll
        for (int i = 0; i < 4; ++i) piece(lds, wid, ptr, kstep(t) * 64 * FF * 2, DM * FF * 2, t, i);
    }
};
#define CSW 4
#define CSD 0
__device__ __forceinline__ void colsum_partial(const Params& p, LAS unsigned char* lds, int lw) {
    const int lane = threadIdx.x & 63, slab = blockIdx.x >> 1, mat = slab >= 56, c0 = (slab - mat * 56) * 64;
    const auto rw = RSRC(mat ? p.wu : p.wg, slab < 112 ? DM * FF * 2 : 0);
    const unsigned vo = (unsigned)(((lw * 256 + (lane >> 3)) * FF + c0 + (lane & 7) * 8) * 2);
    u32x4 v[8]; f32x4 s0 = {0.f, 0.f, 0.f, 0.f}, s1 = s0;
    auto add = [&](const u32x4 w) { s0 += (f32x4){bf_lo(w[0]), bf_hi(w[0]), bf_lo(w[1]), bf_hi(w[1])}; s1 += (f32x4){bf_lo(w[2]), bf_hi(w[2]), bf_lo(w[3]), bf_hi(w[3])}; };
    if (CSD) __builtin_amdgcn_s_sleep(CSD);
#pragma unroll
    for (int i = 0; i < CSW; ++i) v[i] = __builtin_amdgcn_raw_buffer_load_b128(rw, vo, i * 8 * FF * 2, 0);
#pragma unroll
    for (int i = 0; i < 8; ++i) { add(v[i % CSW]); v[i % CSW] = __builtin_amdgcn_raw_buffer_load_b128(rw, vo, (CSW + i) * 8 * FF * 2, 0); sched0(); }
#pragma unroll
    for (int i = CSW; i < 8; ++i) v[i] = __builtin_amdgcn_raw_buffer_load_b128(rw, vo, (8 + i) * 8 * FF * 2, 0);
#pragma nounroll
    for (int j = 16; j < 40; j += 8)
#pragma unroll
        for (int i = 0; i < 8; ++i) { add(v[i]); v[i] = __builtin_amdgcn_raw_buffer_load_b128(rw, j < 32 ? vo : 0x7ffffff0u, (j + i) * 8 * FF * 2, 0); sched0(); }
    LAS float* tab = (LAS float*)(lds + 131072) + (lw * 8 + (lane >> 3)) * 64 + (lane & 7) * 8;
    *(LAS f32x4*)tab = s0; *(LAS f32x4*)(tab + 4) = s1;
    wait_lgkm0();
}
__device__ __forceinline__ void colsum_final(const Params& p, LAS unsigned char* lds, u64 ep) {
    const int lane = threadIdx.x & 63, slab = blockIdx.x >> 1, mat = slab >= 56, c0 = (slab - mat * 56) * 64;
    if (slab >= 112) return;
    const LAS float* tab = (const LAS float*)(lds + 131072);
    float a = 0.f, b = 0.f;
#pragma unroll
    for (int i = 0; i < 32; i += 2) { a += tab[i * 64 + lane]; b += tab[(i + 1) * 64 + lane]; }
    const int f = c0 + lane, t = f / 112, r = f - t * 112, h = r >= 56;
    __hip_atomic_store(p.csum + (mat * 2 + h) * 1792 + t * 56 + r - h * 56, a + b, __ATOMIC_RELAXED, __HIP_MEMORY_SCOPE_AGENT);
    wait_vm<0>();
    if (lane == 0) flag_store(p.flags + (F_CS + slab) * FLAG_STRIDE, ep);
}
#endif

enum { ROW, CHK, TR };
#define NKS2 27
#define FLAGD 1
#define W1E 1
#define LNT0 1
#define LNT1 5
#define NPRE2 1
#define G1PRE 1
#define RE4 1
#define RO4 3
template <int ID> struct Cfg;
template <> struct Cfg<1> { static constexpr int NKT = 16, NKS = 16, FA = ROW, FB = ROW, LDB = DM; };
template <> struct Cfg<2> { static constexpr int NKT = 64, NKS = NKS2, FA = CHK, FB = ROW, LDB = SEQ; };
template <> struct Cfg<4> { static constexpr int NKT = 56, NKS = 0, FA = TR, FB = CHK, LDB = 0; };

#if defined(__HIP_DEVICE_COMPILE__)
template <int ID, int PRE>
__device__ __forceinline__ void g128_load(const Params& p, LAS unsigned char* lds, const int wid, u64& ep) {
    using C = Cfg<ID>;
    constexpr int STAGE = 32768, XLDS = 4 * STAGE;
    const int lane = threadIdx.x & 63, l15 = lane & 15, q = lane >> 4;
    const int xcd = blockIdx.x & 7, idx = blockIdx.x >> 3, im = idx & 3, in = idx >> 2;
    constexpr int NH = ID == 4 ? 2 * (RE4 + RO4) : 0;
    const int ks = (xcd & 1) ^ (ID == 2); const bool recv = ID == 4 || (ID == 2 && ks);
    const int tm = (ID == 1 ? (xcd & 3) : ((xcd >> 1) & 1)) * 4 + im, tn = (xcd >> 2) * 8 + in;
    const int k0 = ID != 1 && ks ? C::NKS : 0, nk = ID == 4 ? 28 - 2 * (ks ? RO4 : RE4) : ID == 2 && ks ? C::NKT - C::NKS : C::NKS;
    const int xb = ID == 4 ? 128 + (blockIdx.x ^ (wid >> 2)) : blockIdx.x >> 1;
    u64* const flag = p.flags + (F_PAIR + xb * 4 + (wid & 3)) * FLAG_STRIDE;
    const unsigned xoff = (unsigned)(ID == 4 ? (128 + xb) * 16384 + (wid & 3) * 4096 : xb * 32768 + (wid & 3) * 8192);
    if constexpr (ID != 1 && PRE < 0) {
        if (wid == 4 && lane == 0) flag_store(p.flags + (ID == 2 ? F_AB + (tn & 7) * 32 + (tn >> 3) * 16 + (xcd & 3) * 4 + im : F_ACT + (tn >> 1) * 32 + (xcd & 3) * 8 + (idx & 7)) * FLAG_STRIDE, ep);
        sched0();
    }
    const int lw = wid - 4, phi = (im + in) & 3;
    const int koff = ID == 1 ? xcd * 2 : ((xcd >> (ID == 2 ? 1 : 2)) & 1) * (nk >> 1);
    const unsigned ksw = (unsigned)(((lane & 7) ^ (lane >> 3)) << 4);
    unsigned voffA[2], voffB;
    if constexpr (C::FA == ROW) voffA[0] = (unsigned)((tm * 128 + lw * 32 + (lane >> 3)) * DM * 2) + ksw;
    if constexpr (C::FA == CHK) voffA[0] = (unsigned)((lw * 2 * ABP + tm * 128 + lane) * 16);
    if constexpr (C::FA == TR) {
#pragma unroll
        for (int i = 0; i < 2; ++i) voffA[i] = (unsigned)((lw * 16 + q) * DM * 2 + tm * 256 + ((l15 ^ ((q | (i << 2)) << 1)) << 4));
    }
    if constexpr (C::FB == ROW) voffB = (unsigned)((((ID == 1 ? tn & 7 : tn)) * 128 + lw * 32 + (lane >> 3)) * C::LDB * 2) + ksw;
    else voffB = (unsigned)((lw * 2 * SEQ + tn * 128 + lane) * 16);
    const auto ra = ID == 1 ? RSRC(p.x, SEQ * DM * 2) : ID == 2 ? RSRC(p.abT, ABT_BYTES) : RSRC(p.wd, FF * DM * 2);
    auto kpos = [&](int t) {
        const int u1 = t - 1, ul = ((nk - 1 - phi) & ~3) + phi;
        int k = ID == 1 ? ((t & 3) != phi ? t : t + 4 < nk ? t + 4 : phi) : t < phi || u1 >= ul ? t : t == phi ? phi + 4 : (u1 & 3) == phi && u1 > phi ? u1 + 4 : u1;
        k += koff; k = k >= nk ? k - nk : k;
        if constexpr (ID == 4) {
            k = ks * 14 + (k < (nk >> 1) ? k : k + 28 - (nk >> 1));
            int u = t - nk + phi; u = u >= NH ? u - NH : u; const int h = u >= RE4 + RO4, v = u - h * (RE4 + RO4);
            return t < nk ? k : h * 28 + (v < RE4 ? 14 - RE4 + v : 28 - RO4 - RE4 + v);
        } else return k + k0;
    };
    const int ktab = kpos(lane);
    struct Pos { int sa, sb, lb; const void* pb; };
    auto prep = [&](int t) {
        const int k = ID == 1 || PRE >= 0 ? kpos(t) : __builtin_amdgcn_readlane(ktab, t);
        Pos x{C::FA == ROW ? k * 128 : C::FA == CHK ? k * 8 * ABP * 16 : k * 64 * DM * 2, C::FB == ROW ? (k & 31) * 128 : k * 8 * SEQ * 16, (t & 3) * STAGE + lw * 4096,
              ID == 1 ? (const void*)(tn < 8 ? p.wre : p.wim) : ID == 2 ? (const void*)(k < 32 ? p.cre : p.cim) : (const void*)p.act};
        asm volatile("" : "+s"(x.sa), "+s"(x.sb), "+s"(x.lb)); if constexpr (ID == 2) asm volatile("" : "+s"(x.pb));
        return x;
    };
    auto issue = [&](const Pos& x, bool doA, bool doB, bool half) {
        const auto rb = RSRC(x.pb, ID == 1 ? DM * DM * 2 : ID == 2 ? SEQ * SEQ * 2 : SEQ * FF * 2);
        LAS unsigned char* base = lds + x.lb;
#pragma unroll
        for (int i = 0; i < 4; ++i) {
            if (doA) {
                if constexpr (C::FA == ROW) DMA16(ra, base + i * 1024, voffA[0], x.sa + i * 8 * DM * 2);
                if constexpr (C::FA == CHK) DMA16_DEV(ra, base + i * 1024, voffA[0], x.sa + (i >> 1) * ABP * 16 + (i & 1) * 1024);
                if constexpr (C::FA == TR) DMA16(ra, base + i * 1024, voffA[i >> 1], x.sa + i * 4 * DM * 2);
            }
            if (doB) {
                if constexpr (C::FB == ROW) DMA16(rb, base + 16384 + i * 1024, voffB, x.sb + i * 8 * C::LDB * 2);
                else if (!(half && (i & 1))) DMA16_DEV(rb, base + 16384 + i * 1024, voffB, x.sb + (i >> 1) * SEQ * 16 + ((i & 1) ^ ks) * 1024);
            }
        }
    };
    auto stage = [&](int t, bool doA, bool doB, bool half = false) { issue(prep(t), doA, doB, half); };
    if constexpr (PRE >= 0) { stage(PRE, ID == 4, ID == 2); return; }
    if constexpr (ID != 1) {
        u64* words = p.flags + (ID == 2 ? F_AB + tm * 32 : F_ACT + (tn >> 1) * 32) * FLAG_STRIDE;
        if constexpr (ID == 4) { if (wid != 4) __builtin_amdgcn_s_sleep(2); stage(1, true, false); stage(2, true, false); }
        if constexpr (ID == 2 && NPRE2 < 3) { if (wid != 4) __builtin_amdgcn_s_sleep(2); if (NPRE2 < 2) stage(1, false, true); stage(2, false, true); }
        if (wid == 4) { const int j = lane & 31; wait_group(words + j * FLAG_STRIDE, ID == 2 ? 2 * j + 2 > k0 && 2 * j < k0 + nk : ((j >> 3) & 1) == ks, ep); }
        bar();
        stage(0, ID == 2, ID == 4); stage(1, ID == 2, ID == 4); stage(2, ID == 2, ID == 4);
        wait_vm<8>(); bar();
        wait_vm<4>();
    } else {
        stage(0, true, true); stage(1, true, true);
        wait_vm<8>(); bar();
        stage(2, true, true);
        const unsigned a = (unsigned)(size_t)lds + XLDS;
        const float e0 = lds_read32<3072>(a), e1 = lds_read32<3076>(a);
        wait_lgkm0(); sched0();
        ep = uniform64(((u64)__float_as_uint(e1) << 32) | __float_as_uint(e0));
    }
    u64 pseen = 0; u64* const pwords = p.flags + (F_ACT + (tn >> 1) * 32 + (lane & 31)) * FLAG_STRIDE;
#pragma nounroll
    for (int t = 0; t < nk - 3; ++t) {
        Pos x; if constexpr (ID != 1 || G1PRE) x = prep(t + 3);
        wait_vm<8>();
        if constexpr (ID == 4) { if (t == nk - 4 && wid == 4 && __any(pseen != ep)) wait_group(pwords, true, ep); }
        bar();
        if constexpr (ID == 1 && !G1PRE) x = prep(t + 3);
        issue(x, true, true, false);
        if constexpr (ID == 4) { if (t == nk - 6 && wid == 4) flag_peek(pseen, pwords); }
        if constexpr (ID == 1) { if (t == LNT1 && wid == 4 && lane == 0) flag_store(p.flags + (F_LN + tm * 16 + tn) * FLAG_STRIDE, ep); }
    }
    if constexpr (NH > 0) {
        wait_vm<8>(); bar(); stage(nk, true, true, true);
#pragma nounroll
        for (int t = nk - 2; t < nk + NH - 3; ++t) { const Pos x = prep(t + 3); wait_vm<6>(); bar(); issue(x, true, true, true); }
    }
    bool got = !recv;
    auto fetch = [&](u64 seen) {
        if (uniform64(seen) != ep) return false;
        asm volatile("" ::: "memory");
#pragma unroll
        for (int i = 0; i < (ID == 4 ? 4 : 8); ++i) DMA16_DEV(RSRC(p.xch, 256 * 32768), lds + XLDS + lw * 8192 + i * 1024, (unsigned)(lane * 16), xoff + i * 1024);
        return true;
    };
    wait_vm<NH ? 6 : 8>(); bar();
    u64* const lnw = p.flags + (F_LN + (lane & 16 ? l15 * 16 + tn : tm * 16 + l15)) * FLAG_STRIDE;
    u64 seen0 = flag_load(ID == 1 ? lnw : flag);
    wait_vm<0>();
    if (!got) got = fetch(seen0);
    if constexpr (ID == 1) { if (wid == 4) {
        if (__any(seen0 != ep)) wait_group(lnw, true, ep);
        asm volatile("" ::: "memory");
#pragma unroll
        for (int i = 0; i < 3; ++i) DMA16_DEV(RSRC(p.ln1, 3 * SEQ * 4), lds + XLDS + i * 1024, lane < 32 ? (unsigned)(lane * 16) : 0x7ffffff0u, (i * SEQ + (i < 2 ? tm : tn) * 128) * 4);
    } }
    if constexpr (ID == 1) g128_load<2, 0>(p, lds, wid, ep);
    bar();
    if constexpr (ID != 1) seen0 = flag_load(flag);
    if constexpr (ID == 2) { if (!got) got = fetch(seen0); }
    if constexpr (ID == 1 && NPRE2 > 1) g128_load<2, 1>(p, lds, wid, ep);
    bar();
    if (!got) got = fetch(seen0);
    if constexpr (ID == 1 && NPRE2 > 2) g128_load<2, 2>(p, lds, wid, ep);
    if (recv) {
#pragma nounroll
        for (int spin = 0; !got && spin < (1 << 20); ++spin) { got = fetch(flag_load(flag)); __builtin_amdgcn_s_sleep(1); }
        wait_vm<0>(); bar();
    }
    if constexpr (ID == 2) { if (!recv) colsum_partial(p, lds, lw); }
    if constexpr (ID == 1) { wait_vm<4 * NPRE2>(); bar(); }
    if constexpr (ID != 4) bar();
}

template <int ID>
__device__ __forceinline__ void g128_compute(const Params& p, LAS unsigned char* lds, const int wid, u64& ep, u32x2& stv, unsigned& sto) {
    using C = Cfg<ID>;
    constexpr int STAGE = 32768, XLDS = 4 * STAGE;
    const int lane = threadIdx.x & 63, l15 = lane & 15, q = lane >> 4;
    const int xcd = blockIdx.x & 7, idx = blockIdx.x >> 3, im = idx & 3, in = idx >> 2;
    constexpr int NH = ID == 4 ? 2 * (RE4 + RO4) : 0;
    const int ks = (xcd & 1) ^ (ID == 2); const bool recv = ID == 4 || (ID == 2 && ks);
    const int tm = (ID == 1 ? (xcd & 3) : ((xcd >> 1) & 1)) * 4 + im, tn = (xcd >> 2) * 8 + in;
    const int k0 = ID != 1 && ks ? C::NKS : 0, nk = ID == 4 ? 28 - 2 * (ks ? RO4 : RE4) : ID == 2 && ks ? C::NKT - C::NKS : C::NKS;
    const int xb = ID == 4 ? 128 + (blockIdx.x ^ (wid >> 2)) : blockIdx.x >> 1;
    u64* const flag = p.flags + (F_PAIR + xb * 4 + (wid & 3)) * FLAG_STRIDE;
    const unsigned xoff = (unsigned)(ID == 4 ? (128 + xb) * 16384 + (wid & 3) * 4096 : xb * 32768 + (wid & 3) * 8192);
    STAMP_BEGIN;
    const int wm = wid & 1, wn = wid >> 1;
    const int eo = (q & 1) * 16 + (q >> 1) * 8;
    f32x4 acc[4][4];
#pragma unroll
    for (int m = 0; m < 4; ++m)
#pragma unroll
        for (int n = 0; n < 4; ++n) acc[m][n] = (f32x4){0.f, 0.f, 0.f, 0.f};
    const int row0 = tn * 128 + (ID == 4 ? ks * 64 + wn * 32 : wn * 64) + l15, col0 = tm * 128 + wm * 64 + eo;
    if constexpr (ID != 1) {
        constexpr int NN = ID == 4 ? 2 : 4;
        if (ID == 4 || !recv) {
            if constexpr (ID == 2) { wait_vm<0>(); bar(); __builtin_amdgcn_s_sleep(4); }
            u32x4 r[NN][2];
#pragma unroll
            for (int n = 0; n < NN; ++n)
#pragma unroll
                for (int mp = 0; mp < 2; ++mp)
                    r[n][mp] = __builtin_amdgcn_raw_buffer_load_b128(RSRC(ID == 2 ? p.x : p.h, SEQ * DM * 2), (unsigned)(ID == 2 ? ((row0 + n * 16) * DM + col0 + mp * 32) * 2 : ((col0 / 8 + mp * 4) * SEQ + row0 + n * 16) * 16), 0, ID == 4 ? SC1 : 0);
            if constexpr (ID == 4) { wait_vm<0>(); bar(); }
#pragma unroll
            for (int n = 0; n < NN; ++n)
#pragma unroll
                for (int mp = 0; mp < 2; ++mp) {
                    unsigned r0 = r[n][mp][0], r1 = r[n][mp][1], r2 = r[n][mp][2], r3 = r[n][mp][3];
                    swap16u(r0, r2); swap16u(r1, r3);
                    acc[2 * mp][n] = (f32x4){bf_lo(r0), bf_hi(r0), bf_lo(r1), bf_hi(r1)}; acc[2 * mp + 1][n] = (f32x4){bf_lo(r2), bf_hi(r2), bf_lo(r3), bf_hi(r3)};
                }
        } else { wait_vm<0>(); bar(); }
    }
    unsigned pfc = 0;
    if constexpr (ID == 1) {
        ep = uniform64(flag_load(p.flags)) + 1;
        if (threadIdx.x == 0) *(LAS u64*)(lds + XLDS + 3072) = ep;
        wait_lgkm0();
        if (idx == 0) {
            unsigned long long pc; int lo, hi;
            asm volatile("s_getpc_b64 %0\n s_mov_b32 %1, _Z7fused_k6Params@rel32@lo+4\n s_mov_b32 %2, _Z6tail_kv@rel32@lo+12" : "=s"(pc), "=s"(lo), "=s"(hi));
            touch(pfc, threadIdx.x * 128, (const void*)(pc + (long long)lo), (unsigned)max(hi - lo, 0)); touch(pfc, threadIdx.x * 128 + 32768, (const void*)(pc + (long long)lo), (unsigned)max(hi - lo, 0));
        }
    }
    constexpr int NA = C::FA == CHK ? 1 : C::FA == ROW ? 2 : 4, NB = C::FB == CHK ? 1 : 2;
    unsigned adA[NA], adB[NB];
    const unsigned lds0 = (unsigned)(size_t)lds, row_sw = lds0 + (unsigned)(l15 * 128 + ((q ^ (lane & 7)) << 4)), chk = lds0 + (unsigned)(q * 2048 + l15 * 16);
#pragma unroll
    for (int i = 0; i < NA; ++i) {
        if constexpr (C::FA == ROW) adA[i] = (row_sw ^ (unsigned)(i * 64)) + (unsigned)(wm * 8192);
        if constexpr (C::FA == CHK) adA[i] = chk + (unsigned)(wm * 1024);
        if constexpr (C::FA == TR) { const int q4 = l15 >> 2; adA[i] = lds0 + (unsigned)((8 * q + q4) * 256 + (((wm * 4 + i) ^ (q4 | ((q & 1) << 2))) << 5) + 8 * (lane & 3)); }
    }
#pragma unroll
    for (int i = 0; i < NB; ++i) adB[i] = C::FB == CHK ? 16384u + chk + (unsigned)(wn * 512) : 16384u + (row_sw ^ (unsigned)(i * 64)) + (unsigned)(wn * 8192);
    constexpr int W = C::FA == TR ? 2 : 1, NR = 4 + 4 * W;
    auto rd = [&](auto ic, Frag (&ra)[4], bf16x8 (&rb)[4], auto kkc) {
        constexpr int kk = decltype(kkc)::value, i = decltype(ic)::value;
        if constexpr (i == 0 || (i > W && i <= W + 3)) { constexpr int n = i == 0 ? 0 : i - W;
            if constexpr (C::FB == CHK) rb[n] = lds_read128<kk * 8192 + (n >> 1) * 1024 + (n & 1) * 256>(adB[0]);
            else rb[n] = lds_read128<n * 2048>(adB[kk]); }
        else { constexpr int j = i <= W ? i - 1 : i - 4, m = j / W;
            if constexpr (C::FA != TR) { bf16x8 v;
                if constexpr (C::FA == CHK) v = lds_read128<kk * 8192 + m * 256>(adA[0]); else v = lds_read128<m * 2048>(adA[kk]);
                const u32x4 w = __builtin_bit_cast(u32x4, v); ra[m].lo = (u32x2){w[0], w[1]}; ra[m].hi = (u32x2){w[2], w[3]}; }
            else if constexpr (j % 2 == 0) ra[m].lo = tr_read<kk * 8192>(adA[m]);
            else ra[m].hi = tr_read<kk * 8192 + 1024>(adA[m]); }
    };
    auto phase = [&](const Frag (&ra)[4], const bf16x8 (&rb)[4], Frag (&na)[4], bf16x8 (&nb)[4], int dstep, auto kkc) {
        static_for<0, 16>([&](auto sc) {
            constexpr int s = decltype(sc)::value, m = s >> 2, n = s & 3;
            if constexpr (n == 0) { wait_lgkm<(3 - m) * W + (s < NR ? s : NR)>(); sched0(); }
            acc[m][n] = MFMA(ra[m].v(), rb[n], acc[m][n]);
            if constexpr (s < NR) rd(sc, na, nb, kkc);
            if constexpr (s >= 12 && decltype(kkc)::value == 1) {
                if constexpr (s - 12 < NA) bump(adA[s - 12], dstep);
                if constexpr (s - 12 < NB) bump(adB[s - 12], dstep);
            }
            sched0();
        });
    };
    Frag a0[4], a1[4]; bf16x8 b0[4], b1[4];
    constexpr std::integral_constant<int, 0> K0{}; constexpr std::integral_constant<int, 1> K1{};
    if constexpr (ID != 1) bar();
    bar();
    STAMP(1);
    static_for<0, NR>([&](auto ic) { rd(ic, a0, b0, K0); });
    sched0();
    const int rx = tm * 128 + tn * 8 + wid * 2 + (lane >> 5), rw = tn * 128 + tm * 8 + wid * 2 + (lane >> 5);
    u32x4 xv[4], wv[4];
    if constexpr (ID == 1) {
#pragma unroll
        for (int i = 0; i < 4; ++i) { xv[i] = __builtin_amdgcn_raw_buffer_load_b128(RSRC(p.x, SEQ * DM * 2), (unsigned)(rx * 2048 + (lane & 31) * 16 + i * 512), 0, 0);
                                      wv[i] = __builtin_amdgcn_raw_buffer_load_b128(RSRC(tn < 8 ? p.wre : p.wim, DM * DM * 2), (unsigned)((rw & 1023) * 2048 + (lane & 31) * 16 + i * 512), 0, 0); }
        sched0();
    }
    auto steps = [&](int t0, int t1) {
#pragma nounroll
        for (int t = t0; t < t1; ++t) {
            phase(a0, b0, a1, b1, (t & 3) == 3 ? -3 * STAGE : STAGE, K1);
            if constexpr (ID == 1) { if (t == LNT1) wait_vm<0>(); }
            bar();
            phase(a1, b1, a0, b0, 0, K0);
        }
    };
    if constexpr (ID == 1) {
        steps(0, LNT0);
        wait_lgkm0(); sched0();
        f32x2 sx = {0.f, 0.f}, sw = sx, sq = sx;
#pragma unroll
        for (int i = 0; i < 16; ++i) { sx += (f32x2){bf_lo(xv[i >> 2][i & 3]), bf_hi(xv[i >> 2][i & 3])}; sw += (f32x2){bf_lo(wv[i >> 2][i & 3]), bf_hi(wv[i >> 2][i & 3])}; }
        const float mean = hsum(sx[0] + sx[1]) * (1.f / DM);
#pragma unroll
        for (int i = 0; i < 16; ++i) { const f32x2 d = (f32x2){bf_lo(xv[i >> 2][i & 3]), bf_hi(xv[i >> 2][i & 3])} - mean; sq += d * d; }
        const float rstd = __builtin_amdgcn_rsqf(hsum(sq[0] + sq[1]) * (1.f / DM) + LN_EPS), wsum = hsum(sw[0] + sw[1]);
        const int j = lane & 31;
        __builtin_amdgcn_raw_buffer_store_b32(__float_as_uint(j == 0 ? mean : j == 1 ? rstd : wsum), RSRC(p.ln1, 3 * SEQ * 4), j < 3 ? (unsigned)((j * SEQ + (j < 2 ? rx : rw)) * 4) : 0x7ffffff0u, 0, SC1);
        sched0();
        steps(LNT0, nk);
    } else steps(0, nk);
    wait_lgkm0(); keep(a0, b0); sched0();
    auto send = [&](int h0) {
        const auto rx = RSRC(p.xch, 256 * 32768);
#pragma unroll
        for (int m = 0; m < 4; ++m)
#pragma unroll
            for (int h = h0; h < 2; ++h) {
                const f32x4 u = acc[m][2 * h], v = acc[m][2 * h + 1];
                __builtin_amdgcn_raw_buffer_store_b128((u32x4){pack_bf16(u[0], u[1]), pack_bf16(u[2], u[3]), pack_bf16(v[0], v[1]), pack_bf16(v[2], v[3])}, rx, xoff + (unsigned)((h0 ? m : m * 2 + h) * 1024 + lane * 16), 0, SC1);
            }
    };
    if constexpr (NH > 0) {
        send(1); sched0();
        auto phaseH = [&](const Frag (&ra)[4], const bf16x8 (&rb)[4], Frag (&na)[4], bf16x8 (&nb)[4], int dstep, auto kkc) {
            static_for<0, 8>([&](auto sc) {
                constexpr int s = decltype(sc)::value;
                if constexpr ((s & 1) == 0) { wait_lgkm<6 - s + (s < 5 ? 2 * s : 10)>(); sched0(); }
                acc[s >> 1][s & 1] = MFMA(ra[s >> 1].v(), rb[s & 1], acc[s >> 1][s & 1]);
                if constexpr (s < 5) { rd(std::integral_constant<int, s < 2 ? 2 * s : 2 * s + 2>{}, na, nb, kkc); rd(std::integral_constant<int, s < 2 ? 2 * s + 1 : 2 * s + 3>{}, na, nb, kkc); }
                if constexpr (s >= 5 && decltype(kkc)::value == 1) { if constexpr (s < 7) { bump(adA[2 * (s - 5)], dstep); bump(adA[2 * (s - 5) + 1], dstep); } else bump(adB[0], dstep); }
                sched0();
            });
        };
#pragma nounroll
        for (int t = nk; t < nk + NH; ++t) {
            phaseH(a0, b0, a1, b1, (t & 3) == 3 ? -3 * STAGE : STAGE, K1);
            bar();
            phaseH(a1, b1, a0, b0, 0, K0);
            if (t == nk + FLAGD) { wait_vm<0>(); if (lane == 0) flag_store(flag, ep); }
        }
        wait_lgkm0();
        asm volatile("" ::"v"(b0[0]), "v"(b0[1]));
#pragma unroll
        for (int i = 0; i < 4; ++i) asm volatile("" ::"v"(a0[i].lo), "v"(a0[i].hi));
        sched0();
    }
    STAMP(2);
    if constexpr (ID == 1) {
        wait_vm<0>(); asm volatile("; pfc %0" ::"v"(pfc));
        bar();
        const auto ro = RSRC(p.abT, ABT_BYTES);
        const float sgn = tn < 8 ? 1.f : -1.f;
        f32x4 mean[4], rs[4]; float ws[4];
        static_for<0, 4>([&](auto ic) { constexpr int i = decltype(ic)::value; const unsigned a = lds0 + (unsigned)(XLDS + wm * 256 + q * 16);
            mean[i] = __builtin_bit_cast(f32x4, lds_read128<i * 64>(a)); rs[i] = __builtin_bit_cast(f32x4, lds_read128<1024 + i * 64>(a));
            ws[i] = lds_read32<2048 + i * 64>(lds0 + (unsigned)(XLDS + wn * 256 + l15 * 4)); });
        wait_lgkm0(); sched0();
#pragma unroll
        for (int m = 0; m < 4; ++m) {
            const f32x4 r = rs[m] * sgn;
#pragma unroll
            for (int n = 0; n < 4; ++n) acc[m][n] = (acc[m][n] - mean[m] * ws[n]) * r;
        }
#pragma unroll
        for (int n = 0; n < 4; ++n) { const int e = (tn & 7) * 128 + wn * 64 + n * 16 + l15;
#pragma unroll
            for (int mp = 0; mp < 2; ++mp) {
                unsigned x0 = pack_bf16(acc[2 * mp][n][0], acc[2 * mp][n][1]), x1 = pack_bf16(acc[2 * mp][n][2], acc[2 * mp][n][3]);
                unsigned y0 = pack_bf16(acc[2 * mp + 1][n][0], acc[2 * mp + 1][n][1]), y1 = pack_bf16(acc[2 * mp + 1][n][2], acc[2 * mp + 1][n][3]);
                swap16u(x0, y0); swap16u(x1, y1);
                const int s = (tn >> 3) * SEQ + col0 + mp * 32;
                __builtin_amdgcn_raw_buffer_store_b128((u32x4){x0, x1, y0, y1}, ro, (unsigned)(((s >> 3) * ABP + e) * 16), 0, STA);
            } }
    } else if (!recv) {
        send(0);
        wait_vm<0>();
        if (lane == 0) flag_store(flag, ep);
        if constexpr (ID == 2) bar();
    } else {
        bar();
        STAMP(4);
        const unsigned px = (unsigned)(size_t)lds + (unsigned)(XLDS + wid * 8192 + lane * 16);
        constexpr int HH = ID == 4 ? 1 : 2;
        u32x4 pw[4 * HH];
        static_for<0, 4 * HH>([&](auto ic) { pw[decltype(ic)::value] = __builtin_bit_cast(u32x4, lds_read128<decltype(ic)::value * 1024>(px)); });
        wait_lgkm0(); sched0();
#pragma unroll
        for (int m = 0; m < 4; ++m)
#pragma unroll
            for (int h = 0; h < HH; ++h) { const u32x4 w = pw[m * HH + h];
                acc[m][2 * h] += (f32x4){bf_lo(w[0]), bf_hi(w[0]), bf_lo(w[1]), bf_hi(w[1])}; acc[m][2 * h + 1] += (f32x4){bf_lo(w[2]), bf_hi(w[2]), bf_lo(w[3]), bf_hi(w[3])}; }
        const auto ro = RSRC(ID == 2 ? p.h : p.out, ID == 2 ? 136 * SEQ * 16 : SEQ * DM * 2);
#pragma unroll
        for (int n = 0; n < 2 * HH; ++n) {
            unsigned o[4][2];
#pragma unroll
            for (int m = 0; m < 4; ++m) { o[m][0] = pack_bf16(acc[m][n][0], acc[m][n][1]); o[m][1] = pack_bf16(acc[m][n][2], acc[m][n][3]); }
#pragma unroll
            for (int mp = 0; mp < 2; ++mp) {
                swap16u(o[2 * mp][0], o[2 * mp + 1][0]); swap16u(o[2 * mp][1], o[2 * mp + 1][1]);
                __builtin_amdgcn_raw_buffer_store_b128((u32x4){o[2 * mp][0], o[2 * mp][1], o[2 * mp + 1][0], o[2 * mp + 1][1]}, ro,
                    (unsigned)(ID == 4 ? ((row0 + n * 16) * DM + col0 + mp * 32) * 2 : ((col0 / 8 + mp * 4) * SEQ + row0 + n * 16) * 16), 0, SC1);
            }
        }
        STAMP(5);
        if constexpr (ID == 2) {
            wait_vm<0>(); bar();
            sto = (unsigned)(((128 + tm) * SEQ + row0 + q * 16) * 16 + wm * 8);
#pragma unroll
            for (int n = 0; n < 4; ++n) {
                const f32x4 s4 = (acc[0][n] + acc[1][n]) + (acc[2][n] + acc[3][n]);
                const float mw = qsum((s4[0] + s4[1]) + (s4[2] + s4[3])) * (1.f / 64.f);
                const f32x4 d0 = acc[0][n] - mw, d1 = acc[1][n] - mw, d2 = acc[2][n] - mw, d3 = acc[3][n] - mw, q4 = (d0 * d0 + d1 * d1) + (d2 * d2 + d3 * d3);
                float m2 = (q4[0] + q4[1]) + (q4[2] + q4[3]);
                m2 = qsum(m2);
                if (q == n) stv = (u32x2){__float_as_uint(mw), __float_as_uint(m2)};
            }
        }
    }
    STAMP_END(ID);
}
#endif

#if defined(__HIP_DEVICE_COMPILE__)
template <int GRP>
__device__ __forceinline__ void gemm3_body(const Params& p, LAS unsigned char* lds, const int wid, u64& ep, u32x2 stv, unsigned sto) {
    constexpr int NK = 16, FA = 7, HB0 = 3 * 32768;
    STAMP_BEGIN;
    const int tid = threadIdx.x, lane = tid & 63;
    const int wm = wid & 1, wn = wid >> 1, l15 = lane & 15, q = lane >> 4;
    const int xcd = blockIdx.x & 7, idx = blockIdx.x >> 3, im = idx & 7, in = idx >> 3;
    const int tm = (xcd & 3) * 8 + im, tn = (xcd >> 2) * 4 + in;
    const G3W gw(p, wid, lane);
    unsigned pf = 0;
    u64* words = p.flags + (F_H + tn * 32) * FLAG_STRIDE;
    if (threadIdx.x == 256 && !(xcd & 1)) flag_store(words + tm * FLAG_STRIDE, ep);
    if (wid != 4) __builtin_amdgcn_s_sleep(2);
    if constexpr (GRP == 1) { const G3W g0(p, wid - 4, lane); g0.early(lds, wid - 4, 0); gw.early(lds, wid, 0); g0.early(lds, wid - 4, 1); gw.early(lds, wid, 1); }
    if ((xcd & 1) && wid == 1) colsum_final(p, lds, ep);
    if (wid == 4) {
        wait_group(words + (lane & 31) * FLAG_STRIDE, (lane & 8) == 0, ep);
        if (blockIdx.x == 0 && lane == 0) flag_store(p.flags, ep);
    }
    bar();
    const unsigned voffB = (unsigned)((wid * SEQ + tn * 256 + lane) * 16);
    struct Src { const void* base; int so; unsigned nrec; };
    auto pin = [](Src x) { asm volatile("" : "+s"(x.base), "+s"(x.so), "+s"(x.nrec)); return x; };
    auto srcW = [&](int t) { return pin(Src{t < NK ? (const void*)gw.ptr : (const void*)(p.csum + GRP * 3584), t < NK ? gw.kstep(t) * 64 * FF * 2 : 0, t < NK ? (unsigned)(DM * FF * 2) : t == NK ? (unsigned)(2 * FF * 2) : 0u}); };
    auto srcH = [&](int t) { Src x{nullptr, (t < NK ? gw.kstep(t) : NK) * 8 * SEQ * 16, 136 * SEQ * 16}; asm volatile("" : "+s"(x.so)); return x; };
    auto stageW = [&](Src x, int slot, int i) { gw.piece(lds, wid, x.base, x.so, x.nrec, slot, i); };
    auto stageH = [&](Src x, int slot, int i) { DMA16_DEV(RSRC(p.h, x.nrec), lds + HB0 + slot * 32768 + wid * 4096 + i * 1024, voffB, x.so + i * 1024); };
#pragma unroll
    for (int t = 0; t < 2; ++t) {
        const Src xh = srcH(t);
#pragma unroll
        for (int i = 0; i < 4; ++i) stageH(xh, t, i);
        if (!W1E && t == 0) gw.early(lds, wid, 1);
    }

    unsigned adA[FA];
    const unsigned lds0 = (unsigned)(size_t)lds;
#pragma unroll
    for (int m = 0; m < FA; ++m) { const int q4 = l15 >> 2, pp = lane & 3, reg = m < 6 ? m & 1 : pp >> 1, ch = m < 6 ? wm * 8 + (m & 6) + (pp >> 1) : 6 + wm;
        adA[m] = lds0 + (unsigned)(reg * 16384 + (8 * q + q4) * 256 + ((ch ^ (((q4 | ((q & 1) << 2)) << 1) | reg)) << 4) + 8 * (pp & 1)); }
    unsigned adB = lds0 + (unsigned)(HB0 + q * 4096 + (wn * 64 + l15) * 16);
    auto rd = [&](auto ic, Frag (&ra)[FA], bf16x8 (&rb)[4], auto kkc) {
        constexpr int kk = decltype(kkc)::value, i = decltype(ic)::value;
        if constexpr (i < 4) rb[i] = lds_read128<kk * 16384 + i * 256>(adB);
        else if constexpr (i % 2 == 0) ra[(i - 4) / 2].lo = tr_read<kk * 8192>(adA[(i - 4) / 2]);
        else ra[(i - 4) / 2].hi = tr_read<kk * 8192 + 1024>(adA[(i - 4) / 2]);
    };
    f32x4 acc[FA][4];
#pragma unroll
    for (int m = 0; m < FA; ++m)
#pragma unroll
        for (int n = 0; n < 4; ++n) acc[m][n] = (f32x4){0.f, 0.f, 0.f, 0.f};
#define DSTR 2
#define LASTP 3
#define PR1 18
#define PR2 6
#define PR3 22

    auto phase = [&](const Frag (&ra)[FA], const bf16x8 (&rb)[4], Frag (&na)[FA], bf16x8 (&nb)[4], int dw, int dh, auto kkc, auto grpc, auto&& dma, auto&& prep) {
        static_for<0, 28>([&](auto sc) {
            constexpr int s = decltype(sc)::value, m = s >> 2, n = s & 3;
            if constexpr (decltype(kkc)::value == 0) { if constexpr (s == 0) __builtin_amdgcn_s_setprio(3); if constexpr (s == PR1) __builtin_amdgcn_s_setprio(2); }
            else { if constexpr (s == PR2) __builtin_amdgcn_s_setprio(1); if constexpr (s == PR3) __builtin_amdgcn_s_setprio(0); }
            acc[m][n] = MFMA(ra[m].v(), rb[n], acc[m][n]);
            sched0();
            if constexpr (s < 18) rd(sc, na, nb, kkc);
            if constexpr (s == 20) prep();
            if constexpr (s % DSTR == 0 && s < 8 * DSTR && decltype(grpc)::value == ((s / DSTR) & 1)) dma(s / (2 * DSTR));
            if constexpr (decltype(kkc)::value == 1 && s >= 18 && s < 26) { if constexpr (s < 25) bump(adA[s - 18], dw); else bump(adB, dh); }
            sched0();
        });
        wait_lgkm0(); sched0();
    };
    Frag a0[FA], a1[FA]; bf16x8 b0[4], b1[4];
    constexpr std::integral_constant<int, 0> K0{}; constexpr std::integral_constant<int, 1> K1{};
    wait_vm<W1E ? 4 : 8>(); bar();
    if constexpr (GRP == 0) __builtin_amdgcn_raw_buffer_store_b64(stv, RSRC(p.h, 136 * SEQ * 16), sto, 0, SC1);
    STAMP(1);
    static_for<0, 18>([&](auto ic) { rd(ic, a0, b0, K0); });
    wait_lgkm0(); sched0();
    const unsigned pfo = (unsigned)(((blockIdx.x * 8 + wid) * 28 + (lane < 28 ? lane : 0)) * 128); const int pft = 1 + idx % 12;
    constexpr std::integral_constant<int, GRP> grpc{};
    Src xw = srcW(2), xh;
    auto phaseA = [&](int t, int sw2) {
        phase(a0, b0, a1, b1, sw2 == 1 ? -65536 : 32768, (t & 1) ? -32768 : 32768, K1, grpc, [&](int i) { stageW(xw, sw2, i);
            if (i == 3) touch(pf, pfo, p.wd, t == pft ? FF * DM * 2 : 0);
        }, [&]() { xh = srcH(t + 2); });
        wait_vm<5>();
    };
    u64 cseen = 0;
    phaseA(0, 2);
    int sw2 = 0;
#pragma nounroll
    for (int t = 0; t < NK - 1; ++t) {
        if (__builtin_expect(t == 1 && threadIdx.x == 0 && !(xcd & 1), 0)) flag_store(p.flags + (F_ST + tn * 32 + tm) * FLAG_STRIDE, ep);
        if (__builtin_expect(t >= 11 && t <= 12 && (wid & 3) == 0, 0)) {
            const int sl = min((tm * 112 >> 6) + (lane & 3), (tm * 112 + 111) >> 6);
            u64* w = p.flags + (lane < 32 ? F_CS + GRP * 56 + sl : F_ST + tn * 32 + (lane & 23)) * FLAG_STRIDE;
            if (t == 11) flag_peek(cseen, w); else if (__any(cseen != ep)) wait_group(w, true, ep);
        }
        bar();
        phase(a1, b1, a0, b0, 0, 0, K0, grpc, [&](int i) { stageH(xh, t & 1, i); }, [&]() { xw = srcW(t + 3); });
        phaseA(t + 1, sw2);
        sw2 = sw2 == 2 ? 0 : sw2 + 1;
    }
    bar();
    __builtin_amdgcn_s_setprio(GRP ? 0 : LASTP);
    f32x4 csv[FA], sp[4][2];
    static_for<0, 16>([&](auto sc) {
        constexpr int s = decltype(sc)::value;
        acc[s >> 2][s & 3] = MFMA(a1[s >> 2].v(), b1[s & 3], acc[s >> 2][s & 3]);
        if constexpr (s < 8) sp[s >> 1][s & 1] = __builtin_bit_cast(f32x4, lds_read128<(s & 1) * 16384 + (s >> 1) * 256>(adB));
        else if constexpr (s < 15) { constexpr int m = s - 8; const int reg = m < 6 ? m & 1 : q >> 1, c4 = m < 6 ? wm * 2 + (m & 6) * 2 + q : (wm ? 0 : 12) + (q & 1);
            csv[m] = __builtin_bit_cast(f32x4, lds_read128<32768>(lds0 + (unsigned)(reg * 16384 + wm * 256 + ((c4 ^ ((wm << 1) | reg)) << 4)))); }
        if constexpr (GRP == 1 && s == 2) g128_load<4, 0>(p, lds, wid, ep);
        sched0();
    });
    wait_lgkm0(); wait_vm<GRP ? 4 : 0>(); asm volatile("; pf %0" ::"v"(pf)); sched0();
    float mean[4], c1[4], ve[4];
#pragma unroll
    for (int n = 0; n < 4; ++n) {
        const f32x4 s0 = sp[n][0], s1 = sp[n][1];
        mean[n] = qsum((s0[0] + s0[2]) + (s1[0] + s1[2])) * (1.f / 16.f);
        const float d0 = s0[0] - mean[n], d1 = s0[2] - mean[n], d2 = s1[0] - mean[n], d3 = s1[2] - mean[n];
        const float m2 = qsum((s0[1] + s0[3]) + (s1[1] + s1[3]) + 64.f * ((d0 * d0 + d1 * d1) + (d2 * d2 + d3 * d3)));
        ve[n] = m2 * (1.f / DM) + LN_EPS; c1[n] = -1.44269504f * __builtin_amdgcn_rsqf(ve[n]);
    }
#pragma unroll
    for (int s = 16; s < 28; ++s) acc[s >> 2][s & 3] = MFMA(a1[s >> 2].v(), b1[s & 3], acc[s >> 2][s & 3]);
#pragma unroll
    for (int s = 16; s < 28; ++s) { __builtin_amdgcn_sched_group_barrier(0x008, 1, 0); __builtin_amdgcn_sched_group_barrier(0x002, 10, 0); }
    sched0();
    STAMP(2);
    const auto ro = RSRC(p.act, SEQ * FF * 2);
    auto silu2 = [&](f32x2 g, f32x2 u, int n) {
        const f32x2 x = g * c1[n], d = (f32x2){__builtin_amdgcn_exp2f(x[0]), __builtin_amdgcn_exp2f(x[1])} * ve[n] + ve[n];
        const f32x2 r = g * u * (f32x2){__builtin_amdgcn_rcpf(d[0]), __builtin_amdgcn_rcpf(d[1])};
        return pack_bf16(r[0], r[1]);
    };
#pragma unroll
    for (int j = 0; j < 3; ++j)
#pragma unroll
        for (int np = 0; np < 2; ++np) {
            unsigned o[2][2];
#pragma unroll
            for (int i = 0; i < 2; ++i) { const int n = 2 * np + i; const f32x4 g = acc[2 * j][n] - mean[n] * csv[2 * j], u = acc[2 * j + 1][n] - mean[n] * csv[2 * j + 1];
                o[i][0] = silu2((f32x2){g[0], g[1]}, (f32x2){u[0], u[1]}, n); o[i][1] = silu2((f32x2){g[2], g[3]}, (f32x2){u[2], u[3]}, n); }
            swap16u(o[0][0], o[1][0]); swap16u(o[0][1], o[1][1]);
            __builtin_amdgcn_raw_buffer_store_b128((u32x4){o[0][0], o[0][1], o[1][0], o[1][1]}, ro, (unsigned)(((tm * 14 + wm * 8 + 2 * j + (q >> 1)) * SEQ + tn * 256 + wn * 64 + np * 32 + (lane & 31)) * 16), 0, STA);
        }
    {
        unsigned o[4];
#pragma unroll
        for (int n = 0; n < 4; ++n) {
            const f32x4 v = acc[6][n] - mean[n] * csv[6];
            float g0 = v[0], g1 = v[1], u0 = v[2], u1 = v[3];
            swap32(g0, u0); swap32(g1, u1);
            o[n] = silu2((f32x2){g0, g1}, (f32x2){u0, u1}, n);
        }
        swap16u(o[0], o[1]); swap16u(o[2], o[3]); swap32u(o[0], o[2]); swap32u(o[1], o[3]);
        __builtin_amdgcn_raw_buffer_store_b128((u32x4){o[0], o[2], o[1], o[3]}, ro, (unsigned)(((tm * 14 + 6 + wm) * SEQ + tn * 256 + wn * 64 + lane) * 16), 0, STA);
    }
    STAMP(5);
    __builtin_amdgcn_s_setprio(0);
    if constexpr (GRP == 1) { wait_vm<0>(); bar(); }
    STAMP_END(3);
}
#endif

__global__ __launch_bounds__(512, 2) void fused_k(const Params p) {
#if defined(__HIP_DEVICE_COMPILE__)
    __shared__ __attribute__((aligned(16))) unsigned char lds_raw[5 * 32768];
    LAS unsigned char* lds = (LAS unsigned char*)lds_raw;
    const int wid = __builtin_amdgcn_readfirstlane(threadIdx.x >> 6);
    u64 ep = 0;
    u32x2 stv = {0u, 0u}; unsigned sto = 0x7ffffff0u;
    if (wid < 4) { g128_compute<1>(p, lds, wid, ep, stv, sto); g128_compute<2>(p, lds, wid, ep, stv, sto); gemm3_body<0>(p, lds, wid, ep, stv, sto); g128_compute<4>(p, lds, wid, ep, stv, sto); }
    else { g128_load<1, -1>(p, lds, wid, ep); g128_load<2, -1>(p, lds, wid, ep); gemm3_body<1>(p, lds, wid, ep, stv, sto); g128_load<4, -1>(p, lds, wid, ep); }
#endif
}
__global__ void tail_k() {}

__global__ void mark_k(const Params p) {
#if defined(__HIP_DEVICE_COMPILE__)
    if (threadIdx.x == 0) ((unsigned long long*)((char*)p.xch + (11u << 20)))[0] = __builtin_amdgcn_s_memrealtime();
#endif
}
template <int M> __global__ void echo_k(const Params p) {
#if defined(__HIP_DEVICE_COMPILE__)
    const unsigned long long* o = (const unsigned long long*)((char*)p.xch + (9u << 20));
    const unsigned long long mk = ((const unsigned long long*)((char*)p.xch + (11u << 20)))[0];
    const unsigned long long t0 = __builtin_amdgcn_s_memrealtime();
    unsigned long long d = 1000 + (M == 1 ? 4 * (o[66 * 16 + 8] - mk) : M == 2 ? 4 * (t0 - o[(768 + 66) * 16 + 11]) : M == 3 ? 4 * (o[66 * 16 + 9] - o[66 * 16 + 8]) : M == 4 ? o[(768 + 66) * 16 + 11] - o[66 * 16 + 8] : 0);
    if (d > 20000) d = 20000;
    while (__builtin_amdgcn_s_memrealtime() - t0 < d) __builtin_amdgcn_s_sleep(1);
#endif
}
extern "C" void kernel_launch(void* const* d_in, const int* in_sizes, int n_in, void* d_out, int out_size, void* d_ws, size_t ws_size, hipStream_t stream) {
    Params p;
    p.x = (const u16*)d_in[0]; p.cre = (const u16*)d_in[1]; p.cim = (const u16*)d_in[2]; p.wre = (const u16*)d_in[3]; p.wim = (const u16*)d_in[4];
    p.wg = (const u16*)d_in[6]; p.wu = (const u16*)d_in[7]; p.wd = (const u16*)d_in[8];
    p.out = (u16*)d_out;
    char* ws = (char*)d_ws;
    p.abT = (u16*)(ws + (ABP == 1024 ? 0u : 48u << 20)); p.h = (u16*)(ws + (8u << 20)); p.act = (u16*)(ws + (16u << 20));
    p.xch = (u16*)(ws + (32u << 20)); p.flags = (unsigned long long*)(ws + (40u << 20)); p.csum = (float*)(ws + (40u << 20) + (512u << 10)); p.ln1 = (float*)(ws + (40u << 20) + (768u << 10));
    mark_k<<<1, 64, 0, stream>>>(p);
    fused_k<<<256, 512, 0, stream>>>(p);
    echo_k<0><<<1, 64, 0, stream>>>(p);
}
```

```cpp
#include <hip/hip_runtime.h>
#include <hip/hip_bf16.h>
#include <type_traits>

#define LAS __attribute__((address_space(3)))
typedef unsigned short u16;
typedef __bf16 bf16x8 __attribute__((ext_vector_type(8)));
typedef __bf16 bf16x2 __attribute__((ext_vector_type(2)));
typedef float f32x4 __attribute__((ext_vector_type(4)));
typedef float f32x2 __attribute__((ext_vector_type(2)));
typedef unsigned u32x4 __attribute__((ext_vector_type(4)));
typedef unsigned u32x2 __attribute__((ext_vector_type(2)));

constexpr int SEQ = 2048, DM = 1024, FF = 3584;
constexpr float LN_EPS = 1e-5f;
constexpr int SC1 = 16;

struct Params {
    const u16* x; const u16* cre; const u16* cim; const u16* wre; const u16* wim;
    const u16* wg; const u16* wu; const u16* wd;
    u16* out; u16* abT; u16* h; u16* act; u16* xch; unsigned long long* flags; float* csum; float* ln1;
};

template <int N> __device__ __forceinline__ void wait_vm() { asm volatile("s_waitcnt vmcnt(%0)" ::"n"(N) : "memory"); }
__device__ __forceinline__ void wait_lgkm0() { __builtin_amdgcn_s_waitcnt(0xC07F); asm volatile("s_waitcnt lgkmcnt(0)" ::: "memory"); }
__device__ __forceinline__ void sched0() { __builtin_amdgcn_sched_barrier(0); }
__device__ __forceinline__ void bar() { sched0(); asm volatile("" ::: "memory"); __builtin_amdgcn_s_barrier(); asm volatile("" ::: "memory"); sched0(); }
__device__ __forceinline__ unsigned pack_bf16(float lo, float hi) { bf16x2 c; c[0] = (__bf16)lo; c[1] = (__bf16)hi; return __builtin_bit_cast(unsigned, c); }
__device__ __forceinline__ float bf_lo(unsigned v) { return __uint_as_float(v << 16); }
__device__ __forceinline__ float bf_hi(unsigned v) { return __uint_as_float(v & 0xffff0000u); }
__device__ __forceinline__ void swap16(float& a, float& b) { auto r = __builtin_amdgcn_permlane16_swap(__float_as_uint(a), __float_as_uint(b), false, false); a = __uint_as_float(r[0]); b = __uint_as_float(r[1]); }
__device__ __forceinline__ void swap32(float& a, float& b) { auto r = __builtin_amdgcn_permlane32_swap(__float_as_uint(a), __float_as_uint(b), false, false); a = __uint_as_float(r[0]); b = __uint_as_float(r[1]); }
__device__ __forceinline__ void swap16u(unsigned& a, unsigned& b) { auto r = __builtin_amdgcn_permlane16_swap(a, b, false, false); a = r[0]; b = r[1]; }
__device__ __forceinline__ void swap32u(unsigned& a, unsigned& b) { auto r = __builtin_amdgcn_permlane32_swap(a, b, false, false); a = r[0]; b = r[1]; }
__device__ __forceinline__ float qsum(float v) { float a = v, b = v; swap16(a, b); a += b; b = a; swap32(a, b); return a + b; }

template <int CTRL> __device__ __forceinline__ float dpp_add(float v) { return v + __int_as_float(__builtin_amdgcn_update_dpp(0, __float_as_int(v), CTRL, 0xf, 0xf, false)); }
__device__ __forceinline__ float hsum(float v) { v = dpp_add<0x128>(v); v = dpp_add<0x124>(v); v = dpp_add<0x122>(v); v = dpp_add<0x121>(v); float b = v; swap16(v, b); return v + b; }

struct Frag { u32x2 lo, hi; __device__ __forceinline__ bf16x8 v() const { return __builtin_bit_cast(bf16x8, __builtin_shufflevector(lo, hi, 0, 1, 2, 3)); } };
template <int OFF> __device__ __forceinline__ u32x2 tr_read(unsigned addr) { u32x2 r; asm volatile("ds_read_b64_tr_b16 %0, %1 offset:%2" : "=v"(r) : "v"(addr), "n"(OFF) : "memory"); return r; }
template <int OFF> __device__ __forceinline__ bf16x8 lds_read128(unsigned addr) { bf16x8 r; asm volatile("ds_read_b128 %0, %1 offset:%2" : "=v"(r) : "v"(addr), "n"(OFF) : "memory"); return r; }
template <int OFF> __device__ __forceinline__ float lds_read32(unsigned addr) { float r; asm volatile("ds_read_b32 %0, %1 offset:%2" : "=v"(r) : "v"(addr), "n"(OFF) : "memory"); return r; }
template <int N> __device__ __forceinline__ void wait_lgkm() { asm volatile("s_waitcnt lgkmcnt(%0)" ::"n"(N) : "memory"); }
template <int I, int N, class F> __device__ __forceinline__ void static_for(F&& f) { if constexpr (I < N) { f(std::integral_constant<int, I>{}); static_for<I + 1, N>(f); } }
template <int NA> __device__ __forceinline__ void keep(const Frag (&a)[NA], const bf16x8 (&b)[4]) {
#pragma unroll
    for (int i = 0; i < NA; ++i) asm volatile("" ::"v"(a[i].lo), "v"(a[i].hi));
#pragma unroll
    for (int i = 0; i < 4; ++i) asm volatile("" ::"v"(b[i]));
}
__device__ __forceinline__ void bump(unsigned& a, int d) { asm volatile("v_add_u32 %0, %1, %0" : "+v"(a) : "s"(d)); }
#define MFMA(a, b, c) __builtin_amdgcn_mfma_f32_16x16x32_bf16(a, b, c, 0, 0, 0)
#define RSRC(ptr, bytes) __builtin_amdgcn_make_buffer_rsrc((void*)(ptr), 0, (bytes), 0x00020000)
#define DMA16(rs, ldsp, voff, soff) __builtin_amdgcn_raw_ptr_buffer_load_lds(rs, (LAS void*)(ldsp), 16, voff, soff, 0, 0)
#define DMA16_DEV(rs, ldsp, voff, soff) __builtin_amdgcn_raw_ptr_buffer_load_lds(rs, (LAS void*)(ldsp), 16, voff, soff, 0, SC1)

typedef unsigned long long u64;
constexpr int FLAG_STRIDE = 8, F_PAIR = 1, F_AB = F_PAIR + 1536, F_H = F_AB + 256, F_ACT = F_H + 256, F_CS = F_ACT + 256, F_LN = F_CS + 128, F_ST = F_LN + 256;
__device__ __forceinline__ u64 flag_load(u64* w) { return __hip_atomic_load(w, __ATOMIC_RELAXED, __HIP_MEMORY_SCOPE_AGENT); }
__device__ __forceinline__ void flag_store(u64* w, u64 v) { __hip_atomic_store(w, v, __ATOMIC_RELAXED, __HIP_MEMORY_SCOPE_AGENT); }
__device__ __forceinline__ u64 uniform64(u64 v) { return ((u64)(unsigned)__builtin_amdgcn_readfirstlane((unsigned)(v >> 32)) << 32) | (unsigned)__builtin_amdgcn_readfirstlane((unsigned)v); }
__device__ __forceinline__ void wait_group(u64* word, bool need, u64 ep) {
#pragma nounroll
    for (int spin = 0; spin < (1 << 20); ++spin) {
        if (!__any(need && flag_load(word) != ep)) break;
        __builtin_amdgcn_s_sleep(2);
    }
}
#if defined(__HIP_DEVICE_COMPILE__)
__device__ __forceinline__ void touch(unsigned& sink, unsigned voff, const void* base, unsigned bytes) {
    asm volatile("s_nop 4\n\tbuffer_load_dword %0, %1, %2, 0 offen" : "+v"(sink) : "v"(voff), "s"(RSRC(base, bytes)) : "memory");
}
__device__ __forceinline__ void flag_peek(u64& v, const u64* w) { asm volatile("global_load_dwordx2 %0, %1, off sc1" : "+v"(v) : "v"(w) : "memory"); }
struct G3W {
    const u16* ptr; unsigned voff[2]; int phi, koff;
    __device__ __forceinline__ G3W(const Params& p, int wid, int lane) {
        const int xcd = blockIdx.x & 7, idx = blockIdx.x >> 3, tm = (xcd & 3) * 8 + (idx & 7), q = lane >> 4;
        ptr = wid < 4 ? p.wg : p.wu; phi = ((idx & 7) + (idx >> 3)) & 3; koff = xcd * 2;
#pragma unroll
        for (int i = 0; i < 2; ++i) { const int j = (lane & 15) ^ (((q | (i << 2)) << 1) | (wid >> 2));
            voff[i] = j < 14 ? (unsigned)(((wid & 3) * 16 + q) * FF * 2 + tm * 224 + j * 16) : 0x7ffffff0u; }
    }
    __device__ __forceinline__ int kstep(int t) const { return (t + koff + ((t & 3) == phi ? 4 : 0)) & 15; }
    __device__ __forceinline__ void piece(LAS unsigned char* lds, int wid, const void* base, int so, unsigned nrec, int slot, int i) const { DMA16_DEV(RSRC(base, nrec), lds + slot * 32768 + wid * 4096 + i * 1024, voff[i >> 1], so + i * 4 * FF * 2); }
    __device__ __forceinline__ void early(LAS unsigned char* lds, int wid, int t) const {
#pragma unroll
        for (int i = 0; i < 4; ++i) piece(lds, wid, ptr, kstep(t) * 64 * FF * 2, DM * FF * 2, t, i);
    }
};
__device__ __forceinline__ void colsum_partial(const Params& p, LAS unsigned char* lds, int lw) {
    const int lane = threadIdx.x & 63, slab = blockIdx.x >> 1, mat = slab >= 56, c0 = (slab - mat * 56) * 64;
    const auto rw = RSRC(mat ? p.wu : p.wg, slab < 112 ? DM * FF * 2 : 0);
    const unsigned vo = (unsigned)(((lw * 256 + (lane >> 3)) * FF + c0 + (lane & 7) * 8) * 2);
    u32x4 v[8]; f32x4 s0 = {0.f, 0.f, 0.f, 0.f}, s1 = s0;
    auto add = [&](const u32x4 w) { s0 += (f32x4){bf_lo(w[0]), bf_hi(w[0]), bf_lo(w[1]), bf_hi(w[1])}; s1 += (f32x4){bf_lo(w[2]), bf_hi(w[2]), bf_lo(w[3]), bf_hi(w[3])}; };
#pragma unroll
    for (int i = 0; i < 4; ++i) v[i] = __builtin_amdgcn_raw_buffer_load_b128(rw, vo, i * 8 * FF * 2, 0);
#pragma unroll
    for (int i = 0; i < 8; ++i) { add(v[i % 4]); v[i % 4] = __builtin_amdgcn_raw_buffer_load_b128(rw, vo, (4 + i) * 8 * FF * 2, 0); sched0(); }
#pragma unroll
    for (int i = 4; i < 8; ++i) v[i] = __builtin_amdgcn_raw_buffer_load_b128(rw, vo, (8 + i) * 8 * FF * 2, 0);
#pragma nounroll
    for (int j = 16; j < 40; j += 8)
#pragma unroll
        for (int i = 0; i < 8; ++i) { add(v[i]); v[i] = __builtin_amdgcn_raw_buffer_load_b128(rw, j < 32 ? vo : 0x7ffffff0u, (j + i) * 8 * FF * 2, 0); sched0(); }
    LAS float* tab = (LAS float*)(lds + 131072) + (lw * 8 + (lane >> 3)) * 64 + (lane & 7) * 8;
    *(LAS f32x4*)tab = s0; *(LAS f32x4*)(tab + 4) = s1;
    wait_lgkm0();
}
__device__ __forceinline__ void colsum_final(const Params& p, LAS unsigned char* lds, u64 ep) {
    const int lane = threadIdx.x & 63, slab = blockIdx.x >> 1, mat = slab >= 56, c0 = (slab - mat * 56) * 64;
    if (slab >= 112) return;
    const LAS float* tab = (const LAS float*)(lds + 131072);
    float a = 0.f, b = 0.f;
#pragma unroll
    for (int i = 0; i < 32; i += 2) { a += tab[i * 64 + lane]; b += tab[(i + 1) * 64 + lane]; }
    const int f = c0 + lane, t = f / 112, r = f - t * 112, h = r >= 56;
    __hip_atomic_store(p.csum + (mat * 2 + h) * 1792 + t * 56 + r - h * 56, a + b, __ATOMIC_RELAXED, __HIP_MEMORY_SCOPE_AGENT);
    wait_vm<0>();
    if (lane == 0) flag_store(p.flags + (F_CS + slab) * FLAG_STRIDE, ep);
}
#endif

enum { ROW, CHK, TR };
constexpr int NKS2 = 27;
constexpr int RE4 = 1, RO4 = 3;
constexpr int FLAGD = 1;
constexpr int LNT0 = 1, LNT1 = 5;
template <int ID> struct Cfg;
template <> struct Cfg<1> { static constexpr int NKT = 16, NKS = 16, FA = ROW, FB = ROW, LDB = DM; };
template <> struct Cfg<2> { static constexpr int NKT = 64, NKS = NKS2, FA = CHK, FB = ROW, LDB = SEQ; };
template <> struct Cfg<4> { static constexpr int NKT = 56, NKS = 0, FA = TR, FB = CHK, LDB = 0; };

#if defined(__HIP_DEVICE_COMPILE__)
template <int ID, bool PRE>
__device__ __forceinline__ void g128_load(const Params& p, LAS unsigned char* lds, const int wid, u64& ep) {
    using C = Cfg<ID>;
    constexpr int STAGE = 32768, XLDS = 4 * STAGE;
    const int lane = threadIdx.x & 63, l15 = lane & 15, q = lane >> 4;
    const int xcd = blockIdx.x & 7, idx = blockIdx.x >> 3, im = idx & 3, in = idx >> 2;
    constexpr int NH = ID == 4 ? 2 * (RE4 + RO4) : 0;
    const int ks = (xcd & 1) ^ (ID == 2); const bool recv = ID == 4 || (ID == 2 && ks);
    const int tm = (ID == 1 ? (xcd & 3) : ((xcd >> 1) & 1)) * 4 + im, tn = (xcd >> 2) * 8 + in;
    const int k0 = ID != 1 && ks ? C::NKS : 0, nk = ID == 4 ? 28 - 2 * (ks ? RO4 : RE4) : ID == 2 && ks ? C::NKT - C::NKS : C::NKS;
    const int xb = ID == 4 ? 128 + (blockIdx.x ^ (wid >> 2)) : blockIdx.x >> 1;
    u64* const flag = p.flags + (F_PAIR + xb * 4 + (wid & 3)) * FLAG_STRIDE;
    const unsigned xoff = (unsigned)(ID == 4 ? (128 + xb) * 16384 + (wid & 3) * 4096 : xb * 32768 + (wid & 3) * 8192);
    if constexpr (ID != 1 && !PRE) {
        if (wid == 4 && lane == 0) flag_store(p.flags + (ID == 2 ? F_AB + (tn & 7) * 32 + (tn >> 3) * 16 + (xcd & 3) * 4 + im : F_ACT + (tn >> 1) * 32 + (xcd & 3) * 8 + (idx & 7)) * FLAG_STRIDE, ep);
        sched0();
    }
    const int lw = wid - 4, phi = (im + in) & 3;
    const int koff = ID == 1 ? xcd * 2 : ((xcd >> (ID == 2 ? 1 : 2)) & 1) * (nk >> 1);
    const unsigned ksw = (unsigned)(((lane & 7) ^ (lane >> 3)) << 4);
    unsigned voffA[2], voffB;
    if constexpr (C::FA == ROW) voffA[0] = (unsigned)((tm * 128 + lw * 32 + (lane >> 3)) * DM * 2) + ksw;
    if constexpr (C::FA == CHK) voffA[0] = (unsigned)((lw * 2 * DM + tm * 128 + lane) * 16);
    if constexpr (C::FA == TR) {
#pragma unroll
        for (int i = 0; i < 2; ++i) voffA[i] = (unsigned)((lw * 16 + q) * DM * 2 + tm * 256 + ((l15 ^ ((q | (i << 2)) << 1)) << 4));
    }
    if constexpr (C::FB == ROW) voffB = (unsigned)((((ID == 1 ? tn & 7 : tn)) * 128 + lw * 32 + (lane >> 3)) * C::LDB * 2) + ksw;
    else voffB = (unsigned)((lw * 2 * SEQ + tn * 128 + lane) * 16);
    const auto ra = ID == 1 ? RSRC(p.x, SEQ * DM * 2) : ID == 2 ? RSRC(p.abT, 2 * SEQ * DM * 2) : RSRC(p.wd, FF * DM * 2);
    auto kpos = [&](int t) {
        const int u1 = t - 1, ul = ((nk - 1 - phi) & ~3) + phi;
        int k = ID == 1 ? ((t & 3) != phi ? t : t + 4 < nk ? t + 4 : phi) : t < phi || u1 >= ul ? t : t == phi ? phi + 4 : (u1 & 3) == phi && u1 > phi ? u1 + 4 : u1;
        k += koff; k = k >= nk ? k - nk : k;
        if constexpr (ID == 4) {
            k = ks * 14 + (k < (nk >> 1) ? k : k + 28 - (nk >> 1));
            int u = t - nk + phi; u = u >= NH ? u - NH : u; const int h = u >= RE4 + RO4, v = u - h * (RE4 + RO4);
            return t < nk ? k : h * 28 + (v < RE4 ? 14 - RE4 + v : 28 - RO4 - RE4 + v);
        } else return k + k0;
    };
    const int ktab = kpos(lane);
    struct Pos { int sa, sb, lb; const void* pb; };
    auto prep = [&](int t) {
        const int k = ID == 1 || PRE ? kpos(t) : __builtin_amdgcn_readlane(ktab, t);
        Pos x{C::FA == ROW ? k * 128 : C::FA == CHK ? k * 8 * DM * 16 : k * 64 * DM * 2, C::FB == ROW ? (k & 31) * 128 : k * 8 * SEQ * 16, (t & 3) * STAGE + lw * 4096,
              ID == 1 ? (const void*)(tn < 8 ? p.wre : p.wim) : ID == 2 ? (const void*)(k < 32 ? p.cre : p.cim) : (const void*)p.act};
        asm volatile("" : "+s"(x.sa), "+s"(x.sb), "+s"(x.lb)); if constexpr (ID == 2) asm volatile("" : "+s"(x.pb));
        return x;
    };
    auto issue = [&](const Pos& x, bool doA, bool doB, bool half) {
        const auto rb = RSRC(x.pb, ID == 1 ? DM * DM * 2 : ID == 2 ? SEQ * SEQ * 2 : SEQ * FF * 2);
        LAS unsigned char* base = lds + x.lb;
#pragma unroll
        for (int i = 0; i < 4; ++i) {
            if (doA) {
                if constexpr (C::FA == ROW) DMA16(ra, base + i * 1024, voffA[0], x.sa + i * 8 * DM * 2);
                if constexpr (C::FA == CHK) DMA16_DEV(ra, base + i * 1024, voffA[0], x.sa + (i >> 1) * DM * 16 + (i & 1) * 1024);
                if constexpr (C::FA == TR) DMA16(ra, base + i * 1024, voffA[i >> 1], x.sa + i * 4 * DM * 2);
            }
            if (doB) {
                if constexpr (C::FB == ROW) DMA16(rb, base + 16384 + i * 1024, voffB, x.sb + i * 8 * C::LDB * 2);
                else if (!(half && (i & 1))) DMA16_DEV(rb, base + 16384 + i * 1024, voffB, x.sb + (i >> 1) * SEQ * 16 + ((i & 1) ^ ks) * 1024);
            }
        }
    };
    auto stage = [&](int t, bool doA, bool doB, bool half = false) { issue(prep(t), doA, doB, half); };
    if constexpr (PRE) { stage(0, ID == 4, ID == 2); return; }
    if constexpr (ID != 1) {
        u64* words = p.flags + (ID == 2 ? F_AB + tm * 32 : F_ACT + (tn >> 1) * 32) * FLAG_STRIDE;
        if (wid != 4) __builtin_amdgcn_s_sleep(2);
        stage(1, ID == 4, ID == 2); stage(2, ID == 4, ID == 2);
        if (wid == 4) { const int j = lane & 31; wait_group(words + j * FLAG_STRIDE, ID == 2 ? 2 * j + 2 > k0 && 2 * j < k0 + nk : ((j >> 3) & 1) == ks, ep); }
        bar();
        stage(0, ID == 2, ID == 4); stage(1, ID == 2, ID == 4); stage(2, ID == 2, ID == 4);
        wait_vm<8>(); bar();
        wait_vm<4>();
    } else {
        stage(0, true, true); stage(1, true, true);
        wait_vm<8>(); bar();
        stage(2, true, true);
        const unsigned a = (unsigned)(size_t)lds + XLDS;
        const float e0 = lds_read32<3072>(a), e1 = lds_read32<3076>(a);
        wait_lgkm0(); sched0();
        ep = uniform64(((u64)__float_as_uint(e1) << 32) | __float_as_uint(e0));
    }
    u64 pseen = 0; u64* const pwords = p.flags + (F_ACT + (tn >> 1) * 32 + (lane & 31)) * FLAG_STRIDE;
#pragma nounroll
    for (int t = 0; t < nk - 3; ++t) {
        const Pos x = prep(t + 3);
        wait_vm<8>();
        if constexpr (ID == 4) { if (t == nk - 4 && wid == 4 && __any(pseen != ep)) wait_group(pwords, true, ep); }
        bar();
        issue(x, true, true, false);
        if constexpr (ID == 4) { if (t == nk - 6 && wid == 4) flag_peek(pseen, pwords); }
        if constexpr (ID == 1) { if (t == LNT1 && wid == 4 && lane == 0) flag_store(p.flags + (F_LN + tm * 16 + tn) * FLAG_STRIDE, ep); }
    }
    if constexpr (NH > 0) {
        wait_vm<8>(); bar(); stage(nk, true, true, true);
#pragma nounroll
        for (int t = nk - 2; t < nk + NH - 3; ++t) { const Pos x = prep(t + 3); wait_vm<6>(); bar(); issue(x, true, true, true); }
    }
    bool got = !recv;
    auto fetch = [&](u64 seen) {
        if (uniform64(seen) != ep) return false;
        asm volatile("" ::: "memory");
#pragma unroll
        for (int i = 0; i < (ID == 4 ? 4 : 8); ++i) DMA16_DEV(RSRC(p.xch, 256 * 32768), lds + XLDS + lw * 8192 + i * 1024, (unsigned)(lane * 16), xoff + i * 1024);
        return true;
    };
    wait_vm<NH ? 6 : 8>(); bar();
    u64* const lnw = p.flags + (F_LN + (lane & 16 ? l15 * 16 + tn : tm * 16 + l15)) * FLAG_STRIDE;
    u64 seen0 = flag_load(ID == 1 ? lnw : flag);
    wait_vm<0>();
    if (!got) got = fetch(seen0);
    if constexpr (ID == 1) { if (wid == 4) {
        if (__any(seen0 != ep)) wait_group(lnw, true, ep);
        asm volatile("" ::: "memory");
#pragma unroll
        for (int i = 0; i < 3; ++i) DMA16_DEV(RSRC(p.ln1, 3 * SEQ * 4), lds + XLDS + i * 1024, lane < 32 ? (unsigned)(lane * 16) : 0x7ffffff0u, (i * SEQ + (i < 2 ? tm : tn) * 128) * 4);
    } }
    if constexpr (ID == 1) g128_load<2, true>(p, lds, wid, ep);
    bar();
    if constexpr (ID != 1) seen0 = flag_load(flag);
    if constexpr (ID == 2) { if (!got) got = fetch(seen0); }
    bar();
    if (!got) got = fetch(seen0);
    if (recv) {
#pragma nounroll
        for (int spin = 0; !got && spin < (1 << 20); ++spin) { got = fetch(flag_load(flag)); __builtin_amdgcn_s_sleep(1); }
        wait_vm<0>(); bar();
    }
    if constexpr (ID == 2) { if (!recv) colsum_partial(p, lds, lw); }
    if constexpr (ID == 1) { wait_vm<4>(); bar(); }
    if constexpr (ID != 4) bar();
}

template <int ID>
__device__ __forceinline__ void g128_compute(const Params& p, LAS unsigned char* lds, const int wid, u64& ep, u32x2& stv, unsigned& sto) {
    using C = Cfg<ID>;
    constexpr int STAGE = 32768, XLDS = 4 * STAGE;
    const int lane = threadIdx.x & 63, l15 = lane & 15, q = lane >> 4;
    const int xcd = blockIdx.x & 7, idx = blockIdx.x >> 3, im = idx & 3, in = idx >> 2;
    constexpr int NH = ID == 4 ? 2 * (RE4 + RO4) : 0;
    const int ks = (xcd & 1) ^ (ID == 2); const bool recv = ID == 4 || (ID == 2 && ks);
    const int tm = (ID == 1 ? (xcd & 3) : ((xcd >> 1) & 1)) * 4 + im, tn = (xcd >> 2) * 8 + in;
    const int k0 = ID != 1 && ks ? C::NKS : 0, nk = ID == 4 ? 28 - 2 * (ks ? RO4 : RE4) : ID == 2 && ks ? C::NKT - C::NKS : C::NKS;
    const int xb = ID == 4 ? 128 + (blockIdx.x ^ (wid >> 2)) : blockIdx.x >> 1;
    u64* const flag = p.flags + (F_PAIR + xb * 4 + (wid & 3)) * FLAG_STRIDE;
    const unsigned xoff = (unsigned)(ID == 4 ? (128 + xb) * 16384 + (wid & 3) * 4096 : xb * 32768 + (wid & 3) * 8192);
    const int wm = wid & 1, wn = wid >> 1;
    const int eo = (q & 1) * 16 + (q >> 1) * 8;
    f32x4 acc[4][4];
#pragma unroll
    for (int m = 0; m < 4; ++m)
#pragma unroll
        for (int n = 0; n < 4; ++n) acc[m][n] = (f32x4){0.f, 0.f, 0.f, 0.f};
    const int row0 = tn * 128 + (ID == 4 ? ks * 64 + wn * 32 : wn * 64) + l15, col0 = tm * 128 + wm * 64 + eo;
    if constexpr (ID != 1) {
        constexpr int NN = ID == 4 ? 2 : 4;
        if (ID == 4 || !recv) {
            if constexpr (ID == 2) { wait_vm<0>(); bar(); __builtin_amdgcn_s_sleep(4); }
            u32x4 r[NN][2];
#pragma unroll
            for (int n = 0; n < NN; ++n)
#pragma unroll
                for (int mp = 0; mp < 2; ++mp)
                    r[n][mp] = __builtin_amdgcn_raw_buffer_load_b128(RSRC(ID == 2 ? p.x : p.h, SEQ * DM * 2), (unsigned)(ID == 2 ? ((row0 + n * 16) * DM + col0 + mp * 32) * 2 : ((col0 / 8 + mp * 4) * SEQ + row0 + n * 16) * 16), 0, ID == 4 ? SC1 : 0);
            if constexpr (ID == 4) { wait_vm<0>(); bar(); }
#pragma unroll
            for (int n = 0; n < NN; ++n)
#pragma unroll
                for (int mp = 0; mp < 2; ++mp) {
                    unsigned r0 = r[n][mp][0], r1 = r[n][mp][1], r2 = r[n][mp][2], r3 = r[n][mp][3];
                    swap16u(r0, r2); swap16u(r1, r3);
                    acc[2 * mp][n] = (f32x4){bf_lo(r0), bf_hi(r0), bf_lo(r1), bf_hi(r1)}; acc[2 * mp + 1][n] = (f32x4){bf_lo(r2), bf_hi(r2), bf_lo(r3), bf_hi(r3)};
                }
        } else { wait_vm<0>(); bar(); }
    }
    unsigned pfc = 0;
    if constexpr (ID == 1) {
        ep = uniform64(flag_load(p.flags)) + 1;
        if (threadIdx.x == 0) *(LAS u64*)(lds + XLDS + 3072) = ep;
        wait_lgkm0();
        if (idx == 0) {
            unsigned long long pc; int lo, hi;
            asm volatile("s_getpc_b64 %0\n s_mov_b32 %1, _Z7fused_k6Params@rel32@lo+4\n s_mov_b32 %2, _Z6tail_kv@rel32@lo+12" : "=s"(pc), "=s"(lo), "=s"(hi));
            touch(pfc, threadIdx.x * 128, (const void*)(pc + (long long)lo), (unsigned)max(hi - lo, 0)); touch(pfc, threadIdx.x * 128 + 32768, (const void*)(pc + (long long)lo), (unsigned)max(hi - lo, 0));
        }
    }
    constexpr int NA = C::FA == CHK ? 1 : C::FA == ROW ? 2 : 4, NB = C::FB == CHK ? 1 : 2;
    unsigned adA[NA], adB[NB];
    const unsigned lds0 = (unsigned)(size_t)lds, row_sw = lds0 + (unsigned)(l15 * 128 + ((q ^ (lane & 7)) << 4)), chk = lds0 + (unsigned)(q * 2048 + l15 * 16);
#pragma unroll
    for (int i = 0; i < NA; ++i) {
        if constexpr (C::FA == ROW) adA[i] = (row_sw ^ (unsigned)(i * 64)) + (unsigned)(wm * 8192);
        if constexpr (C::FA == CHK) adA[i] = chk + (unsigned)(wm * 1024);
        if constexpr (C::FA == TR) { const int q4 = l15 >> 2; adA[i] = lds0 + (unsigned)((8 * q + q4) * 256 + (((wm * 4 + i) ^ (q4 | ((q & 1) << 2))) << 5) + 8 * (lane & 3)); }
    }
#pragma unroll
    for (int i = 0; i < NB; ++i) adB[i] = C::FB == CHK ? 16384u + chk + (unsigned)(wn * 512) : 16384u + (row_sw ^ (unsigned)(i * 64)) + (unsigned)(wn * 8192);
    constexpr int W = C::FA == TR ? 2 : 1, NR = 4 + 4 * W;
    auto rd = [&](auto ic, Frag (&ra)[4], bf16x8 (&rb)[4], auto kkc) {
        constexpr int kk = decltype(kkc)::value, i = decltype(ic)::value;
        if constexpr (i == 0 || (i > W && i <= W + 3)) { constexpr int n = i == 0 ? 0 : i - W;
            if constexpr (C::FB == CHK) rb[n] = lds_read128<kk * 8192 + (n >> 1) * 1024 + (n & 1) * 256>(adB[0]);
            else rb[n] = lds_read128<n * 2048>(adB[kk]); }
        else { constexpr int j = i <= W ? i - 1 : i - 4, m = j / W;
            if constexpr (C::FA != TR) { bf16x8 v;
                if constexpr (C::FA == CHK) v = lds_read128<kk * 8192 + m * 256>(adA[0]); else v = lds_read128<m * 2048>(adA[kk]);
                const u32x4 w = __builtin_bit_cast(u32x4, v); ra[m].lo = (u32x2){w[0], w[1]}; ra[m].hi = (u32x2){w[2], w[3]}; }
            else if constexpr (j % 2 == 0) ra[m].lo = tr_read<kk * 8192>(adA[m]);
            else ra[m].hi = tr_read<kk * 8192 + 1024>(adA[m]); }
    };
    auto phase = [&](const Frag (&ra)[4], const bf16x8 (&rb)[4], Frag (&na)[4], bf16x8 (&nb)[4], int dstep, auto kkc) {
        static_for<0, 16>([&](auto sc) {
            constexpr int s = decltype(sc)::value, m = s >> 2, n = s & 3;
            if constexpr (n == 0) { wait_lgkm<(3 - m) * W + (s < NR ? s : NR)>(); sched0(); }
            acc[m][n] = MFMA(ra[m].v(), rb[n], acc[m][n]);
            if constexpr (s < NR) rd(sc, na, nb, kkc);
            if constexpr (s >= 12 && decltype(kkc)::value == 1) {
                if constexpr (s - 12 < NA) bump(adA[s - 12], dstep);
                if constexpr (s - 12 < NB) bump(adB[s - 12], dstep);
            }
            sched0();
        });
    };
    Frag a0[4], a1[4]; bf16x8 b0[4], b1[4];
    constexpr std::integral_constant<int, 0> K0{}; constexpr std::integral_constant<int, 1> K1{};
    if constexpr (ID != 1) bar();
    bar();
    static_for<0, NR>([&](auto ic) { rd(ic, a0, b0, K0); });
    sched0();
    const int rx = tm * 128 + tn * 8 + wid * 2 + (lane >> 5), rw = tn * 128 + tm * 8 + wid * 2 + (lane >> 5);
    u32x4 xv[4], wv[4];
    if constexpr (ID == 1) {
#pragma unroll
        for (int i = 0; i < 4; ++i) { xv[i] = __builtin_amdgcn_raw_buffer_load_b128(RSRC(p.x, SEQ * DM * 2), (unsigned)(rx * 2048 + (lane & 31) * 16 + i * 512), 0, 0);
                                      wv[i] = __builtin_amdgcn_raw_buffer_load_b128(RSRC(tn < 8 ? p.wre : p.wim, DM * DM * 2), (unsigned)((rw & 1023) * 2048 + (lane & 31) * 16 + i * 512), 0, 0); }
        sched0();
    }
    auto steps = [&](int t0, int t1) {
#pragma nounroll
        for (int t = t0; t < t1; ++t) {
            phase(a0, b0, a1, b1, (t & 3) == 3 ? -3 * STAGE : STAGE, K1);
            if constexpr (ID == 1) { if (t == LNT1) wait_vm<0>(); }
            bar();
            phase(a1, b1, a0, b0, 0, K0);
        }
    };
    if constexpr (ID == 1) {
        steps(0, LNT0);
        wait_lgkm0(); sched0();
        f32x2 sx = {0.f, 0.f}, sw = sx, sq = sx;
#pragma unroll
        for (int i = 0; i < 16; ++i) { sx += (f32x2){bf_lo(xv[i >> 2][i & 3]), bf_hi(xv[i >> 2][i & 3])}; sw += (f32x2){bf_lo(wv[i >> 2][i & 3]), bf_hi(wv[i >> 2][i & 3])}; }
        const float mean = hsum(sx[0] + sx[1]) * (1.f / DM);
#pragma unroll
        for (int i = 0; i < 16; ++i) { const f32x2 d = (f32x2){bf_lo(xv[i >> 2][i & 3]), bf_hi(xv[i >> 2][i & 3])} - mean; sq += d * d; }
        const float rstd = __builtin_amdgcn_rsqf(hsum(sq[0] + sq[1]) * (1.f / DM) + LN_EPS), wsum = hsum(sw[0] + sw[1]);
        const int j = lane & 31;
        __builtin_amdgcn_raw_buffer_store_b32(__float_as_uint(j == 0 ? mean : j == 1 ? rstd : wsum), RSRC(p.ln1, 3 * SEQ * 4), j < 3 ? (unsigned)((j * SEQ + (j < 2 ? rx : rw)) * 4) : 0x7ffffff0u, 0, SC1);
        sched0();
        steps(LNT0, nk);
    } else steps(0, nk);
    wait_lgkm0(); keep(a0, b0); sched0();
    auto send = [&](int h0) {
        const auto rx = RSRC(p.xch, 256 * 32768);
#pragma unroll
        for (int m = 0; m < 4; ++m)
#pragma unroll
            for (int h = h0; h < 2; ++h) {
                const f32x4 u = acc[m][2 * h], v = acc[m][2 * h + 1];
                __builtin_amdgcn_raw_buffer_store_b128((u32x4){pack_bf16(u[0], u[1]), pack_bf16(u[2], u[3]), pack_bf16(v[0], v[1]), pack_bf16(v[2], v[3])}, rx, xoff + (unsigned)((h0 ? m : m * 2 + h) * 1024 + lane * 16), 0, SC1);
            }
    };
    if constexpr (NH > 0) {
        send(1); sched0();
        auto phaseH = [&](const Frag (&ra)[4], const bf16x8 (&rb)[4], Frag (&na)[4], bf16x8 (&nb)[4], int dstep, auto kkc) {
            static_for<0, 8>([&](auto sc) {
                constexpr int s = decltype(sc)::value;
                if constexpr ((s & 1) == 0) { wait_lgkm<6 - s + (s < 5 ? 2 * s : 10)>(); sched0(); }
                acc[s >> 1][s & 1] = MFMA(ra[s >> 1].v(), rb[s & 1], acc[s >> 1][s & 1]);
                if constexpr (s < 5) { rd(std::integral_constant<int, s < 2 ? 2 * s : 2 * s + 2>{}, na, nb, kkc); rd(std::integral_constant<int, s < 2 ? 2 * s + 1 : 2 * s + 3>{}, na, nb, kkc); }
                if constexpr (s >= 5 && decltype(kkc)::value == 1) { if constexpr (s < 7) { bump(adA[2 * (s - 5)], dstep); bump(adA[2 * (s - 5) + 1], dstep); } else bump(adB[0], dstep); }
                sched0();
            });
        };
#pragma nounroll
        for (int t = nk; t < nk + NH; ++t) {
            phaseH(a0, b0, a1, b1, (t & 3) == 3 ? -3 * STAGE : STAGE, K1);
            bar();
            phaseH(a1, b1, a0, b0, 0, K0);
            if (t == nk + FLAGD) { wait_vm<0>(); if (lane == 0) flag_store(flag, ep); }
        }
        wait_lgkm0();
        asm volatile("" ::"v"(b0[0]), "v"(b0[1]));
#pragma unroll
        for (int i = 0; i < 4; ++i) asm volatile("" ::"v"(a0[i].lo), "v"(a0[i].hi));
        sched0();
    }
    if constexpr (ID == 1) {
        wait_vm<0>(); asm volatile("; pfc %0" ::"v"(pfc));
        bar();
        const auto ro = RSRC(p.abT, 2 * SEQ * DM * 2);
        const float sgn = tn < 8 ? 1.f : -1.f;
        f32x4 mean[4], rs[4]; float ws[4];
        static_for<0, 4>([&](auto ic) { constexpr int i = decltype(ic)::value; const unsigned a = lds0 + (unsigned)(XLDS + wm * 256 + q * 16);
            mean[i] = __builtin_bit_cast(f32x4, lds_read128<i * 64>(a)); rs[i] = __builtin_bit_cast(f32x4, lds_read128<1024 + i * 64>(a));
            ws[i] = lds_read32<2048 + i * 64>(lds0 + (unsigned)(XLDS + wn * 256 + l15 * 4)); });
        wait_lgkm0(); sched0();
#pragma unroll
        for (int m = 0; m < 4; ++m) {
            const f32x4 r = rs[m] * sgn;
#pragma unroll
            for (int n = 0; n < 4; ++n) acc[m][n] = (acc[m][n] - mean[m] * ws[n]) * r;
        }
#pragma unroll
        for (int n = 0; n < 4; ++n) { const int e = (tn & 7) * 128 + wn * 64 + n * 16 + l15;
#pragma unroll
            for (int mp = 0; mp < 2; ++mp) {
                unsigned x0 = pack_bf16(acc[2 * mp][n][0], acc[2 * mp][n][1]), x1 = pack_bf16(acc[2 * mp][n][2], acc[2 * mp][n][3]);
                unsigned y0 = pack_bf16(acc[2 * mp + 1][n][0], acc[2 * mp + 1][n][1]), y1 = pack_bf16(acc[2 * mp + 1][n][2], acc[2 * mp + 1][n][3]);
                swap16u(x0, y0); swap16u(x1, y1);
                const int s = (tn >> 3) * SEQ + col0 + mp * 32;
                __builtin_amdgcn_raw_buffer_store_b128((u32x4){x0, x1, y0, y1}, ro, (unsigned)(((s >> 3) * DM + e) * 16), 0, SC1);
            } }
    } else if (!recv) {
        send(0);
        wait_vm<0>();
        if (lane == 0) flag_store(flag, ep);
        if constexpr (ID == 2) bar();
    } else {
        bar();
        const unsigned px = (unsigned)(size_t)lds + (unsigned)(XLDS + wid * 8192 + lane * 16);
        constexpr int HH = ID == 4 ? 1 : 2;
        u32x4 pw[4 * HH];
        static_for<0, 4 * HH>([&](auto ic) { pw[decltype(ic)::value] = __builtin_bit_cast(u32x4, lds_read128<decltype(ic)::value * 1024>(px)); });
        wait_lgkm0(); sched0();
#pragma unroll
        for (int m = 0; m < 4; ++m)
#pragma unroll
            for (int h = 0; h < HH; ++h) { const u32x4 w = pw[m * HH + h];
                acc[m][2 * h] += (f32x4){bf_lo(w[0]), bf_hi(w[0]), bf_lo(w[1]), bf_hi(w[1])}; acc[m][2 * h + 1] += (f32x4){bf_lo(w[2]), bf_hi(w[2]), bf_lo(w[3]), bf_hi(w[3])}; }
        const auto ro = RSRC(ID == 2 ? p.h : p.out, ID == 2 ? 136 * SEQ * 16 : SEQ * DM * 2);
#pragma unroll
        for (int n = 0; n < 2 * HH; ++n) {
            unsigned o[4][2];
#pragma unroll
            for (int m = 0; m < 4; ++m) { o[m][0] = pack_bf16(acc[m][n][0], acc[m][n][1]); o[m][1] = pack_bf16(acc[m][n][2], acc[m][n][3]); }
#pragma unroll
            for (int mp = 0; mp < 2; ++mp) {
                swap16u(o[2 * mp][0], o[2 * mp + 1][0]); swap16u(o[2 * mp][1], o[2 * mp + 1][1]);
                __builtin_amdgcn_raw_buffer_store_b128((u32x4){o[2 * mp][0], o[2 * mp][1], o[2 * mp + 1][0], o[2 * mp + 1][1]}, ro,
                    (unsigned)(ID == 4 ? ((row0 + n * 16) * DM + col0 + mp * 32) * 2 : ((col0 / 8 + mp * 4) * SEQ + row0 + n * 16) * 16), 0, SC1);
            }
        }
        if constexpr (ID == 2) {
            wait_vm<0>(); bar();
            sto = (unsigned)(((128 + tm) * SEQ + row0 + q * 16) * 16 + wm * 8);
#pragma unroll
            for (int n = 0; n < 4; ++n) {
                const f32x4 s4 = (acc[0][n] + acc[1][n]) + (acc[2][n] + acc[3][n]);
                const float mw = qsum((s4[0] + s4[1]) + (s4[2] + s4[3])) * (1.f / 64.f);
                const f32x4 d0 = acc[0][n] - mw, d1 = acc[1][n] - mw, d2 = acc[2][n] - mw, d3 = acc[3][n] - mw, q4 = (d0 * d0 + d1 * d1) + (d2 * d2 + d3 * d3);
                float m2 = (q4[0] + q4[1]) + (q4[2] + q4[3]);
                m2 = qsum(m2);
                if (q == n) stv = (u32x2){__float_as_uint(mw), __float_as_uint(m2)};
            }
        }
    }
}
#endif

#if defined(__HIP_DEVICE_COMPILE__)
template <int GRP>
__device__ __forceinline__ void gemm3_body(const Params& p, LAS unsigned char* lds, const int wid, u64& ep, u32x2 stv, unsigned sto) {
    constexpr int NK = 16, FA = 7, HB0 = 3 * 32768;
    const int lane = threadIdx.x & 63;
    const int wm = wid & 1, wn = wid >> 1, l15 = lane & 15, q = lane >> 4;
    const int xcd = blockIdx.x & 7, idx = blockIdx.x >> 3, im = idx & 7, in = idx >> 3;
    const int tm = (xcd & 3) * 8 + im, tn = (xcd >> 2) * 4 + in;
    const G3W gw(p, wid, lane);
    unsigned pf = 0;
    u64* words = p.flags + (F_H + tn * 32) * FLAG_STRIDE;
    if (threadIdx.x == 256 && !(xcd & 1)) flag_store(words + tm * FLAG_STRIDE, ep);
    if (wid != 4) __builtin_amdgcn_s_sleep(2);
    if constexpr (GRP == 1) { const G3W g0(p, wid - 4, lane); g0.early(lds, wid - 4, 0); gw.early(lds, wid, 0); g0.early(lds, wid - 4, 1); gw.early(lds, wid, 1); }
    if ((xcd & 1) && wid == 1) colsum_final(p, lds, ep);
    if (wid == 4) {
        wait_group(words + (lane & 31) * FLAG_STRIDE, (lane & 8) == 0, ep);
        if (blockIdx.x == 0 && lane == 0) flag_store(p.flags, ep);
    }
    bar();
    const unsigned voffB = (unsigned)((wid * SEQ + tn * 256 + lane) * 16);
    struct Src { const void* base; int so; unsigned nrec; };
    auto pin = [](Src x) { asm volatile("" : "+s"(x.base), "+s"(x.so), "+s"(x.nrec)); return x; };
    auto srcW = [&](int t) { return pin(Src{t < NK ? (const void*)gw.ptr : (const void*)(p.csum + GRP * 3584), (gw.kstep(t) * 64 * FF * 2) & -(int)(t < NK), t < NK ? (unsigned)(DM * FF * 2) : t == NK ? (unsigned)(2 * FF * 2) : 0u}); };
    auto srcH = [&](int t) { Src x{nullptr, (t < NK ? gw.kstep(t) : NK) * 8 * SEQ * 16, 136 * SEQ * 16}; asm volatile("" : "+s"(x.so)); return x; };
    auto stageW = [&](Src x, int slot, int i) { gw.piece(lds, wid, x.base, x.so, x.nrec, slot, i); };
    auto stageH = [&](Src x, int slot, int i) { DMA16_DEV(RSRC(p.h, x.nrec), lds + HB0 + slot * 32768 + wid * 4096 + i * 1024, voffB, x.so + i * 1024); };
#pragma unroll
    for (int t = 0; t < 2; ++t) {
        const Src xh = srcH(t);
#pragma unroll
        for (int i = 0; i < 4; ++i) stageH(xh, t, i);
    }

    unsigned adA[FA];
    const unsigned lds0 = (unsigned)(size_t)lds;
#pragma unroll
    for (int m = 0; m < FA; ++m) { const int q4 = l15 >> 2, pp = lane & 3, reg = m < 6 ? m & 1 : pp >> 1, ch = m < 6 ? wm * 8 + (m & 6) + (pp >> 1) : 6 + wm;
        adA[m] = lds0 + (unsigned)(reg * 16384 + (8 * q + q4) * 256 + ((ch ^ (((q4 | ((q & 1) << 2)) << 1) | reg)) << 4) + 8 * (pp & 1)); }
    unsigned adB = lds0 + (unsigned)(HB0 + q * 4096 + (wn * 64 + l15) * 16);
    auto rd = [&](auto ic, Frag (&ra)[FA], bf16x8 (&rb)[4], auto kkc) {
        constexpr int kk = decltype(kkc)::value, i = decltype(ic)::value;
        if constexpr (i < 4) rb[i] = lds_read128<kk * 16384 + i * 256>(adB);
        else if constexpr (i % 2 == 0) ra[(i - 4) / 2].lo = tr_read<kk * 8192>(adA[(i - 4) / 2]);
        else ra[(i - 4) / 2].hi = tr_read<kk * 8192 + 1024>(adA[(i - 4) / 2]);
    };
    f32x4 acc[FA][4];
#pragma unroll
    for (int m = 0; m < FA; ++m)
#pragma unroll
        for (int n = 0; n < 4; ++n) acc[m][n] = (f32x4){0.f, 0.f, 0.f, 0.f};
    auto phase = [&](const Frag (&ra)[FA], const bf16x8 (&rb)[4], Frag (&na)[FA], bf16x8 (&nb)[4], int dw, int dh, auto kkc, auto&& dma, auto&& prep) {
        static_for<0, 28>([&](auto sc) {
            constexpr int s = decltype(sc)::value, m = s >> 2, n = s & 3;
            if constexpr (decltype(kkc)::value == 0) { if constexpr (s == 0) __builtin_amdgcn_s_setprio(3); if constexpr (s == 18) __builtin_amdgcn_s_setprio(2); }
            else { if constexpr (s == 6) __builtin_amdgcn_s_setprio(1); if constexpr (s == 22) __builtin_amdgcn_s_setprio(0); }
            acc[m][n] = MFMA(ra[m].v(), rb[n], acc[m][n]);
            sched0();
            if constexpr (s < 18) rd(sc, na, nb, kkc);
            if constexpr (s == 20) prep();
            if constexpr (s % 4 == 2 * GRP && s < 16) dma(s / 4);
            if constexpr (decltype(kkc)::value == 1 && s >= 18 && s < 26) { if constexpr (s < 25) bump(adA[s - 18], dw); else bump(adB, dh); }
            sched0();
        });
        wait_lgkm0(); sched0();
    };
    Frag a0[FA], a1[FA]; bf16x8 b0[4], b1[4];
    constexpr std::integral_constant<int, 0> K0{}; constexpr std::integral_constant<int, 1> K1{};
    wait_vm<4>(); bar();
    if constexpr (GRP == 0) __builtin_amdgcn_raw_buffer_store_b64(stv, RSRC(p.h, 136 * SEQ * 16), sto, 0, SC1);
    static_for<0, 18>([&](auto ic) { rd(ic, a0, b0, K0); });
    wait_lgkm0(); sched0();
    const unsigned pfo = (unsigned)(((blockIdx.x * 8 + wid) * 28 + (lane < 28 ? lane : 0)) * 128); const int pft = 1 + idx % 12;
    Src xw = srcW(2), xh;
    auto phaseA = [&](int t, int sw2) {
        phase(a0, b0, a1, b1, sw2 == 1 ? -65536 : 32768, (t & 1) ? -32768 : 32768, K1, [&](int i) { stageW(xw, sw2, i);
            if (i == 3) touch(pf, pfo, p.wd, t == pft ? FF * DM * 2 : 0);
        }, [&]() { xh = srcH(t + 2); });
        wait_vm<5>();
    };
    u64 cseen = 0;
    phaseA(0, 2);
    if constexpr (GRP == 0) wait_vm<0>();
    int sw2 = 2;
#pragma nounroll
    for (int t = 0; t < NK - 1; ++t) {
        bar();
        phase(a1, b1, a0, b0, 0, 0, K0, [&](int i) { stageH(xh, t & 1, i); }, [&]() { xw = srcW(t + 3); });
        if (__builtin_expect(t == 0 && threadIdx.x == 0 && !(xcd & 1), 0)) flag_store(p.flags + (F_ST + tn * 32 + tm) * FLAG_STRIDE, ep);
        if (__builtin_expect(t >= 11 && t <= 12 && (wid & 3) == 0, 0)) {
            const int sl = min((tm * 112 >> 6) + (lane & 3), (tm * 112 + 111) >> 6);
            u64* w = p.flags + (lane < 32 ? F_CS + GRP * 56 + sl : F_ST + tn * 32 + (lane & 23)) * FLAG_STRIDE;
            if (t == 11) flag_peek(cseen, w); else if (__any(cseen != ep)) wait_group(w, true, ep);
        }
        sw2 = sw2 == 2 ? 0 : sw2 + 1;
        phaseA(t + 1, sw2);
    }
    bar();
    __builtin_amdgcn_s_setprio(GRP ? 0 : 3);
    f32x4 csv[FA], sp[4][2];
    static_for<0, 16>([&](auto sc) {
        constexpr int s = decltype(sc)::value;
        acc[s >> 2][s & 3] = MFMA(a1[s >> 2].v(), b1[s & 3], acc[s >> 2][s & 3]);
        if constexpr (s < 8) sp[s >> 1][s & 1] = __builtin_bit_cast(f32x4, lds_read128<(s & 1) * 16384 + (s >> 1) * 256>(adB));
        else if constexpr (s < 15) { constexpr int m = s - 8; const int reg = m < 6 ? m & 1 : q >> 1, c4 = m < 6 ? wm * 2 + (m & 6) * 2 + q : (wm ? 0 : 12) + (q & 1);
            csv[m] = __builtin_bit_cast(f32x4, lds_read128<32768>(lds0 + (unsigned)(reg * 16384 + wm * 256 + ((c4 ^ ((wm << 1) | reg)) << 4)))); }
        if constexpr (GRP == 1 && s == 2) g128_load<4, true>(p, lds, wid, ep);
        sched0();
    });
    wait_lgkm0(); wait_vm<GRP ? 4 : 0>(); asm volatile("; pf %0" ::"v"(pf)); sched0();
    float mean[4], c1[4], ve[4];
#pragma unroll
    for (int n = 0; n < 4; ++n) {
        const f32x4 s0 = sp[n][0], s1 = sp[n][1];
        mean[n] = qsum((s0[0] + s0[2]) + (s1[0] + s1[2])) * (1.f / 16.f);
        const float d0 = s0[0] - mean[n], d1 = s0[2] - mean[n], d2 = s1[0] - mean[n], d3 = s1[2] - mean[n];
        const float m2 = qsum((s0[1] + s0[3]) + (s1[1] + s1[3]) + 64.f * ((d0 * d0 + d1 * d1) + (d2 * d2 + d3 * d3)));
        ve[n] = m2 * (1.f / DM) + LN_EPS; c1[n] = -1.44269504f * __builtin_amdgcn_rsqf(ve[n]);
    }
#pragma unroll
    for (int s = 16; s < 28; ++s) acc[s >> 2][s & 3] = MFMA(a1[s >> 2].v(), b1[s & 3], acc[s >> 2][s & 3]);
#pragma unroll
    for (int s = 16; s < 28; ++s) { __builtin_amdgcn_sched_group_barrier(0x008, 1, 0); __builtin_amdgcn_sched_group_barrier(0x002, 10, 0); }
    sched0();
    const auto ro = RSRC(p.act, SEQ * FF * 2);
    auto silu2 = [&](f32x2 g, f32x2 u, int n) {
        const f32x2 x = g * c1[n], d = (f32x2){__builtin_amdgcn_exp2f(x[0]), __builtin_amdgcn_exp2f(x[1])} * ve[n] + ve[n];
        const f32x2 r = g * u * (f32x2){__builtin_amdgcn_rcpf(d[0]), __builtin_amdgcn_rcpf(d[1])};
        return pack_bf16(r[0], r[1]);
    };
#pragma unroll
    for (int j = 0; j < 3; ++j)
#pragma unroll
        for (int np = 0; np < 2; ++np) {
            unsigned o[2][2];
#pragma unroll
            for (int i = 0; i < 2; ++i) { const int n = 2 * np + i; const f32x4 g = acc[2 * j][n] - mean[n] * csv[2 * j], u = acc[2 * j + 1][n] - mean[n] * csv[2 * j + 1];
                o[i][0] = silu2((f32x2){g[0], g[1]}, (f32x2){u[0], u[1]}, n); o[i][1] = silu2((f32x2){g[2], g[3]}, (f32x2){u[2], u[3]}, n); }
            swap16u(o[0][0], o[1][0]); swap16u(o[0][1], o[1][1]);
            __builtin_amdgcn_raw_buffer_store_b128((u32x4){o[0][0], o[0][1], o[1][0], o[1][1]}, ro, (unsigned)(((tm * 14 + wm * 8 + 2 * j + (q >> 1)) * SEQ + tn * 256 + wn * 64 + np * 32 + (lane & 31)) * 16), 0, SC1);
        }
    {
        unsigned o[4];
#pragma unroll
        for (int n = 0; n < 4; ++n) {
            const f32x4 v = acc[6][n] - mean[n] * csv[6];
            float g0 = v[0], g1 = v[1], u0 = v[2], u1 = v[3];
            swap32(g0, u0); swap32(g1, u1);
            o[n] = silu2((f32x2){g0, g1}, (f32x2){u0, u1}, n);
        }
        swap16u(o[0], o[1]); swap16u(o[2], o[3]); swap32u(o[0], o[2]); swap32u(o[1], o[3]);
        __builtin_amdgcn_raw_buffer_store_b128((u32x4){o[0], o[2], o[1], o[3]}, ro, (unsigned)(((tm * 14 + 6 + wm) * SEQ + tn * 256 + wn * 64 + lane) * 16), 0, SC1);
    }
    __builtin_amdgcn_s_setprio(0);
    if constexpr (GRP == 1) { wait_vm<0>(); bar(); }
}
#endif

__global__ __launch_bounds__(512, 2) void fused_k(const Params p) {
#if defined(__HIP_DEVICE_COMPILE__)
    __shared__ __attribute__((aligned(16))) unsigned char lds_raw[5 * 32768];
    LAS unsigned char* lds = (LAS unsigned char*)lds_raw;
    const int wid = __builtin_amdgcn_readfirstlane(threadIdx.x >> 6);
    u64 ep = 0;
    u32x2 stv = {0u, 0u}; unsigned sto = 0x7ffffff0u;
    if (wid < 4) { g128_compute<1>(p, lds, wid, ep, stv, sto); g128_compute<2>(p, lds, wid, ep, stv, sto); gemm3_body<0>(p, lds, wid, ep, stv, sto); g128_compute<4>(p, lds, wid, ep, stv, sto); }
    else { g128_load<1, false>(p, lds, wid, ep); g128_load<2, false>(p, lds, wid, ep); gemm3_body<1>(p, lds, wid, ep, stv, sto); g128_load<4, false>(p, lds, wid, ep); }
#endif
}
__global__ void tail_k() {}

extern "C" void kernel_launch(void* const* d_in, const int* in_sizes, int n_in, void* d_out, int out_size, void* d_ws, size_t ws_size, hipStream_t stream) {
    Params p;
    p.x = (const u16*)d_in[0]; p.cre = (const u16*)d_in[1]; p.cim = (const u16*)d_in[2]; p.wre = (const u16*)d_in[3]; p.wim = (const u16*)d_in[4];
    p.wg = (const u16*)d_in[6]; p.wu = (const u16*)d_in[7]; p.wd = (const u16*)d_in[8];
    p.out = (u16*)d_out;
    char* ws = (char*)d_ws;
    p.abT = (u16*)ws; p.h = (u16*)(ws + (8u << 20)); p.act = (u16*)(ws + (16u << 20));
    p.xch = (u16*)(ws + (32u << 20)); p.flags = (unsigned long long*)(ws + (40u << 20)); p.csum = (float*)(ws + (40u << 20) + (512u << 10)); p.ln1 = (float*)(ws + (40u << 20) + (768u << 10));
    fused_k<<<256, 512, 0, stream>>>(p);
}
```
